```python
import torch

def _softmax(x, axis=-1):
    return torch.softmax(x, dim=axis)
def _gelu(x):
    return torch.nn.functional.gelu(x, approximate="tanh")
def _silu(x):
    return torch.nn.functional.silu(x)
def _sigmoid(x):
    return torch.sigmoid(x)
def _layernorm(x, axis=-1, eps=1e-5):
    m = x.mean(dim=axis, keepdim=True)
    v = x.var(dim=axis, keepdim=True, unbiased=False)
    return (x - m) / torch.sqrt(v + eps)
def _scatter_add(tgt, idx, src, axis=0):
    return tgt.clone().index_add_(axis, idx, src)
def _segment_sum(src, seg, n):
    out = torch.zeros((n,) + src.shape[1:], dtype=src.dtype)
    return out.index_add_(0, seg, src)
def _pad(x, pw):
    flat = tuple(v for pair in reversed(pw) for v in pair)
    return torch.nn.functional.pad(x, flat)

kernel_name = 'concat_heads__mha_block_cached__gqa_block_cached__near_ridge'

def setup_inputs():

    _w = (lambda a: (lambda t: t.to(torch.bfloat16) if t.is_floating_point() else t)(torch.as_tensor(a)))
    def _A_setup():
        B, Sq, D = 16, 112, 512
        H, Sk, Dh = 32, 512, 128
        return {
            "x": _w((lambda *s: torch.randn(*s, dtype=torch.bfloat16) * 0.8)(B, Sq, D)),
            "k_cache": _w((lambda *s: torch.randn(*s, dtype=torch.bfloat16) * 0.8)(B, H, Sk, Dh)),
            "v_cache": _w((lambda *s: torch.randn(*s, dtype=torch.bfloat16) * 0.05)(B, H, Sk, Dh)),
            "wq": _w((lambda *s: torch.randn(*s, dtype=torch.bfloat16) * 0.8)(D, H * Dh) * (D ** -0.5)),
            "wo": _w((lambda *s: torch.randn(*s, dtype=torch.bfloat16) * 0.05)(H * Dh, D) * ((H * Dh) ** -0.5)),
        }
    _A_in = _A_setup()

    def _B_setup():
        B, Sq, D = 16, 112, 512
        Hq, Hkv, Sk, Dh = 32, 32, 512, 128
        assert Hq % Hkv == 0, (Hq, Hkv)
        return {
            "x": _w((lambda *s: torch.randn(*s, dtype=torch.bfloat16) * 0.8)(B, Sq, D)),
            "k_cache": _w((lambda *s: torch.randn(*s, dtype=torch.bfloat16) * 0.8)(B, Hkv, Sk, Dh)),
            "v_cache": _w((lambda *s: torch.randn(*s, dtype=torch.bfloat16) * 0.05)(B, Hkv, Sk, Dh)),
            "wq": _w((lambda *s: torch.randn(*s, dtype=torch.bfloat16) * 0.8)(D, Hq * Dh) * (D ** -0.5)),
            "wo": _w((lambda *s: torch.randn(*s, dtype=torch.bfloat16) * 0.05)(Hq * Dh, D) * ((Hq * Dh) ** -0.5)),
        }
    _B_in = _B_setup()

    return {
        "A_x": _A_in["x"],
        "A_k_cache": _A_in["k_cache"],
        "A_v_cache": _A_in["v_cache"],
        "A_wq": _A_in["wq"],
        "A_wo": _A_in["wo"],
        "B_x": _B_in["x"],
        "B_k_cache": _B_in["k_cache"],
        "B_v_cache": _B_in["v_cache"],
        "B_wq": _B_in["wq"],
        "B_wo": _B_in["wo"],
    }

def reference(A_x, A_k_cache, A_v_cache, A_wq, A_wo, B_x, B_k_cache, B_v_cache, B_wq, B_wo):
    def _A_ref(x, k_cache, v_cache, wq, wo):
        B, Sq, D = x.shape
        H, Dh = 32, 128
        q = (lambda x, *d: x.permute(*d))((x @ wq).reshape(B, Sq, H, Dh), 0, 2, 1, 3)
        scores = torch.einsum("bhqd,bhkd->bhqk", q, k_cache) * (Dh ** -0.5)
        p = _softmax(scores, axis=-1)
        o = torch.einsum("bhqk,bhkd->bhqd", p, v_cache)
        return (lambda x, *d: x.permute(*d))(o, 0, 2, 1, 3).reshape(B, Sq, H * Dh) @ wo

    def _B_ref(x, k_cache, v_cache, wq, wo):
        B, Sq, D = x.shape
        Hq, Dh = 32, 128
        Hkv = k_cache.shape[1]
        rep = Hq // Hkv
        q = (lambda x, *d: x.permute(*d))((x @ wq).reshape(B, Sq, Hq, Dh), 0, 2, 1, 3)
        k = (lambda x, n, axis: torch.repeat_interleave(x, n, dim=axis))(k_cache, rep, axis=1)
        v = (lambda x, n, axis: torch.repeat_interleave(x, n, dim=axis))(v_cache, rep, axis=1)
        scores = torch.einsum("bhqd,bhkd->bhqk", q, k) * (Dh ** -0.5)
        p = _softmax(scores, axis=-1)
        o = torch.einsum("bhqk,bhkd->bhqd", p, v)
        return (lambda x, *d: x.permute(*d))(o, 0, 2, 1, 3).reshape(B, Sq, Hq * Dh) @ wo

    a = _A_ref(A_x, A_k_cache, A_v_cache, A_wq, A_wo)
    b = _B_ref(B_x, B_k_cache, B_v_cache, B_wq, B_wo)
    return torch.concatenate([a, b], axis=-1)
```

```cpp
#include <hip/hip_runtime.h>
#include <hip/hip_bf16.h>
#include <stdint.h>

typedef short bf16x8 __attribute__((ext_vector_type(8)));
typedef float f32x4 __attribute__((ext_vector_type(4)));
typedef unsigned short u16;

constexpr int B = 16, SQ = 112, D = 512, H = 32, SK = 512, DH = 128;

__device__ __forceinline__ u16 f2bf(float f) {
  __hip_bfloat16 b = __float2bfloat16(f);
  return *reinterpret_cast<u16*>(&b);
}
__device__ __forceinline__ float bf2f(u16 v) { return __uint_as_float(((unsigned)v) << 16); }

__global__ void gemm_naive(const u16* __restrict__ A, int lda, const u16* __restrict__ Bm, int ldb, u16* __restrict__ C, int ldc, int K) {
  int lane = threadIdx.x & 63, wid = threadIdx.x >> 6;
  int m0 = blockIdx.y * 16;
  int n0 = blockIdx.x * 64 + wid * 16;
  int r = lane & 15, g = lane >> 4;
  f32x4 acc = {0, 0, 0, 0};
  for (int k0 = 0; k0 < K; k0 += 32) {
    bf16x8 a = *reinterpret_cast<const bf16x8*>(A + (size_t)(m0 + r) * lda + k0 + 8 * g);
    bf16x8 b;
    for (int j = 0; j < 8; ++j) b[j] = (short)Bm[(size_t)(k0 + 8 * g + j) * ldb + n0 + r];
    acc = __builtin_amdgcn_mfma_f32_16x16x32_bf16(a, b, acc, 0, 0, 0);
  }
  for (int j = 0; j < 4; ++j) C[(size_t)(m0 + 4 * g + j) * ldc + n0 + r] = f2bf(acc[j]);
}

__global__ __launch_bounds__(256) void attn_naive(const u16* __restrict__ Q, const u16* __restrict__ Kc, const u16* __restrict__ Vc, u16* __restrict__ O) {
  __shared__ float S[16][SK + 4];
  __shared__ u16 P[16][SK + 8];
  __shared__ float rs[16];
  int lane = threadIdx.x & 63, wid = threadIdx.x >> 6;
  int qt = blockIdx.x, h = blockIdx.y, b = blockIdx.z;
  int r = lane & 15, g = lane >> 4;
  const u16* q = Q + (size_t)(b * SQ + qt * 16) * (H * DH) + h * DH;
  const u16* k = Kc + (size_t)(b * H + h) * SK * DH;
  const u16* v = Vc + (size_t)(b * H + h) * SK * DH;
  for (int kt = wid; kt < SK / 16; kt += 4) {
    f32x4 acc = {0, 0, 0, 0};
    for (int d0 = 0; d0 < DH; d0 += 32) {
      bf16x8 a = *reinterpret_cast<const bf16x8*>(q + (size_t)r * (H * DH) + d0 + 8 * g);
      bf16x8 bb = *reinterpret_cast<const bf16x8*>(k + (size_t)(kt * 16 + r) * DH + d0 + 8 * g);
      acc = __builtin_amdgcn_mfma_f32_16x16x32_bf16(a, bb, acc, 0, 0, 0);
    }
    for (int j = 0; j < 4; ++j) S[4 * g + j][kt * 16 + r] = acc[j] * 0.08838834764831845f;
  }
  __syncthreads();
  for (int rr = wid * 4; rr < wid * 4 + 4; ++rr) {
    float m = -1e30f;
    for (int j = lane; j < SK; j += 64) m = fmaxf(m, S[rr][j]);
    for (int o = 32; o; o >>= 1) m = fmaxf(m, __shfl_xor(m, o));
    float s = 0;
    for (int j = lane; j < SK; j += 64) { float e = __expf(S[rr][j] - m); S[rr][j] = e; s += e; }
    for (int o = 32; o; o >>= 1) s += __shfl_xor(s, o);
    float inv = 1.f / s;
    for (int j = lane; j < SK; j += 64) P[rr][j] = f2bf(S[rr][j] * inv);
  }
  __syncthreads();
  for (int dt = wid; dt < DH / 16; dt += 4) {
    f32x4 acc = {0, 0, 0, 0};
#pragma unroll 2
    for (int k0 = 0; k0 < SK; k0 += 32) {
      bf16x8 a = *reinterpret_cast<const bf16x8*>(&P[r][k0 + 8 * g]);
      bf16x8 bb;
      for (int j = 0; j < 8; ++j) bb[j] = (short)v[(size_t)(k0 + 8 * g + j) * DH + dt * 16 + r];
      acc = __builtin_amdgcn_mfma_f32_16x16x32_bf16(a, bb, acc, 0, 0, 0);
    }
    for (int j = 0; j < 4; ++j) O[(size_t)(b * SQ + qt * 16 + 4 * g + j) * (H * DH) + h * DH + dt * 16 + r] = f2bf(acc[j]);
  }
}

extern "C" void kernel_launch(void* const* d_in, const int* in_sizes, int n_in, void* d_out, int out_size, void* d_ws, size_t ws_size,
                              hipStream_t stream) {
  u16* ws = (u16*)d_ws;
  u16* Q = ws;
  u16* O = ws + (size_t)B * SQ * H * DH;
  for (int br = 0; br < 2; ++br) {
    const u16* x = (const u16*)d_in[5 * br + 0];
    const u16* k = (const u16*)d_in[5 * br + 1];
    const u16* v = (const u16*)d_in[5 * br + 2];
    const u16* wq = (const u16*)d_in[5 * br + 3];
    const u16* wo = (const u16*)d_in[5 * br + 4];
    gemm_naive<<<dim3(H * DH / 64, B * SQ / 16), 256, 0, stream>>>(x, D, wq, H * DH, Q, H * DH, D);
    attn_naive<<<dim3(SQ / 16, H, B), 256, 0, stream>>>(Q, k, v, O);
    gemm_naive<<<dim3(D / 64, B * SQ / 16), 256, 0, stream>>>(O, H * DH, wo, D, (u16*)d_out + br * D, 2 * D, H * DH);
  }
}
```

```cpp
#include <hip/hip_runtime.h>
#include <hip/hip_bf16.h>
#include <stdint.h>

typedef unsigned short u16;
typedef short bf16x8 __attribute__((ext_vector_type(8)));
typedef float f32x4 __attribute__((ext_vector_type(4)));
typedef unsigned u32x2 __attribute__((ext_vector_type(2)));
typedef unsigned u32x4 __attribute__((ext_vector_type(4)));

constexpr int NB = 16, SQ = 112, DM = 512, NH = 32, SK = 512, DH = 128;
constexpr int O_ITEM = 7 * 4 * 64 * 8;
constexpr int LDS1 = 163840;
constexpr int FLAG_OFF = 159744;
constexpr int P_ITEM = 16 * 7 * 64 * 8;
typedef __attribute__((address_space(1))) unsigned gu32;
#define RLX_AGENT __ATOMIC_RELAXED, __HIP_MEMORY_SCOPE_AGENT
constexpr size_t P_OFF = (size_t)32 * NH * O_ITEM;

__device__ __forceinline__ float bf2f(u16 v) { return __uint_as_float(((unsigned)v) << 16); }
__device__ __forceinline__ u16 f2bf(float f) { __hip_bfloat16 b = __float2bfloat16(f); return *reinterpret_cast<u16*>(&b); }
__device__ __forceinline__ unsigned rfl(unsigned x) { return __builtin_amdgcn_readfirstlane(x); }

__device__ __forceinline__ void wg_map(unsigned wg, unsigned& grp, unsigned& hg) {
  const unsigned x = wg & 7, i = wg >> 3;
  hg = 2 * (x & 3) + (i & 1);
  grp = (x >> 2) * 16 + (i >> 1);
}

__device__ __forceinline__ void safe_head(const u16* __restrict__ x, const u16* __restrict__ wq, const u16* __restrict__ K, const u16* __restrict__ V,
                                       u16* __restrict__ Oit, int h, char* lds) {
  u16* Qs = (u16*)lds;
  float* Ps = (float*)(lds + SQ * DH * 2);
  const int tid = threadIdx.x, lane = tid & 63, wid = tid >> 6;
  for (int e = tid; e < SQ * DH; e += 256) {
    const int q = e >> 7, d = e & 127;
    float a = 0.f;
    for (int k = 0; k < DM; ++k) a = fmaf(bf2f(x[q * DM + k]), bf2f(wq[(size_t)k * (NH * DH) + h * DH + d]), a);
    Qs[e] = f2bf(a * 0.12751743082f);
  }
  __syncthreads();
  float* P = Ps + wid * SK;
  for (int q = wid; q < SQ; q += 4) {
    float m = -INFINITY;
    for (int i = 0; i < SK / 64; ++i) {
      const int key = lane + 64 * i;
      float s = 0.f;
      for (int d = 0; d < DH; ++d) s = fmaf(bf2f(Qs[q * DH + d]), bf2f(K[key * DH + d]), s);
      P[key] = s; m = fmaxf(m, s);
      if (s != s) m = s;
    }
    for (int o = 32; o; o >>= 1) { const float t = __shfl_xor(m, o); m = (t != t || m != m) ? (m + t) : fmaxf(m, t); }
    float l = 0.f;
    for (int i = 0; i < SK / 64; ++i) {
      const int key = lane + 64 * i;
      const float p = bf2f(f2bf(exp2f(P[key] - m)));
      P[key] = p; l += p;
    }
    for (int o = 32; o; o >>= 1) l += __shfl_xor(l, o);
    __builtin_amdgcn_s_waitcnt(0);
    __builtin_amdgcn_wave_barrier();
    const float inv = 1.f / l;
    for (int dd = 0; dd < 2; ++dd) {
      const int d = lane + 64 * dd;
      float a = 0.f;
      for (int key = 0; key < SK; ++key) a = fmaf(P[key], bf2f(V[key * DH + d]), a);
      const int j = q >> 4, s = d >> 5, r = d & 31, g = (r & 15) >> 2, e = (r & 3) + (r >= 16 ? 4 : 0);
      Oit[((j * 4 + s) * 64 + (q & 15) + 16 * g) * 8 + e] = f2bf(a * inv);
    }
    __builtin_amdgcn_s_waitcnt(0);
    __builtin_amdgcn_wave_barrier();
  }
  __syncthreads();
}

__device__ __forceinline__ void safe_block(const u16* __restrict__ x, const u16* __restrict__ wq, const u16* __restrict__ K, const u16* __restrict__ V,
                                           const u16* __restrict__ wo, u16* __restrict__ O, u16* __restrict__ P, int hg, char* lds) {
  for (int hl = 0; hl < 4; ++hl)
    safe_head(x, wq, K + (size_t)hl * SK * DH, V + (size_t)hl * SK * DH, O + (size_t)hl * O_ITEM, hg * 4 + hl, lds);
  __threadfence_block();
  __syncthreads();
  for (int c2 = threadIdx.x; c2 < 16 * 7 * 64 * 4; c2 += 256) {
    unsigned pk = 0;
    for (int u = 0; u < 2; ++u) {
      const int c = 2 * c2 + u;
      const int e = c & 7, lane = (c >> 3) & 63, j = (c >> 9) % 7, pm = (c >> 9) / 7;
      const int g = lane >> 4, n = 32 * pm + (e < 4 ? 4 * g + e : 16 + 4 * g + e - 4), q = lane & 15;
      float a = 0.f;
      for (int k = 0; k < 512; ++k) {
        const int hl = k >> 7, d = k & 127, s = d >> 5, r = d & 31, gg = (r & 15) >> 2, ee = (r & 3) + (r >= 16 ? 4 : 0);
        a = fmaf(bf2f(O[(size_t)hl * O_ITEM + ((j * 4 + s) * 64 + q + 16 * gg) * 8 + ee]), bf2f(wo[(size_t)(hg * 512 + k) * DM + n]), a);
      }
      pk |= (unsigned)f2bf(a) << (16 * u);
    }
    __hip_atomic_store((gu32*)P + c2, pk, RLX_AGENT);
  }
}

__global__ __launch_bounds__(256, 1) void attn_kernel(const u16* __restrict__ xA, const u16* __restrict__ kA, const u16* __restrict__ vA, const u16* __restrict__ wqA,
                                                      const u16* __restrict__ woA,
                                                      const u16* __restrict__ xB, const u16* __restrict__ kB, const u16* __restrict__ vB, const u16* __restrict__ wqB,
                                                      const u16* __restrict__ woB, u16* __restrict__ ws) {
  extern __shared__ __attribute__((aligned(16))) char lds[];
  unsigned grp, hg;
  wg_map(blockIdx.x, grp, hg);
  const unsigned br = grp >> 4, b = grp & 15;
  const u16* x = (br ? xB : xA) + (size_t)b * SQ * DM;
  const u16* K = (br ? kB : kA) + ((size_t)b * NH + hg * 4) * SK * DH;
  const u16* V = (br ? vB : vA) + ((size_t)b * NH + hg * 4) * SK * DH;
  const u16* wq = (br ? wqB : wqA);
  const u16* wo = (br ? woB : woA);
  u16* O = ws + ((size_t)grp * NH + hg * 4) * O_ITEM;
  u16* P = ws + P_OFF + ((size_t)grp * 8 + hg) * P_ITEM;
  const unsigned wid = rfl(threadIdx.x >> 6);
  const unsigned head = hg * 4 + wid;
  const unsigned nw = DM * NH * DH * 2 - head * DH * 2;
  const unsigned ldsb = rfl((unsigned)(size_t)lds);
  const u16* kp = K + (size_t)wid * SK * DH;
  const u16* vp = V + (size_t)wid * SK * DH;
  const u16* wp = wq + head * DH;
  const u16* wop = wo + (size_t)hg * 512 * DM;
  const u16* pto = wop + (size_t)(b * 32 + wid * 8) * DM;
  const u16* wpa = wq + (hg * 4 + (wid & 1)) * DH;
  const unsigned nwa = DM * NH * DH * 2 - (hg * 4 + (wid & 1)) * DH * 2;
  unsigned bad;
  asm volatile(
    "s_mov_b64 s[52:53], %[xp]\n"
    "s_and_b32 s53, s53, 0xffff\n"
    "s_mov_b32 s54, 114688\n"
    "s_mov_b32 s55, 0x20000\n"
    "s_mov_b64 s[48:49], %[wpa]\n"
    "s_and_b32 s49, s49, 0xffff\n"
    "s_mov_b32 s50, %[nwa]\n"
    "s_mov_b32 s51, 0x20000\n"
    "s_mov_b64 s[40:41], %[kp]\n"
    "s_and_b32 s41, s41, 0xffff\n"
    "s_mov_b32 s42, 0x20000\n"
    "s_mov_b32 s43, 0x20000\n"
    "s_mov_b64 s[44:45], %[vp]\n"
    "s_and_b32 s45, s45, 0xffff\n"
    "s_mov_b32 s46, 0x20000\n"
    "s_mov_b32 s47, 0x20000\n"
    "s_lshl_b32 s64, %[wid], 15\n"
    "s_add_u32 s64, s64, %[lds]\n"
    "s_lshl_b32 s65, %[wid], 12\n"
    "s_add_u32 s65, s65, %[lds]\n"
    "s_add_u32 s65, s65, 131072\n"
    "s_lshl_b32 s72, %[wid], 12\n"
    "s_add_u32 s72, s72, %[lds]\n"
    "s_add_u32 s72, s72, 16384\n"
    "s_mov_b32 s66, 0\n"
    "s_mov_b32 s67, 0\n"
    "s_mov_b32 s68, 0\n"
    "s_lshl_b32 s69, %[wid], 15\n"
    "s_and_b32 s88, %[wid], 1\n"
    "s_add_u32 s88, s88, 2\n"
    "s_lshl_b32 s88, s88, 15\n"
    "s_add_u32 s88, s88, %[lds]\n"
    "s_mov_b32 s71, s88\n"
    "s_lshr_b32 s74, %[wid], 1\n"
    "s_lshl_b32 s89, s74, 17\n"
    "s_lshl_b32 s74, s74, 12\n"
    "s_add_u32 s88, s88, s74\n"
    "v_mbcnt_lo_u32_b32 v25, -1, 0\n"
    "v_mbcnt_hi_u32_b32 v25, -1, v25\n"
    "v_and_b32 v34, 15, v25\n"
    "v_lshrrev_b32 v35, 4, v25\n"
    "v_and_b32 v32, 7, v25\n"
    "v_xor_b32 v32, v32, v35\n"
    "v_lshlrev_b32 v32, 4, v32\n"
    "v_lshrrev_b32 v33, 3, v25\n"
    "v_lshl_add_u32 v28, v33, 10, v32\n"
    "v_xor_b32 v29, 64, v28\n"
    "v_lshlrev_b32 v32, 1, v35\n"
    "v_xor_b32 v32, v32, v34\n"
    "v_lshlrev_b32 v32, 4, v32\n"
    "v_lshl_add_u32 v26, v35, 13, v32\n"
    "v_xor_b32 v27, 128, v26\n"
    "v_lshl_add_u32 v20, v35, 8, v32\n"
    "v_xor_b32 v21, 128, v20\n"
    "v_add_u32 v22, 4096, v20\n"
    "v_add_u32 v23, 4096, v21\n"
    "s_cmp_ge_u32 %[wid], 2\n"
    "s_cbranch_scc1 .LL_start_%=\n"
    "s_add_u32 m0, s72, 0\n"
    "s_nop 0\n"
    "buffer_load_dwordx4 v28, s[52:55], s69 offen offset:0 lds\n"
    "s_add_u32 s76, s69, 7168\n"
    "buffer_load_dwordx4 v29, s[52:55], s76 offen offset:1024 lds\n"
    "s_add_u32 s76, s69, 14336\n"
    "buffer_load_dwordx4 v28, s[52:55], s76 offen offset:2048 lds\n"
    "s_add_u32 s76, s69, 21504\n"
    "buffer_load_dwordx4 v29, s[52:55], s76 offen offset:3072 lds\n"
    "s_add_u32 s69, s69, 128\n"
    "s_add_u32 m0, s88, 24576\n"
    "s_nop 0\n"
    "buffer_load_dwordx4 v26, s[48:51], s89 offen offset:0 lds\n"
    "s_add_u32 s74, s89, 31744\n"
    "buffer_load_dwordx4 v26, s[48:51], s74 offen offset:1024 lds\n"
    "s_add_u32 s74, s89, 63488\n"
    "buffer_load_dwordx4 v27, s[48:51], s74 offen offset:2048 lds\n"
    "s_add_u32 s74, s89, 95232\n"
    "buffer_load_dwordx4 v27, s[48:51], s74 offen offset:3072 lds\n"
    "s_add_u32 s89, s89, 262144\n"
    "s_add_u32 m0, s88, 0\n"
    "s_nop 0\n"
    "buffer_load_dwordx4 v26, s[48:51], s89 offen offset:0 lds\n"
    "s_add_u32 s74, s89, 31744\n"
    "buffer_load_dwordx4 v26, s[48:51], s74 offen offset:1024 lds\n"
    "s_add_u32 s74, s89, 63488\n"
    "buffer_load_dwordx4 v27, s[48:51], s74 offen offset:2048 lds\n"
    "s_add_u32 s74, s89, 95232\n"
    "buffer_load_dwordx4 v27, s[48:51], s74 offen offset:3072 lds\n"
    "s_add_u32 s89, s89, 262144\n"
    "s_add_u32 m0, s72, 32768\n"
    "s_nop 0\n"
    "buffer_load_dwordx4 v28, s[52:55], s69 offen offset:0 lds\n"
    "s_add_u32 s76, s69, 7168\n"
    "buffer_load_dwordx4 v29, s[52:55], s76 offen offset:1024 lds\n"
    "s_add_u32 s76, s69, 14336\n"
    "buffer_load_dwordx4 v28, s[52:55], s76 offen offset:2048 lds\n"
    "s_add_u32 s76, s69, 21504\n"
    "buffer_load_dwordx4 v29, s[52:55], s76 offen offset:3072 lds\n"
    "s_add_u32 s69, s69, 128\n"
    "s_add_u32 m0, s88, 8192\n"
    "s_nop 0\n"
    "buffer_load_dwordx4 v26, s[48:51], s89 offen offset:0 lds\n"
    "s_add_u32 s74, s89, 31744\n"
    "buffer_load_dwordx4 v26, s[48:51], s74 offen offset:1024 lds\n"
    "s_add_u32 s74, s89, 63488\n"
    "buffer_load_dwordx4 v27, s[48:51], s74 offen offset:2048 lds\n"
    "s_add_u32 s74, s89, 95232\n"
    "buffer_load_dwordx4 v27, s[48:51], s74 offen offset:3072 lds\n"
    "s_add_u32 s89, s89, 262144\n"
    "s_add_u32 m0, s88, 16384\n"
    "s_nop 0\n"
    "buffer_load_dwordx4 v26, s[48:51], s89 offen offset:0 lds\n"
    "s_add_u32 s74, s89, 31744\n"
    "buffer_load_dwordx4 v26, s[48:51], s74 offen offset:1024 lds\n"
    "s_add_u32 s74, s89, 63488\n"
    "buffer_load_dwordx4 v27, s[48:51], s74 offen offset:2048 lds\n"
    "s_add_u32 s74, s89, 95232\n"
    "buffer_load_dwordx4 v27, s[48:51], s74 offen offset:3072 lds\n"
    "s_add_u32 s89, s89, 262144\n"
    "v_and_b32 v32, 7, v34\n"
    "v_lshlrev_b32 v32, 1, v32\n"
    "v_xor_b32 v32, v32, v35\n"
    "v_lshlrev_b32 v32, 4, v32\n"
    "v_lshl_add_u32 v32, v34, 8, v32\n"
    "v_xor_b32 v16, 0, v32\n"
    "v_add_u32 v16, s64, v16\n"
    "v_xor_b32 v17, 64, v32\n"
    "v_add_u32 v17, s64, v17\n"
    "v_xor_b32 v18, 128, v32\n"
    "v_add_u32 v18, s64, v18\n"
    "v_xor_b32 v19, 192, v32\n"
    "v_add_u32 v19, s64, v19\n"
    "v_lshrrev_b32 v37, 2, v34\n"
    "v_and_b32 v38, 3, v34\n"
    "v_and_b32 v32, 1, v35\n"
    "v_lshl_add_u32 v32, v32, 2, v37\n"
    "v_lshlrev_b32 v32, 1, v32\n"
    "v_lshrrev_b32 v33, 1, v38\n"
    "v_xor_b32 v32, v32, v33\n"
    "v_lshlrev_b32 v32, 4, v32\n"
    "v_and_b32 v33, 1, v38\n"
    "v_lshl_add_u32 v32, v33, 3, v32\n"
    "v_lshl_add_u32 v33, v35, 3, v37\n"
    "v_lshlrev_b32 v33, 8, v33\n"
    "v_xor_b32 v8, 0, v32\n"
    "v_add3_u32 v8, v8, v33, s71\n"
    "v_xor_b32 v9, 32, v32\n"
    "v_add3_u32 v9, v9, v33, s71\n"
    "v_xor_b32 v10, 64, v32\n"
    "v_add3_u32 v10, v10, v33, s71\n"
    "v_xor_b32 v11, 96, v32\n"
    "v_add3_u32 v11, v11, v33, s71\n"
    "v_xor_b32 v12, 128, v32\n"
    "v_add3_u32 v12, v12, v33, s71\n"
    "v_xor_b32 v13, 160, v32\n"
    "v_add3_u32 v13, v13, v33, s71\n"
    "v_xor_b32 v14, 192, v32\n"
    "v_add3_u32 v14, v14, v33, s71\n"
    "v_xor_b32 v15, 224, v32\n"
    "v_add3_u32 v15, v15, v33, s71\n"
    "v_lshrrev_b32 v32, 1, v34\n"
    "v_xor_b32 v32, v32, v35\n"
    "v_lshlrev_b32 v32, 4, v32\n"
    "v_lshl_add_u32 v32, v34, 7, v32\n"
    "s_add_u32 s74, %[lds], 131072\n"
    "s_add_u32 s76, %[lds], 16384\n"
    "v_add_u32 v30, s74, v32\n"
    "v_add_u32 v6, s76, v32\n"
    "v_xor_b32 v32, 64, v32\n"
    "v_add_u32 v31, s74, v32\n"
    "v_add_u32 v7, s76, v32\n"
    "v_lshlrev_b32 v24, 4, v25\n"
    "s_mov_b32 s80, 0x3e0293ee\n"
    "s_mov_b32 s81, 0x3e0293ee\n"
    "s_mov_b32 s75, 0\n"
    "s_mov_b64 s[82:83], 0\n"
    "s_mov_b32 s93, 0x53800000\n"
    "v_mov_b64 v[40:41], 0\n"
    "v_mov_b64 v[42:43], 0\n"
    "v_mov_b64 v[44:45], 0\n"
    "v_mov_b64 v[46:47], 0\n"
    "v_mov_b64 v[48:49], 0\n"
    "v_mov_b64 v[50:51], 0\n"
    "v_mov_b64 v[52:53], 0\n"
    "v_mov_b64 v[54:55], 0\n"
    "v_mov_b64 v[56:57], 0\n"
    "v_mov_b64 v[58:59], 0\n"
    "v_mov_b64 v[60:61], 0\n"
    "v_mov_b64 v[62:63], 0\n"
    "v_mov_b64 v[64:65], 0\n"
    "v_mov_b64 v[66:67], 0\n"
    "v_mov_b64 v[68:69], 0\n"
    "v_mov_b64 v[70:71], 0\n"
    "v_mov_b64 v[72:73], 0\n"
    "v_mov_b64 v[74:75], 0\n"
    "v_mov_b64 v[76:77], 0\n"
    "v_mov_b64 v[78:79], 0\n"
    "v_mov_b64 v[80:81], 0\n"
    "v_mov_b64 v[82:83], 0\n"
    "v_mov_b64 v[84:85], 0\n"
    "v_mov_b64 v[86:87], 0\n"
    "v_mov_b64 v[88:89], 0\n"
    "v_mov_b64 v[90:91], 0\n"
    "v_mov_b64 v[92:93], 0\n"
    "v_mov_b64 v[94:95], 0\n"
    "v_mov_b64 v[96:97], 0\n"
    "v_mov_b64 v[98:99], 0\n"
    "v_mov_b64 v[100:101], 0\n"
    "v_mov_b64 v[102:103], 0\n"
    "v_mov_b64 v[104:105], 0\n"
    "v_mov_b64 v[106:107], 0\n"
    "v_mov_b64 v[108:109], 0\n"
    "v_mov_b64 v[110:111], 0\n"
    "v_mov_b64 v[112:113], 0\n"
    "v_mov_b64 v[114:115], 0\n"
    "v_mov_b64 v[116:117], 0\n"
    "v_mov_b64 v[118:119], 0\n"
    "v_mov_b64 v[120:121], 0\n"
    "v_mov_b64 v[122:123], 0\n"
    "v_mov_b64 v[124:125], 0\n"
    "v_mov_b64 v[126:127], 0\n"
    "v_mov_b64 v[128:129], 0\n"
    "v_mov_b64 v[130:131], 0\n"
    "v_mov_b64 v[132:133], 0\n"
    "v_mov_b64 v[134:135], 0\n"
    "v_mov_b64 v[136:137], 0\n"
    "v_mov_b64 v[138:139], 0\n"
    "v_mov_b64 v[140:141], 0\n"
    "v_mov_b64 v[142:143], 0\n"
    "v_mov_b64 v[144:145], 0\n"
    "v_mov_b64 v[146:147], 0\n"
    "v_mov_b64 v[148:149], 0\n"
    "v_mov_b64 v[150:151], 0\n"
    "v_mov_b64 v[152:153], 0\n"
    "v_mov_b64 v[154:155], 0\n"
    "v_mov_b64 v[156:157], 0\n"
    "v_mov_b64 v[158:159], 0\n"
    "v_mov_b64 v[160:161], 0\n"
    "v_mov_b64 v[162:163], 0\n"
    "v_mov_b64 v[164:165], 0\n"
    "v_mov_b64 v[166:167], 0\n"
    "v_mov_b64 v[168:169], 0\n"
    "v_mov_b64 v[170:171], 0\n"
    "v_mov_b64 v[172:173], 0\n"
    "v_mov_b64 v[174:175], 0\n"
    "v_mov_b64 v[176:177], 0\n"
    "v_mov_b64 v[178:179], 0\n"
    "v_mov_b64 v[180:181], 0\n"
    "v_mov_b64 v[182:183], 0\n"
    "v_mov_b64 v[184:185], 0\n"
    "v_mov_b64 v[186:187], 0\n"
    "v_mov_b64 v[188:189], 0\n"
    "v_mov_b64 v[190:191], 0\n"
    "v_mov_b64 v[192:193], 0\n"
    "v_mov_b64 v[194:195], 0\n"
    "v_mov_b64 v[196:197], 0\n"
    "v_mov_b64 v[198:199], 0\n"
    "v_mov_b64 v[200:201], 0\n"
    "v_mov_b64 v[202:203], 0\n"
    "v_mov_b64 v[204:205], 0\n"
    "v_mov_b64 v[206:207], 0\n"
    "v_mov_b64 v[208:209], 0\n"
    "v_mov_b64 v[210:211], 0\n"
    "v_mov_b64 v[212:213], 0\n"
    "v_mov_b64 v[214:215], 0\n"
    "v_mov_b64 v[216:217], 0\n"
    "v_mov_b64 v[218:219], 0\n"
    "v_mov_b64 v[220:221], 0\n"
    "v_mov_b64 v[222:223], 0\n"
    "v_mov_b64 v[224:225], 0\n"
    "v_mov_b64 v[226:227], 0\n"
    "v_mov_b64 v[228:229], 0\n"
    "v_mov_b64 v[230:231], 0\n"
    "v_mov_b64 v[232:233], 0\n"
    "v_mov_b64 v[234:235], 0\n"
    "v_mov_b64 v[236:237], 0\n"
    "v_mov_b64 v[238:239], 0\n"
    "v_mov_b64 v[240:241], 0\n"
    "v_mov_b64 v[242:243], 0\n"
    "v_mov_b64 v[244:245], 0\n"
    "v_mov_b64 v[246:247], 0\n"
    "v_mov_b64 v[248:249], 0\n"
    "v_mov_b64 v[250:251], 0\n"
    "v_mov_b64 v[252:253], 0\n"
    "v_mov_b64 v[254:255], 0\n"
    "v_mov_b64 v[32:33], 0\n"
    "v_mov_b64 v[34:35], 0\n"
    "v_mov_b64 v[36:37], 0\n"
    "v_mov_b64 v[38:39], 0\n"
    "s_waitcnt vmcnt(16)\n"
    "s_barrier\n"
    "ds_read_b64_tr_b16 a[0:1], v8 offset:24576\n"
    "ds_read_b64_tr_b16 a[2:3], v8 offset:25600\n"
    "ds_read_b64_tr_b16 a[4:5], v9 offset:24576\n"
    "ds_read_b64_tr_b16 a[6:7], v9 offset:25600\n"
    "ds_read_b64_tr_b16 a[8:9], v10 offset:24576\n"
    "ds_read_b64_tr_b16 a[10:11], v10 offset:25600\n"
    "ds_read_b64_tr_b16 a[12:13], v11 offset:24576\n"
    "ds_read_b64_tr_b16 a[14:15], v11 offset:25600\n"
    "ds_read_b64_tr_b16 a[16:17], v12 offset:24576\n"
    "ds_read_b64_tr_b16 a[18:19], v12 offset:25600\n"
    "ds_read_b64_tr_b16 a[20:21], v13 offset:24576\n"
    "ds_read_b64_tr_b16 a[22:23], v13 offset:25600\n"
    "ds_read_b64_tr_b16 a[24:25], v14 offset:24576\n"
    "ds_read_b64_tr_b16 a[26:27], v14 offset:25600\n"
    "ds_read_b64_tr_b16 a[28:29], v15 offset:24576\n"
    "ds_read_b64_tr_b16 a[30:31], v15 offset:25600\n"
    "ds_read_b128 a[64:67], v6 offset:0\n"
    "ds_read_b128 a[68:71], v6 offset:2048\n"
    "ds_read_b128 a[72:75], v6 offset:4096\n"
    "ds_read_b128 a[76:79], v6 offset:6144\n"
    "s_add_u32 m0, s65, 0\n"
    "s_nop 0\n"
    "buffer_load_dwordx4 v28, s[52:55], s69 offen offset:0 lds\n"
    "s_add_u32 s76, s69, 7168\n"
    "buffer_load_dwordx4 v29, s[52:55], s76 offen offset:1024 lds\n"
    "s_add_u32 s76, s69, 14336\n"
    "buffer_load_dwordx4 v28, s[52:55], s76 offen offset:2048 lds\n"
    "s_add_u32 s76, s69, 21504\n"
    "buffer_load_dwordx4 v29, s[52:55], s76 offen offset:3072 lds\n"
    "s_add_u32 s69, s69, 128\n"
    "s_add_u32 m0, s65, 16384\n"
    "s_nop 0\n"
    "buffer_load_dwordx4 v28, s[52:55], s69 offen offset:0 lds\n"
    "s_add_u32 s76, s69, 7168\n"
    "buffer_load_dwordx4 v29, s[52:55], s76 offen offset:1024 lds\n"
    "s_add_u32 s76, s69, 14336\n"
    "buffer_load_dwordx4 v28, s[52:55], s76 offen offset:2048 lds\n"
    "s_add_u32 s76, s69, 21504\n"
    "buffer_load_dwordx4 v29, s[52:55], s76 offen offset:3072 lds\n"
    "s_add_u32 s69, s69, 128\n"
    "s_waitcnt lgkmcnt(0)\n"
    "s_waitcnt vmcnt(20)\n"
    "s_barrier\n"
    "v_mfma_f32_16x16x32_bf16 v[32:35], a[0:3], a[64:67], v[32:35]\n"
    "s_add_u32 m0, s88, 24576\n"
    "v_mfma_f32_16x16x32_bf16 v[64:67], a[0:3], a[68:71], v[64:67]\n"
    "ds_read_b64_tr_b16 a[32:33], v8 offset:0\n"
    "v_mfma_f32_16x16x32_bf16 v[96:99], a[0:3], a[72:75], v[96:99]\n"
    "ds_read_b64_tr_b16 a[34:35], v8 offset:1024\n"
    "v_mfma_f32_16x16x32_bf16 v[128:131], a[0:3], a[76:79], v[128:131]\n"
    "ds_read_b64_tr_b16 a[36:37], v9 offset:0\n"
    "v_mfma_f32_16x16x32_bf16 v[36:39], a[4:7], a[64:67], v[36:39]\n"
    "buffer_load_dwordx4 v26, s[48:51], s89 offen offset:0 lds\n"
    "v_mfma_f32_16x16x32_bf16 v[68:71], a[4:7], a[68:71], v[68:71]\n"
    "ds_read_b64_tr_b16 a[38:39], v9 offset:1024\n"
    "v_mfma_f32_16x16x32_bf16 v[100:103], a[4:7], a[72:75], v[100:103]\n"
    "ds_read_b64_tr_b16 a[40:41], v10 offset:0\n"
    "v_mfma_f32_16x16x32_bf16 v[132:135], a[4:7], a[76:79], v[132:135]\n"
    "s_add_u32 s74, s89, 31744\n"
    "ds_read_b64_tr_b16 a[42:43], v10 offset:1024\n"
    "v_mfma_f32_16x16x32_bf16 v[40:43], a[8:11], a[64:67], v[40:43]\n"
    "ds_read_b64_tr_b16 a[44:45], v11 offset:0\n"
    "v_mfma_f32_16x16x32_bf16 v[72:75], a[8:11], a[68:71], v[72:75]\n"
    "buffer_load_dwordx4 v26, s[48:51], s74 offen offset:1024 lds\n"
    "v_mfma_f32_16x16x32_bf16 v[104:107], a[8:11], a[72:75], v[104:107]\n"
    "ds_read_b64_tr_b16 a[46:47], v11 offset:1024\n"
    "v_mfma_f32_16x16x32_bf16 v[136:139], a[8:11], a[76:79], v[136:139]\n"
    "ds_read_b64_tr_b16 a[48:49], v12 offset:0\n"
    "v_mfma_f32_16x16x32_bf16 v[44:47], a[12:15], a[64:67], v[44:47]\n"
    "s_add_u32 s74, s89, 63488\n"
    "v_mfma_f32_16x16x32_bf16 v[76:79], a[12:15], a[68:71], v[76:79]\n"
    "ds_read_b64_tr_b16 a[50:51], v12 offset:1024\n"
    "v_mfma_f32_16x16x32_bf16 v[108:111], a[12:15], a[72:75], v[108:111]\n"
    "ds_read_b64_tr_b16 a[52:53], v13 offset:0\n"
    "v_mfma_f32_16x16x32_bf16 v[140:143], a[12:15], a[76:79], v[140:143]\n"
    "ds_read_b64_tr_b16 a[54:55], v13 offset:1024\n"
    "v_mfma_f32_16x16x32_bf16 v[48:51], a[16:19], a[64:67], v[48:51]\n"
    "buffer_load_dwordx4 v27, s[48:51], s74 offen offset:2048 lds\n"
    "v_mfma_f32_16x16x32_bf16 v[80:83], a[16:19], a[68:71], v[80:83]\n"
    "ds_read_b64_tr_b16 a[56:57], v14 offset:0\n"
    "v_mfma_f32_16x16x32_bf16 v[112:115], a[16:19], a[72:75], v[112:115]\n"
    "ds_read_b64_tr_b16 a[58:59], v14 offset:1024\n"
    "v_mfma_f32_16x16x32_bf16 v[144:147], a[16:19], a[76:79], v[144:147]\n"
    "s_add_u32 s74, s89, 95232\n"
    "v_mfma_f32_16x16x32_bf16 v[52:55], a[20:23], a[64:67], v[52:55]\n"
    "ds_read_b64_tr_b16 a[60:61], v15 offset:0\n"
    "v_mfma_f32_16x16x32_bf16 v[84:87], a[20:23], a[68:71], v[84:87]\n"
    "ds_read_b64_tr_b16 a[62:63], v15 offset:1024\n"
    "v_mfma_f32_16x16x32_bf16 v[116:119], a[20:23], a[72:75], v[116:119]\n"
    "buffer_load_dwordx4 v27, s[48:51], s74 offen offset:3072 lds\n"
    "ds_read_b128 a[92:95], v7 offset:0\n"
    "v_mfma_f32_16x16x32_bf16 v[148:151], a[20:23], a[76:79], v[148:151]\n"
    "ds_read_b128 a[96:99], v7 offset:2048\n"
    "v_mfma_f32_16x16x32_bf16 v[56:59], a[24:27], a[64:67], v[56:59]\n"
    "s_add_u32 s89, s89, 262144\n"
    "v_mfma_f32_16x16x32_bf16 v[88:91], a[24:27], a[68:71], v[88:91]\n"
    "ds_read_b128 a[100:103], v7 offset:4096\n"
    "v_mfma_f32_16x16x32_bf16 v[120:123], a[24:27], a[72:75], v[120:123]\n"
    "ds_read_b128 a[104:107], v7 offset:6144\n"
    "v_mfma_f32_16x16x32_bf16 v[152:155], a[24:27], a[76:79], v[152:155]\n"
    "v_mfma_f32_16x16x32_bf16 v[60:63], a[28:31], a[64:67], v[60:63]\n"
    "v_mfma_f32_16x16x32_bf16 v[92:95], a[28:31], a[68:71], v[92:95]\n"
    "v_mfma_f32_16x16x32_bf16 v[124:127], a[28:31], a[72:75], v[124:127]\n"
    "v_mfma_f32_16x16x32_bf16 v[156:159], a[28:31], a[76:79], v[156:159]\n"
    "s_waitcnt lgkmcnt(0)\n"
    "s_waitcnt vmcnt(16)\n"
    "s_barrier\n"
    "v_mfma_f32_16x16x32_bf16 v[32:35], a[32:35], a[92:95], v[32:35]\n"
    "s_add_u32 m0, s72, 0\n"
    "v_mfma_f32_16x16x32_bf16 v[64:67], a[32:35], a[96:99], v[64:67]\n"
    "ds_read_b64_tr_b16 a[0:1], v8 offset:8192\n"
    "ds_read_b64_tr_b16 a[2:3], v8 offset:9216\n"
    "v_mfma_f32_16x16x32_bf16 v[96:99], a[32:35], a[100:103], v[96:99]\n"
    "buffer_load_dwordx4 v28, s[52:55], s69 offen offset:0 lds\n"
    "v_mfma_f32_16x16x32_bf16 v[128:131], a[32:35], a[104:107], v[128:131]\n"
    "ds_read_b64_tr_b16 a[4:5], v9 offset:8192\n"
    "s_add_u32 s76, s69, 7168\n"
    "v_mfma_f32_16x16x32_bf16 v[36:39], a[36:39], a[92:95], v[36:39]\n"
    "ds_read_b64_tr_b16 a[6:7], v9 offset:9216\n"
    "v_mfma_f32_16x16x32_bf16 v[68:71], a[36:39], a[96:99], v[68:71]\n"
    "buffer_load_dwordx4 v29, s[52:55], s76 offen offset:1024 lds\n"
    "v_mfma_f32_16x16x32_bf16 v[100:103], a[36:39], a[100:103], v[100:103]\n"
    "ds_read_b64_tr_b16 a[8:9], v10 offset:8192\n"
    "s_add_u32 s76, s69, 14336\n"
    "v_mfma_f32_16x16x32_bf16 v[132:135], a[36:39], a[104:107], v[132:135]\n"
    "ds_read_b64_tr_b16 a[10:11], v10 offset:9216\n"
    "v_mfma_f32_16x16x32_bf16 v[40:43], a[40:43], a[92:95], v[40:43]\n"
    "buffer_load_dwordx4 v28, s[52:55], s76 offen offset:2048 lds\n"
    "ds_read_b64_tr_b16 a[12:13], v11 offset:8192\n"
    "v_mfma_f32_16x16x32_bf16 v[72:75], a[40:43], a[96:99], v[72:75]\n"
    "s_add_u32 s76, s69, 21504\n"
    "v_mfma_f32_16x16x32_bf16 v[104:107], a[40:43], a[100:103], v[104:107]\n"
    "ds_read_b64_tr_b16 a[14:15], v11 offset:9216\n"
    "v_mfma_f32_16x16x32_bf16 v[136:139], a[40:43], a[104:107], v[136:139]\n"
    "buffer_load_dwordx4 v29, s[52:55], s76 offen offset:3072 lds\n"
    "ds_read_b64_tr_b16 a[16:17], v12 offset:8192\n"
    "v_mfma_f32_16x16x32_bf16 v[44:47], a[44:47], a[92:95], v[44:47]\n"
    "s_add_u32 s69, s69, 128\n"
    "v_mfma_f32_16x16x32_bf16 v[76:79], a[44:47], a[96:99], v[76:79]\n"
    "ds_read_b64_tr_b16 a[18:19], v12 offset:9216\n"
    "s_add_u32 m0, s88, 0\n"
    "v_mfma_f32_16x16x32_bf16 v[108:111], a[44:47], a[100:103], v[108:111]\n"
    "ds_read_b64_tr_b16 a[20:21], v13 offset:8192\n"
    "v_mfma_f32_16x16x32_bf16 v[140:143], a[44:47], a[104:107], v[140:143]\n"
    "ds_read_b64_tr_b16 a[22:23], v13 offset:9216\n"
    "v_mfma_f32_16x16x32_bf16 v[48:51], a[48:51], a[92:95], v[48:51]\n"
    "buffer_load_dwordx4 v26, s[48:51], s89 offen offset:0 lds\n"
    "ds_read_b64_tr_b16 a[24:25], v14 offset:8192\n"
    "v_mfma_f32_16x16x32_bf16 v[80:83], a[48:51], a[96:99], v[80:83]\n"
    "s_add_u32 s74, s89, 31744\n"
    "v_mfma_f32_16x16x32_bf16 v[112:115], a[48:51], a[100:103], v[112:115]\n"
    "ds_read_b64_tr_b16 a[26:27], v14 offset:9216\n"
    "buffer_load_dwordx4 v26, s[48:51], s74 offen offset:1024 lds\n"
    "v_mfma_f32_16x16x32_bf16 v[144:147], a[48:51], a[104:107], v[144:147]\n"
    "ds_read_b64_tr_b16 a[28:29], v15 offset:8192\n"
    "v_mfma_f32_16x16x32_bf16 v[52:55], a[52:55], a[92:95], v[52:55]\n"
    "s_add_u32 s74, s89, 63488\n"
    "v_mfma_f32_16x16x32_bf16 v[84:87], a[52:55], a[96:99], v[84:87]\n"
    "ds_read_b64_tr_b16 a[30:31], v15 offset:9216\n"
    "buffer_load_dwordx4 v27, s[48:51], s74 offen offset:2048 lds\n"
    "v_mfma_f32_16x16x32_bf16 v[116:119], a[52:55], a[100:103], v[116:119]\n"
    "ds_read_b128 a[64:67], v6 offset:32768\n"
    "v_mfma_f32_16x16x32_bf16 v[148:151], a[52:55], a[104:107], v[148:151]\n"
    "s_add_u32 s74, s89, 95232\n"
    "ds_read_b128 a[68:71], v6 offset:34816\n"
    "v_mfma_f32_16x16x32_bf16 v[56:59], a[56:59], a[92:95], v[56:59]\n"
    "buffer_load_dwordx4 v27, s[48:51], s74 offen offset:3072 lds\n"
    "v_mfma_f32_16x16x32_bf16 v[88:91], a[56:59], a[96:99], v[88:91]\n"
    "ds_read_b128 a[72:75], v6 offset:36864\n"
    "v_mfma_f32_16x16x32_bf16 v[120:123], a[56:59], a[100:103], v[120:123]\n"
    "s_add_u32 s89, s89, 262144\n"
    "ds_read_b128 a[76:79], v6 offset:38912\n"
    "v_mfma_f32_16x16x32_bf16 v[152:155], a[56:59], a[104:107], v[152:155]\n"
    "v_mfma_f32_16x16x32_bf16 v[60:63], a[60:63], a[92:95], v[60:63]\n"
    "v_mfma_f32_16x16x32_bf16 v[92:95], a[60:63], a[96:99], v[92:95]\n"
    "v_mfma_f32_16x16x32_bf16 v[124:127], a[60:63], a[100:103], v[124:127]\n"
    "v_mfma_f32_16x16x32_bf16 v[156:159], a[60:63], a[104:107], v[156:159]\n"
    "s_waitcnt lgkmcnt(0)\n"
    "s_waitcnt vmcnt(20)\n"
    "s_barrier\n"
    "v_mfma_f32_16x16x32_bf16 v[32:35], a[0:3], a[64:67], v[32:35]\n"
    "s_add_u32 m0, s88, 8192\n"
    "v_mfma_f32_16x16x32_bf16 v[64:67], a[0:3], a[68:71], v[64:67]\n"
    "ds_read_b64_tr_b16 a[32:33], v8 offset:16384\n"
    "v_mfma_f32_16x16x32_bf16 v[96:99], a[0:3], a[72:75], v[96:99]\n"
    "ds_read_b64_tr_b16 a[34:35], v8 offset:17408\n"
    "v_mfma_f32_16x16x32_bf16 v[128:131], a[0:3], a[76:79], v[128:131]\n"
    "ds_read_b64_tr_b16 a[36:37], v9 offset:16384\n"
    "v_mfma_f32_16x16x32_bf16 v[36:39], a[4:7], a[64:67], v[36:39]\n"
    "buffer_load_dwordx4 v26, s[48:51], s89 offen offset:0 lds\n"
    "v_mfma_f32_16x16x32_bf16 v[68:71], a[4:7], a[68:71], v[68:71]\n"
    "ds_read_b64_tr_b16 a[38:39], v9 offset:17408\n"
    "v_mfma_f32_16x16x32_bf16 v[100:103], a[4:7], a[72:75], v[100:103]\n"
    "ds_read_b64_tr_b16 a[40:41], v10 offset:16384\n"
    "v_mfma_f32_16x16x32_bf16 v[132:135], a[4:7], a[76:79], v[132:135]\n"
    "s_add_u32 s74, s89, 31744\n"
    "ds_read_b64_tr_b16 a[42:43], v10 offset:17408\n"
    "v_mfma_f32_16x16x32_bf16 v[40:43], a[8:11], a[64:67], v[40:43]\n"
    "ds_read_b64_tr_b16 a[44:45], v11 offset:16384\n"
    "v_mfma_f32_16x16x32_bf16 v[72:75], a[8:11], a[68:71], v[72:75]\n"
    "buffer_load_dwordx4 v26, s[48:51], s74 offen offset:1024 lds\n"
    "v_mfma_f32_16x16x32_bf16 v[104:107], a[8:11], a[72:75], v[104:107]\n"
    "ds_read_b64_tr_b16 a[46:47], v11 offset:17408\n"
    "v_mfma_f32_16x16x32_bf16 v[136:139], a[8:11], a[76:79], v[136:139]\n"
    "ds_read_b64_tr_b16 a[48:49], v12 offset:16384\n"
    "v_mfma_f32_16x16x32_bf16 v[44:47], a[12:15], a[64:67], v[44:47]\n"
    "s_add_u32 s74, s89, 63488\n"
    "v_mfma_f32_16x16x32_bf16 v[76:79], a[12:15], a[68:71], v[76:79]\n"
    "ds_read_b64_tr_b16 a[50:51], v12 offset:17408\n"
    "v_mfma_f32_16x16x32_bf16 v[108:111], a[12:15], a[72:75], v[108:111]\n"
    "ds_read_b64_tr_b16 a[52:53], v13 offset:16384\n"
    "v_mfma_f32_16x16x32_bf16 v[140:143], a[12:15], a[76:79], v[140:143]\n"
    "ds_read_b64_tr_b16 a[54:55], v13 offset:17408\n"
    "v_mfma_f32_16x16x32_bf16 v[48:51], a[16:19], a[64:67], v[48:51]\n"
    "buffer_load_dwordx4 v27, s[48:51], s74 offen offset:2048 lds\n"
    "v_mfma_f32_16x16x32_bf16 v[80:83], a[16:19], a[68:71], v[80:83]\n"
    "ds_read_b64_tr_b16 a[56:57], v14 offset:16384\n"
    "v_mfma_f32_16x16x32_bf16 v[112:115], a[16:19], a[72:75], v[112:115]\n"
    "ds_read_b64_tr_b16 a[58:59], v14 offset:17408\n"
    "v_mfma_f32_16x16x32_bf16 v[144:147], a[16:19], a[76:79], v[144:147]\n"
    "s_add_u32 s74, s89, 95232\n"
    "v_mfma_f32_16x16x32_bf16 v[52:55], a[20:23], a[64:67], v[52:55]\n"
    "ds_read_b64_tr_b16 a[60:61], v15 offset:16384\n"
    "v_mfma_f32_16x16x32_bf16 v[84:87], a[20:23], a[68:71], v[84:87]\n"
    "ds_read_b64_tr_b16 a[62:63], v15 offset:17408\n"
    "v_mfma_f32_16x16x32_bf16 v[116:119], a[20:23], a[72:75], v[116:119]\n"
    "buffer_load_dwordx4 v27, s[48:51], s74 offen offset:3072 lds\n"
    "ds_read_b128 a[92:95], v7 offset:32768\n"
    "v_mfma_f32_16x16x32_bf16 v[148:151], a[20:23], a[76:79], v[148:151]\n"
    "ds_read_b128 a[96:99], v7 offset:34816\n"
    "v_mfma_f32_16x16x32_bf16 v[56:59], a[24:27], a[64:67], v[56:59]\n"
    "s_add_u32 s89, s89, 262144\n"
    "v_mfma_f32_16x16x32_bf16 v[88:91], a[24:27], a[68:71], v[88:91]\n"
    "ds_read_b128 a[100:103], v7 offset:36864\n"
    "v_mfma_f32_16x16x32_bf16 v[120:123], a[24:27], a[72:75], v[120:123]\n"
    "ds_read_b128 a[104:107], v7 offset:38912\n"
    "v_mfma_f32_16x16x32_bf16 v[152:155], a[24:27], a[76:79], v[152:155]\n"
    "v_mfma_f32_16x16x32_bf16 v[60:63], a[28:31], a[64:67], v[60:63]\n"
    "v_mfma_f32_16x16x32_bf16 v[92:95], a[28:31], a[68:71], v[92:95]\n"
    "v_mfma_f32_16x16x32_bf16 v[124:127], a[28:31], a[72:75], v[124:127]\n"
    "v_mfma_f32_16x16x32_bf16 v[156:159], a[28:31], a[76:79], v[156:159]\n"
    "s_waitcnt lgkmcnt(0)\n"
    "s_waitcnt vmcnt(12)\n"
    "s_barrier\n"
    "v_mfma_f32_16x16x32_bf16 v[32:35], a[32:35], a[92:95], v[32:35]\n"
    "s_add_u32 m0, s72, 32768\n"
    "v_mfma_f32_16x16x32_bf16 v[64:67], a[32:35], a[96:99], v[64:67]\n"
    "ds_read_b64_tr_b16 a[0:1], v8 offset:24576\n"
    "ds_read_b64_tr_b16 a[2:3], v8 offset:25600\n"
    "v_mfma_f32_16x16x32_bf16 v[96:99], a[32:35], a[100:103], v[96:99]\n"
    "buffer_load_dwordx4 v28, s[52:55], s69 offen offset:0 lds\n"
    "v_mfma_f32_16x16x32_bf16 v[128:131], a[32:35], a[104:107], v[128:131]\n"
    "ds_read_b64_tr_b16 a[4:5], v9 offset:24576\n"
    "s_add_u32 s76, s69, 7168\n"
    "v_mfma_f32_16x16x32_bf16 v[36:39], a[36:39], a[92:95], v[36:39]\n"
    "ds_read_b64_tr_b16 a[6:7], v9 offset:25600\n"
    "v_mfma_f32_16x16x32_bf16 v[68:71], a[36:39], a[96:99], v[68:71]\n"
    "buffer_load_dwordx4 v29, s[52:55], s76 offen offset:1024 lds\n"
    "v_mfma_f32_16x16x32_bf16 v[100:103], a[36:39], a[100:103], v[100:103]\n"
    "ds_read_b64_tr_b16 a[8:9], v10 offset:24576\n"
    "s_add_u32 s76, s69, 14336\n"
    "v_mfma_f32_16x16x32_bf16 v[132:135], a[36:39], a[104:107], v[132:135]\n"
    "ds_read_b64_tr_b16 a[10:11], v10 offset:25600\n"
    "v_mfma_f32_16x16x32_bf16 v[40:43], a[40:43], a[92:95], v[40:43]\n"
    "buffer_load_dwordx4 v28, s[52:55], s76 offen offset:2048 lds\n"
    "ds_read_b64_tr_b16 a[12:13], v11 offset:24576\n"
    "v_mfma_f32_16x16x32_bf16 v[72:75], a[40:43], a[96:99], v[72:75]\n"
    "s_add_u32 s76, s69, 21504\n"
    "v_mfma_f32_16x16x32_bf16 v[104:107], a[40:43], a[100:103], v[104:107]\n"
    "ds_read_b64_tr_b16 a[14:15], v11 offset:25600\n"
    "v_mfma_f32_16x16x32_bf16 v[136:139], a[40:43], a[104:107], v[136:139]\n"
    "buffer_load_dwordx4 v29, s[52:55], s76 offen offset:3072 lds\n"
    "ds_read_b64_tr_b16 a[16:17], v12 offset:24576\n"
    "v_mfma_f32_16x16x32_bf16 v[44:47], a[44:47], a[92:95], v[44:47]\n"
    "s_add_u32 s69, s69, 128\n"
    "v_mfma_f32_16x16x32_bf16 v[76:79], a[44:47], a[96:99], v[76:79]\n"
    "ds_read_b64_tr_b16 a[18:19], v12 offset:25600\n"
    "s_add_u32 m0, s88, 16384\n"
    "v_mfma_f32_16x16x32_bf16 v[108:111], a[44:47], a[100:103], v[108:111]\n"
    "ds_read_b64_tr_b16 a[20:21], v13 offset:24576\n"
    "v_mfma_f32_16x16x32_bf16 v[140:143], a[44:47], a[104:107], v[140:143]\n"
    "ds_read_b64_tr_b16 a[22:23], v13 offset:25600\n"
    "v_mfma_f32_16x16x32_bf16 v[48:51], a[48:51], a[92:95], v[48:51]\n"
    "buffer_load_dwordx4 v26, s[48:51], s89 offen offset:0 lds\n"
    "ds_read_b64_tr_b16 a[24:25], v14 offset:24576\n"
    "v_mfma_f32_16x16x32_bf16 v[80:83], a[48:51], a[96:99], v[80:83]\n"
    "s_add_u32 s74, s89, 31744\n"
    "v_mfma_f32_16x16x32_bf16 v[112:115], a[48:51], a[100:103], v[112:115]\n"
    "ds_read_b64_tr_b16 a[26:27], v14 offset:25600\n"
    "buffer_load_dwordx4 v26, s[48:51], s74 offen offset:1024 lds\n"
    "v_mfma_f32_16x16x32_bf16 v[144:147], a[48:51], a[104:107], v[144:147]\n"
    "ds_read_b64_tr_b16 a[28:29], v15 offset:24576\n"
    "v_mfma_f32_16x16x32_bf16 v[52:55], a[52:55], a[92:95], v[52:55]\n"
    "s_add_u32 s74, s89, 63488\n"
    "v_mfma_f32_16x16x32_bf16 v[84:87], a[52:55], a[96:99], v[84:87]\n"
    "ds_read_b64_tr_b16 a[30:31], v15 offset:25600\n"
    "buffer_load_dwordx4 v27, s[48:51], s74 offen offset:2048 lds\n"
    "v_mfma_f32_16x16x32_bf16 v[116:119], a[52:55], a[100:103], v[116:119]\n"
    "ds_read_b128 a[64:67], v30 offset:0\n"
    "v_mfma_f32_16x16x32_bf16 v[148:151], a[52:55], a[104:107], v[148:151]\n"
    "s_add_u32 s74, s89, 95232\n"
    "ds_read_b128 a[68:71], v30 offset:2048\n"
    "v_mfma_f32_16x16x32_bf16 v[56:59], a[56:59], a[92:95], v[56:59]\n"
    "buffer_load_dwordx4 v27, s[48:51], s74 offen offset:3072 lds\n"
    "v_mfma_f32_16x16x32_bf16 v[88:91], a[56:59], a[96:99], v[88:91]\n"
    "ds_read_b128 a[72:75], v30 offset:4096\n"
    "v_mfma_f32_16x16x32_bf16 v[120:123], a[56:59], a[100:103], v[120:123]\n"
    "s_add_u32 s89, s89, 262144\n"
    "ds_read_b128 a[76:79], v30 offset:6144\n"
    "v_mfma_f32_16x16x32_bf16 v[152:155], a[56:59], a[104:107], v[152:155]\n"
    "v_mfma_f32_16x16x32_bf16 v[60:63], a[60:63], a[92:95], v[60:63]\n"
    "v_mfma_f32_16x16x32_bf16 v[92:95], a[60:63], a[96:99], v[92:95]\n"
    "v_mfma_f32_16x16x32_bf16 v[124:127], a[60:63], a[100:103], v[124:127]\n"
    "v_mfma_f32_16x16x32_bf16 v[156:159], a[60:63], a[104:107], v[156:159]\n"
    "s_waitcnt lgkmcnt(0)\n"
    "s_waitcnt vmcnt(12)\n"
    "s_barrier\n"
    "v_mfma_f32_16x16x32_bf16 v[32:35], a[0:3], a[64:67], v[32:35]\n"
    "s_add_u32 m0, s88, 24576\n"
    "v_mfma_f32_16x16x32_bf16 v[64:67], a[0:3], a[68:71], v[64:67]\n"
    "ds_read_b64_tr_b16 a[32:33], v8 offset:0\n"
    "v_mfma_f32_16x16x32_bf16 v[96:99], a[0:3], a[72:75], v[96:99]\n"
    "ds_read_b64_tr_b16 a[34:35], v8 offset:1024\n"
    "v_mfma_f32_16x16x32_bf16 v[128:131], a[0:3], a[76:79], v[128:131]\n"
    "ds_read_b64_tr_b16 a[36:37], v9 offset:0\n"
    "v_mfma_f32_16x16x32_bf16 v[36:39], a[4:7], a[64:67], v[36:39]\n"
    "buffer_load_dwordx4 v26, s[48:51], s89 offen offset:0 lds\n"
    "v_mfma_f32_16x16x32_bf16 v[68:71], a[4:7], a[68:71], v[68:71]\n"
    "ds_read_b64_tr_b16 a[38:39], v9 offset:1024\n"
    "v_mfma_f32_16x16x32_bf16 v[100:103], a[4:7], a[72:75], v[100:103]\n"
    "ds_read_b64_tr_b16 a[40:41], v10 offset:0\n"
    "v_mfma_f32_16x16x32_bf16 v[132:135], a[4:7], a[76:79], v[132:135]\n"
    "s_add_u32 s74, s89, 31744\n"
    "ds_read_b64_tr_b16 a[42:43], v10 offset:1024\n"
    "v_mfma_f32_16x16x32_bf16 v[40:43], a[8:11], a[64:67], v[40:43]\n"
    "ds_read_b64_tr_b16 a[44:45], v11 offset:0\n"
    "v_mfma_f32_16x16x32_bf16 v[72:75], a[8:11], a[68:71], v[72:75]\n"
    "buffer_load_dwordx4 v26, s[48:51], s74 offen offset:1024 lds\n"
    "v_mfma_f32_16x16x32_bf16 v[104:107], a[8:11], a[72:75], v[104:107]\n"
    "ds_read_b64_tr_b16 a[46:47], v11 offset:1024\n"
    "v_mfma_f32_16x16x32_bf16 v[136:139], a[8:11], a[76:79], v[136:139]\n"
    "ds_read_b64_tr_b16 a[48:49], v12 offset:0\n"
    "v_mfma_f32_16x16x32_bf16 v[44:47], a[12:15], a[64:67], v[44:47]\n"
    "s_add_u32 s74, s89, 63488\n"
    "v_mfma_f32_16x16x32_bf16 v[76:79], a[12:15], a[68:71], v[76:79]\n"
    "ds_read_b64_tr_b16 a[50:51], v12 offset:1024\n"
    "v_mfma_f32_16x16x32_bf16 v[108:111], a[12:15], a[72:75], v[108:111]\n"
    "ds_read_b64_tr_b16 a[52:53], v13 offset:0\n"
    "v_mfma_f32_16x16x32_bf16 v[140:143], a[12:15], a[76:79], v[140:143]\n"
    "ds_read_b64_tr_b16 a[54:55], v13 offset:1024\n"
    "v_mfma_f32_16x16x32_bf16 v[48:51], a[16:19], a[64:67], v[48:51]\n"
    "buffer_load_dwordx4 v27, s[48:51], s74 offen offset:2048 lds\n"
    "v_mfma_f32_16x16x32_bf16 v[80:83], a[16:19], a[68:71], v[80:83]\n"
    "ds_read_b64_tr_b16 a[56:57], v14 offset:0\n"
    "v_mfma_f32_16x16x32_bf16 v[112:115], a[16:19], a[72:75], v[112:115]\n"
    "ds_read_b64_tr_b16 a[58:59], v14 offset:1024\n"
    "v_mfma_f32_16x16x32_bf16 v[144:147], a[16:19], a[76:79], v[144:147]\n"
    "s_add_u32 s74, s89, 95232\n"
    "v_mfma_f32_16x16x32_bf16 v[52:55], a[20:23], a[64:67], v[52:55]\n"
    "ds_read_b64_tr_b16 a[60:61], v15 offset:0\n"
    "v_mfma_f32_16x16x32_bf16 v[84:87], a[20:23], a[68:71], v[84:87]\n"
    "ds_read_b64_tr_b16 a[62:63], v15 offset:1024\n"
    "v_mfma_f32_16x16x32_bf16 v[116:119], a[20:23], a[72:75], v[116:119]\n"
    "buffer_load_dwordx4 v27, s[48:51], s74 offen offset:3072 lds\n"
    "ds_read_b128 a[92:95], v31 offset:0\n"
    "v_mfma_f32_16x16x32_bf16 v[148:151], a[20:23], a[76:79], v[148:151]\n"
    "ds_read_b128 a[96:99], v31 offset:2048\n"
    "v_mfma_f32_16x16x32_bf16 v[56:59], a[24:27], a[64:67], v[56:59]\n"
    "s_add_u32 s89, s89, 262144\n"
    "v_mfma_f32_16x16x32_bf16 v[88:91], a[24:27], a[68:71], v[88:91]\n"
    "ds_read_b128 a[100:103], v31 offset:4096\n"
    "v_mfma_f32_16x16x32_bf16 v[120:123], a[24:27], a[72:75], v[120:123]\n"
    "ds_read_b128 a[104:107], v31 offset:6144\n"
    "v_mfma_f32_16x16x32_bf16 v[152:155], a[24:27], a[76:79], v[152:155]\n"
    "v_mfma_f32_16x16x32_bf16 v[60:63], a[28:31], a[64:67], v[60:63]\n"
    "v_mfma_f32_16x16x32_bf16 v[92:95], a[28:31], a[68:71], v[92:95]\n"
    "v_mfma_f32_16x16x32_bf16 v[124:127], a[28:31], a[72:75], v[124:127]\n"
    "v_mfma_f32_16x16x32_bf16 v[156:159], a[28:31], a[76:79], v[156:159]\n"
    "s_waitcnt lgkmcnt(0)\n"
    "s_waitcnt vmcnt(12)\n"
    "s_barrier\n"
    "v_mfma_f32_16x16x32_bf16 v[32:35], a[32:35], a[92:95], v[32:35]\n"
    "s_add_u32 m0, s65, 0\n"
    "v_mfma_f32_16x16x32_bf16 v[64:67], a[32:35], a[96:99], v[64:67]\n"
    "ds_read_b64_tr_b16 a[0:1], v8 offset:8192\n"
    "ds_read_b64_tr_b16 a[2:3], v8 offset:9216\n"
    "v_mfma_f32_16x16x32_bf16 v[96:99], a[32:35], a[100:103], v[96:99]\n"
    "buffer_load_dwordx4 v28, s[52:55], s69 offen offset:0 lds\n"
    "v_mfma_f32_16x16x32_bf16 v[128:131], a[32:35], a[104:107], v[128:131]\n"
    "ds_read_b64_tr_b16 a[4:5], v9 offset:8192\n"
    "s_add_u32 s76, s69, 7168\n"
    "v_mfma_f32_16x16x32_bf16 v[36:39], a[36:39], a[92:95], v[36:39]\n"
    "ds_read_b64_tr_b16 a[6:7], v9 offset:9216\n"
    "v_mfma_f32_16x16x32_bf16 v[68:71], a[36:39], a[96:99], v[68:71]\n"
    "buffer_load_dwordx4 v29, s[52:55], s76 offen offset:1024 lds\n"
    "v_mfma_f32_16x16x32_bf16 v[100:103], a[36:39], a[100:103], v[100:103]\n"
    "ds_read_b64_tr_b16 a[8:9], v10 offset:8192\n"
    "s_add_u32 s76, s69, 14336\n"
    "v_mfma_f32_16x16x32_bf16 v[132:135], a[36:39], a[104:107], v[132:135]\n"
    "ds_read_b64_tr_b16 a[10:11], v10 offset:9216\n"
    "v_mfma_f32_16x16x32_bf16 v[40:43], a[40:43], a[92:95], v[40:43]\n"
    "buffer_load_dwordx4 v28, s[52:55], s76 offen offset:2048 lds\n"
    "ds_read_b64_tr_b16 a[12:13], v11 offset:8192\n"
    "v_mfma_f32_16x16x32_bf16 v[72:75], a[40:43], a[96:99], v[72:75]\n"
    "s_add_u32 s76, s69, 21504\n"
    "v_mfma_f32_16x16x32_bf16 v[104:107], a[40:43], a[100:103], v[104:107]\n"
    "ds_read_b64_tr_b16 a[14:15], v11 offset:9216\n"
    "v_mfma_f32_16x16x32_bf16 v[136:139], a[40:43], a[104:107], v[136:139]\n"
    "buffer_load_dwordx4 v29, s[52:55], s76 offen offset:3072 lds\n"
    "ds_read_b64_tr_b16 a[16:17], v12 offset:8192\n"
    "v_mfma_f32_16x16x32_bf16 v[44:47], a[44:47], a[92:95], v[44:47]\n"
    "s_add_u32 s69, s69, 128\n"
    "v_mfma_f32_16x16x32_bf16 v[76:79], a[44:47], a[96:99], v[76:79]\n"
    "ds_read_b64_tr_b16 a[18:19], v12 offset:9216\n"
    "s_add_u32 m0, s88, 0\n"
    "v_mfma_f32_16x16x32_bf16 v[108:111], a[44:47], a[100:103], v[108:111]\n"
    "ds_read_b64_tr_b16 a[20:21], v13 offset:8192\n"
    "v_mfma_f32_16x16x32_bf16 v[140:143], a[44:47], a[104:107], v[140:143]\n"
    "ds_read_b64_tr_b16 a[22:23], v13 offset:9216\n"
    "v_mfma_f32_16x16x32_bf16 v[48:51], a[48:51], a[92:95], v[48:51]\n"
    "buffer_load_dwordx4 v26, s[48:51], s89 offen offset:0 lds\n"
    "ds_read_b64_tr_b16 a[24:25], v14 offset:8192\n"
    "v_mfma_f32_16x16x32_bf16 v[80:83], a[48:51], a[96:99], v[80:83]\n"
    "s_add_u32 s74, s89, 31744\n"
    "v_mfma_f32_16x16x32_bf16 v[112:115], a[48:51], a[100:103], v[112:115]\n"
    "ds_read_b64_tr_b16 a[26:27], v14 offset:9216\n"
    "buffer_load_dwordx4 v26, s[48:51], s74 offen offset:1024 lds\n"
    "v_mfma_f32_16x16x32_bf16 v[144:147], a[48:51], a[104:107], v[144:147]\n"
    "ds_read_b64_tr_b16 a[28:29], v15 offset:8192\n"
    "v_mfma_f32_16x16x32_bf16 v[52:55], a[52:55], a[92:95], v[52:55]\n"
    "s_add_u32 s74, s89, 63488\n"
    "v_mfma_f32_16x16x32_bf16 v[84:87], a[52:55], a[96:99], v[84:87]\n"
    "ds_read_b64_tr_b16 a[30:31], v15 offset:9216\n"
    "buffer_load_dwordx4 v27, s[48:51], s74 offen offset:2048 lds\n"
    "v_mfma_f32_16x16x32_bf16 v[116:119], a[52:55], a[100:103], v[116:119]\n"
    "ds_read_b128 a[64:67], v30 offset:16384\n"
    "v_mfma_f32_16x16x32_bf16 v[148:151], a[52:55], a[104:107], v[148:151]\n"
    "s_add_u32 s74, s89, 95232\n"
    "ds_read_b128 a[68:71], v30 offset:18432\n"
    "v_mfma_f32_16x16x32_bf16 v[56:59], a[56:59], a[92:95], v[56:59]\n"
    "buffer_load_dwordx4 v27, s[48:51], s74 offen offset:3072 lds\n"
    "v_mfma_f32_16x16x32_bf16 v[88:91], a[56:59], a[96:99], v[88:91]\n"
    "ds_read_b128 a[72:75], v30 offset:20480\n"
    "v_mfma_f32_16x16x32_bf16 v[120:123], a[56:59], a[100:103], v[120:123]\n"
    "s_add_u32 s89, s89, 262144\n"
    "ds_read_b128 a[76:79], v30 offset:22528\n"
    "v_mfma_f32_16x16x32_bf16 v[152:155], a[56:59], a[104:107], v[152:155]\n"
    "v_mfma_f32_16x16x32_bf16 v[60:63], a[60:63], a[92:95], v[60:63]\n"
    "v_mfma_f32_16x16x32_bf16 v[92:95], a[60:63], a[96:99], v[92:95]\n"
    "v_mfma_f32_16x16x32_bf16 v[124:127], a[60:63], a[100:103], v[124:127]\n"
    "v_mfma_f32_16x16x32_bf16 v[156:159], a[60:63], a[104:107], v[156:159]\n"
    "s_waitcnt lgkmcnt(0)\n"
    "s_waitcnt vmcnt(12)\n"
    "s_barrier\n"
    "v_mfma_f32_16x16x32_bf16 v[32:35], a[0:3], a[64:67], v[32:35]\n"
    "s_add_u32 m0, s88, 8192\n"
    "v_mfma_f32_16x16x32_bf16 v[64:67], a[0:3], a[68:71], v[64:67]\n"
    "ds_read_b64_tr_b16 a[32:33], v8 offset:16384\n"
    "v_mfma_f32_16x16x32_bf16 v[96:99], a[0:3], a[72:75], v[96:99]\n"
    "ds_read_b64_tr_b16 a[34:35], v8 offset:17408\n"
    "v_mfma_f32_16x16x32_bf16 v[128:131], a[0:3], a[76:79], v[128:131]\n"
    "ds_read_b64_tr_b16 a[36:37], v9 offset:16384\n"
    "v_mfma_f32_16x16x32_bf16 v[36:39], a[4:7], a[64:67], v[36:39]\n"
    "buffer_load_dwordx4 v26, s[48:51], s89 offen offset:0 lds\n"
    "v_mfma_f32_16x16x32_bf16 v[68:71], a[4:7], a[68:71], v[68:71]\n"
    "ds_read_b64_tr_b16 a[38:39], v9 offset:17408\n"
    "v_mfma_f32_16x16x32_bf16 v[100:103], a[4:7], a[72:75], v[100:103]\n"
    "ds_read_b64_tr_b16 a[40:41], v10 offset:16384\n"
    "v_mfma_f32_16x16x32_bf16 v[132:135], a[4:7], a[76:79], v[132:135]\n"
    "s_add_u32 s74, s89, 31744\n"
    "ds_read_b64_tr_b16 a[42:43], v10 offset:17408\n"
    "v_mfma_f32_16x16x32_bf16 v[40:43], a[8:11], a[64:67], v[40:43]\n"
    "ds_read_b64_tr_b16 a[44:45], v11 offset:16384\n"
    "v_mfma_f32_16x16x32_bf16 v[72:75], a[8:11], a[68:71], v[72:75]\n"
    "buffer_load_dwordx4 v26, s[48:51], s74 offen offset:1024 lds\n"
    "v_mfma_f32_16x16x32_bf16 v[104:107], a[8:11], a[72:75], v[104:107]\n"
    "ds_read_b64_tr_b16 a[46:47], v11 offset:17408\n"
    "v_mfma_f32_16x16x32_bf16 v[136:139], a[8:11], a[76:79], v[136:139]\n"
    "ds_read_b64_tr_b16 a[48:49], v12 offset:16384\n"
    "v_mfma_f32_16x16x32_bf16 v[44:47], a[12:15], a[64:67], v[44:47]\n"
    "s_add_u32 s74, s89, 63488\n"
    "v_mfma_f32_16x16x32_bf16 v[76:79], a[12:15], a[68:71], v[76:79]\n"
    "ds_read_b64_tr_b16 a[50:51], v12 offset:17408\n"
    "v_mfma_f32_16x16x32_bf16 v[108:111], a[12:15], a[72:75], v[108:111]\n"
    "ds_read_b64_tr_b16 a[52:53], v13 offset:16384\n"
    "v_mfma_f32_16x16x32_bf16 v[140:143], a[12:15], a[76:79], v[140:143]\n"
    "ds_read_b64_tr_b16 a[54:55], v13 offset:17408\n"
    "v_mfma_f32_16x16x32_bf16 v[48:51], a[16:19], a[64:67], v[48:51]\n"
    "buffer_load_dwordx4 v27, s[48:51], s74 offen offset:2048 lds\n"
    "v_mfma_f32_16x16x32_bf16 v[80:83], a[16:19], a[68:71], v[80:83]\n"
    "ds_read_b64_tr_b16 a[56:57], v14 offset:16384\n"
    "v_mfma_f32_16x16x32_bf16 v[112:115], a[16:19], a[72:75], v[112:115]\n"
    "ds_read_b64_tr_b16 a[58:59], v14 offset:17408\n"
    "v_mfma_f32_16x16x32_bf16 v[144:147], a[16:19], a[76:79], v[144:147]\n"
    "s_add_u32 s74, s89, 95232\n"
    "v_mfma_f32_16x16x32_bf16 v[52:55], a[20:23], a[64:67], v[52:55]\n"
    "ds_read_b64_tr_b16 a[60:61], v15 offset:16384\n"
    "v_mfma_f32_16x16x32_bf16 v[84:87], a[20:23], a[68:71], v[84:87]\n"
    "ds_read_b64_tr_b16 a[62:63], v15 offset:17408\n"
    "v_mfma_f32_16x16x32_bf16 v[116:119], a[20:23], a[72:75], v[116:119]\n"
    "buffer_load_dwordx4 v27, s[48:51], s74 offen offset:3072 lds\n"
    "ds_read_b128 a[92:95], v31 offset:16384\n"
    "v_mfma_f32_16x16x32_bf16 v[148:151], a[20:23], a[76:79], v[148:151]\n"
    "ds_read_b128 a[96:99], v31 offset:18432\n"
    "v_mfma_f32_16x16x32_bf16 v[56:59], a[24:27], a[64:67], v[56:59]\n"
    "s_add_u32 s89, s89, 262144\n"
    "v_mfma_f32_16x16x32_bf16 v[88:91], a[24:27], a[68:71], v[88:91]\n"
    "ds_read_b128 a[100:103], v31 offset:20480\n"
    "v_mfma_f32_16x16x32_bf16 v[120:123], a[24:27], a[72:75], v[120:123]\n"
    "ds_read_b128 a[104:107], v31 offset:22528\n"
    "v_mfma_f32_16x16x32_bf16 v[152:155], a[24:27], a[76:79], v[152:155]\n"
    "v_mfma_f32_16x16x32_bf16 v[60:63], a[28:31], a[64:67], v[60:63]\n"
    "v_mfma_f32_16x16x32_bf16 v[92:95], a[28:31], a[68:71], v[92:95]\n"
    "v_mfma_f32_16x16x32_bf16 v[124:127], a[28:31], a[72:75], v[124:127]\n"
    "v_mfma_f32_16x16x32_bf16 v[156:159], a[28:31], a[76:79], v[156:159]\n"
    "s_waitcnt lgkmcnt(0)\n"
    "s_waitcnt vmcnt(12)\n"
    "s_barrier\n"
    "v_mfma_f32_16x16x32_bf16 v[32:35], a[32:35], a[92:95], v[32:35]\n"
    "s_add_u32 m0, s65, 16384\n"
    "v_mfma_f32_16x16x32_bf16 v[64:67], a[32:35], a[96:99], v[64:67]\n"
    "ds_read_b64_tr_b16 a[0:1], v8 offset:24576\n"
    "ds_read_b64_tr_b16 a[2:3], v8 offset:25600\n"
    "v_mfma_f32_16x16x32_bf16 v[96:99], a[32:35], a[100:103], v[96:99]\n"
    "buffer_load_dwordx4 v28, s[52:55], s69 offen offset:0 lds\n"
    "v_mfma_f32_16x16x32_bf16 v[128:131], a[32:35], a[104:107], v[128:131]\n"
    "ds_read_b64_tr_b16 a[4:5], v9 offset:24576\n"
    "s_add_u32 s76, s69, 7168\n"
    "v_mfma_f32_16x16x32_bf16 v[36:39], a[36:39], a[92:95], v[36:39]\n"
    "ds_read_b64_tr_b16 a[6:7], v9 offset:25600\n"
    "v_mfma_f32_16x16x32_bf16 v[68:71], a[36:39], a[96:99], v[68:71]\n"
    "buffer_load_dwordx4 v29, s[52:55], s76 offen offset:1024 lds\n"
    "v_mfma_f32_16x16x32_bf16 v[100:103], a[36:39], a[100:103], v[100:103]\n"
    "ds_read_b64_tr_b16 a[8:9], v10 offset:24576\n"
    "s_add_u32 s76, s69, 14336\n"
    "v_mfma_f32_16x16x32_bf16 v[132:135], a[36:39], a[104:107], v[132:135]\n"
    "ds_read_b64_tr_b16 a[10:11], v10 offset:25600\n"
    "v_mfma_f32_16x16x32_bf16 v[40:43], a[40:43], a[92:95], v[40:43]\n"
    "buffer_load_dwordx4 v28, s[52:55], s76 offen offset:2048 lds\n"
    "ds_read_b64_tr_b16 a[12:13], v11 offset:24576\n"
    "v_mfma_f32_16x16x32_bf16 v[72:75], a[40:43], a[96:99], v[72:75]\n"
    "s_add_u32 s76, s69, 21504\n"
    "v_mfma_f32_16x16x32_bf16 v[104:107], a[40:43], a[100:103], v[104:107]\n"
    "ds_read_b64_tr_b16 a[14:15], v11 offset:25600\n"
    "v_mfma_f32_16x16x32_bf16 v[136:139], a[40:43], a[104:107], v[136:139]\n"
    "buffer_load_dwordx4 v29, s[52:55], s76 offen offset:3072 lds\n"
    "ds_read_b64_tr_b16 a[16:17], v12 offset:24576\n"
    "v_mfma_f32_16x16x32_bf16 v[44:47], a[44:47], a[92:95], v[44:47]\n"
    "s_add_u32 s69, s69, 128\n"
    "v_mfma_f32_16x16x32_bf16 v[76:79], a[44:47], a[96:99], v[76:79]\n"
    "ds_read_b64_tr_b16 a[18:19], v12 offset:25600\n"
    "s_add_u32 m0, s88, 16384\n"
    "v_mfma_f32_16x16x32_bf16 v[108:111], a[44:47], a[100:103], v[108:111]\n"
    "ds_read_b64_tr_b16 a[20:21], v13 offset:24576\n"
    "v_mfma_f32_16x16x32_bf16 v[140:143], a[44:47], a[104:107], v[140:143]\n"
    "ds_read_b64_tr_b16 a[22:23], v13 offset:25600\n"
    "v_mfma_f32_16x16x32_bf16 v[48:51], a[48:51], a[92:95], v[48:51]\n"
    "buffer_load_dwordx4 v26, s[48:51], s89 offen offset:0 lds\n"
    "ds_read_b64_tr_b16 a[24:25], v14 offset:24576\n"
    "v_mfma_f32_16x16x32_bf16 v[80:83], a[48:51], a[96:99], v[80:83]\n"
    "s_add_u32 s74, s89, 31744\n"
    "v_mfma_f32_16x16x32_bf16 v[112:115], a[48:51], a[100:103], v[112:115]\n"
    "ds_read_b64_tr_b16 a[26:27], v14 offset:25600\n"
    "buffer_load_dwordx4 v26, s[48:51], s74 offen offset:1024 lds\n"
    "v_mfma_f32_16x16x32_bf16 v[144:147], a[48:51], a[104:107], v[144:147]\n"
    "ds_read_b64_tr_b16 a[28:29], v15 offset:24576\n"
    "v_mfma_f32_16x16x32_bf16 v[52:55], a[52:55], a[92:95], v[52:55]\n"
    "s_add_u32 s74, s89, 63488\n"
    "v_mfma_f32_16x16x32_bf16 v[84:87], a[52:55], a[96:99], v[84:87]\n"
    "ds_read_b64_tr_b16 a[30:31], v15 offset:25600\n"
    "buffer_load_dwordx4 v27, s[48:51], s74 offen offset:2048 lds\n"
    "v_mfma_f32_16x16x32_bf16 v[116:119], a[52:55], a[100:103], v[116:119]\n"
    "ds_read_b128 a[64:67], v6 offset:0\n"
    "v_mfma_f32_16x16x32_bf16 v[148:151], a[52:55], a[104:107], v[148:151]\n"
    "s_add_u32 s74, s89, 95232\n"
    "ds_read_b128 a[68:71], v6 offset:2048\n"
    "v_mfma_f32_16x16x32_bf16 v[56:59], a[56:59], a[92:95], v[56:59]\n"
    "buffer_load_dwordx4 v27, s[48:51], s74 offen offset:3072 lds\n"
    "v_mfma_f32_16x16x32_bf16 v[88:91], a[56:59], a[96:99], v[88:91]\n"
    "ds_read_b128 a[72:75], v6 offset:4096\n"
    "v_mfma_f32_16x16x32_bf16 v[120:123], a[56:59], a[100:103], v[120:123]\n"
    "s_add_u32 s89, s89, 262144\n"
    "ds_read_b128 a[76:79], v6 offset:6144\n"
    "v_mfma_f32_16x16x32_bf16 v[152:155], a[56:59], a[104:107], v[152:155]\n"
    "v_mfma_f32_16x16x32_bf16 v[60:63], a[60:63], a[92:95], v[60:63]\n"
    "v_mfma_f32_16x16x32_bf16 v[92:95], a[60:63], a[96:99], v[92:95]\n"
    "v_mfma_f32_16x16x32_bf16 v[124:127], a[60:63], a[100:103], v[124:127]\n"
    "v_mfma_f32_16x16x32_bf16 v[156:159], a[60:63], a[104:107], v[156:159]\n"
    "s_waitcnt lgkmcnt(0)\n"
    "s_waitcnt vmcnt(12)\n"
    "s_barrier\n"
    "v_mfma_f32_16x16x32_bf16 v[32:35], a[0:3], a[64:67], v[32:35]\n"
    "s_add_u32 m0, s88, 24576\n"
    "v_mfma_f32_16x16x32_bf16 v[64:67], a[0:3], a[68:71], v[64:67]\n"
    "ds_read_b64_tr_b16 a[32:33], v8 offset:0\n"
    "v_mfma_f32_16x16x32_bf16 v[96:99], a[0:3], a[72:75], v[96:99]\n"
    "ds_read_b64_tr_b16 a[34:35], v8 offset:1024\n"
    "v_mfma_f32_16x16x32_bf16 v[128:131], a[0:3], a[76:79], v[128:131]\n"
    "ds_read_b64_tr_b16 a[36:37], v9 offset:0\n"
    "v_mfma_f32_16x16x32_bf16 v[36:39], a[4:7], a[64:67], v[36:39]\n"
    "buffer_load_dwordx4 v26, s[48:51], s89 offen offset:0 lds\n"
    "v_mfma_f32_16x16x32_bf16 v[68:71], a[4:7], a[68:71], v[68:71]\n"
    "ds_read_b64_tr_b16 a[38:39], v9 offset:1024\n"
    "v_mfma_f32_16x16x32_bf16 v[100:103], a[4:7], a[72:75], v[100:103]\n"
    "ds_read_b64_tr_b16 a[40:41], v10 offset:0\n"
    "v_mfma_f32_16x16x32_bf16 v[132:135], a[4:7], a[76:79], v[132:135]\n"
    "s_add_u32 s74, s89, 31744\n"
    "ds_read_b64_tr_b16 a[42:43], v10 offset:1024\n"
    "v_mfma_f32_16x16x32_bf16 v[40:43], a[8:11], a[64:67], v[40:43]\n"
    "ds_read_b64_tr_b16 a[44:45], v11 offset:0\n"
    "v_mfma_f32_16x16x32_bf16 v[72:75], a[8:11], a[68:71], v[72:75]\n"
    "buffer_load_dwordx4 v26, s[48:51], s74 offen offset:1024 lds\n"
    "v_mfma_f32_16x16x32_bf16 v[104:107], a[8:11], a[72:75], v[104:107]\n"
    "ds_read_b64_tr_b16 a[46:47], v11 offset:1024\n"
    "v_mfma_f32_16x16x32_bf16 v[136:139], a[8:11], a[76:79], v[136:139]\n"
    "ds_read_b64_tr_b16 a[48:49], v12 offset:0\n"
    "v_mfma_f32_16x16x32_bf16 v[44:47], a[12:15], a[64:67], v[44:47]\n"
    "s_add_u32 s74, s89, 63488\n"
    "v_mfma_f32_16x16x32_bf16 v[76:79], a[12:15], a[68:71], v[76:79]\n"
    "ds_read_b64_tr_b16 a[50:51], v12 offset:1024\n"
    "v_mfma_f32_16x16x32_bf16 v[108:111], a[12:15], a[72:75], v[108:111]\n"
    "ds_read_b64_tr_b16 a[52:53], v13 offset:0\n"
    "v_mfma_f32_16x16x32_bf16 v[140:143], a[12:15], a[76:79], v[140:143]\n"
    "ds_read_b64_tr_b16 a[54:55], v13 offset:1024\n"
    "v_mfma_f32_16x16x32_bf16 v[48:51], a[16:19], a[64:67], v[48:51]\n"
    "buffer_load_dwordx4 v27, s[48:51], s74 offen offset:2048 lds\n"
    "v_mfma_f32_16x16x32_bf16 v[80:83], a[16:19], a[68:71], v[80:83]\n"
    "ds_read_b64_tr_b16 a[56:57], v14 offset:0\n"
    "v_mfma_f32_16x16x32_bf16 v[112:115], a[16:19], a[72:75], v[112:115]\n"
    "ds_read_b64_tr_b16 a[58:59], v14 offset:1024\n"
    "v_mfma_f32_16x16x32_bf16 v[144:147], a[16:19], a[76:79], v[144:147]\n"
    "s_add_u32 s74, s89, 95232\n"
    "v_mfma_f32_16x16x32_bf16 v[52:55], a[20:23], a[64:67], v[52:55]\n"
    "ds_read_b64_tr_b16 a[60:61], v15 offset:0\n"
    "v_mfma_f32_16x16x32_bf16 v[84:87], a[20:23], a[68:71], v[84:87]\n"
    "ds_read_b64_tr_b16 a[62:63], v15 offset:1024\n"
    "v_mfma_f32_16x16x32_bf16 v[116:119], a[20:23], a[72:75], v[116:119]\n"
    "buffer_load_dwordx4 v27, s[48:51], s74 offen offset:3072 lds\n"
    "ds_read_b128 a[92:95], v7 offset:0\n"
    "v_mfma_f32_16x16x32_bf16 v[148:151], a[20:23], a[76:79], v[148:151]\n"
    "ds_read_b128 a[96:99], v7 offset:2048\n"
    "v_mfma_f32_16x16x32_bf16 v[56:59], a[24:27], a[64:67], v[56:59]\n"
    "s_add_u32 s89, s89, 262144\n"
    "v_mfma_f32_16x16x32_bf16 v[88:91], a[24:27], a[68:71], v[88:91]\n"
    "ds_read_b128 a[100:103], v7 offset:4096\n"
    "v_mfma_f32_16x16x32_bf16 v[120:123], a[24:27], a[72:75], v[120:123]\n"
    "ds_read_b128 a[104:107], v7 offset:6144\n"
    "v_mfma_f32_16x16x32_bf16 v[152:155], a[24:27], a[76:79], v[152:155]\n"
    "v_mfma_f32_16x16x32_bf16 v[60:63], a[28:31], a[64:67], v[60:63]\n"
    "v_mfma_f32_16x16x32_bf16 v[92:95], a[28:31], a[68:71], v[92:95]\n"
    "v_mfma_f32_16x16x32_bf16 v[124:127], a[28:31], a[72:75], v[124:127]\n"
    "v_mfma_f32_16x16x32_bf16 v[156:159], a[28:31], a[76:79], v[156:159]\n"
    "s_waitcnt lgkmcnt(0)\n"
    "s_waitcnt vmcnt(12)\n"
    "s_barrier\n"
    "v_mfma_f32_16x16x32_bf16 v[32:35], a[32:35], a[92:95], v[32:35]\n"
    "s_add_u32 m0, s88, 0\n"
    "v_mfma_f32_16x16x32_bf16 v[64:67], a[32:35], a[96:99], v[64:67]\n"
    "ds_read_b64_tr_b16 a[0:1], v8 offset:8192\n"
    "v_mfma_f32_16x16x32_bf16 v[96:99], a[32:35], a[100:103], v[96:99]\n"
    "ds_read_b64_tr_b16 a[2:3], v8 offset:9216\n"
    "v_mfma_f32_16x16x32_bf16 v[128:131], a[32:35], a[104:107], v[128:131]\n"
    "ds_read_b64_tr_b16 a[4:5], v9 offset:8192\n"
    "v_mfma_f32_16x16x32_bf16 v[36:39], a[36:39], a[92:95], v[36:39]\n"
    "buffer_load_dwordx4 v26, s[48:51], s89 offen offset:0 lds\n"
    "v_mfma_f32_16x16x32_bf16 v[68:71], a[36:39], a[96:99], v[68:71]\n"
    "ds_read_b64_tr_b16 a[6:7], v9 offset:9216\n"
    "v_mfma_f32_16x16x32_bf16 v[100:103], a[36:39], a[100:103], v[100:103]\n"
    "ds_read_b64_tr_b16 a[8:9], v10 offset:8192\n"
    "v_mfma_f32_16x16x32_bf16 v[132:135], a[36:39], a[104:107], v[132:135]\n"
    "s_add_u32 s74, s89, 31744\n"
    "ds_read_b64_tr_b16 a[10:11], v10 offset:9216\n"
    "v_mfma_f32_16x16x32_bf16 v[40:43], a[40:43], a[92:95], v[40:43]\n"
    "ds_read_b64_tr_b16 a[12:13], v11 offset:8192\n"
    "v_mfma_f32_16x16x32_bf16 v[72:75], a[40:43], a[96:99], v[72:75]\n"
    "buffer_load_dwordx4 v26, s[48:51], s74 offen offset:1024 lds\n"
    "v_mfma_f32_16x16x32_bf16 v[104:107], a[40:43], a[100:103], v[104:107]\n"
    "ds_read_b64_tr_b16 a[14:15], v11 offset:9216\n"
    "v_mfma_f32_16x16x32_bf16 v[136:139], a[40:43], a[104:107], v[136:139]\n"
    "ds_read_b64_tr_b16 a[16:17], v12 offset:8192\n"
    "v_mfma_f32_16x16x32_bf16 v[44:47], a[44:47], a[92:95], v[44:47]\n"
    "s_add_u32 s74, s89, 63488\n"
    "v_mfma_f32_16x16x32_bf16 v[76:79], a[44:47], a[96:99], v[76:79]\n"
    "ds_read_b64_tr_b16 a[18:19], v12 offset:9216\n"
    "v_mfma_f32_16x16x32_bf16 v[108:111], a[44:47], a[100:103], v[108:111]\n"
    "ds_read_b64_tr_b16 a[20:21], v13 offset:8192\n"
    "v_mfma_f32_16x16x32_bf16 v[140:143], a[44:47], a[104:107], v[140:143]\n"
    "ds_read_b64_tr_b16 a[22:23], v13 offset:9216\n"
    "v_mfma_f32_16x16x32_bf16 v[48:51], a[48:51], a[92:95], v[48:51]\n"
    "buffer_load_dwordx4 v27, s[48:51], s74 offen offset:2048 lds\n"
    "v_mfma_f32_16x16x32_bf16 v[80:83], a[48:51], a[96:99], v[80:83]\n"
    "ds_read_b64_tr_b16 a[24:25], v14 offset:8192\n"
    "v_mfma_f32_16x16x32_bf16 v[112:115], a[48:51], a[100:103], v[112:115]\n"
    "ds_read_b64_tr_b16 a[26:27], v14 offset:9216\n"
    "v_mfma_f32_16x16x32_bf16 v[144:147], a[48:51], a[104:107], v[144:147]\n"
    "s_add_u32 s74, s89, 95232\n"
    "v_mfma_f32_16x16x32_bf16 v[52:55], a[52:55], a[92:95], v[52:55]\n"
    "ds_read_b64_tr_b16 a[28:29], v15 offset:8192\n"
    "v_mfma_f32_16x16x32_bf16 v[84:87], a[52:55], a[96:99], v[84:87]\n"
    "ds_read_b64_tr_b16 a[30:31], v15 offset:9216\n"
    "v_mfma_f32_16x16x32_bf16 v[116:119], a[52:55], a[100:103], v[116:119]\n"
    "buffer_load_dwordx4 v27, s[48:51], s74 offen offset:3072 lds\n"
    "ds_read_b128 a[64:67], v6 offset:32768\n"
    "v_mfma_f32_16x16x32_bf16 v[148:151], a[52:55], a[104:107], v[148:151]\n"
    "ds_read_b128 a[68:71], v6 offset:34816\n"
    "v_mfma_f32_16x16x32_bf16 v[56:59], a[56:59], a[92:95], v[56:59]\n"
    "s_add_u32 s89, s89, 262144\n"
    "v_mfma_f32_16x16x32_bf16 v[88:91], a[56:59], a[96:99], v[88:91]\n"
    "ds_read_b128 a[72:75], v6 offset:36864\n"
    "v_mfma_f32_16x16x32_bf16 v[120:123], a[56:59], a[100:103], v[120:123]\n"
    "ds_read_b128 a[76:79], v6 offset:38912\n"
    "v_mfma_f32_16x16x32_bf16 v[152:155], a[56:59], a[104:107], v[152:155]\n"
    "v_mfma_f32_16x16x32_bf16 v[60:63], a[60:63], a[92:95], v[60:63]\n"
    "v_mfma_f32_16x16x32_bf16 v[92:95], a[60:63], a[96:99], v[92:95]\n"
    "v_mfma_f32_16x16x32_bf16 v[124:127], a[60:63], a[100:103], v[124:127]\n"
    "v_mfma_f32_16x16x32_bf16 v[156:159], a[60:63], a[104:107], v[156:159]\n"
    "s_waitcnt lgkmcnt(0)\n"
    "s_waitcnt vmcnt(8)\n"
    "s_barrier\n"
    "v_mfma_f32_16x16x32_bf16 v[32:35], a[0:3], a[64:67], v[32:35]\n"
    "s_add_u32 m0, s88, 8192\n"
    "v_mfma_f32_16x16x32_bf16 v[64:67], a[0:3], a[68:71], v[64:67]\n"
    "ds_read_b64_tr_b16 a[32:33], v8 offset:16384\n"
    "v_mfma_f32_16x16x32_bf16 v[96:99], a[0:3], a[72:75], v[96:99]\n"
    "ds_read_b64_tr_b16 a[34:35], v8 offset:17408\n"
    "v_mfma_f32_16x16x32_bf16 v[128:131], a[0:3], a[76:79], v[128:131]\n"
    "ds_read_b64_tr_b16 a[36:37], v9 offset:16384\n"
    "v_mfma_f32_16x16x32_bf16 v[36:39], a[4:7], a[64:67], v[36:39]\n"
    "buffer_load_dwordx4 v26, s[48:51], s89 offen offset:0 lds\n"
    "v_mfma_f32_16x16x32_bf16 v[68:71], a[4:7], a[68:71], v[68:71]\n"
    "ds_read_b64_tr_b16 a[38:39], v9 offset:17408\n"
    "v_mfma_f32_16x16x32_bf16 v[100:103], a[4:7], a[72:75], v[100:103]\n"
    "ds_read_b64_tr_b16 a[40:41], v10 offset:16384\n"
    "v_mfma_f32_16x16x32_bf16 v[132:135], a[4:7], a[76:79], v[132:135]\n"
    "s_add_u32 s74, s89, 31744\n"
    "ds_read_b64_tr_b16 a[42:43], v10 offset:17408\n"
    "v_mfma_f32_16x16x32_bf16 v[40:43], a[8:11], a[64:67], v[40:43]\n"
    "ds_read_b64_tr_b16 a[44:45], v11 offset:16384\n"
    "v_mfma_f32_16x16x32_bf16 v[72:75], a[8:11], a[68:71], v[72:75]\n"
    "buffer_load_dwordx4 v26, s[48:51], s74 offen offset:1024 lds\n"
    "v_mfma_f32_16x16x32_bf16 v[104:107], a[8:11], a[72:75], v[104:107]\n"
    "ds_read_b64_tr_b16 a[46:47], v11 offset:17408\n"
    "v_mfma_f32_16x16x32_bf16 v[136:139], a[8:11], a[76:79], v[136:139]\n"
    "ds_read_b64_tr_b16 a[48:49], v12 offset:16384\n"
    "v_mfma_f32_16x16x32_bf16 v[44:47], a[12:15], a[64:67], v[44:47]\n"
    "s_add_u32 s74, s89, 63488\n"
    "v_mfma_f32_16x16x32_bf16 v[76:79], a[12:15], a[68:71], v[76:79]\n"
    "ds_read_b64_tr_b16 a[50:51], v12 offset:17408\n"
    "v_mfma_f32_16x16x32_bf16 v[108:111], a[12:15], a[72:75], v[108:111]\n"
    "ds_read_b64_tr_b16 a[52:53], v13 offset:16384\n"
    "v_mfma_f32_16x16x32_bf16 v[140:143], a[12:15], a[76:79], v[140:143]\n"
    "ds_read_b64_tr_b16 a[54:55], v13 offset:17408\n"
    "v_mfma_f32_16x16x32_bf16 v[48:51], a[16:19], a[64:67], v[48:51]\n"
    "buffer_load_dwordx4 v27, s[48:51], s74 offen offset:2048 lds\n"
    "v_mfma_f32_16x16x32_bf16 v[80:83], a[16:19], a[68:71], v[80:83]\n"
    "ds_read_b64_tr_b16 a[56:57], v14 offset:16384\n"
    "v_mfma_f32_16x16x32_bf16 v[112:115], a[16:19], a[72:75], v[112:115]\n"
    "ds_read_b64_tr_b16 a[58:59], v14 offset:17408\n"
    "v_mfma_f32_16x16x32_bf16 v[144:147], a[16:19], a[76:79], v[144:147]\n"
    "s_add_u32 s74, s89, 95232\n"
    "v_mfma_f32_16x16x32_bf16 v[52:55], a[20:23], a[64:67], v[52:55]\n"
    "ds_read_b64_tr_b16 a[60:61], v15 offset:16384\n"
    "v_mfma_f32_16x16x32_bf16 v[84:87], a[20:23], a[68:71], v[84:87]\n"
    "ds_read_b64_tr_b16 a[62:63], v15 offset:17408\n"
    "v_mfma_f32_16x16x32_bf16 v[116:119], a[20:23], a[72:75], v[116:119]\n"
    "buffer_load_dwordx4 v27, s[48:51], s74 offen offset:3072 lds\n"
    "ds_read_b128 a[92:95], v7 offset:32768\n"
    "v_mfma_f32_16x16x32_bf16 v[148:151], a[20:23], a[76:79], v[148:151]\n"
    "ds_read_b128 a[96:99], v7 offset:34816\n"
    "v_mfma_f32_16x16x32_bf16 v[56:59], a[24:27], a[64:67], v[56:59]\n"
    "s_add_u32 s89, s89, 262144\n"
    "v_mfma_f32_16x16x32_bf16 v[88:91], a[24:27], a[68:71], v[88:91]\n"
    "ds_read_b128 a[100:103], v7 offset:36864\n"
    "v_mfma_f32_16x16x32_bf16 v[120:123], a[24:27], a[72:75], v[120:123]\n"
    "ds_read_b128 a[104:107], v7 offset:38912\n"
    "v_mfma_f32_16x16x32_bf16 v[152:155], a[24:27], a[76:79], v[152:155]\n"
    "v_mfma_f32_16x16x32_bf16 v[60:63], a[28:31], a[64:67], v[60:63]\n"
    "v_mfma_f32_16x16x32_bf16 v[92:95], a[28:31], a[68:71], v[92:95]\n"
    "v_mfma_f32_16x16x32_bf16 v[124:127], a[28:31], a[72:75], v[124:127]\n"
    "v_mfma_f32_16x16x32_bf16 v[156:159], a[28:31], a[76:79], v[156:159]\n"
    "s_waitcnt lgkmcnt(0)\n"
    "s_waitcnt vmcnt(8)\n"
    "s_barrier\n"
    "v_mfma_f32_16x16x32_bf16 v[32:35], a[32:35], a[92:95], v[32:35]\n"
    "s_add_u32 m0, s88, 16384\n"
    "v_mfma_f32_16x16x32_bf16 v[64:67], a[32:35], a[96:99], v[64:67]\n"
    "ds_read_b64_tr_b16 a[0:1], v8 offset:24576\n"
    "v_mfma_f32_16x16x32_bf16 v[96:99], a[32:35], a[100:103], v[96:99]\n"
    "ds_read_b64_tr_b16 a[2:3], v8 offset:25600\n"
    "v_mfma_f32_16x16x32_bf16 v[128:131], a[32:35], a[104:107], v[128:131]\n"
    "ds_read_b64_tr_b16 a[4:5], v9 offset:24576\n"
    "v_mfma_f32_16x16x32_bf16 v[36:39], a[36:39], a[92:95], v[36:39]\n"
    "buffer_load_dwordx4 v26, s[48:51], s89 offen offset:0 lds\n"
    "v_mfma_f32_16x16x32_bf16 v[68:71], a[36:39], a[96:99], v[68:71]\n"
    "ds_read_b64_tr_b16 a[6:7], v9 offset:25600\n"
    "v_mfma_f32_16x16x32_bf16 v[100:103], a[36:39], a[100:103], v[100:103]\n"
    "ds_read_b64_tr_b16 a[8:9], v10 offset:24576\n"
    "v_mfma_f32_16x16x32_bf16 v[132:135], a[36:39], a[104:107], v[132:135]\n"
    "s_add_u32 s74, s89, 31744\n"
    "ds_read_b64_tr_b16 a[10:11], v10 offset:25600\n"
    "v_mfma_f32_16x16x32_bf16 v[40:43], a[40:43], a[92:95], v[40:43]\n"
    "ds_read_b64_tr_b16 a[12:13], v11 offset:24576\n"
    "v_mfma_f32_16x16x32_bf16 v[72:75], a[40:43], a[96:99], v[72:75]\n"
    "buffer_load_dwordx4 v26, s[48:51], s74 offen offset:1024 lds\n"
    "v_mfma_f32_16x16x32_bf16 v[104:107], a[40:43], a[100:103], v[104:107]\n"
    "ds_read_b64_tr_b16 a[14:15], v11 offset:25600\n"
    "v_mfma_f32_16x16x32_bf16 v[136:139], a[40:43], a[104:107], v[136:139]\n"
    "ds_read_b64_tr_b16 a[16:17], v12 offset:24576\n"
    "v_mfma_f32_16x16x32_bf16 v[44:47], a[44:47], a[92:95], v[44:47]\n"
    "s_add_u32 s74, s89, 63488\n"
    "v_mfma_f32_16x16x32_bf16 v[76:79], a[44:47], a[96:99], v[76:79]\n"
    "ds_read_b64_tr_b16 a[18:19], v12 offset:25600\n"
    "v_mfma_f32_16x16x32_bf16 v[108:111], a[44:47], a[100:103], v[108:111]\n"
    "ds_read_b64_tr_b16 a[20:21], v13 offset:24576\n"
    "v_mfma_f32_16x16x32_bf16 v[140:143], a[44:47], a[104:107], v[140:143]\n"
    "ds_read_b64_tr_b16 a[22:23], v13 offset:25600\n"
    "v_mfma_f32_16x16x32_bf16 v[48:51], a[48:51], a[92:95], v[48:51]\n"
    "buffer_load_dwordx4 v27, s[48:51], s74 offen offset:2048 lds\n"
    "v_mfma_f32_16x16x32_bf16 v[80:83], a[48:51], a[96:99], v[80:83]\n"
    "ds_read_b64_tr_b16 a[24:25], v14 offset:24576\n"
    "v_mfma_f32_16x16x32_bf16 v[112:115], a[48:51], a[100:103], v[112:115]\n"
    "ds_read_b64_tr_b16 a[26:27], v14 offset:25600\n"
    "v_mfma_f32_16x16x32_bf16 v[144:147], a[48:51], a[104:107], v[144:147]\n"
    "s_add_u32 s74, s89, 95232\n"
    "v_mfma_f32_16x16x32_bf16 v[52:55], a[52:55], a[92:95], v[52:55]\n"
    "ds_read_b64_tr_b16 a[28:29], v15 offset:24576\n"
    "v_mfma_f32_16x16x32_bf16 v[84:87], a[52:55], a[96:99], v[84:87]\n"
    "ds_read_b64_tr_b16 a[30:31], v15 offset:25600\n"
    "v_mfma_f32_16x16x32_bf16 v[116:119], a[52:55], a[100:103], v[116:119]\n"
    "buffer_load_dwordx4 v27, s[48:51], s74 offen offset:3072 lds\n"
    "ds_read_b128 a[64:67], v30 offset:0\n"
    "v_mfma_f32_16x16x32_bf16 v[148:151], a[52:55], a[104:107], v[148:151]\n"
    "ds_read_b128 a[68:71], v30 offset:2048\n"
    "v_mfma_f32_16x16x32_bf16 v[56:59], a[56:59], a[92:95], v[56:59]\n"
    "s_add_u32 s89, s89, 262144\n"
    "v_mfma_f32_16x16x32_bf16 v[88:91], a[56:59], a[96:99], v[88:91]\n"
    "ds_read_b128 a[72:75], v30 offset:4096\n"
    "v_mfma_f32_16x16x32_bf16 v[120:123], a[56:59], a[100:103], v[120:123]\n"
    "ds_read_b128 a[76:79], v30 offset:6144\n"
    "v_mfma_f32_16x16x32_bf16 v[152:155], a[56:59], a[104:107], v[152:155]\n"
    "v_mfma_f32_16x16x32_bf16 v[60:63], a[60:63], a[92:95], v[60:63]\n"
    "v_mfma_f32_16x16x32_bf16 v[92:95], a[60:63], a[96:99], v[92:95]\n"
    "v_mfma_f32_16x16x32_bf16 v[124:127], a[60:63], a[100:103], v[124:127]\n"
    "v_mfma_f32_16x16x32_bf16 v[156:159], a[60:63], a[104:107], v[156:159]\n"
    "s_waitcnt lgkmcnt(0)\n"
    "s_waitcnt vmcnt(8)\n"
    "s_barrier\n"
    "v_mfma_f32_16x16x32_bf16 v[32:35], a[0:3], a[64:67], v[32:35]\n"
    "s_add_u32 m0, s64, 0\n"
    "v_mfma_f32_16x16x32_bf16 v[64:67], a[0:3], a[68:71], v[64:67]\n"
    "ds_read_b64_tr_b16 a[32:33], v8 offset:0\n"
    "v_mfma_f32_16x16x32_bf16 v[96:99], a[0:3], a[72:75], v[96:99]\n"
    "ds_read_b64_tr_b16 a[34:35], v8 offset:1024\n"
    "v_mfma_f32_16x16x32_bf16 v[128:131], a[0:3], a[76:79], v[128:131]\n"
    "buffer_load_dwordx4 v20, s[40:43], s66 offen offset:0 nt lds\n"
    "ds_read_b64_tr_b16 a[36:37], v9 offset:0\n"
    "v_mfma_f32_16x16x32_bf16 v[36:39], a[4:7], a[64:67], v[36:39]\n"
    "ds_read_b64_tr_b16 a[38:39], v9 offset:1024\n"
    "v_mfma_f32_16x16x32_bf16 v[68:71], a[4:7], a[68:71], v[68:71]\n"
    "buffer_load_dwordx4 v21, s[40:43], s66 offen offset:1024 nt lds\n"
    "v_mfma_f32_16x16x32_bf16 v[100:103], a[4:7], a[72:75], v[100:103]\n"
    "ds_read_b64_tr_b16 a[40:41], v10 offset:0\n"
    "v_mfma_f32_16x16x32_bf16 v[132:135], a[4:7], a[76:79], v[132:135]\n"
    "ds_read_b64_tr_b16 a[42:43], v10 offset:1024\n"
    "v_mfma_f32_16x16x32_bf16 v[40:43], a[8:11], a[64:67], v[40:43]\n"
    "buffer_load_dwordx4 v20, s[40:43], s66 offen offset:2048 nt lds\n"
    "v_mfma_f32_16x16x32_bf16 v[72:75], a[8:11], a[68:71], v[72:75]\n"
    "ds_read_b64_tr_b16 a[44:45], v11 offset:0\n"
    "v_mfma_f32_16x16x32_bf16 v[104:107], a[8:11], a[72:75], v[104:107]\n"
    "ds_read_b64_tr_b16 a[46:47], v11 offset:1024\n"
    "buffer_load_dwordx4 v21, s[40:43], s66 offen offset:3072 nt lds\n"
    "v_mfma_f32_16x16x32_bf16 v[136:139], a[8:11], a[76:79], v[136:139]\n"
    "ds_read_b64_tr_b16 a[48:49], v12 offset:0\n"
    "v_mfma_f32_16x16x32_bf16 v[44:47], a[12:15], a[64:67], v[44:47]\n"
    "ds_read_b64_tr_b16 a[50:51], v12 offset:1024\n"
    "v_mfma_f32_16x16x32_bf16 v[76:79], a[12:15], a[68:71], v[76:79]\n"
    "s_add_u32 m0, s64, 4096\n"
    "v_mfma_f32_16x16x32_bf16 v[108:111], a[12:15], a[72:75], v[108:111]\n"
    "ds_read_b64_tr_b16 a[52:53], v13 offset:0\n"
    "v_mfma_f32_16x16x32_bf16 v[140:143], a[12:15], a[76:79], v[140:143]\n"
    "buffer_load_dwordx4 v22, s[40:43], s66 offen offset:0 nt lds\n"
    "v_mfma_f32_16x16x32_bf16 v[48:51], a[16:19], a[64:67], v[48:51]\n"
    "ds_read_b64_tr_b16 a[54:55], v13 offset:1024\n"
    "v_mfma_f32_16x16x32_bf16 v[80:83], a[16:19], a[68:71], v[80:83]\n"
    "ds_read_b64_tr_b16 a[56:57], v14 offset:0\n"
    "buffer_load_dwordx4 v23, s[40:43], s66 offen offset:1024 nt lds\n"
    "v_mfma_f32_16x16x32_bf16 v[112:115], a[16:19], a[72:75], v[112:115]\n"
    "ds_read_b64_tr_b16 a[58:59], v14 offset:1024\n"
    "v_mfma_f32_16x16x32_bf16 v[144:147], a[16:19], a[76:79], v[144:147]\n"
    "ds_read_b64_tr_b16 a[60:61], v15 offset:0\n"
    "v_mfma_f32_16x16x32_bf16 v[52:55], a[20:23], a[64:67], v[52:55]\n"
    "buffer_load_dwordx4 v22, s[40:43], s66 offen offset:2048 nt lds\n"
    "v_mfma_f32_16x16x32_bf16 v[84:87], a[20:23], a[68:71], v[84:87]\n"
    "ds_read_b64_tr_b16 a[62:63], v15 offset:1024\n"
    "v_mfma_f32_16x16x32_bf16 v[116:119], a[20:23], a[72:75], v[116:119]\n"
    "ds_read_b128 a[92:95], v31 offset:0\n"
    "v_mfma_f32_16x16x32_bf16 v[148:151], a[20:23], a[76:79], v[148:151]\n"
    "buffer_load_dwordx4 v23, s[40:43], s66 offen offset:3072 nt lds\n"
    "v_mfma_f32_16x16x32_bf16 v[56:59], a[24:27], a[64:67], v[56:59]\n"
    "ds_read_b128 a[96:99], v31 offset:2048\n"
    "v_mfma_f32_16x16x32_bf16 v[88:91], a[24:27], a[68:71], v[88:91]\n"
    "ds_read_b128 a[100:103], v31 offset:4096\n"
    "s_add_u32 s66, s66, 8192\n"
    "v_mfma_f32_16x16x32_bf16 v[120:123], a[24:27], a[72:75], v[120:123]\n"
    "ds_read_b128 a[104:107], v31 offset:6144\n"
    "v_mfma_f32_16x16x32_bf16 v[152:155], a[24:27], a[76:79], v[152:155]\n"
    "v_mfma_f32_16x16x32_bf16 v[60:63], a[28:31], a[64:67], v[60:63]\n"
    "v_mfma_f32_16x16x32_bf16 v[92:95], a[28:31], a[68:71], v[92:95]\n"
    "v_mfma_f32_16x16x32_bf16 v[124:127], a[28:31], a[72:75], v[124:127]\n"
    "v_mfma_f32_16x16x32_bf16 v[156:159], a[28:31], a[76:79], v[156:159]\n"
    "s_waitcnt lgkmcnt(0)\n"
    "s_waitcnt vmcnt(12)\n"
    "s_barrier\n"
    "v_mfma_f32_16x16x32_bf16 v[32:35], a[32:35], a[92:95], v[32:35]\n"
    "s_add_u32 m0, s64, 8192\n"
    "v_mfma_f32_16x16x32_bf16 v[64:67], a[32:35], a[96:99], v[64:67]\n"
    "ds_read_b64_tr_b16 a[0:1], v8 offset:8192\n"
    "v_mfma_f32_16x16x32_bf16 v[96:99], a[32:35], a[100:103], v[96:99]\n"
    "ds_read_b64_tr_b16 a[2:3], v8 offset:9216\n"
    "v_mfma_f32_16x16x32_bf16 v[128:131], a[32:35], a[104:107], v[128:131]\n"
    "buffer_load_dwordx4 v20, s[44:47], s67 offen offset:0 nt lds\n"
    "ds_read_b64_tr_b16 a[4:5], v9 offset:8192\n"
    "v_mfma_f32_16x16x32_bf16 v[36:39], a[36:39], a[92:95], v[36:39]\n"
    "ds_read_b64_tr_b16 a[6:7], v9 offset:9216\n"
    "v_mfma_f32_16x16x32_bf16 v[68:71], a[36:39], a[96:99], v[68:71]\n"
    "buffer_load_dwordx4 v21, s[44:47], s67 offen offset:1024 nt lds\n"
    "v_mfma_f32_16x16x32_bf16 v[100:103], a[36:39], a[100:103], v[100:103]\n"
    "ds_read_b64_tr_b16 a[8:9], v10 offset:8192\n"
    "v_mfma_f32_16x16x32_bf16 v[132:135], a[36:39], a[104:107], v[132:135]\n"
    "ds_read_b64_tr_b16 a[10:11], v10 offset:9216\n"
    "v_mfma_f32_16x16x32_bf16 v[40:43], a[40:43], a[92:95], v[40:43]\n"
    "buffer_load_dwordx4 v20, s[44:47], s67 offen offset:2048 nt lds\n"
    "v_mfma_f32_16x16x32_bf16 v[72:75], a[40:43], a[96:99], v[72:75]\n"
    "ds_read_b64_tr_b16 a[12:13], v11 offset:8192\n"
    "v_mfma_f32_16x16x32_bf16 v[104:107], a[40:43], a[100:103], v[104:107]\n"
    "ds_read_b64_tr_b16 a[14:15], v11 offset:9216\n"
    "buffer_load_dwordx4 v21, s[44:47], s67 offen offset:3072 nt lds\n"
    "v_mfma_f32_16x16x32_bf16 v[136:139], a[40:43], a[104:107], v[136:139]\n"
    "ds_read_b64_tr_b16 a[16:17], v12 offset:8192\n"
    "v_mfma_f32_16x16x32_bf16 v[44:47], a[44:47], a[92:95], v[44:47]\n"
    "ds_read_b64_tr_b16 a[18:19], v12 offset:9216\n"
    "v_mfma_f32_16x16x32_bf16 v[76:79], a[44:47], a[96:99], v[76:79]\n"
    "s_add_u32 m0, s64, 12288\n"
    "v_mfma_f32_16x16x32_bf16 v[108:111], a[44:47], a[100:103], v[108:111]\n"
    "ds_read_b64_tr_b16 a[20:21], v13 offset:8192\n"
    "v_mfma_f32_16x16x32_bf16 v[140:143], a[44:47], a[104:107], v[140:143]\n"
    "buffer_load_dwordx4 v22, s[44:47], s67 offen offset:0 nt lds\n"
    "v_mfma_f32_16x16x32_bf16 v[48:51], a[48:51], a[92:95], v[48:51]\n"
    "ds_read_b64_tr_b16 a[22:23], v13 offset:9216\n"
    "v_mfma_f32_16x16x32_bf16 v[80:83], a[48:51], a[96:99], v[80:83]\n"
    "ds_read_b64_tr_b16 a[24:25], v14 offset:8192\n"
    "buffer_load_dwordx4 v23, s[44:47], s67 offen offset:1024 nt lds\n"
    "v_mfma_f32_16x16x32_bf16 v[112:115], a[48:51], a[100:103], v[112:115]\n"
    "ds_read_b64_tr_b16 a[26:27], v14 offset:9216\n"
    "v_mfma_f32_16x16x32_bf16 v[144:147], a[48:51], a[104:107], v[144:147]\n"
    "ds_read_b64_tr_b16 a[28:29], v15 offset:8192\n"
    "v_mfma_f32_16x16x32_bf16 v[52:55], a[52:55], a[92:95], v[52:55]\n"
    "buffer_load_dwordx4 v22, s[44:47], s67 offen offset:2048 nt lds\n"
    "v_mfma_f32_16x16x32_bf16 v[84:87], a[52:55], a[96:99], v[84:87]\n"
    "ds_read_b64_tr_b16 a[30:31], v15 offset:9216\n"
    "v_mfma_f32_16x16x32_bf16 v[116:119], a[52:55], a[100:103], v[116:119]\n"
    "ds_read_b128 a[64:67], v30 offset:16384\n"
    "v_mfma_f32_16x16x32_bf16 v[148:151], a[52:55], a[104:107], v[148:151]\n"
    "buffer_load_dwordx4 v23, s[44:47], s67 offen offset:3072 nt lds\n"
    "v_mfma_f32_16x16x32_bf16 v[56:59], a[56:59], a[92:95], v[56:59]\n"
    "ds_read_b128 a[68:71], v30 offset:18432\n"
    "v_mfma_f32_16x16x32_bf16 v[88:91], a[56:59], a[96:99], v[88:91]\n"
    "ds_read_b128 a[72:75], v30 offset:20480\n"
    "s_add_u32 s67, s67, 8192\n"
    "v_mfma_f32_16x16x32_bf16 v[120:123], a[56:59], a[100:103], v[120:123]\n"
    "ds_read_b128 a[76:79], v30 offset:22528\n"
    "v_mfma_f32_16x16x32_bf16 v[152:155], a[56:59], a[104:107], v[152:155]\n"
    "v_mfma_f32_16x16x32_bf16 v[60:63], a[60:63], a[92:95], v[60:63]\n"
    "v_mfma_f32_16x16x32_bf16 v[92:95], a[60:63], a[96:99], v[92:95]\n"
    "v_mfma_f32_16x16x32_bf16 v[124:127], a[60:63], a[100:103], v[124:127]\n"
    "v_mfma_f32_16x16x32_bf16 v[156:159], a[60:63], a[104:107], v[156:159]\n"
    "s_waitcnt lgkmcnt(0)\n"
    "s_waitcnt vmcnt(16)\n"
    "s_barrier\n"
    "v_mfma_f32_16x16x32_bf16 v[32:35], a[0:3], a[64:67], v[32:35]\n"
    "s_add_u32 m0, s64, 16384\n"
    "v_mfma_f32_16x16x32_bf16 v[64:67], a[0:3], a[68:71], v[64:67]\n"
    "ds_read_b64_tr_b16 a[32:33], v8 offset:16384\n"
    "v_mfma_f32_16x16x32_bf16 v[96:99], a[0:3], a[72:75], v[96:99]\n"
    "ds_read_b64_tr_b16 a[34:35], v8 offset:17408\n"
    "v_mfma_f32_16x16x32_bf16 v[128:131], a[0:3], a[76:79], v[128:131]\n"
    "buffer_load_dwordx4 v20, s[40:43], s66 offen offset:0 nt lds\n"
    "ds_read_b64_tr_b16 a[36:37], v9 offset:16384\n"
    "v_mfma_f32_16x16x32_bf16 v[36:39], a[4:7], a[64:67], v[36:39]\n"
    "ds_read_b64_tr_b16 a[38:39], v9 offset:17408\n"
    "v_mfma_f32_16x16x32_bf16 v[68:71], a[4:7], a[68:71], v[68:71]\n"
    "buffer_load_dwordx4 v21, s[40:43], s66 offen offset:1024 nt lds\n"
    "v_mfma_f32_16x16x32_bf16 v[100:103], a[4:7], a[72:75], v[100:103]\n"
    "ds_read_b64_tr_b16 a[40:41], v10 offset:16384\n"
    "v_mfma_f32_16x16x32_bf16 v[132:135], a[4:7], a[76:79], v[132:135]\n"
    "ds_read_b64_tr_b16 a[42:43], v10 offset:17408\n"
    "v_mfma_f32_16x16x32_bf16 v[40:43], a[8:11], a[64:67], v[40:43]\n"
    "buffer_load_dwordx4 v20, s[40:43], s66 offen offset:2048 nt lds\n"
    "v_mfma_f32_16x16x32_bf16 v[72:75], a[8:11], a[68:71], v[72:75]\n"
    "ds_read_b64_tr_b16 a[44:45], v11 offset:16384\n"
    "v_mfma_f32_16x16x32_bf16 v[104:107], a[8:11], a[72:75], v[104:107]\n"
    "ds_read_b64_tr_b16 a[46:47], v11 offset:17408\n"
    "buffer_load_dwordx4 v21, s[40:43], s66 offen offset:3072 nt lds\n"
    "v_mfma_f32_16x16x32_bf16 v[136:139], a[8:11], a[76:79], v[136:139]\n"
    "ds_read_b64_tr_b16 a[48:49], v12 offset:16384\n"
    "v_mfma_f32_16x16x32_bf16 v[44:47], a[12:15], a[64:67], v[44:47]\n"
    "ds_read_b64_tr_b16 a[50:51], v12 offset:17408\n"
    "v_mfma_f32_16x16x32_bf16 v[76:79], a[12:15], a[68:71], v[76:79]\n"
    "s_add_u32 m0, s64, 20480\n"
    "v_mfma_f32_16x16x32_bf16 v[108:111], a[12:15], a[72:75], v[108:111]\n"
    "ds_read_b64_tr_b16 a[52:53], v13 offset:16384\n"
    "v_mfma_f32_16x16x32_bf16 v[140:143], a[12:15], a[76:79], v[140:143]\n"
    "buffer_load_dwordx4 v22, s[40:43], s66 offen offset:0 nt lds\n"
    "v_mfma_f32_16x16x32_bf16 v[48:51], a[16:19], a[64:67], v[48:51]\n"
    "ds_read_b64_tr_b16 a[54:55], v13 offset:17408\n"
    "v_mfma_f32_16x16x32_bf16 v[80:83], a[16:19], a[68:71], v[80:83]\n"
    "ds_read_b64_tr_b16 a[56:57], v14 offset:16384\n"
    "buffer_load_dwordx4 v23, s[40:43], s66 offen offset:1024 nt lds\n"
    "v_mfma_f32_16x16x32_bf16 v[112:115], a[16:19], a[72:75], v[112:115]\n"
    "ds_read_b64_tr_b16 a[58:59], v14 offset:17408\n"
    "v_mfma_f32_16x16x32_bf16 v[144:147], a[16:19], a[76:79], v[144:147]\n"
    "ds_read_b64_tr_b16 a[60:61], v15 offset:16384\n"
    "v_mfma_f32_16x16x32_bf16 v[52:55], a[20:23], a[64:67], v[52:55]\n"
    "buffer_load_dwordx4 v22, s[40:43], s66 offen offset:2048 nt lds\n"
    "v_mfma_f32_16x16x32_bf16 v[84:87], a[20:23], a[68:71], v[84:87]\n"
    "ds_read_b64_tr_b16 a[62:63], v15 offset:17408\n"
    "v_mfma_f32_16x16x32_bf16 v[116:119], a[20:23], a[72:75], v[116:119]\n"
    "ds_read_b128 a[92:95], v31 offset:16384\n"
    "v_mfma_f32_16x16x32_bf16 v[148:151], a[20:23], a[76:79], v[148:151]\n"
    "buffer_load_dwordx4 v23, s[40:43], s66 offen offset:3072 nt lds\n"
    "v_mfma_f32_16x16x32_bf16 v[56:59], a[24:27], a[64:67], v[56:59]\n"
    "ds_read_b128 a[96:99], v31 offset:18432\n"
    "v_mfma_f32_16x16x32_bf16 v[88:91], a[24:27], a[68:71], v[88:91]\n"
    "ds_read_b128 a[100:103], v31 offset:20480\n"
    "s_add_u32 s66, s66, 8192\n"
    "v_mfma_f32_16x16x32_bf16 v[120:123], a[24:27], a[72:75], v[120:123]\n"
    "ds_read_b128 a[104:107], v31 offset:22528\n"
    "v_mfma_f32_16x16x32_bf16 v[152:155], a[24:27], a[76:79], v[152:155]\n"
    "v_mfma_f32_16x16x32_bf16 v[60:63], a[28:31], a[64:67], v[60:63]\n"
    "v_mfma_f32_16x16x32_bf16 v[92:95], a[28:31], a[68:71], v[92:95]\n"
    "v_mfma_f32_16x16x32_bf16 v[124:127], a[28:31], a[72:75], v[124:127]\n"
    "v_mfma_f32_16x16x32_bf16 v[156:159], a[28:31], a[76:79], v[156:159]\n"
    "s_waitcnt lgkmcnt(0)\n"
    "s_barrier\n"
    "v_mfma_f32_16x16x32_bf16 v[32:35], a[32:35], a[92:95], v[32:35]\n"
    "v_mfma_f32_16x16x32_bf16 v[36:39], a[36:39], a[92:95], v[36:39]\n"
    "v_mfma_f32_16x16x32_bf16 v[40:43], a[40:43], a[92:95], v[40:43]\n"
    "v_mfma_f32_16x16x32_bf16 v[44:47], a[44:47], a[92:95], v[44:47]\n"
    "v_mfma_f32_16x16x32_bf16 v[48:51], a[48:51], a[92:95], v[48:51]\n"
    "v_mfma_f32_16x16x32_bf16 v[52:55], a[52:55], a[92:95], v[52:55]\n"
    "v_mfma_f32_16x16x32_bf16 v[56:59], a[56:59], a[92:95], v[56:59]\n"
    "v_mfma_f32_16x16x32_bf16 v[60:63], a[60:63], a[92:95], v[60:63]\n"
    "v_mfma_f32_16x16x32_bf16 v[64:67], a[32:35], a[96:99], v[64:67]\n"
    "v_pk_mul_f32 v[32:33], v[32:33], s[80:81]\n"
    "v_pk_mul_f32 v[34:35], v[34:35], s[80:81]\n"
    "v_pk_mul_f32 v[36:37], v[36:37], s[80:81]\n"
    "v_pk_mul_f32 v[38:39], v[38:39], s[80:81]\n"
    "v_pk_mul_f32 v[40:41], v[40:41], s[80:81]\n"
    "v_mfma_f32_16x16x32_bf16 v[68:71], a[36:39], a[96:99], v[68:71]\n"
    "v_pk_mul_f32 v[42:43], v[42:43], s[80:81]\n"
    "v_pk_mul_f32 v[44:45], v[44:45], s[80:81]\n"
    "v_pk_mul_f32 v[46:47], v[46:47], s[80:81]\n"
    "v_pk_mul_f32 v[48:49], v[48:49], s[80:81]\n"
    "v_pk_mul_f32 v[50:51], v[50:51], s[80:81]\n"
    "v_mfma_f32_16x16x32_bf16 v[72:75], a[40:43], a[96:99], v[72:75]\n"
    "v_pk_mul_f32 v[52:53], v[52:53], s[80:81]\n"
    "v_pk_mul_f32 v[54:55], v[54:55], s[80:81]\n"
    "v_pk_mul_f32 v[56:57], v[56:57], s[80:81]\n"
    "v_pk_mul_f32 v[58:59], v[58:59], s[80:81]\n"
    "v_pk_mul_f32 v[60:61], v[60:61], s[80:81]\n"
    "v_mfma_f32_16x16x32_bf16 v[76:79], a[44:47], a[96:99], v[76:79]\n"
    "v_pk_mul_f32 v[62:63], v[62:63], s[80:81]\n"
    "v_cvt_pk_bf16_f32 v32, v32, v33\n"
    "v_cvt_pk_bf16_f32 v33, v34, v35\n"
    "v_cvt_pk_bf16_f32 v34, v36, v37\n"
    "v_cvt_pk_bf16_f32 v35, v38, v39\n"
    "v_cvt_pk_bf16_f32 v36, v40, v41\n"
    "v_cvt_pk_bf16_f32 v37, v42, v43\n"
    "v_cvt_pk_bf16_f32 v38, v44, v45\n"
    "v_cvt_pk_bf16_f32 v39, v46, v47\n"
    "v_mfma_f32_16x16x32_bf16 v[80:83], a[48:51], a[96:99], v[80:83]\n"
    "v_cvt_pk_bf16_f32 v40, v48, v49\n"
    "v_cvt_pk_bf16_f32 v41, v50, v51\n"
    "v_cvt_pk_bf16_f32 v42, v52, v53\n"
    "v_cvt_pk_bf16_f32 v43, v54, v55\n"
    "v_cvt_pk_bf16_f32 v44, v56, v57\n"
    "v_cvt_pk_bf16_f32 v45, v58, v59\n"
    "v_cvt_pk_bf16_f32 v46, v60, v61\n"
    "v_cvt_pk_bf16_f32 v47, v62, v63\n"
    "v_permlane32_swap_b32 v32, v34\n"
    "v_mfma_f32_16x16x32_bf16 v[84:87], a[52:55], a[96:99], v[84:87]\n"
    "v_permlane32_swap_b32 v33, v35\n"
    "v_permlane32_swap_b32 v36, v38\n"
    "v_permlane32_swap_b32 v37, v39\n"
    "v_permlane32_swap_b32 v40, v42\n"
    "v_permlane32_swap_b32 v41, v43\n"
    "v_mfma_f32_16x16x32_bf16 v[88:91], a[56:59], a[96:99], v[88:91]\n"
    "v_permlane32_swap_b32 v44, v46\n"
    "v_permlane32_swap_b32 v45, v47\n"
    "v_permlane16_swap_b32 v32, v34\n"
    "v_permlane16_swap_b32 v33, v35\n"
    "v_permlane16_swap_b32 v36, v38\n"
    "v_mfma_f32_16x16x32_bf16 v[92:95], a[60:63], a[96:99], v[92:95]\n"
    "v_permlane16_swap_b32 v37, v39\n"
    "v_permlane16_swap_b32 v40, v42\n"
    "v_permlane16_swap_b32 v41, v43\n"
    "v_permlane16_swap_b32 v44, v46\n"
    "v_permlane16_swap_b32 v45, v47\n"
    "v_mfma_f32_16x16x32_bf16 v[96:99], a[32:35], a[100:103], v[96:99]\n"
    "v_pk_mul_f32 v[64:65], v[64:65], s[80:81]\n"
    "v_pk_mul_f32 v[66:67], v[66:67], s[80:81]\n"
    "v_pk_mul_f32 v[68:69], v[68:69], s[80:81]\n"
    "v_pk_mul_f32 v[70:71], v[70:71], s[80:81]\n"
    "v_pk_mul_f32 v[72:73], v[72:73], s[80:81]\n"
    "v_mfma_f32_16x16x32_bf16 v[100:103], a[36:39], a[100:103], v[100:103]\n"
    "v_pk_mul_f32 v[74:75], v[74:75], s[80:81]\n"
    "v_pk_mul_f32 v[76:77], v[76:77], s[80:81]\n"
    "v_pk_mul_f32 v[78:79], v[78:79], s[80:81]\n"
    "v_pk_mul_f32 v[80:81], v[80:81], s[80:81]\n"
    "v_pk_mul_f32 v[82:83], v[82:83], s[80:81]\n"
    "v_mfma_f32_16x16x32_bf16 v[104:107], a[40:43], a[100:103], v[104:107]\n"
    "v_pk_mul_f32 v[84:85], v[84:85], s[80:81]\n"
    "v_pk_mul_f32 v[86:87], v[86:87], s[80:81]\n"
    "v_pk_mul_f32 v[88:89], v[88:89], s[80:81]\n"
    "v_pk_mul_f32 v[90:91], v[90:91], s[80:81]\n"
    "v_pk_mul_f32 v[92:93], v[92:93], s[80:81]\n"
    "v_mfma_f32_16x16x32_bf16 v[108:111], a[44:47], a[100:103], v[108:111]\n"
    "v_pk_mul_f32 v[94:95], v[94:95], s[80:81]\n"
    "v_cvt_pk_bf16_f32 v48, v64, v65\n"
    "v_cvt_pk_bf16_f32 v49, v66, v67\n"
    "v_cvt_pk_bf16_f32 v50, v68, v69\n"
    "v_cvt_pk_bf16_f32 v51, v70, v71\n"
    "v_cvt_pk_bf16_f32 v52, v72, v73\n"
    "v_cvt_pk_bf16_f32 v53, v74, v75\n"
    "v_cvt_pk_bf16_f32 v54, v76, v77\n"
    "v_cvt_pk_bf16_f32 v55, v78, v79\n"
    "v_mfma_f32_16x16x32_bf16 v[112:115], a[48:51], a[100:103], v[112:115]\n"
    "v_cvt_pk_bf16_f32 v56, v80, v81\n"
    "v_cvt_pk_bf16_f32 v57, v82, v83\n"
    "v_cvt_pk_bf16_f32 v58, v84, v85\n"
    "v_cvt_pk_bf16_f32 v59, v86, v87\n"
    "v_cvt_pk_bf16_f32 v60, v88, v89\n"
    "v_cvt_pk_bf16_f32 v61, v90, v91\n"
    "v_cvt_pk_bf16_f32 v62, v92, v93\n"
    "v_cvt_pk_bf16_f32 v63, v94, v95\n"
    "v_permlane32_swap_b32 v48, v50\n"
    "v_mfma_f32_16x16x32_bf16 v[116:119], a[52:55], a[100:103], v[116:119]\n"
    "v_permlane32_swap_b32 v49, v51\n"
    "v_permlane32_swap_b32 v52, v54\n"
    "v_permlane32_swap_b32 v53, v55\n"
    "v_permlane32_swap_b32 v56, v58\n"
    "v_permlane32_swap_b32 v57, v59\n"
    "v_mfma_f32_16x16x32_bf16 v[120:123], a[56:59], a[100:103], v[120:123]\n"
    "v_permlane32_swap_b32 v60, v62\n"
    "v_permlane32_swap_b32 v61, v63\n"
    "v_permlane16_swap_b32 v48, v50\n"
    "v_permlane16_swap_b32 v49, v51\n"
    "v_permlane16_swap_b32 v52, v54\n"
    "v_mfma_f32_16x16x32_bf16 v[124:127], a[60:63], a[100:103], v[124:127]\n"
    "v_permlane16_swap_b32 v53, v55\n"
    "v_permlane16_swap_b32 v56, v58\n"
    "v_permlane16_swap_b32 v57, v59\n"
    "v_permlane16_swap_b32 v60, v62\n"
    "v_permlane16_swap_b32 v61, v63\n"
    "v_mfma_f32_16x16x32_bf16 v[128:131], a[32:35], a[104:107], v[128:131]\n"
    "v_pk_mul_f32 v[96:97], v[96:97], s[80:81]\n"
    "v_pk_mul_f32 v[98:99], v[98:99], s[80:81]\n"
    "v_pk_mul_f32 v[100:101], v[100:101], s[80:81]\n"
    "v_pk_mul_f32 v[102:103], v[102:103], s[80:81]\n"
    "v_pk_mul_f32 v[104:105], v[104:105], s[80:81]\n"
    "v_mfma_f32_16x16x32_bf16 v[132:135], a[36:39], a[104:107], v[132:135]\n"
    "v_pk_mul_f32 v[106:107], v[106:107], s[80:81]\n"
    "v_pk_mul_f32 v[108:109], v[108:109], s[80:81]\n"
    "v_pk_mul_f32 v[110:111], v[110:111], s[80:81]\n"
    "v_pk_mul_f32 v[112:113], v[112:113], s[80:81]\n"
    "v_pk_mul_f32 v[114:115], v[114:115], s[80:81]\n"
    "v_mfma_f32_16x16x32_bf16 v[136:139], a[40:43], a[104:107], v[136:139]\n"
    "v_pk_mul_f32 v[116:117], v[116:117], s[80:81]\n"
    "v_pk_mul_f32 v[118:119], v[118:119], s[80:81]\n"
    "v_pk_mul_f32 v[120:121], v[120:121], s[80:81]\n"
    "v_pk_mul_f32 v[122:123], v[122:123], s[80:81]\n"
    "v_pk_mul_f32 v[124:125], v[124:125], s[80:81]\n"
    "v_mfma_f32_16x16x32_bf16 v[140:143], a[44:47], a[104:107], v[140:143]\n"
    "v_pk_mul_f32 v[126:127], v[126:127], s[80:81]\n"
    "v_cvt_pk_bf16_f32 v64, v96, v97\n"
    "v_cvt_pk_bf16_f32 v65, v98, v99\n"
    "v_cvt_pk_bf16_f32 v66, v100, v101\n"
    "v_cvt_pk_bf16_f32 v67, v102, v103\n"
    "v_cvt_pk_bf16_f32 v68, v104, v105\n"
    "v_cvt_pk_bf16_f32 v69, v106, v107\n"
    "v_cvt_pk_bf16_f32 v70, v108, v109\n"
    "v_cvt_pk_bf16_f32 v71, v110, v111\n"
    "v_mfma_f32_16x16x32_bf16 v[144:147], a[48:51], a[104:107], v[144:147]\n"
    "v_cvt_pk_bf16_f32 v72, v112, v113\n"
    "v_cvt_pk_bf16_f32 v73, v114, v115\n"
    "v_cvt_pk_bf16_f32 v74, v116, v117\n"
    "v_cvt_pk_bf16_f32 v75, v118, v119\n"
    "v_cvt_pk_bf16_f32 v76, v120, v121\n"
    "v_cvt_pk_bf16_f32 v77, v122, v123\n"
    "v_cvt_pk_bf16_f32 v78, v124, v125\n"
    "v_cvt_pk_bf16_f32 v79, v126, v127\n"
    "v_permlane32_swap_b32 v64, v66\n"
    "v_mfma_f32_16x16x32_bf16 v[148:151], a[52:55], a[104:107], v[148:151]\n"
    "v_permlane32_swap_b32 v65, v67\n"
    "v_permlane32_swap_b32 v68, v70\n"
    "v_permlane32_swap_b32 v69, v71\n"
    "v_permlane32_swap_b32 v72, v74\n"
    "v_permlane32_swap_b32 v73, v75\n"
    "v_mfma_f32_16x16x32_bf16 v[152:155], a[56:59], a[104:107], v[152:155]\n"
    "v_permlane32_swap_b32 v76, v78\n"
    "v_permlane32_swap_b32 v77, v79\n"
    "v_permlane16_swap_b32 v64, v66\n"
    "v_permlane16_swap_b32 v65, v67\n"
    "v_permlane16_swap_b32 v68, v70\n"
    "v_mfma_f32_16x16x32_bf16 v[156:159], a[60:63], a[104:107], v[156:159]\n"
    "v_permlane16_swap_b32 v69, v71\n"
    "v_permlane16_swap_b32 v72, v74\n"
    "v_permlane16_swap_b32 v73, v75\n"
    "v_permlane16_swap_b32 v76, v78\n"
    "v_permlane16_swap_b32 v77, v79\n"
    "v_pk_mul_f32 v[128:129], v[128:129], s[80:81]\n"
    "v_pk_mul_f32 v[130:131], v[130:131], s[80:81]\n"
    "v_pk_mul_f32 v[132:133], v[132:133], s[80:81]\n"
    "v_pk_mul_f32 v[134:135], v[134:135], s[80:81]\n"
    "v_pk_mul_f32 v[136:137], v[136:137], s[80:81]\n"
    "v_pk_mul_f32 v[138:139], v[138:139], s[80:81]\n"
    "v_pk_mul_f32 v[140:141], v[140:141], s[80:81]\n"
    "v_pk_mul_f32 v[142:143], v[142:143], s[80:81]\n"
    "v_pk_mul_f32 v[144:145], v[144:145], s[80:81]\n"
    "v_pk_mul_f32 v[146:147], v[146:147], s[80:81]\n"
    "v_pk_mul_f32 v[148:149], v[148:149], s[80:81]\n"
    "v_pk_mul_f32 v[150:151], v[150:151], s[80:81]\n"
    "v_pk_mul_f32 v[152:153], v[152:153], s[80:81]\n"
    "v_pk_mul_f32 v[154:155], v[154:155], s[80:81]\n"
    "v_pk_mul_f32 v[156:157], v[156:157], s[80:81]\n"
    "v_pk_mul_f32 v[158:159], v[158:159], s[80:81]\n"
    "v_cvt_pk_bf16_f32 v80, v128, v129\n"
    "v_cvt_pk_bf16_f32 v81, v130, v131\n"
    "v_cvt_pk_bf16_f32 v82, v132, v133\n"
    "v_cvt_pk_bf16_f32 v83, v134, v135\n"
    "v_cvt_pk_bf16_f32 v84, v136, v137\n"
    "v_cvt_pk_bf16_f32 v85, v138, v139\n"
    "v_cvt_pk_bf16_f32 v86, v140, v141\n"
    "v_cvt_pk_bf16_f32 v87, v142, v143\n"
    "v_cvt_pk_bf16_f32 v88, v144, v145\n"
    "v_cvt_pk_bf16_f32 v89, v146, v147\n"
    "v_cvt_pk_bf16_f32 v90, v148, v149\n"
    "v_cvt_pk_bf16_f32 v91, v150, v151\n"
    "v_cvt_pk_bf16_f32 v92, v152, v153\n"
    "v_cvt_pk_bf16_f32 v93, v154, v155\n"
    "v_cvt_pk_bf16_f32 v94, v156, v157\n"
    "v_cvt_pk_bf16_f32 v95, v158, v159\n"
    "v_permlane32_swap_b32 v80, v82\n"
    "v_permlane32_swap_b32 v81, v83\n"
    "v_permlane32_swap_b32 v84, v86\n"
    "v_permlane32_swap_b32 v85, v87\n"
    "v_permlane32_swap_b32 v88, v90\n"
    "v_permlane32_swap_b32 v89, v91\n"
    "v_permlane32_swap_b32 v92, v94\n"
    "v_permlane32_swap_b32 v93, v95\n"
    "v_permlane16_swap_b32 v80, v82\n"
    "v_permlane16_swap_b32 v81, v83\n"
    "v_permlane16_swap_b32 v84, v86\n"
    "v_permlane16_swap_b32 v85, v87\n"
    "v_permlane16_swap_b32 v88, v90\n"
    "v_permlane16_swap_b32 v89, v91\n"
    "v_permlane16_swap_b32 v92, v94\n"
    "v_permlane16_swap_b32 v93, v95\n"
    "s_waitcnt lgkmcnt(0)\n"
    "s_barrier\n"
    "s_mul_i32 s74, %[wid], 16384\n"
    "s_add_u32 s74, s74, %[lds]\n"
    "s_add_u32 s74, s74, 131072\n"
    "v_add_u32 v6, s74, v24\n"
    "ds_read_b128 v[96:99], v6 offset:0\n"
    "ds_read_b128 v[100:103], v6 offset:1024\n"
    "ds_read_b128 v[104:107], v6 offset:2048\n"
    "ds_read_b128 v[108:111], v6 offset:3072\n"
    "ds_read_b128 v[112:115], v6 offset:4096\n"
    "ds_read_b128 v[116:119], v6 offset:5120\n"
    "ds_read_b128 v[120:123], v6 offset:6144\n"
    "ds_read_b128 v[124:127], v6 offset:7168\n"
    "ds_read_b128 v[128:131], v6 offset:8192\n"
    "ds_read_b128 v[132:135], v6 offset:9216\n"
    "ds_read_b128 v[136:139], v6 offset:10240\n"
    "ds_read_b128 v[140:143], v6 offset:11264\n"
    "v_mov_b64 v[248:249], 0\n"
    "v_mov_b64 v[250:251], 0\n"
    "v_mov_b32 v7, %[lds]\n"
    "v_add_u32 v7, 146432, v7\n"
    "ds_write_b128 v7, v[248:251]\n"
    "s_waitcnt vmcnt(16)\n"
    "ds_read_b128 a[224:227], v16 offset:0\n"
    "ds_read_b128 a[240:243], v16 offset:4096\n"
    "ds_read_b128 a[228:231], v17 offset:0\n"
    "ds_read_b128 a[244:247], v17 offset:4096\n"
    "ds_read_b128 a[232:235], v18 offset:0\n"
    "ds_read_b128 a[248:251], v18 offset:4096\n"
    "ds_read_b128 a[236:239], v19 offset:0\n"
    "ds_read_b128 a[252:255], v19 offset:4096\n"
    "s_waitcnt lgkmcnt(0)\n"
    "s_barrier\n"
    "s_add_u32 m0, s64, 24576\n"
    "s_nop 0\n"
    "buffer_load_dwordx4 v20, s[44:47], s67 offen offset:0 nt lds\n"
    "buffer_load_dwordx4 v21, s[44:47], s67 offen offset:1024 nt lds\n"
    "buffer_load_dwordx4 v20, s[44:47], s67 offen offset:2048 nt lds\n"
    "buffer_load_dwordx4 v21, s[44:47], s67 offen offset:3072 nt lds\n"
    "s_add_u32 m0, s64, 28672\n"
    "s_nop 0\n"
    "buffer_load_dwordx4 v22, s[44:47], s67 offen offset:0 nt lds\n"
    "buffer_load_dwordx4 v23, s[44:47], s67 offen offset:1024 nt lds\n"
    "buffer_load_dwordx4 v22, s[44:47], s67 offen offset:2048 nt lds\n"
    "buffer_load_dwordx4 v23, s[44:47], s67 offen offset:3072 nt lds\n"
    "s_add_u32 s67, s67, 8192\n"
    "v_subrev_u32 v8, 65536, v8\n"
    "v_subrev_u32 v9, 65536, v9\n"
    "v_subrev_u32 v10, 65536, v10\n"
    "v_subrev_u32 v11, 65536, v11\n"
    "v_subrev_u32 v12, 65536, v12\n"
    "v_subrev_u32 v13, 65536, v13\n"
    "v_subrev_u32 v14, 65536, v14\n"
    "v_subrev_u32 v15, 65536, v15\n"
    "v_lshrrev_b32 v26, 4, v25\n"
    "v_lshlrev_b32 v26, 10, v26\n"
    "v_sub_u32 v8, v8, v26\n"
    "v_sub_u32 v9, v9, v26\n"
    "v_sub_u32 v10, v10, v26\n"
    "v_sub_u32 v11, v11, v26\n"
    "v_sub_u32 v12, v12, v26\n"
    "v_sub_u32 v13, v13, v26\n"
    "v_sub_u32 v14, v14, v26\n"
    "v_sub_u32 v15, v15, v26\n"
    "v_mov_b32 v228, 0x3f803f80\n"
    "v_mov_b32 v229, v228\n"
    "v_mov_b32 v230, v228\n"
    "v_mov_b32 v231, v228\n"
    "s_mov_b32 s88, 31744\n"
    "s_mov_b64 s[60:61], %[pto]\n"
    "s_and_b32 s61, s61, 0xffff\n"
    "s_mov_b32 s62, 8192\n"
    "s_mov_b32 s63, 0x20000\n"
    "s_mov_b32 s86, 0\n"
    "s_lshl_b32 s73, %[wid], 14\n"
    "s_add_u32 s73, s73, 7168\n"
    "s_cmp_ge_u32 %[wid], 2\n"
    "s_cselect_b32 s73, s88, s73\n"
    "s_add_u32 s73, s73, %[lds]\n"
    "s_add_u32 s73, s73, 131072\n"
    "v_mov_b32 v27, v24\n"
    "s_branch .LW_att_%=\n"
    ".LL_start_%=:\n"
    "s_add_u32 m0, s72, 0\n"
    "s_nop 0\n"
    "buffer_load_dwordx4 v28, s[52:55], s69 offen offset:0 lds\n"
    "s_add_u32 s76, s69, 7168\n"
    "buffer_load_dwordx4 v29, s[52:55], s76 offen offset:1024 lds\n"
    "s_add_u32 s76, s69, 14336\n"
    "buffer_load_dwordx4 v28, s[52:55], s76 offen offset:2048 lds\n"
    "s_add_u32 s76, s69, 21504\n"
    "buffer_load_dwordx4 v29, s[52:55], s76 offen offset:3072 lds\n"
    "s_add_u32 s69, s69, 128\n"
    "s_add_u32 m0, s88, 24576\n"
    "s_nop 0\n"
    "buffer_load_dwordx4 v26, s[48:51], s89 offen offset:0 lds\n"
    "s_add_u32 s74, s89, 31744\n"
    "buffer_load_dwordx4 v26, s[48:51], s74 offen offset:1024 lds\n"
    "s_add_u32 s74, s89, 63488\n"
    "buffer_load_dwordx4 v27, s[48:51], s74 offen offset:2048 lds\n"
    "s_add_u32 s74, s89, 95232\n"
    "buffer_load_dwordx4 v27, s[48:51], s74 offen offset:3072 lds\n"
    "s_add_u32 s89, s89, 262144\n"
    "s_add_u32 m0, s88, 0\n"
    "s_nop 0\n"
    "buffer_load_dwordx4 v26, s[48:51], s89 offen offset:0 lds\n"
    "s_add_u32 s74, s89, 31744\n"
    "buffer_load_dwordx4 v26, s[48:51], s74 offen offset:1024 lds\n"
    "s_add_u32 s74, s89, 63488\n"
    "buffer_load_dwordx4 v27, s[48:51], s74 offen offset:2048 lds\n"
    "s_add_u32 s74, s89, 95232\n"
    "buffer_load_dwordx4 v27, s[48:51], s74 offen offset:3072 lds\n"
    "s_add_u32 s89, s89, 262144\n"
    "s_add_u32 m0, s72, 32768\n"
    "s_nop 0\n"
    "buffer_load_dwordx4 v28, s[52:55], s69 offen offset:0 lds\n"
    "s_add_u32 s76, s69, 7168\n"
    "buffer_load_dwordx4 v29, s[52:55], s76 offen offset:1024 lds\n"
    "s_add_u32 s76, s69, 14336\n"
    "buffer_load_dwordx4 v28, s[52:55], s76 offen offset:2048 lds\n"
    "s_add_u32 s76, s69, 21504\n"
    "buffer_load_dwordx4 v29, s[52:55], s76 offen offset:3072 lds\n"
    "s_add_u32 s69, s69, 128\n"
    "s_add_u32 m0, s88, 8192\n"
    "s_nop 0\n"
    "buffer_load_dwordx4 v26, s[48:51], s89 offen offset:0 lds\n"
    "s_add_u32 s74, s89, 31744\n"
    "buffer_load_dwordx4 v26, s[48:51], s74 offen offset:1024 lds\n"
    "s_add_u32 s74, s89, 63488\n"
    "buffer_load_dwordx4 v27, s[48:51], s74 offen offset:2048 lds\n"
    "s_add_u32 s74, s89, 95232\n"
    "buffer_load_dwordx4 v27, s[48:51], s74 offen offset:3072 lds\n"
    "s_add_u32 s89, s89, 262144\n"
    "s_add_u32 m0, s88, 16384\n"
    "s_nop 0\n"
    "buffer_load_dwordx4 v26, s[48:51], s89 offen offset:0 lds\n"
    "s_add_u32 s74, s89, 31744\n"
    "buffer_load_dwordx4 v26, s[48:51], s74 offen offset:1024 lds\n"
    "s_add_u32 s74, s89, 63488\n"
    "buffer_load_dwordx4 v27, s[48:51], s74 offen offset:2048 lds\n"
    "s_add_u32 s74, s89, 95232\n"
    "buffer_load_dwordx4 v27, s[48:51], s74 offen offset:3072 lds\n"
    "s_add_u32 s89, s89, 262144\n"
    "v_and_b32 v32, 7, v34\n"
    "v_lshlrev_b32 v32, 1, v32\n"
    "v_xor_b32 v32, v32, v35\n"
    "v_lshlrev_b32 v32, 4, v32\n"
    "v_lshl_add_u32 v32, v34, 8, v32\n"
    "v_xor_b32 v16, 0, v32\n"
    "v_add_u32 v16, s64, v16\n"
    "v_xor_b32 v17, 64, v32\n"
    "v_add_u32 v17, s64, v17\n"
    "v_xor_b32 v18, 128, v32\n"
    "v_add_u32 v18, s64, v18\n"
    "v_xor_b32 v19, 192, v32\n"
    "v_add_u32 v19, s64, v19\n"
    "v_lshrrev_b32 v37, 2, v34\n"
    "v_and_b32 v38, 3, v34\n"
    "v_and_b32 v32, 1, v35\n"
    "v_lshl_add_u32 v32, v32, 2, v37\n"
    "v_lshlrev_b32 v32, 1, v32\n"
    "v_lshrrev_b32 v33, 1, v38\n"
    "v_xor_b32 v32, v32, v33\n"
    "v_lshlrev_b32 v32, 4, v32\n"
    "v_and_b32 v33, 1, v38\n"
    "v_lshl_add_u32 v32, v33, 3, v32\n"
    "v_lshl_add_u32 v33, v35, 3, v37\n"
    "v_lshlrev_b32 v33, 8, v33\n"
    "v_xor_b32 v8, 0, v32\n"
    "v_add3_u32 v8, v8, v33, s71\n"
    "v_xor_b32 v9, 32, v32\n"
    "v_add3_u32 v9, v9, v33, s71\n"
    "v_xor_b32 v10, 64, v32\n"
    "v_add3_u32 v10, v10, v33, s71\n"
    "v_xor_b32 v11, 96, v32\n"
    "v_add3_u32 v11, v11, v33, s71\n"
    "v_xor_b32 v12, 128, v32\n"
    "v_add3_u32 v12, v12, v33, s71\n"
    "v_xor_b32 v13, 160, v32\n"
    "v_add3_u32 v13, v13, v33, s71\n"
    "v_xor_b32 v14, 192, v32\n"
    "v_add3_u32 v14, v14, v33, s71\n"
    "v_xor_b32 v15, 224, v32\n"
    "v_add3_u32 v15, v15, v33, s71\n"
    "v_lshrrev_b32 v32, 1, v34\n"
    "v_xor_b32 v32, v32, v35\n"
    "v_lshlrev_b32 v32, 4, v32\n"
    "v_lshl_add_u32 v32, v34, 7, v32\n"
    "s_add_u32 s74, %[lds], 131072\n"
    "s_add_u32 s76, %[lds], 16384\n"
    "v_add_u32 v30, s74, v32\n"
    "v_add_u32 v6, s76, v32\n"
    "v_xor_b32 v32, 64, v32\n"
    "v_add_u32 v31, s74, v32\n"
    "v_add_u32 v7, s76, v32\n"
    "v_lshlrev_b32 v24, 4, v25\n"
    "s_mov_b32 s80, 0x3e0293ee\n"
    "s_mov_b32 s81, 0x3e0293ee\n"
    "s_mov_b32 s75, 0\n"
    "s_mov_b64 s[82:83], 0\n"
    "s_mov_b32 s93, 0x53800000\n"
    "v_mov_b64 v[40:41], 0\n"
    "v_mov_b64 v[42:43], 0\n"
    "v_mov_b64 v[44:45], 0\n"
    "v_mov_b64 v[46:47], 0\n"
    "v_mov_b64 v[48:49], 0\n"
    "v_mov_b64 v[50:51], 0\n"
    "v_mov_b64 v[52:53], 0\n"
    "v_mov_b64 v[54:55], 0\n"
    "v_mov_b64 v[56:57], 0\n"
    "v_mov_b64 v[58:59], 0\n"
    "v_mov_b64 v[60:61], 0\n"
    "v_mov_b64 v[62:63], 0\n"
    "v_mov_b64 v[64:65], 0\n"
    "v_mov_b64 v[66:67], 0\n"
    "v_mov_b64 v[68:69], 0\n"
    "v_mov_b64 v[70:71], 0\n"
    "v_mov_b64 v[72:73], 0\n"
    "v_mov_b64 v[74:75], 0\n"
    "v_mov_b64 v[76:77], 0\n"
    "v_mov_b64 v[78:79], 0\n"
    "v_mov_b64 v[80:81], 0\n"
    "v_mov_b64 v[82:83], 0\n"
    "v_mov_b64 v[84:85], 0\n"
    "v_mov_b64 v[86:87], 0\n"
    "v_mov_b64 v[88:89], 0\n"
    "v_mov_b64 v[90:91], 0\n"
    "v_mov_b64 v[92:93], 0\n"
    "v_mov_b64 v[94:95], 0\n"
    "v_mov_b64 v[96:97], 0\n"
    "v_mov_b64 v[98:99], 0\n"
    "v_mov_b64 v[100:101], 0\n"
    "v_mov_b64 v[102:103], 0\n"
    "v_mov_b64 v[104:105], 0\n"
    "v_mov_b64 v[106:107], 0\n"
    "v_mov_b64 v[108:109], 0\n"
    "v_mov_b64 v[110:111], 0\n"
    "v_mov_b64 v[112:113], 0\n"
    "v_mov_b64 v[114:115], 0\n"
    "v_mov_b64 v[116:117], 0\n"
    "v_mov_b64 v[118:119], 0\n"
    "v_mov_b64 v[120:121], 0\n"
    "v_mov_b64 v[122:123], 0\n"
    "v_mov_b64 v[124:125], 0\n"
    "v_mov_b64 v[126:127], 0\n"
    "v_mov_b64 v[128:129], 0\n"
    "v_mov_b64 v[130:131], 0\n"
    "v_mov_b64 v[132:133], 0\n"
    "v_mov_b64 v[134:135], 0\n"
    "v_mov_b64 v[136:137], 0\n"
    "v_mov_b64 v[138:139], 0\n"
    "v_mov_b64 v[140:141], 0\n"
    "v_mov_b64 v[142:143], 0\n"
    "v_mov_b64 v[144:145], 0\n"
    "v_mov_b64 v[146:147], 0\n"
    "v_mov_b64 v[148:149], 0\n"
    "v_mov_b64 v[150:151], 0\n"
    "v_mov_b64 v[152:153], 0\n"
    "v_mov_b64 v[154:155], 0\n"
    "v_mov_b64 v[156:157], 0\n"
    "v_mov_b64 v[158:159], 0\n"
    "v_mov_b64 v[160:161], 0\n"
    "v_mov_b64 v[162:163], 0\n"
    "v_mov_b64 v[164:165], 0\n"
    "v_mov_b64 v[166:167], 0\n"
    "v_mov_b64 v[168:169], 0\n"
    "v_mov_b64 v[170:171], 0\n"
    "v_mov_b64 v[172:173], 0\n"
    "v_mov_b64 v[174:175], 0\n"
    "v_mov_b64 v[176:177], 0\n"
    "v_mov_b64 v[178:179], 0\n"
    "v_mov_b64 v[180:181], 0\n"
    "v_mov_b64 v[182:183], 0\n"
    "v_mov_b64 v[184:185], 0\n"
    "v_mov_b64 v[186:187], 0\n"
    "v_mov_b64 v[188:189], 0\n"
    "v_mov_b64 v[190:191], 0\n"
    "v_mov_b64 v[192:193], 0\n"
    "v_mov_b64 v[194:195], 0\n"
    "v_mov_b64 v[196:197], 0\n"
    "v_mov_b64 v[198:199], 0\n"
    "v_mov_b64 v[200:201], 0\n"
    "v_mov_b64 v[202:203], 0\n"
    "v_mov_b64 v[204:205], 0\n"
    "v_mov_b64 v[206:207], 0\n"
    "v_mov_b64 v[208:209], 0\n"
    "v_mov_b64 v[210:211], 0\n"
    "v_mov_b64 v[212:213], 0\n"
    "v_mov_b64 v[214:215], 0\n"
    "v_mov_b64 v[216:217], 0\n"
    "v_mov_b64 v[218:219], 0\n"
    "v_mov_b64 v[220:221], 0\n"
    "v_mov_b64 v[222:223], 0\n"
    "v_mov_b64 v[224:225], 0\n"
    "v_mov_b64 v[226:227], 0\n"
    "v_mov_b64 v[228:229], 0\n"
    "v_mov_b64 v[230:231], 0\n"
    "v_mov_b64 v[232:233], 0\n"
    "v_mov_b64 v[234:235], 0\n"
    "v_mov_b64 v[236:237], 0\n"
    "v_mov_b64 v[238:239], 0\n"
    "v_mov_b64 v[240:241], 0\n"
    "v_mov_b64 v[242:243], 0\n"
    "v_mov_b64 v[244:245], 0\n"
    "v_mov_b64 v[246:247], 0\n"
    "v_mov_b64 v[248:249], 0\n"
    "v_mov_b64 v[250:251], 0\n"
    "v_mov_b64 v[252:253], 0\n"
    "v_mov_b64 v[254:255], 0\n"
    "v_mov_b64 v[32:33], 0\n"
    "v_mov_b64 v[34:35], 0\n"
    "v_mov_b64 v[36:37], 0\n"
    "v_mov_b64 v[38:39], 0\n"
    "s_waitcnt vmcnt(16)\n"
    "s_barrier\n"
    "ds_read_b64_tr_b16 a[0:1], v8 offset:24576\n"
    "ds_read_b64_tr_b16 a[2:3], v8 offset:25600\n"
    "ds_read_b64_tr_b16 a[4:5], v9 offset:24576\n"
    "ds_read_b64_tr_b16 a[6:7], v9 offset:25600\n"
    "ds_read_b64_tr_b16 a[8:9], v10 offset:24576\n"
    "ds_read_b64_tr_b16 a[10:11], v10 offset:25600\n"
    "ds_read_b64_tr_b16 a[12:13], v11 offset:24576\n"
    "ds_read_b64_tr_b16 a[14:15], v11 offset:25600\n"
    "ds_read_b64_tr_b16 a[16:17], v12 offset:24576\n"
    "ds_read_b64_tr_b16 a[18:19], v12 offset:25600\n"
    "ds_read_b64_tr_b16 a[20:21], v13 offset:24576\n"
    "ds_read_b64_tr_b16 a[22:23], v13 offset:25600\n"
    "ds_read_b64_tr_b16 a[24:25], v14 offset:24576\n"
    "ds_read_b64_tr_b16 a[26:27], v14 offset:25600\n"
    "ds_read_b64_tr_b16 a[28:29], v15 offset:24576\n"
    "ds_read_b64_tr_b16 a[30:31], v15 offset:25600\n"
    "ds_read_b128 a[64:67], v6 offset:8192\n"
    "ds_read_b128 a[68:71], v6 offset:10240\n"
    "ds_read_b128 a[72:75], v6 offset:12288\n"
    "s_add_u32 m0, s65, 0\n"
    "s_nop 0\n"
    "buffer_load_dwordx4 v28, s[52:55], s69 offen offset:0 lds\n"
    "s_add_u32 s76, s69, 7168\n"
    "buffer_load_dwordx4 v29, s[52:55], s76 offen offset:1024 lds\n"
    "s_add_u32 s76, s69, 14336\n"
    "buffer_load_dwordx4 v28, s[52:55], s76 offen offset:2048 lds\n"
    "s_add_u32 s76, s69, 21504\n"
    "buffer_load_dwordx4 v29, s[52:55], s76 offen offset:3072 lds\n"
    "s_add_u32 s69, s69, 128\n"
    "s_add_u32 m0, s65, 16384\n"
    "s_nop 0\n"
    "buffer_load_dwordx4 v28, s[52:55], s69 offen offset:0 lds\n"
    "s_add_u32 s76, s69, 7168\n"
    "buffer_load_dwordx4 v29, s[52:55], s76 offen offset:1024 lds\n"
    "s_add_u32 s76, s69, 14336\n"
    "buffer_load_dwordx4 v28, s[52:55], s76 offen offset:2048 lds\n"
    "s_add_u32 s76, s69, 21504\n"
    "buffer_load_dwordx4 v29, s[52:55], s76 offen offset:3072 lds\n"
    "s_add_u32 s69, s69, 128\n"
    "s_waitcnt lgkmcnt(0)\n"
    "s_waitcnt vmcnt(20)\n"
    "s_barrier\n"
    "v_mfma_f32_16x16x32_bf16 v[32:35], a[0:3], a[64:67], v[32:35]\n"
    "s_add_u32 m0, s88, 24576\n"
    "v_mfma_f32_16x16x32_bf16 v[64:67], a[0:3], a[68:71], v[64:67]\n"
    "ds_read_b64_tr_b16 a[32:33], v8 offset:0\n"
    "ds_read_b64_tr_b16 a[34:35], v8 offset:1024\n"
    "v_mfma_f32_16x16x32_bf16 v[96:99], a[0:3], a[72:75], v[96:99]\n"
    "ds_read_b64_tr_b16 a[36:37], v9 offset:0\n"
    "v_mfma_f32_16x16x32_bf16 v[36:39], a[4:7], a[64:67], v[36:39]\n"
    "buffer_load_dwordx4 v26, s[48:51], s89 offen offset:0 lds\n"
    "v_mfma_f32_16x16x32_bf16 v[68:71], a[4:7], a[68:71], v[68:71]\n"
    "ds_read_b64_tr_b16 a[38:39], v9 offset:1024\n"
    "ds_read_b64_tr_b16 a[40:41], v10 offset:0\n"
    "v_mfma_f32_16x16x32_bf16 v[100:103], a[4:7], a[72:75], v[100:103]\n"
    "s_add_u32 s74, s89, 31744\n"
    "v_mfma_f32_16x16x32_bf16 v[40:43], a[8:11], a[64:67], v[40:43]\n"
    "ds_read_b64_tr_b16 a[42:43], v10 offset:1024\n"
    "ds_read_b64_tr_b16 a[44:45], v11 offset:0\n"
    "v_mfma_f32_16x16x32_bf16 v[72:75], a[8:11], a[68:71], v[72:75]\n"
    "buffer_load_dwordx4 v26, s[48:51], s74 offen offset:1024 lds\n"
    "v_mfma_f32_16x16x32_bf16 v[104:107], a[8:11], a[72:75], v[104:107]\n"
    "ds_read_b64_tr_b16 a[46:47], v11 offset:1024\n"
    "v_mfma_f32_16x16x32_bf16 v[44:47], a[12:15], a[64:67], v[44:47]\n"
    "ds_read_b64_tr_b16 a[48:49], v12 offset:0\n"
    "s_add_u32 s74, s89, 63488\n"
    "v_mfma_f32_16x16x32_bf16 v[76:79], a[12:15], a[68:71], v[76:79]\n"
    "ds_read_b64_tr_b16 a[50:51], v12 offset:1024\n"
    "v_mfma_f32_16x16x32_bf16 v[108:111], a[12:15], a[72:75], v[108:111]\n"
    "ds_read_b64_tr_b16 a[52:53], v13 offset:0\n"
    "v_mfma_f32_16x16x32_bf16 v[48:51], a[16:19], a[64:67], v[48:51]\n"
    "buffer_load_dwordx4 v27, s[48:51], s74 offen offset:2048 lds\n"
    "ds_read_b64_tr_b16 a[54:55], v13 offset:1024\n"
    "v_mfma_f32_16x16x32_bf16 v[80:83], a[16:19], a[68:71], v[80:83]\n"
    "ds_read_b64_tr_b16 a[56:57], v14 offset:0\n"
    "v_mfma_f32_16x16x32_bf16 v[112:115], a[16:19], a[72:75], v[112:115]\n"
    "s_add_u32 s74, s89, 95232\n"
    "ds_read_b64_tr_b16 a[58:59], v14 offset:1024\n"
    "v_mfma_f32_16x16x32_bf16 v[52:55], a[20:23], a[64:67], v[52:55]\n"
    "ds_read_b64_tr_b16 a[60:61], v15 offset:0\n"
    "v_mfma_f32_16x16x32_bf16 v[84:87], a[20:23], a[68:71], v[84:87]\n"
    "buffer_load_dwordx4 v27, s[48:51], s74 offen offset:3072 lds\n"
    "v_mfma_f32_16x16x32_bf16 v[116:119], a[20:23], a[72:75], v[116:119]\n"
    "ds_read_b64_tr_b16 a[62:63], v15 offset:1024\n"
    "ds_read_b128 a[92:95], v7 offset:8192\n"
    "v_mfma_f32_16x16x32_bf16 v[56:59], a[24:27], a[64:67], v[56:59]\n"
    "s_add_u32 s89, s89, 262144\n"
    "v_mfma_f32_16x16x32_bf16 v[88:91], a[24:27], a[68:71], v[88:91]\n"
    "ds_read_b128 a[96:99], v7 offset:10240\n"
    "v_mfma_f32_16x16x32_bf16 v[120:123], a[24:27], a[72:75], v[120:123]\n"
    "ds_read_b128 a[100:103], v7 offset:12288\n"
    "v_mfma_f32_16x16x32_bf16 v[60:63], a[28:31], a[64:67], v[60:63]\n"
    "v_mfma_f32_16x16x32_bf16 v[92:95], a[28:31], a[68:71], v[92:95]\n"
    "v_mfma_f32_16x16x32_bf16 v[124:127], a[28:31], a[72:75], v[124:127]\n"
    "s_waitcnt lgkmcnt(0)\n"
    "s_waitcnt vmcnt(16)\n"
    "s_barrier\n"
    "v_mfma_f32_16x16x32_bf16 v[32:35], a[32:35], a[92:95], v[32:35]\n"
    "s_add_u32 m0, s72, 0\n"
    "ds_read_b64_tr_b16 a[0:1], v8 offset:8192\n"
    "v_mfma_f32_16x16x32_bf16 v[64:67], a[32:35], a[96:99], v[64:67]\n"
    "ds_read_b64_tr_b16 a[2:3], v8 offset:9216\n"
    "buffer_load_dwordx4 v28, s[52:55], s69 offen offset:0 lds\n"
    "v_mfma_f32_16x16x32_bf16 v[96:99], a[32:35], a[100:103], v[96:99]\n"
    "ds_read_b64_tr_b16 a[4:5], v9 offset:8192\n"
    "v_mfma_f32_16x16x32_bf16 v[36:39], a[36:39], a[92:95], v[36:39]\n"
    "s_add_u32 s76, s69, 7168\n"
    "ds_read_b64_tr_b16 a[6:7], v9 offset:9216\n"
    "v_mfma_f32_16x16x32_bf16 v[68:71], a[36:39], a[96:99], v[68:71]\n"
    "buffer_load_dwordx4 v29, s[52:55], s76 offen offset:1024 lds\n"
    "ds_read_b64_tr_b16 a[8:9], v10 offset:8192\n"
    "v_mfma_f32_16x16x32_bf16 v[100:103], a[36:39], a[100:103], v[100:103]\n"
    "s_add_u32 s76, s69, 14336\n"
    "ds_read_b64_tr_b16 a[10:11], v10 offset:9216\n"
    "v_mfma_f32_16x16x32_bf16 v[40:43], a[40:43], a[92:95], v[40:43]\n"
    "buffer_load_dwordx4 v28, s[52:55], s76 offen offset:2048 lds\n"
    "ds_read_b64_tr_b16 a[12:13], v11 offset:8192\n"
    "v_mfma_f32_16x16x32_bf16 v[72:75], a[40:43], a[96:99], v[72:75]\n"
    "s_add_u32 s76, s69, 21504\n"
    "ds_read_b64_tr_b16 a[14:15], v11 offset:9216\n"
    "v_mfma_f32_16x16x32_bf16 v[104:107], a[40:43], a[100:103], v[104:107]\n"
    "buffer_load_dwordx4 v29, s[52:55], s76 offen offset:3072 lds\n"
    "v_mfma_f32_16x16x32_bf16 v[44:47], a[44:47], a[92:95], v[44:47]\n"
    "ds_read_b64_tr_b16 a[16:17], v12 offset:8192\n"
    "s_add_u32 s69, s69, 128\n"
    "v_mfma_f32_16x16x32_bf16 v[76:79], a[44:47], a[96:99], v[76:79]\n"
    "ds_read_b64_tr_b16 a[18:19], v12 offset:9216\n"
    "s_add_u32 m0, s88, 0\n"
    "v_mfma_f32_16x16x32_bf16 v[108:111], a[44:47], a[100:103], v[108:111]\n"
    "ds_read_b64_tr_b16 a[20:21], v13 offset:8192\n"
    "buffer_load_dwordx4 v26, s[48:51], s89 offen offset:0 lds\n"
    "v_mfma_f32_16x16x32_bf16 v[48:51], a[48:51], a[92:95], v[48:51]\n"
    "ds_read_b64_tr_b16 a[22:23], v13 offset:9216\n"
    "s_add_u32 s74, s89, 31744\n"
    "v_mfma_f32_16x16x32_bf16 v[80:83], a[48:51], a[96:99], v[80:83]\n"
    "ds_read_b64_tr_b16 a[24:25], v14 offset:8192\n"
    "v_mfma_f32_16x16x32_bf16 v[112:115], a[48:51], a[100:103], v[112:115]\n"
    "buffer_load_dwordx4 v26, s[48:51], s74 offen offset:1024 lds\n"
    "ds_read_b64_tr_b16 a[26:27], v14 offset:9216\n"
    "v_mfma_f32_16x16x32_bf16 v[52:55], a[52:55], a[92:95], v[52:55]\n"
    "s_add_u32 s74, s89, 63488\n"
    "ds_read_b64_tr_b16 a[28:29], v15 offset:8192\n"
    "v_mfma_f32_16x16x32_bf16 v[84:87], a[52:55], a[96:99], v[84:87]\n"
    "buffer_load_dwordx4 v27, s[48:51], s74 offen offset:2048 lds\n"
    "ds_read_b64_tr_b16 a[30:31], v15 offset:9216\n"
    "v_mfma_f32_16x16x32_bf16 v[116:119], a[52:55], a[100:103], v[116:119]\n"
    "s_add_u32 s74, s89, 95232\n"
    "ds_read_b128 a[64:67], v6 offset:40960\n"
    "v_mfma_f32_16x16x32_bf16 v[56:59], a[56:59], a[92:95], v[56:59]\n"
    "buffer_load_dwordx4 v27, s[48:51], s74 offen offset:3072 lds\n"
    "v_mfma_f32_16x16x32_bf16 v[88:91], a[56:59], a[96:99], v[88:91]\n"
    "ds_read_b128 a[68:71], v6 offset:43008\n"
    "s_add_u32 s89, s89, 262144\n"
    "v_mfma_f32_16x16x32_bf16 v[120:123], a[56:59], a[100:103], v[120:123]\n"
    "ds_read_b128 a[72:75], v6 offset:45056\n"
    "v_mfma_f32_16x16x32_bf16 v[60:63], a[60:63], a[92:95], v[60:63]\n"
    "v_mfma_f32_16x16x32_bf16 v[92:95], a[60:63], a[96:99], v[92:95]\n"
    "v_mfma_f32_16x16x32_bf16 v[124:127], a[60:63], a[100:103], v[124:127]\n"
    "s_waitcnt lgkmcnt(0)\n"
    "s_waitcnt vmcnt(20)\n"
    "s_barrier\n"
    "v_mfma_f32_16x16x32_bf16 v[32:35], a[0:3], a[64:67], v[32:35]\n"
    "s_add_u32 m0, s88, 8192\n"
    "v_mfma_f32_16x16x32_bf16 v[64:67], a[0:3], a[68:71], v[64:67]\n"
    "ds_read_b64_tr_b16 a[32:33], v8 offset:16384\n"
    "ds_read_b64_tr_b16 a[34:35], v8 offset:17408\n"
    "v_mfma_f32_16x16x32_bf16 v[96:99], a[0:3], a[72:75], v[96:99]\n"
    "ds_read_b64_tr_b16 a[36:37], v9 offset:16384\n"
    "v_mfma_f32_16x16x32_bf16 v[36:39], a[4:7], a[64:67], v[36:39]\n"
    "buffer_load_dwordx4 v26, s[48:51], s89 offen offset:0 lds\n"
    "v_mfma_f32_16x16x32_bf16 v[68:71], a[4:7], a[68:71], v[68:71]\n"
    "ds_read_b64_tr_b16 a[38:39], v9 offset:17408\n"
    "ds_read_b64_tr_b16 a[40:41], v10 offset:16384\n"
    "v_mfma_f32_16x16x32_bf16 v[100:103], a[4:7], a[72:75], v[100:103]\n"
    "s_add_u32 s74, s89, 31744\n"
    "v_mfma_f32_16x16x32_bf16 v[40:43], a[8:11], a[64:67], v[40:43]\n"
    "ds_read_b64_tr_b16 a[42:43], v10 offset:17408\n"
    "ds_read_b64_tr_b16 a[44:45], v11 offset:16384\n"
    "v_mfma_f32_16x16x32_bf16 v[72:75], a[8:11], a[68:71], v[72:75]\n"
    "buffer_load_dwordx4 v26, s[48:51], s74 offen offset:1024 lds\n"
    "v_mfma_f32_16x16x32_bf16 v[104:107], a[8:11], a[72:75], v[104:107]\n"
    "ds_read_b64_tr_b16 a[46:47], v11 offset:17408\n"
    "v_mfma_f32_16x16x32_bf16 v[44:47], a[12:15], a[64:67], v[44:47]\n"
    "ds_read_b64_tr_b16 a[48:49], v12 offset:16384\n"
    "s_add_u32 s74, s89, 63488\n"
    "v_mfma_f32_16x16x32_bf16 v[76:79], a[12:15], a[68:71], v[76:79]\n"
    "ds_read_b64_tr_b16 a[50:51], v12 offset:17408\n"
    "v_mfma_f32_16x16x32_bf16 v[108:111], a[12:15], a[72:75], v[108:111]\n"
    "ds_read_b64_tr_b16 a[52:53], v13 offset:16384\n"
    "v_mfma_f32_16x16x32_bf16 v[48:51], a[16:19], a[64:67], v[48:51]\n"
    "buffer_load_dwordx4 v27, s[48:51], s74 offen offset:2048 lds\n"
    "ds_read_b64_tr_b16 a[54:55], v13 offset:17408\n"
    "v_mfma_f32_16x16x32_bf16 v[80:83], a[16:19], a[68:71], v[80:83]\n"
    "ds_read_b64_tr_b16 a[56:57], v14 offset:16384\n"
    "v_mfma_f32_16x16x32_bf16 v[112:115], a[16:19], a[72:75], v[112:115]\n"
    "s_add_u32 s74, s89, 95232\n"
    "ds_read_b64_tr_b16 a[58:59], v14 offset:17408\n"
    "v_mfma_f32_16x16x32_bf16 v[52:55], a[20:23], a[64:67], v[52:55]\n"
    "ds_read_b64_tr_b16 a[60:61], v15 offset:16384\n"
    "v_mfma_f32_16x16x32_bf16 v[84:87], a[20:23], a[68:71], v[84:87]\n"
    "buffer_load_dwordx4 v27, s[48:51], s74 offen offset:3072 lds\n"
    "v_mfma_f32_16x16x32_bf16 v[116:119], a[20:23], a[72:75], v[116:119]\n"
    "ds_read_b64_tr_b16 a[62:63], v15 offset:17408\n"
    "ds_read_b128 a[92:95], v7 offset:40960\n"
    "v_mfma_f32_16x16x32_bf16 v[56:59], a[24:27], a[64:67], v[56:59]\n"
    "s_add_u32 s89, s89, 262144\n"
    "v_mfma_f32_16x16x32_bf16 v[88:91], a[24:27], a[68:71], v[88:91]\n"
    "ds_read_b128 a[96:99], v7 offset:43008\n"
    "v_mfma_f32_16x16x32_bf16 v[120:123], a[24:27], a[72:75], v[120:123]\n"
    "ds_read_b128 a[100:103], v7 offset:45056\n"
    "v_mfma_f32_16x16x32_bf16 v[60:63], a[28:31], a[64:67], v[60:63]\n"
    "v_mfma_f32_16x16x32_bf16 v[92:95], a[28:31], a[68:71], v[92:95]\n"
    "v_mfma_f32_16x16x32_bf16 v[124:127], a[28:31], a[72:75], v[124:127]\n"
    "s_waitcnt lgkmcnt(0)\n"
    "s_waitcnt vmcnt(12)\n"
    "s_barrier\n"
    "v_mfma_f32_16x16x32_bf16 v[32:35], a[32:35], a[92:95], v[32:35]\n"
    "s_add_u32 m0, s72, 32768\n"
    "ds_read_b64_tr_b16 a[0:1], v8 offset:24576\n"
    "v_mfma_f32_16x16x32_bf16 v[64:67], a[32:35], a[96:99], v[64:67]\n"
    "ds_read_b64_tr_b16 a[2:3], v8 offset:25600\n"
    "buffer_load_dwordx4 v28, s[52:55], s69 offen offset:0 lds\n"
    "v_mfma_f32_16x16x32_bf16 v[96:99], a[32:35], a[100:103], v[96:99]\n"
    "ds_read_b64_tr_b16 a[4:5], v9 offset:24576\n"
    "v_mfma_f32_16x16x32_bf16 v[36:39], a[36:39], a[92:95], v[36:39]\n"
    "s_add_u32 s76, s69, 7168\n"
    "ds_read_b64_tr_b16 a[6:7], v9 offset:25600\n"
    "v_mfma_f32_16x16x32_bf16 v[68:71], a[36:39], a[96:99], v[68:71]\n"
    "buffer_load_dwordx4 v29, s[52:55], s76 offen offset:1024 lds\n"
    "ds_read_b64_tr_b16 a[8:9], v10 offset:24576\n"
    "v_mfma_f32_16x16x32_bf16 v[100:103], a[36:39], a[100:103], v[100:103]\n"
    "s_add_u32 s76, s69, 14336\n"
    "ds_read_b64_tr_b16 a[10:11], v10 offset:25600\n"
    "v_mfma_f32_16x16x32_bf16 v[40:43], a[40:43], a[92:95], v[40:43]\n"
    "buffer_load_dwordx4 v28, s[52:55], s76 offen offset:2048 lds\n"
    "ds_read_b64_tr_b16 a[12:13], v11 offset:24576\n"
    "v_mfma_f32_16x16x32_bf16 v[72:75], a[40:43], a[96:99], v[72:75]\n"
    "s_add_u32 s76, s69, 21504\n"
    "ds_read_b64_tr_b16 a[14:15], v11 offset:25600\n"
    "v_mfma_f32_16x16x32_bf16 v[104:107], a[40:43], a[100:103], v[104:107]\n"
    "buffer_load_dwordx4 v29, s[52:55], s76 offen offset:3072 lds\n"
    "v_mfma_f32_16x16x32_bf16 v[44:47], a[44:47], a[92:95], v[44:47]\n"
    "ds_read_b64_tr_b16 a[16:17], v12 offset:24576\n"
    "s_add_u32 s69, s69, 128\n"
    "v_mfma_f32_16x16x32_bf16 v[76:79], a[44:47], a[96:99], v[76:79]\n"
    "ds_read_b64_tr_b16 a[18:19], v12 offset:25600\n"
    "s_add_u32 m0, s88, 16384\n"
    "v_mfma_f32_16x16x32_bf16 v[108:111], a[44:47], a[100:103], v[108:111]\n"
    "ds_read_b64_tr_b16 a[20:21], v13 offset:24576\n"
    "buffer_load_dwordx4 v26, s[48:51], s89 offen offset:0 lds\n"
    "v_mfma_f32_16x16x32_bf16 v[48:51], a[48:51], a[92:95], v[48:51]\n"
    "ds_read_b64_tr_b16 a[22:23], v13 offset:25600\n"
    "s_add_u32 s74, s89, 31744\n"
    "v_mfma_f32_16x16x32_bf16 v[80:83], a[48:51], a[96:99], v[80:83]\n"
    "ds_read_b64_tr_b16 a[24:25], v14 offset:24576\n"
    "v_mfma_f32_16x16x32_bf16 v[112:115], a[48:51], a[100:103], v[112:115]\n"
    "buffer_load_dwordx4 v26, s[48:51], s74 offen offset:1024 lds\n"
    "ds_read_b64_tr_b16 a[26:27], v14 offset:25600\n"
    "v_mfma_f32_16x16x32_bf16 v[52:55], a[52:55], a[92:95], v[52:55]\n"
    "s_add_u32 s74, s89, 63488\n"
    "ds_read_b64_tr_b16 a[28:29], v15 offset:24576\n"
    "v_mfma_f32_16x16x32_bf16 v[84:87], a[52:55], a[96:99], v[84:87]\n"
    "buffer_load_dwordx4 v27, s[48:51], s74 offen offset:2048 lds\n"
    "ds_read_b64_tr_b16 a[30:31], v15 offset:25600\n"
    "v_mfma_f32_16x16x32_bf16 v[116:119], a[52:55], a[100:103], v[116:119]\n"
    "s_add_u32 s74, s89, 95232\n"
    "ds_read_b128 a[64:67], v30 offset:8192\n"
    "v_mfma_f32_16x16x32_bf16 v[56:59], a[56:59], a[92:95], v[56:59]\n"
    "buffer_load_dwordx4 v27, s[48:51], s74 offen offset:3072 lds\n"
    "v_mfma_f32_16x16x32_bf16 v[88:91], a[56:59], a[96:99], v[88:91]\n"
    "ds_read_b128 a[68:71], v30 offset:10240\n"
    "s_add_u32 s89, s89, 262144\n"
    "v_mfma_f32_16x16x32_bf16 v[120:123], a[56:59], a[100:103], v[120:123]\n"
    "ds_read_b128 a[72:75], v30 offset:12288\n"
    "v_mfma_f32_16x16x32_bf16 v[60:63], a[60:63], a[92:95], v[60:63]\n"
    "v_mfma_f32_16x16x32_bf16 v[92:95], a[60:63], a[96:99], v[92:95]\n"
    "v_mfma_f32_16x16x32_bf16 v[124:127], a[60:63], a[100:103], v[124:127]\n"
    "s_waitcnt lgkmcnt(0)\n"
    "s_waitcnt vmcnt(12)\n"
    "s_barrier\n"
    "v_mfma_f32_16x16x32_bf16 v[32:35], a[0:3], a[64:67], v[32:35]\n"
    "s_add_u32 m0, s88, 24576\n"
    "v_mfma_f32_16x16x32_bf16 v[64:67], a[0:3], a[68:71], v[64:67]\n"
    "ds_read_b64_tr_b16 a[32:33], v8 offset:0\n"
    "ds_read_b64_tr_b16 a[34:35], v8 offset:1024\n"
    "v_mfma_f32_16x16x32_bf16 v[96:99], a[0:3], a[72:75], v[96:99]\n"
    "ds_read_b64_tr_b16 a[36:37], v9 offset:0\n"
    "v_mfma_f32_16x16x32_bf16 v[36:39], a[4:7], a[64:67], v[36:39]\n"
    "buffer_load_dwordx4 v26, s[48:51], s89 offen offset:0 lds\n"
    "v_mfma_f32_16x16x32_bf16 v[68:71], a[4:7], a[68:71], v[68:71]\n"
    "ds_read_b64_tr_b16 a[38:39], v9 offset:1024\n"
    "ds_read_b64_tr_b16 a[40:41], v10 offset:0\n"
    "v_mfma_f32_16x16x32_bf16 v[100:103], a[4:7], a[72:75], v[100:103]\n"
    "s_add_u32 s74, s89, 31744\n"
    "v_mfma_f32_16x16x32_bf16 v[40:43], a[8:11], a[64:67], v[40:43]\n"
    "ds_read_b64_tr_b16 a[42:43], v10 offset:1024\n"
    "ds_read_b64_tr_b16 a[44:45], v11 offset:0\n"
    "v_mfma_f32_16x16x32_bf16 v[72:75], a[8:11], a[68:71], v[72:75]\n"
    "buffer_load_dwordx4 v26, s[48:51], s74 offen offset:1024 lds\n"
    "v_mfma_f32_16x16x32_bf16 v[104:107], a[8:11], a[72:75], v[104:107]\n"
    "ds_read_b64_tr_b16 a[46:47], v11 offset:1024\n"
    "v_mfma_f32_16x16x32_bf16 v[44:47], a[12:15], a[64:67], v[44:47]\n"
    "ds_read_b64_tr_b16 a[48:49], v12 offset:0\n"
    "s_add_u32 s74, s89, 63488\n"
    "v_mfma_f32_16x16x32_bf16 v[76:79], a[12:15], a[68:71], v[76:79]\n"
    "ds_read_b64_tr_b16 a[50:51], v12 offset:1024\n"
    "v_mfma_f32_16x16x32_bf16 v[108:111], a[12:15], a[72:75], v[108:111]\n"
    "ds_read_b64_tr_b16 a[52:53], v13 offset:0\n"
    "v_mfma_f32_16x16x32_bf16 v[48:51], a[16:19], a[64:67], v[48:51]\n"
    "buffer_load_dwordx4 v27, s[48:51], s74 offen offset:2048 lds\n"
    "ds_read_b64_tr_b16 a[54:55], v13 offset:1024\n"
    "v_mfma_f32_16x16x32_bf16 v[80:83], a[16:19], a[68:71], v[80:83]\n"
    "ds_read_b64_tr_b16 a[56:57], v14 offset:0\n"
    "v_mfma_f32_16x16x32_bf16 v[112:115], a[16:19], a[72:75], v[112:115]\n"
    "s_add_u32 s74, s89, 95232\n"
    "ds_read_b64_tr_b16 a[58:59], v14 offset:1024\n"
    "v_mfma_f32_16x16x32_bf16 v[52:55], a[20:23], a[64:67], v[52:55]\n"
    "ds_read_b64_tr_b16 a[60:61], v15 offset:0\n"
    "v_mfma_f32_16x16x32_bf16 v[84:87], a[20:23], a[68:71], v[84:87]\n"
    "buffer_load_dwordx4 v27, s[48:51], s74 offen offset:3072 lds\n"
    "v_mfma_f32_16x16x32_bf16 v[116:119], a[20:23], a[72:75], v[116:119]\n"
    "ds_read_b64_tr_b16 a[62:63], v15 offset:1024\n"
    "ds_read_b128 a[92:95], v31 offset:8192\n"
    "v_mfma_f32_16x16x32_bf16 v[56:59], a[24:27], a[64:67], v[56:59]\n"
    "s_add_u32 s89, s89, 262144\n"
    "v_mfma_f32_16x16x32_bf16 v[88:91], a[24:27], a[68:71], v[88:91]\n"
    "ds_read_b128 a[96:99], v31 offset:10240\n"
    "v_mfma_f32_16x16x32_bf16 v[120:123], a[24:27], a[72:75], v[120:123]\n"
    "ds_read_b128 a[100:103], v31 offset:12288\n"
    "v_mfma_f32_16x16x32_bf16 v[60:63], a[28:31], a[64:67], v[60:63]\n"
    "v_mfma_f32_16x16x32_bf16 v[92:95], a[28:31], a[68:71], v[92:95]\n"
    "v_mfma_f32_16x16x32_bf16 v[124:127], a[28:31], a[72:75], v[124:127]\n"
    "s_waitcnt lgkmcnt(0)\n"
    "s_waitcnt vmcnt(12)\n"
    "s_barrier\n"
    "v_mfma_f32_16x16x32_bf16 v[32:35], a[32:35], a[92:95], v[32:35]\n"
    "s_add_u32 m0, s65, 0\n"
    "ds_read_b64_tr_b16 a[0:1], v8 offset:8192\n"
    "v_mfma_f32_16x16x32_bf16 v[64:67], a[32:35], a[96:99], v[64:67]\n"
    "ds_read_b64_tr_b16 a[2:3], v8 offset:9216\n"
    "buffer_load_dwordx4 v28, s[52:55], s69 offen offset:0 lds\n"
    "v_mfma_f32_16x16x32_bf16 v[96:99], a[32:35], a[100:103], v[96:99]\n"
    "ds_read_b64_tr_b16 a[4:5], v9 offset:8192\n"
    "v_mfma_f32_16x16x32_bf16 v[36:39], a[36:39], a[92:95], v[36:39]\n"
    "s_add_u32 s76, s69, 7168\n"
    "ds_read_b64_tr_b16 a[6:7], v9 offset:9216\n"
    "v_mfma_f32_16x16x32_bf16 v[68:71], a[36:39], a[96:99], v[68:71]\n"
    "buffer_load_dwordx4 v29, s[52:55], s76 offen offset:1024 lds\n"
    "ds_read_b64_tr_b16 a[8:9], v10 offset:8192\n"
    "v_mfma_f32_16x16x32_bf16 v[100:103], a[36:39], a[100:103], v[100:103]\n"
    "s_add_u32 s76, s69, 14336\n"
    "ds_read_b64_tr_b16 a[10:11], v10 offset:9216\n"
    "v_mfma_f32_16x16x32_bf16 v[40:43], a[40:43], a[92:95], v[40:43]\n"
    "buffer_load_dwordx4 v28, s[52:55], s76 offen offset:2048 lds\n"
    "ds_read_b64_tr_b16 a[12:13], v11 offset:8192\n"
    "v_mfma_f32_16x16x32_bf16 v[72:75], a[40:43], a[96:99], v[72:75]\n"
    "s_add_u32 s76, s69, 21504\n"
    "ds_read_b64_tr_b16 a[14:15], v11 offset:9216\n"
    "v_mfma_f32_16x16x32_bf16 v[104:107], a[40:43], a[100:103], v[104:107]\n"
    "buffer_load_dwordx4 v29, s[52:55], s76 offen offset:3072 lds\n"
    "v_mfma_f32_16x16x32_bf16 v[44:47], a[44:47], a[92:95], v[44:47]\n"
    "ds_read_b64_tr_b16 a[16:17], v12 offset:8192\n"
    "s_add_u32 s69, s69, 128\n"
    "v_mfma_f32_16x16x32_bf16 v[76:79], a[44:47], a[96:99], v[76:79]\n"
    "ds_read_b64_tr_b16 a[18:19], v12 offset:9216\n"
    "s_add_u32 m0, s88, 0\n"
    "v_mfma_f32_16x16x32_bf16 v[108:111], a[44:47], a[100:103], v[108:111]\n"
    "ds_read_b64_tr_b16 a[20:21], v13 offset:8192\n"
    "buffer_load_dwordx4 v26, s[48:51], s89 offen offset:0 lds\n"
    "v_mfma_f32_16x16x32_bf16 v[48:51], a[48:51], a[92:95], v[48:51]\n"
    "ds_read_b64_tr_b16 a[22:23], v13 offset:9216\n"
    "s_add_u32 s74, s89, 31744\n"
    "v_mfma_f32_16x16x32_bf16 v[80:83], a[48:51], a[96:99], v[80:83]\n"
    "ds_read_b64_tr_b16 a[24:25], v14 offset:8192\n"
    "v_mfma_f32_16x16x32_bf16 v[112:115], a[48:51], a[100:103], v[112:115]\n"
    "buffer_load_dwordx4 v26, s[48:51], s74 offen offset:1024 lds\n"
    "ds_read_b64_tr_b16 a[26:27], v14 offset:9216\n"
    "v_mfma_f32_16x16x32_bf16 v[52:55], a[52:55], a[92:95], v[52:55]\n"
    "s_add_u32 s74, s89, 63488\n"
    "ds_read_b64_tr_b16 a[28:29], v15 offset:8192\n"
    "v_mfma_f32_16x16x32_bf16 v[84:87], a[52:55], a[96:99], v[84:87]\n"
    "buffer_load_dwordx4 v27, s[48:51], s74 offen offset:2048 lds\n"
    "ds_read_b64_tr_b16 a[30:31], v15 offset:9216\n"
    "v_mfma_f32_16x16x32_bf16 v[116:119], a[52:55], a[100:103], v[116:119]\n"
    "s_add_u32 s74, s89, 95232\n"
    "ds_read_b128 a[64:67], v30 offset:24576\n"
    "v_mfma_f32_16x16x32_bf16 v[56:59], a[56:59], a[92:95], v[56:59]\n"
    "buffer_load_dwordx4 v27, s[48:51], s74 offen offset:3072 lds\n"
    "v_mfma_f32_16x16x32_bf16 v[88:91], a[56:59], a[96:99], v[88:91]\n"
    "ds_read_b128 a[68:71], v30 offset:26624\n"
    "s_add_u32 s89, s89, 262144\n"
    "v_mfma_f32_16x16x32_bf16 v[120:123], a[56:59], a[100:103], v[120:123]\n"
    "ds_read_b128 a[72:75], v30 offset:28672\n"
    "v_mfma_f32_16x16x32_bf16 v[60:63], a[60:63], a[92:95], v[60:63]\n"
    "v_mfma_f32_16x16x32_bf16 v[92:95], a[60:63], a[96:99], v[92:95]\n"
    "v_mfma_f32_16x16x32_bf16 v[124:127], a[60:63], a[100:103], v[124:127]\n"
    "s_waitcnt lgkmcnt(0)\n"
    "s_waitcnt vmcnt(12)\n"
    "s_barrier\n"
    "v_mfma_f32_16x16x32_bf16 v[32:35], a[0:3], a[64:67], v[32:35]\n"
    "s_add_u32 m0, s88, 8192\n"
    "v_mfma_f32_16x16x32_bf16 v[64:67], a[0:3], a[68:71], v[64:67]\n"
    "ds_read_b64_tr_b16 a[32:33], v8 offset:16384\n"
    "ds_read_b64_tr_b16 a[34:35], v8 offset:17408\n"
    "v_mfma_f32_16x16x32_bf16 v[96:99], a[0:3], a[72:75], v[96:99]\n"
    "ds_read_b64_tr_b16 a[36:37], v9 offset:16384\n"
    "v_mfma_f32_16x16x32_bf16 v[36:39], a[4:7], a[64:67], v[36:39]\n"
    "buffer_load_dwordx4 v26, s[48:51], s89 offen offset:0 lds\n"
    "v_mfma_f32_16x16x32_bf16 v[68:71], a[4:7], a[68:71], v[68:71]\n"
    "ds_read_b64_tr_b16 a[38:39], v9 offset:17408\n"
    "ds_read_b64_tr_b16 a[40:41], v10 offset:16384\n"
    "v_mfma_f32_16x16x32_bf16 v[100:103], a[4:7], a[72:75], v[100:103]\n"
    "s_add_u32 s74, s89, 31744\n"
    "v_mfma_f32_16x16x32_bf16 v[40:43], a[8:11], a[64:67], v[40:43]\n"
    "ds_read_b64_tr_b16 a[42:43], v10 offset:17408\n"
    "ds_read_b64_tr_b16 a[44:45], v11 offset:16384\n"
    "v_mfma_f32_16x16x32_bf16 v[72:75], a[8:11], a[68:71], v[72:75]\n"
    "buffer_load_dwordx4 v26, s[48:51], s74 offen offset:1024 lds\n"
    "v_mfma_f32_16x16x32_bf16 v[104:107], a[8:11], a[72:75], v[104:107]\n"
    "ds_read_b64_tr_b16 a[46:47], v11 offset:17408\n"
    "v_mfma_f32_16x16x32_bf16 v[44:47], a[12:15], a[64:67], v[44:47]\n"
    "ds_read_b64_tr_b16 a[48:49], v12 offset:16384\n"
    "s_add_u32 s74, s89, 63488\n"
    "v_mfma_f32_16x16x32_bf16 v[76:79], a[12:15], a[68:71], v[76:79]\n"
    "ds_read_b64_tr_b16 a[50:51], v12 offset:17408\n"
    "v_mfma_f32_16x16x32_bf16 v[108:111], a[12:15], a[72:75], v[108:111]\n"
    "ds_read_b64_tr_b16 a[52:53], v13 offset:16384\n"
    "v_mfma_f32_16x16x32_bf16 v[48:51], a[16:19], a[64:67], v[48:51]\n"
    "buffer_load_dwordx4 v27, s[48:51], s74 offen offset:2048 lds\n"
    "ds_read_b64_tr_b16 a[54:55], v13 offset:17408\n"
    "v_mfma_f32_16x16x32_bf16 v[80:83], a[16:19], a[68:71], v[80:83]\n"
    "ds_read_b64_tr_b16 a[56:57], v14 offset:16384\n"
    "v_mfma_f32_16x16x32_bf16 v[112:115], a[16:19], a[72:75], v[112:115]\n"
    "s_add_u32 s74, s89, 95232\n"
    "ds_read_b64_tr_b16 a[58:59], v14 offset:17408\n"
    "v_mfma_f32_16x16x32_bf16 v[52:55], a[20:23], a[64:67], v[52:55]\n"
    "ds_read_b64_tr_b16 a[60:61], v15 offset:16384\n"
    "v_mfma_f32_16x16x32_bf16 v[84:87], a[20:23], a[68:71], v[84:87]\n"
    "buffer_load_dwordx4 v27, s[48:51], s74 offen offset:3072 lds\n"
    "v_mfma_f32_16x16x32_bf16 v[116:119], a[20:23], a[72:75], v[116:119]\n"
    "ds_read_b64_tr_b16 a[62:63], v15 offset:17408\n"
    "ds_read_b128 a[92:95], v31 offset:24576\n"
    "v_mfma_f32_16x16x32_bf16 v[56:59], a[24:27], a[64:67], v[56:59]\n"
    "s_add_u32 s89, s89, 262144\n"
    "v_mfma_f32_16x16x32_bf16 v[88:91], a[24:27], a[68:71], v[88:91]\n"
    "ds_read_b128 a[96:99], v31 offset:26624\n"
    "v_mfma_f32_16x16x32_bf16 v[120:123], a[24:27], a[72:75], v[120:123]\n"
    "ds_read_b128 a[100:103], v31 offset:28672\n"
    "v_mfma_f32_16x16x32_bf16 v[60:63], a[28:31], a[64:67], v[60:63]\n"
    "v_mfma_f32_16x16x32_bf16 v[92:95], a[28:31], a[68:71], v[92:95]\n"
    "v_mfma_f32_16x16x32_bf16 v[124:127], a[28:31], a[72:75], v[124:127]\n"
    "s_waitcnt lgkmcnt(0)\n"
    "s_waitcnt vmcnt(12)\n"
    "s_barrier\n"
    "v_mfma_f32_16x16x32_bf16 v[32:35], a[32:35], a[92:95], v[32:35]\n"
    "s_add_u32 m0, s65, 16384\n"
    "ds_read_b64_tr_b16 a[0:1], v8 offset:24576\n"
    "v_mfma_f32_16x16x32_bf16 v[64:67], a[32:35], a[96:99], v[64:67]\n"
    "ds_read_b64_tr_b16 a[2:3], v8 offset:25600\n"
    "buffer_load_dwordx4 v28, s[52:55], s69 offen offset:0 lds\n"
    "v_mfma_f32_16x16x32_bf16 v[96:99], a[32:35], a[100:103], v[96:99]\n"
    "ds_read_b64_tr_b16 a[4:5], v9 offset:24576\n"
    "v_mfma_f32_16x16x32_bf16 v[36:39], a[36:39], a[92:95], v[36:39]\n"
    "s_add_u32 s76, s69, 7168\n"
    "ds_read_b64_tr_b16 a[6:7], v9 offset:25600\n"
    "v_mfma_f32_16x16x32_bf16 v[68:71], a[36:39], a[96:99], v[68:71]\n"
    "buffer_load_dwordx4 v29, s[52:55], s76 offen offset:1024 lds\n"
    "ds_read_b64_tr_b16 a[8:9], v10 offset:24576\n"
    "v_mfma_f32_16x16x32_bf16 v[100:103], a[36:39], a[100:103], v[100:103]\n"
    "s_add_u32 s76, s69, 14336\n"
    "ds_read_b64_tr_b16 a[10:11], v10 offset:25600\n"
    "v_mfma_f32_16x16x32_bf16 v[40:43], a[40:43], a[92:95], v[40:43]\n"
    "buffer_load_dwordx4 v28, s[52:55], s76 offen offset:2048 lds\n"
    "ds_read_b64_tr_b16 a[12:13], v11 offset:24576\n"
    "v_mfma_f32_16x16x32_bf16 v[72:75], a[40:43], a[96:99], v[72:75]\n"
    "s_add_u32 s76, s69, 21504\n"
    "ds_read_b64_tr_b16 a[14:15], v11 offset:25600\n"
    "v_mfma_f32_16x16x32_bf16 v[104:107], a[40:43], a[100:103], v[104:107]\n"
    "buffer_load_dwordx4 v29, s[52:55], s76 offen offset:3072 lds\n"
    "v_mfma_f32_16x16x32_bf16 v[44:47], a[44:47], a[92:95], v[44:47]\n"
    "ds_read_b64_tr_b16 a[16:17], v12 offset:24576\n"
    "s_add_u32 s69, s69, 128\n"
    "v_mfma_f32_16x16x32_bf16 v[76:79], a[44:47], a[96:99], v[76:79]\n"
    "ds_read_b64_tr_b16 a[18:19], v12 offset:25600\n"
    "s_add_u32 m0, s88, 16384\n"
    "v_mfma_f32_16x16x32_bf16 v[108:111], a[44:47], a[100:103], v[108:111]\n"
    "ds_read_b64_tr_b16 a[20:21], v13 offset:24576\n"
    "buffer_load_dwordx4 v26, s[48:51], s89 offen offset:0 lds\n"
    "v_mfma_f32_16x16x32_bf16 v[48:51], a[48:51], a[92:95], v[48:51]\n"
    "ds_read_b64_tr_b16 a[22:23], v13 offset:25600\n"
    "s_add_u32 s74, s89, 31744\n"
    "v_mfma_f32_16x16x32_bf16 v[80:83], a[48:51], a[96:99], v[80:83]\n"
    "ds_read_b64_tr_b16 a[24:25], v14 offset:24576\n"
    "v_mfma_f32_16x16x32_bf16 v[112:115], a[48:51], a[100:103], v[112:115]\n"
    "buffer_load_dwordx4 v26, s[48:51], s74 offen offset:1024 lds\n"
    "ds_read_b64_tr_b16 a[26:27], v14 offset:25600\n"
    "v_mfma_f32_16x16x32_bf16 v[52:55], a[52:55], a[92:95], v[52:55]\n"
    "s_add_u32 s74, s89, 63488\n"
    "ds_read_b64_tr_b16 a[28:29], v15 offset:24576\n"
    "v_mfma_f32_16x16x32_bf16 v[84:87], a[52:55], a[96:99], v[84:87]\n"
    "buffer_load_dwordx4 v27, s[48:51], s74 offen offset:2048 lds\n"
    "ds_read_b64_tr_b16 a[30:31], v15 offset:25600\n"
    "v_mfma_f32_16x16x32_bf16 v[116:119], a[52:55], a[100:103], v[116:119]\n"
    "s_add_u32 s74, s89, 95232\n"
    "ds_read_b128 a[64:67], v6 offset:8192\n"
    "v_mfma_f32_16x16x32_bf16 v[56:59], a[56:59], a[92:95], v[56:59]\n"
    "buffer_load_dwordx4 v27, s[48:51], s74 offen offset:3072 lds\n"
    "v_mfma_f32_16x16x32_bf16 v[88:91], a[56:59], a[96:99], v[88:91]\n"
    "ds_read_b128 a[68:71], v6 offset:10240\n"
    "s_add_u32 s89, s89, 262144\n"
    "v_mfma_f32_16x16x32_bf16 v[120:123], a[56:59], a[100:103], v[120:123]\n"
    "ds_read_b128 a[72:75], v6 offset:12288\n"
    "v_mfma_f32_16x16x32_bf16 v[60:63], a[60:63], a[92:95], v[60:63]\n"
    "v_mfma_f32_16x16x32_bf16 v[92:95], a[60:63], a[96:99], v[92:95]\n"
    "v_mfma_f32_16x16x32_bf16 v[124:127], a[60:63], a[100:103], v[124:127]\n"
    "s_waitcnt lgkmcnt(0)\n"
    "s_waitcnt vmcnt(12)\n"
    "s_barrier\n"
    "v_mfma_f32_16x16x32_bf16 v[32:35], a[0:3], a[64:67], v[32:35]\n"
    "s_add_u32 m0, s88, 24576\n"
    "v_mfma_f32_16x16x32_bf16 v[64:67], a[0:3], a[68:71], v[64:67]\n"
    "ds_read_b64_tr_b16 a[32:33], v8 offset:0\n"
    "ds_read_b64_tr_b16 a[34:35], v8 offset:1024\n"
    "v_mfma_f32_16x16x32_bf16 v[96:99], a[0:3], a[72:75], v[96:99]\n"
    "ds_read_b64_tr_b16 a[36:37], v9 offset:0\n"
    "v_mfma_f32_16x16x32_bf16 v[36:39], a[4:7], a[64:67], v[36:39]\n"
    "buffer_load_dwordx4 v26, s[48:51], s89 offen offset:0 lds\n"
    "v_mfma_f32_16x16x32_bf16 v[68:71], a[4:7], a[68:71], v[68:71]\n"
    "ds_read_b64_tr_b16 a[38:39], v9 offset:1024\n"
    "ds_read_b64_tr_b16 a[40:41], v10 offset:0\n"
    "v_mfma_f32_16x16x32_bf16 v[100:103], a[4:7], a[72:75], v[100:103]\n"
    "s_add_u32 s74, s89, 31744\n"
    "v_mfma_f32_16x16x32_bf16 v[40:43], a[8:11], a[64:67], v[40:43]\n"
    "ds_read_b64_tr_b16 a[42:43], v10 offset:1024\n"
    "ds_read_b64_tr_b16 a[44:45], v11 offset:0\n"
    "v_mfma_f32_16x16x32_bf16 v[72:75], a[8:11], a[68:71], v[72:75]\n"
    "buffer_load_dwordx4 v26, s[48:51], s74 offen offset:1024 lds\n"
    "v_mfma_f32_16x16x32_bf16 v[104:107], a[8:11], a[72:75], v[104:107]\n"
    "ds_read_b64_tr_b16 a[46:47], v11 offset:1024\n"
    "v_mfma_f32_16x16x32_bf16 v[44:47], a[12:15], a[64:67], v[44:47]\n"
    "ds_read_b64_tr_b16 a[48:49], v12 offset:0\n"
    "s_add_u32 s74, s89, 63488\n"
    "v_mfma_f32_16x16x32_bf16 v[76:79], a[12:15], a[68:71], v[76:79]\n"
    "ds_read_b64_tr_b16 a[50:51], v12 offset:1024\n"
    "v_mfma_f32_16x16x32_bf16 v[108:111], a[12:15], a[72:75], v[108:111]\n"
    "ds_read_b64_tr_b16 a[52:53], v13 offset:0\n"
    "v_mfma_f32_16x16x32_bf16 v[48:51], a[16:19], a[64:67], v[48:51]\n"
    "buffer_load_dwordx4 v27, s[48:51], s74 offen offset:2048 lds\n"
    "ds_read_b64_tr_b16 a[54:55], v13 offset:1024\n"
    "v_mfma_f32_16x16x32_bf16 v[80:83], a[16:19], a[68:71], v[80:83]\n"
    "ds_read_b64_tr_b16 a[56:57], v14 offset:0\n"
    "v_mfma_f32_16x16x32_bf16 v[112:115], a[16:19], a[72:75], v[112:115]\n"
    "s_add_u32 s74, s89, 95232\n"
    "ds_read_b64_tr_b16 a[58:59], v14 offset:1024\n"
    "v_mfma_f32_16x16x32_bf16 v[52:55], a[20:23], a[64:67], v[52:55]\n"
    "ds_read_b64_tr_b16 a[60:61], v15 offset:0\n"
    "v_mfma_f32_16x16x32_bf16 v[84:87], a[20:23], a[68:71], v[84:87]\n"
    "buffer_load_dwordx4 v27, s[48:51], s74 offen offset:3072 lds\n"
    "v_mfma_f32_16x16x32_bf16 v[116:119], a[20:23], a[72:75], v[116:119]\n"
    "ds_read_b64_tr_b16 a[62:63], v15 offset:1024\n"
    "ds_read_b128 a[92:95], v7 offset:8192\n"
    "v_mfma_f32_16x16x32_bf16 v[56:59], a[24:27], a[64:67], v[56:59]\n"
    "s_add_u32 s89, s89, 262144\n"
    "v_mfma_f32_16x16x32_bf16 v[88:91], a[24:27], a[68:71], v[88:91]\n"
    "ds_read_b128 a[96:99], v7 offset:10240\n"
    "v_mfma_f32_16x16x32_bf16 v[120:123], a[24:27], a[72:75], v[120:123]\n"
    "ds_read_b128 a[100:103], v7 offset:12288\n"
    "v_mfma_f32_16x16x32_bf16 v[60:63], a[28:31], a[64:67], v[60:63]\n"
    "v_mfma_f32_16x16x32_bf16 v[92:95], a[28:31], a[68:71], v[92:95]\n"
    "v_mfma_f32_16x16x32_bf16 v[124:127], a[28:31], a[72:75], v[124:127]\n"
    "s_waitcnt lgkmcnt(0)\n"
    "s_waitcnt vmcnt(12)\n"
    "s_barrier\n"
    "v_mfma_f32_16x16x32_bf16 v[32:35], a[32:35], a[92:95], v[32:35]\n"
    "s_add_u32 m0, s88, 0\n"
    "v_mfma_f32_16x16x32_bf16 v[64:67], a[32:35], a[96:99], v[64:67]\n"
    "ds_read_b64_tr_b16 a[0:1], v8 offset:8192\n"
    "ds_read_b64_tr_b16 a[2:3], v8 offset:9216\n"
    "v_mfma_f32_16x16x32_bf16 v[96:99], a[32:35], a[100:103], v[96:99]\n"
    "ds_read_b64_tr_b16 a[4:5], v9 offset:8192\n"
    "v_mfma_f32_16x16x32_bf16 v[36:39], a[36:39], a[92:95], v[36:39]\n"
    "buffer_load_dwordx4 v26, s[48:51], s89 offen offset:0 lds\n"
    "v_mfma_f32_16x16x32_bf16 v[68:71], a[36:39], a[96:99], v[68:71]\n"
    "ds_read_b64_tr_b16 a[6:7], v9 offset:9216\n"
    "ds_read_b64_tr_b16 a[8:9], v10 offset:8192\n"
    "v_mfma_f32_16x16x32_bf16 v[100:103], a[36:39], a[100:103], v[100:103]\n"
    "s_add_u32 s74, s89, 31744\n"
    "v_mfma_f32_16x16x32_bf16 v[40:43], a[40:43], a[92:95], v[40:43]\n"
    "ds_read_b64_tr_b16 a[10:11], v10 offset:9216\n"
    "ds_read_b64_tr_b16 a[12:13], v11 offset:8192\n"
    "v_mfma_f32_16x16x32_bf16 v[72:75], a[40:43], a[96:99], v[72:75]\n"
    "buffer_load_dwordx4 v26, s[48:51], s74 offen offset:1024 lds\n"
    "v_mfma_f32_16x16x32_bf16 v[104:107], a[40:43], a[100:103], v[104:107]\n"
    "ds_read_b64_tr_b16 a[14:15], v11 offset:9216\n"
    "v_mfma_f32_16x16x32_bf16 v[44:47], a[44:47], a[92:95], v[44:47]\n"
    "ds_read_b64_tr_b16 a[16:17], v12 offset:8192\n"
    "s_add_u32 s74, s89, 63488\n"
    "v_mfma_f32_16x16x32_bf16 v[76:79], a[44:47], a[96:99], v[76:79]\n"
    "ds_read_b64_tr_b16 a[18:19], v12 offset:9216\n"
    "v_mfma_f32_16x16x32_bf16 v[108:111], a[44:47], a[100:103], v[108:111]\n"
    "ds_read_b64_tr_b16 a[20:21], v13 offset:8192\n"
    "v_mfma_f32_16x16x32_bf16 v[48:51], a[48:51], a[92:95], v[48:51]\n"
    "buffer_load_dwordx4 v27, s[48:51], s74 offen offset:2048 lds\n"
    "ds_read_b64_tr_b16 a[22:23], v13 offset:9216\n"
    "v_mfma_f32_16x16x32_bf16 v[80:83], a[48:51], a[96:99], v[80:83]\n"
    "ds_read_b64_tr_b16 a[24:25], v14 offset:8192\n"
    "v_mfma_f32_16x16x32_bf16 v[112:115], a[48:51], a[100:103], v[112:115]\n"
    "s_add_u32 s74, s89, 95232\n"
    "ds_read_b64_tr_b16 a[26:27], v14 offset:9216\n"
    "v_mfma_f32_16x16x32_bf16 v[52:55], a[52:55], a[92:95], v[52:55]\n"
    "ds_read_b64_tr_b16 a[28:29], v15 offset:8192\n"
    "v_mfma_f32_16x16x32_bf16 v[84:87], a[52:55], a[96:99], v[84:87]\n"
    "buffer_load_dwordx4 v27, s[48:51], s74 offen offset:3072 lds\n"
    "v_mfma_f32_16x16x32_bf16 v[116:119], a[52:55], a[100:103], v[116:119]\n"
    "ds_read_b64_tr_b16 a[30:31], v15 offset:9216\n"
    "ds_read_b128 a[64:67], v6 offset:40960\n"
    "v_mfma_f32_16x16x32_bf16 v[56:59], a[56:59], a[92:95], v[56:59]\n"
    "s_add_u32 s89, s89, 262144\n"
    "v_mfma_f32_16x16x32_bf16 v[88:91], a[56:59], a[96:99], v[88:91]\n"
    "ds_read_b128 a[68:71], v6 offset:43008\n"
    "v_mfma_f32_16x16x32_bf16 v[120:123], a[56:59], a[100:103], v[120:123]\n"
    "ds_read_b128 a[72:75], v6 offset:45056\n"
    "v_mfma_f32_16x16x32_bf16 v[60:63], a[60:63], a[92:95], v[60:63]\n"
    "v_mfma_f32_16x16x32_bf16 v[92:95], a[60:63], a[96:99], v[92:95]\n"
    "v_mfma_f32_16x16x32_bf16 v[124:127], a[60:63], a[100:103], v[124:127]\n"
    "s_waitcnt lgkmcnt(0)\n"
    "s_waitcnt vmcnt(8)\n"
    "s_barrier\n"
    "v_mfma_f32_16x16x32_bf16 v[32:35], a[0:3], a[64:67], v[32:35]\n"
    "s_add_u32 m0, s88, 8192\n"
    "v_mfma_f32_16x16x32_bf16 v[64:67], a[0:3], a[68:71], v[64:67]\n"
    "ds_read_b64_tr_b16 a[32:33], v8 offset:16384\n"
    "ds_read_b64_tr_b16 a[34:35], v8 offset:17408\n"
    "v_mfma_f32_16x16x32_bf16 v[96:99], a[0:3], a[72:75], v[96:99]\n"
    "ds_read_b64_tr_b16 a[36:37], v9 offset:16384\n"
    "v_mfma_f32_16x16x32_bf16 v[36:39], a[4:7], a[64:67], v[36:39]\n"
    "buffer_load_dwordx4 v26, s[48:51], s89 offen offset:0 lds\n"
    "v_mfma_f32_16x16x32_bf16 v[68:71], a[4:7], a[68:71], v[68:71]\n"
    "ds_read_b64_tr_b16 a[38:39], v9 offset:17408\n"
    "ds_read_b64_tr_b16 a[40:41], v10 offset:16384\n"
    "v_mfma_f32_16x16x32_bf16 v[100:103], a[4:7], a[72:75], v[100:103]\n"
    "s_add_u32 s74, s89, 31744\n"
    "v_mfma_f32_16x16x32_bf16 v[40:43], a[8:11], a[64:67], v[40:43]\n"
    "ds_read_b64_tr_b16 a[42:43], v10 offset:17408\n"
    "ds_read_b64_tr_b16 a[44:45], v11 offset:16384\n"
    "v_mfma_f32_16x16x32_bf16 v[72:75], a[8:11], a[68:71], v[72:75]\n"
    "buffer_load_dwordx4 v26, s[48:51], s74 offen offset:1024 lds\n"
    "v_mfma_f32_16x16x32_bf16 v[104:107], a[8:11], a[72:75], v[104:107]\n"
    "ds_read_b64_tr_b16 a[46:47], v11 offset:17408\n"
    "v_mfma_f32_16x16x32_bf16 v[44:47], a[12:15], a[64:67], v[44:47]\n"
    "ds_read_b64_tr_b16 a[48:49], v12 offset:16384\n"
    "s_add_u32 s74, s89, 63488\n"
    "v_mfma_f32_16x16x32_bf16 v[76:79], a[12:15], a[68:71], v[76:79]\n"
    "ds_read_b64_tr_b16 a[50:51], v12 offset:17408\n"
    "v_mfma_f32_16x16x32_bf16 v[108:111], a[12:15], a[72:75], v[108:111]\n"
    "ds_read_b64_tr_b16 a[52:53], v13 offset:16384\n"
    "v_mfma_f32_16x16x32_bf16 v[48:51], a[16:19], a[64:67], v[48:51]\n"
    "buffer_load_dwordx4 v27, s[48:51], s74 offen offset:2048 lds\n"
    "ds_read_b64_tr_b16 a[54:55], v13 offset:17408\n"
    "v_mfma_f32_16x16x32_bf16 v[80:83], a[16:19], a[68:71], v[80:83]\n"
    "ds_read_b64_tr_b16 a[56:57], v14 offset:16384\n"
    "v_mfma_f32_16x16x32_bf16 v[112:115], a[16:19], a[72:75], v[112:115]\n"
    "s_add_u32 s74, s89, 95232\n"
    "ds_read_b64_tr_b16 a[58:59], v14 offset:17408\n"
    "v_mfma_f32_16x16x32_bf16 v[52:55], a[20:23], a[64:67], v[52:55]\n"
    "ds_read_b64_tr_b16 a[60:61], v15 offset:16384\n"
    "v_mfma_f32_16x16x32_bf16 v[84:87], a[20:23], a[68:71], v[84:87]\n"
    "buffer_load_dwordx4 v27, s[48:51], s74 offen offset:3072 lds\n"
    "v_mfma_f32_16x16x32_bf16 v[116:119], a[20:23], a[72:75], v[116:119]\n"
    "ds_read_b64_tr_b16 a[62:63], v15 offset:17408\n"
    "ds_read_b128 a[92:95], v7 offset:40960\n"
    "v_mfma_f32_16x16x32_bf16 v[56:59], a[24:27], a[64:67], v[56:59]\n"
    "s_add_u32 s89, s89, 262144\n"
    "v_mfma_f32_16x16x32_bf16 v[88:91], a[24:27], a[68:71], v[88:91]\n"
    "ds_read_b128 a[96:99], v7 offset:43008\n"
    "v_mfma_f32_16x16x32_bf16 v[120:123], a[24:27], a[72:75], v[120:123]\n"
    "ds_read_b128 a[100:103], v7 offset:45056\n"
    "v_mfma_f32_16x16x32_bf16 v[60:63], a[28:31], a[64:67], v[60:63]\n"
    "v_mfma_f32_16x16x32_bf16 v[92:95], a[28:31], a[68:71], v[92:95]\n"
    "v_mfma_f32_16x16x32_bf16 v[124:127], a[28:31], a[72:75], v[124:127]\n"
    "s_waitcnt lgkmcnt(0)\n"
    "s_waitcnt vmcnt(8)\n"
    "s_barrier\n"
    "v_mfma_f32_16x16x32_bf16 v[32:35], a[32:35], a[92:95], v[32:35]\n"
    "s_add_u32 m0, s88, 16384\n"
    "v_mfma_f32_16x16x32_bf16 v[64:67], a[32:35], a[96:99], v[64:67]\n"
    "ds_read_b64_tr_b16 a[0:1], v8 offset:24576\n"
    "ds_read_b64_tr_b16 a[2:3], v8 offset:25600\n"
    "v_mfma_f32_16x16x32_bf16 v[96:99], a[32:35], a[100:103], v[96:99]\n"
    "ds_read_b64_tr_b16 a[4:5], v9 offset:24576\n"
    "v_mfma_f32_16x16x32_bf16 v[36:39], a[36:39], a[92:95], v[36:39]\n"
    "buffer_load_dwordx4 v26, s[48:51], s89 offen offset:0 lds\n"
    "v_mfma_f32_16x16x32_bf16 v[68:71], a[36:39], a[96:99], v[68:71]\n"
    "ds_read_b64_tr_b16 a[6:7], v9 offset:25600\n"
    "ds_read_b64_tr_b16 a[8:9], v10 offset:24576\n"
    "v_mfma_f32_16x16x32_bf16 v[100:103], a[36:39], a[100:103], v[100:103]\n"
    "s_add_u32 s74, s89, 31744\n"
    "v_mfma_f32_16x16x32_bf16 v[40:43], a[40:43], a[92:95], v[40:43]\n"
    "ds_read_b64_tr_b16 a[10:11], v10 offset:25600\n"
    "ds_read_b64_tr_b16 a[12:13], v11 offset:24576\n"
    "v_mfma_f32_16x16x32_bf16 v[72:75], a[40:43], a[96:99], v[72:75]\n"
    "buffer_load_dwordx4 v26, s[48:51], s74 offen offset:1024 lds\n"
    "v_mfma_f32_16x16x32_bf16 v[104:107], a[40:43], a[100:103], v[104:107]\n"
    "ds_read_b64_tr_b16 a[14:15], v11 offset:25600\n"
    "v_mfma_f32_16x16x32_bf16 v[44:47], a[44:47], a[92:95], v[44:47]\n"
    "ds_read_b64_tr_b16 a[16:17], v12 offset:24576\n"
    "s_add_u32 s74, s89, 63488\n"
    "v_mfma_f32_16x16x32_bf16 v[76:79], a[44:47], a[96:99], v[76:79]\n"
    "ds_read_b64_tr_b16 a[18:19], v12 offset:25600\n"
    "v_mfma_f32_16x16x32_bf16 v[108:111], a[44:47], a[100:103], v[108:111]\n"
    "ds_read_b64_tr_b16 a[20:21], v13 offset:24576\n"
    "v_mfma_f32_16x16x32_bf16 v[48:51], a[48:51], a[92:95], v[48:51]\n"
    "buffer_load_dwordx4 v27, s[48:51], s74 offen offset:2048 lds\n"
    "ds_read_b64_tr_b16 a[22:23], v13 offset:25600\n"
    "v_mfma_f32_16x16x32_bf16 v[80:83], a[48:51], a[96:99], v[80:83]\n"
    "ds_read_b64_tr_b16 a[24:25], v14 offset:24576\n"
    "v_mfma_f32_16x16x32_bf16 v[112:115], a[48:51], a[100:103], v[112:115]\n"
    "s_add_u32 s74, s89, 95232\n"
    "ds_read_b64_tr_b16 a[26:27], v14 offset:25600\n"
    "v_mfma_f32_16x16x32_bf16 v[52:55], a[52:55], a[92:95], v[52:55]\n"
    "ds_read_b64_tr_b16 a[28:29], v15 offset:24576\n"
    "v_mfma_f32_16x16x32_bf16 v[84:87], a[52:55], a[96:99], v[84:87]\n"
    "buffer_load_dwordx4 v27, s[48:51], s74 offen offset:3072 lds\n"
    "v_mfma_f32_16x16x32_bf16 v[116:119], a[52:55], a[100:103], v[116:119]\n"
    "ds_read_b64_tr_b16 a[30:31], v15 offset:25600\n"
    "ds_read_b128 a[64:67], v30 offset:8192\n"
    "v_mfma_f32_16x16x32_bf16 v[56:59], a[56:59], a[92:95], v[56:59]\n"
    "s_add_u32 s89, s89, 262144\n"
    "v_mfma_f32_16x16x32_bf16 v[88:91], a[56:59], a[96:99], v[88:91]\n"
    "ds_read_b128 a[68:71], v30 offset:10240\n"
    "v_mfma_f32_16x16x32_bf16 v[120:123], a[56:59], a[100:103], v[120:123]\n"
    "ds_read_b128 a[72:75], v30 offset:12288\n"
    "v_mfma_f32_16x16x32_bf16 v[60:63], a[60:63], a[92:95], v[60:63]\n"
    "v_mfma_f32_16x16x32_bf16 v[92:95], a[60:63], a[96:99], v[92:95]\n"
    "v_mfma_f32_16x16x32_bf16 v[124:127], a[60:63], a[100:103], v[124:127]\n"
    "s_waitcnt lgkmcnt(0)\n"
    "s_waitcnt vmcnt(8)\n"
    "s_barrier\n"
    "v_mfma_f32_16x16x32_bf16 v[32:35], a[0:3], a[64:67], v[32:35]\n"
    "s_mov_b64 s[60:61], %[wp]\n"
    "ds_read_b64_tr_b16 a[32:33], v8 offset:0\n"
    "v_mfma_f32_16x16x32_bf16 v[64:67], a[0:3], a[68:71], v[64:67]\n"
    "s_and_b32 s61, s61, 0xffff\n"
    "ds_read_b64_tr_b16 a[34:35], v8 offset:1024\n"
    "v_mfma_f32_16x16x32_bf16 v[96:99], a[0:3], a[72:75], v[96:99]\n"
    "s_mov_b32 s62, %[nw]\n"
    "ds_read_b64_tr_b16 a[36:37], v9 offset:0\n"
    "v_mfma_f32_16x16x32_bf16 v[36:39], a[4:7], a[64:67], v[36:39]\n"
    "s_mov_b32 s63, 0x20000\n"
    "ds_read_b64_tr_b16 a[38:39], v9 offset:1024\n"
    "v_mfma_f32_16x16x32_bf16 v[68:71], a[4:7], a[68:71], v[68:71]\n"
    "s_add_u32 m0, s64, 24576\n"
    "ds_read_b64_tr_b16 a[40:41], v10 offset:0\n"
    "v_mfma_f32_16x16x32_bf16 v[100:103], a[4:7], a[72:75], v[100:103]\n"
    "buffer_load_dwordx4 v26, s[60:63], s68 offen offset:0 lds\n"
    "ds_read_b64_tr_b16 a[42:43], v10 offset:1024\n"
    "v_mfma_f32_16x16x32_bf16 v[40:43], a[8:11], a[64:67], v[40:43]\n"
    "s_add_u32 s74, s68, 31744\n"
    "ds_read_b64_tr_b16 a[44:45], v11 offset:0\n"
    "v_mfma_f32_16x16x32_bf16 v[72:75], a[8:11], a[68:71], v[72:75]\n"
    "buffer_load_dwordx4 v26, s[60:63], s74 offen offset:1024 lds\n"
    "s_add_u32 s74, s68, 63488\n"
    "v_mfma_f32_16x16x32_bf16 v[104:107], a[8:11], a[72:75], v[104:107]\n"
    "ds_read_b64_tr_b16 a[46:47], v11 offset:1024\n"
    "buffer_load_dwordx4 v27, s[60:63], s74 offen offset:2048 lds\n"
    "v_mfma_f32_16x16x32_bf16 v[44:47], a[12:15], a[64:67], v[44:47]\n"
    "ds_read_b64_tr_b16 a[48:49], v12 offset:0\n"
    "s_add_u32 s74, s68, 95232\n"
    "v_mfma_f32_16x16x32_bf16 v[76:79], a[12:15], a[68:71], v[76:79]\n"
    "ds_read_b64_tr_b16 a[50:51], v12 offset:1024\n"
    "buffer_load_dwordx4 v27, s[60:63], s74 offen offset:3072 lds\n"
    "v_mfma_f32_16x16x32_bf16 v[108:111], a[12:15], a[72:75], v[108:111]\n"
    "ds_read_b64_tr_b16 a[52:53], v13 offset:0\n"
    "s_add_u32 m0, s64, 28672\n"
    "v_mfma_f32_16x16x32_bf16 v[48:51], a[16:19], a[64:67], v[48:51]\n"
    "ds_read_b64_tr_b16 a[54:55], v13 offset:1024\n"
    "s_add_u32 s74, s68, 131072\n"
    "v_mfma_f32_16x16x32_bf16 v[80:83], a[16:19], a[68:71], v[80:83]\n"
    "ds_read_b64_tr_b16 a[56:57], v14 offset:0\n"
    "buffer_load_dwordx4 v26, s[60:63], s74 offen offset:0 lds\n"
    "v_mfma_f32_16x16x32_bf16 v[112:115], a[16:19], a[72:75], v[112:115]\n"
    "s_add_u32 s74, s68, 162816\n"
    "ds_read_b64_tr_b16 a[58:59], v14 offset:1024\n"
    "v_mfma_f32_16x16x32_bf16 v[52:55], a[20:23], a[64:67], v[52:55]\n"
    "buffer_load_dwordx4 v26, s[60:63], s74 offen offset:1024 lds\n"
    "ds_read_b64_tr_b16 a[60:61], v15 offset:0\n"
    "v_mfma_f32_16x16x32_bf16 v[84:87], a[20:23], a[68:71], v[84:87]\n"
    "s_add_u32 s74, s68, 194560\n"
    "ds_read_b64_tr_b16 a[62:63], v15 offset:1024\n"
    "v_mfma_f32_16x16x32_bf16 v[116:119], a[20:23], a[72:75], v[116:119]\n"
    "buffer_load_dwordx4 v27, s[60:63], s74 offen offset:2048 lds\n"
    "ds_read_b128 a[92:95], v31 offset:8192\n"
    "v_mfma_f32_16x16x32_bf16 v[56:59], a[24:27], a[64:67], v[56:59]\n"
    "s_add_u32 s74, s68, 226304\n"
    "ds_read_b128 a[96:99], v31 offset:10240\n"
    "v_mfma_f32_16x16x32_bf16 v[88:91], a[24:27], a[68:71], v[88:91]\n"
    "buffer_load_dwordx4 v27, s[60:63], s74 offen offset:3072 lds\n"
    "ds_read_b128 a[100:103], v31 offset:12288\n"
    "v_mfma_f32_16x16x32_bf16 v[120:123], a[24:27], a[72:75], v[120:123]\n"
    "s_add_u32 s68, s68, 262144\n"
    "v_mfma_f32_16x16x32_bf16 v[60:63], a[28:31], a[64:67], v[60:63]\n"
    "v_mfma_f32_16x16x32_bf16 v[92:95], a[28:31], a[68:71], v[92:95]\n"
    "v_mfma_f32_16x16x32_bf16 v[124:127], a[28:31], a[72:75], v[124:127]\n"
    "s_waitcnt lgkmcnt(0)\n"
    "s_waitcnt vmcnt(12)\n"
    "s_barrier\n"
    "v_mfma_f32_16x16x32_bf16 v[32:35], a[32:35], a[92:95], v[32:35]\n"
    "ds_read_b64_tr_b16 a[0:1], v8 offset:8192\n"
    "v_mfma_f32_16x16x32_bf16 v[64:67], a[32:35], a[96:99], v[64:67]\n"
    "ds_read_b64_tr_b16 a[2:3], v8 offset:9216\n"
    "v_mfma_f32_16x16x32_bf16 v[96:99], a[32:35], a[100:103], v[96:99]\n"
    "ds_read_b64_tr_b16 a[4:5], v9 offset:8192\n"
    "v_mfma_f32_16x16x32_bf16 v[36:39], a[36:39], a[92:95], v[36:39]\n"
    "ds_read_b64_tr_b16 a[6:7], v9 offset:9216\n"
    "v_mfma_f32_16x16x32_bf16 v[68:71], a[36:39], a[96:99], v[68:71]\n"
    "ds_read_b64_tr_b16 a[8:9], v10 offset:8192\n"
    "v_mfma_f32_16x16x32_bf16 v[100:103], a[36:39], a[100:103], v[100:103]\n"
    "ds_read_b64_tr_b16 a[10:11], v10 offset:9216\n"
    "v_mfma_f32_16x16x32_bf16 v[40:43], a[40:43], a[92:95], v[40:43]\n"
    "ds_read_b64_tr_b16 a[12:13], v11 offset:8192\n"
    "v_mfma_f32_16x16x32_bf16 v[72:75], a[40:43], a[96:99], v[72:75]\n"
    "v_mfma_f32_16x16x32_bf16 v[104:107], a[40:43], a[100:103], v[104:107]\n"
    "ds_read_b64_tr_b16 a[14:15], v11 offset:9216\n"
    "v_mfma_f32_16x16x32_bf16 v[44:47], a[44:47], a[92:95], v[44:47]\n"
    "ds_read_b64_tr_b16 a[16:17], v12 offset:8192\n"
    "v_mfma_f32_16x16x32_bf16 v[76:79], a[44:47], a[96:99], v[76:79]\n"
    "ds_read_b64_tr_b16 a[18:19], v12 offset:9216\n"
    "v_mfma_f32_16x16x32_bf16 v[108:111], a[44:47], a[100:103], v[108:111]\n"
    "ds_read_b64_tr_b16 a[20:21], v13 offset:8192\n"
    "v_mfma_f32_16x16x32_bf16 v[48:51], a[48:51], a[92:95], v[48:51]\n"
    "ds_read_b64_tr_b16 a[22:23], v13 offset:9216\n"
    "v_mfma_f32_16x16x32_bf16 v[80:83], a[48:51], a[96:99], v[80:83]\n"
    "ds_read_b64_tr_b16 a[24:25], v14 offset:8192\n"
    "v_mfma_f32_16x16x32_bf16 v[112:115], a[48:51], a[100:103], v[112:115]\n"
    "ds_read_b64_tr_b16 a[26:27], v14 offset:9216\n"
    "v_mfma_f32_16x16x32_bf16 v[52:55], a[52:55], a[92:95], v[52:55]\n"
    "ds_read_b64_tr_b16 a[28:29], v15 offset:8192\n"
    "v_mfma_f32_16x16x32_bf16 v[84:87], a[52:55], a[96:99], v[84:87]\n"
    "ds_read_b64_tr_b16 a[30:31], v15 offset:9216\n"
    "v_mfma_f32_16x16x32_bf16 v[116:119], a[52:55], a[100:103], v[116:119]\n"
    "ds_read_b128 a[64:67], v30 offset:24576\n"
    "v_mfma_f32_16x16x32_bf16 v[56:59], a[56:59], a[92:95], v[56:59]\n"
    "ds_read_b128 a[68:71], v30 offset:26624\n"
    "v_mfma_f32_16x16x32_bf16 v[88:91], a[56:59], a[96:99], v[88:91]\n"
    "ds_read_b128 a[72:75], v30 offset:28672\n"
    "v_mfma_f32_16x16x32_bf16 v[120:123], a[56:59], a[100:103], v[120:123]\n"
    "v_mfma_f32_16x16x32_bf16 v[60:63], a[60:63], a[92:95], v[60:63]\n"
    "v_mfma_f32_16x16x32_bf16 v[92:95], a[60:63], a[96:99], v[92:95]\n"
    "v_mfma_f32_16x16x32_bf16 v[124:127], a[60:63], a[100:103], v[124:127]\n"
    "s_waitcnt lgkmcnt(0)\n"
    "s_waitcnt vmcnt(8)\n"
    "s_barrier\n"
    "v_mfma_f32_16x16x32_bf16 v[32:35], a[0:3], a[64:67], v[32:35]\n"
    "s_add_u32 m0, s64, 8192\n"
    "ds_read_b64_tr_b16 a[32:33], v8 offset:16384\n"
    "v_mfma_f32_16x16x32_bf16 v[64:67], a[0:3], a[68:71], v[64:67]\n"
    "ds_read_b64_tr_b16 a[34:35], v8 offset:17408\n"
    "buffer_load_dwordx4 v26, s[60:63], s68 offen offset:0 lds\n"
    "v_mfma_f32_16x16x32_bf16 v[96:99], a[0:3], a[72:75], v[96:99]\n"
    "ds_read_b64_tr_b16 a[36:37], v9 offset:16384\n"
    "v_mfma_f32_16x16x32_bf16 v[36:39], a[4:7], a[64:67], v[36:39]\n"
    "s_add_u32 s74, s68, 31744\n"
    "ds_read_b64_tr_b16 a[38:39], v9 offset:17408\n"
    "v_mfma_f32_16x16x32_bf16 v[68:71], a[4:7], a[68:71], v[68:71]\n"
    "buffer_load_dwordx4 v26, s[60:63], s74 offen offset:1024 lds\n"
    "ds_read_b64_tr_b16 a[40:41], v10 offset:16384\n"
    "v_mfma_f32_16x16x32_bf16 v[100:103], a[4:7], a[72:75], v[100:103]\n"
    "s_add_u32 s74, s68, 63488\n"
    "ds_read_b64_tr_b16 a[42:43], v10 offset:17408\n"
    "v_mfma_f32_16x16x32_bf16 v[40:43], a[8:11], a[64:67], v[40:43]\n"
    "buffer_load_dwordx4 v27, s[60:63], s74 offen offset:2048 lds\n"
    "ds_read_b64_tr_b16 a[44:45], v11 offset:16384\n"
    "v_mfma_f32_16x16x32_bf16 v[72:75], a[8:11], a[68:71], v[72:75]\n"
    "s_add_u32 s74, s68, 95232\n"
    "ds_read_b64_tr_b16 a[46:47], v11 offset:17408\n"
    "v_mfma_f32_16x16x32_bf16 v[104:107], a[8:11], a[72:75], v[104:107]\n"
    "buffer_load_dwordx4 v27, s[60:63], s74 offen offset:3072 lds\n"
    "v_mfma_f32_16x16x32_bf16 v[44:47], a[12:15], a[64:67], v[44:47]\n"
    "ds_read_b64_tr_b16 a[48:49], v12 offset:16384\n"
    "s_add_u32 m0, s64, 12288\n"
    "v_mfma_f32_16x16x32_bf16 v[76:79], a[12:15], a[68:71], v[76:79]\n"
    "ds_read_b64_tr_b16 a[50:51], v12 offset:17408\n"
    "s_add_u32 s74, s68, 131072\n"
    "v_mfma_f32_16x16x32_bf16 v[108:111], a[12:15], a[72:75], v[108:111]\n"
    "ds_read_b64_tr_b16 a[52:53], v13 offset:16384\n"
    "buffer_load_dwordx4 v26, s[60:63], s74 offen offset:0 lds\n"
    "v_mfma_f32_16x16x32_bf16 v[48:51], a[16:19], a[64:67], v[48:51]\n"
    "ds_read_b64_tr_b16 a[54:55], v13 offset:17408\n"
    "s_add_u32 s74, s68, 162816\n"
    "v_mfma_f32_16x16x32_bf16 v[80:83], a[16:19], a[68:71], v[80:83]\n"
    "ds_read_b64_tr_b16 a[56:57], v14 offset:16384\n"
    "v_mfma_f32_16x16x32_bf16 v[112:115], a[16:19], a[72:75], v[112:115]\n"
    "buffer_load_dwordx4 v26, s[60:63], s74 offen offset:1024 lds\n"
    "ds_read_b64_tr_b16 a[58:59], v14 offset:17408\n"
    "v_mfma_f32_16x16x32_bf16 v[52:55], a[20:23], a[64:67], v[52:55]\n"
    "s_add_u32 s74, s68, 194560\n"
    "ds_read_b64_tr_b16 a[60:61], v15 offset:16384\n"
    "v_mfma_f32_16x16x32_bf16 v[84:87], a[20:23], a[68:71], v[84:87]\n"
    "buffer_load_dwordx4 v27, s[60:63], s74 offen offset:2048 lds\n"
    "ds_read_b64_tr_b16 a[62:63], v15 offset:17408\n"
    "v_mfma_f32_16x16x32_bf16 v[116:119], a[20:23], a[72:75], v[116:119]\n"
    "s_add_u32 s74, s68, 226304\n"
    "ds_read_b128 a[92:95], v31 offset:24576\n"
    "v_mfma_f32_16x16x32_bf16 v[56:59], a[24:27], a[64:67], v[56:59]\n"
    "buffer_load_dwordx4 v27, s[60:63], s74 offen offset:3072 lds\n"
    "v_mfma_f32_16x16x32_bf16 v[88:91], a[24:27], a[68:71], v[88:91]\n"
    "ds_read_b128 a[96:99], v31 offset:26624\n"
    "s_add_u32 s68, s68, 262144\n"
    "v_mfma_f32_16x16x32_bf16 v[120:123], a[24:27], a[72:75], v[120:123]\n"
    "ds_read_b128 a[100:103], v31 offset:28672\n"
    "v_mfma_f32_16x16x32_bf16 v[60:63], a[28:31], a[64:67], v[60:63]\n"
    "v_mfma_f32_16x16x32_bf16 v[92:95], a[28:31], a[68:71], v[92:95]\n"
    "v_mfma_f32_16x16x32_bf16 v[124:127], a[28:31], a[72:75], v[124:127]\n"
    "s_waitcnt lgkmcnt(0)\n"
    "s_barrier\n"
    "v_mfma_f32_16x16x32_bf16 v[32:35], a[32:35], a[92:95], v[32:35]\n"
    "v_mfma_f32_16x16x32_bf16 v[36:39], a[36:39], a[92:95], v[36:39]\n"
    "v_mfma_f32_16x16x32_bf16 v[40:43], a[40:43], a[92:95], v[40:43]\n"
    "v_mfma_f32_16x16x32_bf16 v[44:47], a[44:47], a[92:95], v[44:47]\n"
    "v_mfma_f32_16x16x32_bf16 v[48:51], a[48:51], a[92:95], v[48:51]\n"
    "v_mfma_f32_16x16x32_bf16 v[52:55], a[52:55], a[92:95], v[52:55]\n"
    "v_mfma_f32_16x16x32_bf16 v[56:59], a[56:59], a[92:95], v[56:59]\n"
    "v_mfma_f32_16x16x32_bf16 v[60:63], a[60:63], a[92:95], v[60:63]\n"
    "v_mfma_f32_16x16x32_bf16 v[64:67], a[32:35], a[96:99], v[64:67]\n"
    "v_pk_mul_f32 v[32:33], v[32:33], s[80:81]\n"
    "v_pk_mul_f32 v[34:35], v[34:35], s[80:81]\n"
    "v_pk_mul_f32 v[36:37], v[36:37], s[80:81]\n"
    "v_pk_mul_f32 v[38:39], v[38:39], s[80:81]\n"
    "v_pk_mul_f32 v[40:41], v[40:41], s[80:81]\n"
    "v_mfma_f32_16x16x32_bf16 v[68:71], a[36:39], a[96:99], v[68:71]\n"
    "v_pk_mul_f32 v[42:43], v[42:43], s[80:81]\n"
    "v_pk_mul_f32 v[44:45], v[44:45], s[80:81]\n"
    "v_pk_mul_f32 v[46:47], v[46:47], s[80:81]\n"
    "v_pk_mul_f32 v[48:49], v[48:49], s[80:81]\n"
    "v_pk_mul_f32 v[50:51], v[50:51], s[80:81]\n"
    "v_mfma_f32_16x16x32_bf16 v[72:75], a[40:43], a[96:99], v[72:75]\n"
    "v_pk_mul_f32 v[52:53], v[52:53], s[80:81]\n"
    "v_pk_mul_f32 v[54:55], v[54:55], s[80:81]\n"
    "v_pk_mul_f32 v[56:57], v[56:57], s[80:81]\n"
    "v_pk_mul_f32 v[58:59], v[58:59], s[80:81]\n"
    "v_pk_mul_f32 v[60:61], v[60:61], s[80:81]\n"
    "v_mfma_f32_16x16x32_bf16 v[76:79], a[44:47], a[96:99], v[76:79]\n"
    "v_pk_mul_f32 v[62:63], v[62:63], s[80:81]\n"
    "v_cvt_pk_bf16_f32 v32, v32, v33\n"
    "v_cvt_pk_bf16_f32 v33, v34, v35\n"
    "v_cvt_pk_bf16_f32 v34, v36, v37\n"
    "v_cvt_pk_bf16_f32 v35, v38, v39\n"
    "v_cvt_pk_bf16_f32 v36, v40, v41\n"
    "v_cvt_pk_bf16_f32 v37, v42, v43\n"
    "v_cvt_pk_bf16_f32 v38, v44, v45\n"
    "v_cvt_pk_bf16_f32 v39, v46, v47\n"
    "v_mfma_f32_16x16x32_bf16 v[80:83], a[48:51], a[96:99], v[80:83]\n"
    "v_cvt_pk_bf16_f32 v40, v48, v49\n"
    "v_cvt_pk_bf16_f32 v41, v50, v51\n"
    "v_cvt_pk_bf16_f32 v42, v52, v53\n"
    "v_cvt_pk_bf16_f32 v43, v54, v55\n"
    "v_cvt_pk_bf16_f32 v44, v56, v57\n"
    "v_cvt_pk_bf16_f32 v45, v58, v59\n"
    "v_cvt_pk_bf16_f32 v46, v60, v61\n"
    "v_cvt_pk_bf16_f32 v47, v62, v63\n"
    "v_permlane32_swap_b32 v32, v34\n"
    "v_mfma_f32_16x16x32_bf16 v[84:87], a[52:55], a[96:99], v[84:87]\n"
    "v_permlane32_swap_b32 v33, v35\n"
    "v_permlane32_swap_b32 v36, v38\n"
    "v_permlane32_swap_b32 v37, v39\n"
    "v_permlane32_swap_b32 v40, v42\n"
    "v_permlane32_swap_b32 v41, v43\n"
    "v_mfma_f32_16x16x32_bf16 v[88:91], a[56:59], a[96:99], v[88:91]\n"
    "v_permlane32_swap_b32 v44, v46\n"
    "v_permlane32_swap_b32 v45, v47\n"
    "v_permlane16_swap_b32 v32, v34\n"
    "v_permlane16_swap_b32 v33, v35\n"
    "v_permlane16_swap_b32 v36, v38\n"
    "v_mfma_f32_16x16x32_bf16 v[92:95], a[60:63], a[96:99], v[92:95]\n"
    "v_permlane16_swap_b32 v37, v39\n"
    "v_permlane16_swap_b32 v40, v42\n"
    "v_permlane16_swap_b32 v41, v43\n"
    "v_permlane16_swap_b32 v44, v46\n"
    "v_permlane16_swap_b32 v45, v47\n"
    "v_mfma_f32_16x16x32_bf16 v[96:99], a[32:35], a[100:103], v[96:99]\n"
    "v_pk_mul_f32 v[64:65], v[64:65], s[80:81]\n"
    "v_pk_mul_f32 v[66:67], v[66:67], s[80:81]\n"
    "v_pk_mul_f32 v[68:69], v[68:69], s[80:81]\n"
    "v_pk_mul_f32 v[70:71], v[70:71], s[80:81]\n"
    "v_pk_mul_f32 v[72:73], v[72:73], s[80:81]\n"
    "v_mfma_f32_16x16x32_bf16 v[100:103], a[36:39], a[100:103], v[100:103]\n"
    "v_pk_mul_f32 v[74:75], v[74:75], s[80:81]\n"
    "v_pk_mul_f32 v[76:77], v[76:77], s[80:81]\n"
    "v_pk_mul_f32 v[78:79], v[78:79], s[80:81]\n"
    "v_pk_mul_f32 v[80:81], v[80:81], s[80:81]\n"
    "v_pk_mul_f32 v[82:83], v[82:83], s[80:81]\n"
    "v_mfma_f32_16x16x32_bf16 v[104:107], a[40:43], a[100:103], v[104:107]\n"
    "v_pk_mul_f32 v[84:85], v[84:85], s[80:81]\n"
    "v_pk_mul_f32 v[86:87], v[86:87], s[80:81]\n"
    "v_pk_mul_f32 v[88:89], v[88:89], s[80:81]\n"
    "v_pk_mul_f32 v[90:91], v[90:91], s[80:81]\n"
    "v_pk_mul_f32 v[92:93], v[92:93], s[80:81]\n"
    "v_mfma_f32_16x16x32_bf16 v[108:111], a[44:47], a[100:103], v[108:111]\n"
    "v_pk_mul_f32 v[94:95], v[94:95], s[80:81]\n"
    "v_cvt_pk_bf16_f32 v48, v64, v65\n"
    "v_cvt_pk_bf16_f32 v49, v66, v67\n"
    "v_cvt_pk_bf16_f32 v50, v68, v69\n"
    "v_cvt_pk_bf16_f32 v51, v70, v71\n"
    "v_cvt_pk_bf16_f32 v52, v72, v73\n"
    "v_cvt_pk_bf16_f32 v53, v74, v75\n"
    "v_cvt_pk_bf16_f32 v54, v76, v77\n"
    "v_cvt_pk_bf16_f32 v55, v78, v79\n"
    "v_mfma_f32_16x16x32_bf16 v[112:115], a[48:51], a[100:103], v[112:115]\n"
    "v_cvt_pk_bf16_f32 v56, v80, v81\n"
    "v_cvt_pk_bf16_f32 v57, v82, v83\n"
    "v_cvt_pk_bf16_f32 v58, v84, v85\n"
    "v_cvt_pk_bf16_f32 v59, v86, v87\n"
    "v_cvt_pk_bf16_f32 v60, v88, v89\n"
    "v_cvt_pk_bf16_f32 v61, v90, v91\n"
    "v_cvt_pk_bf16_f32 v62, v92, v93\n"
    "v_cvt_pk_bf16_f32 v63, v94, v95\n"
    "v_permlane32_swap_b32 v48, v50\n"
    "v_mfma_f32_16x16x32_bf16 v[116:119], a[52:55], a[100:103], v[116:119]\n"
    "v_permlane32_swap_b32 v49, v51\n"
    "v_permlane32_swap_b32 v52, v54\n"
    "v_permlane32_swap_b32 v53, v55\n"
    "v_permlane32_swap_b32 v56, v58\n"
    "v_permlane32_swap_b32 v57, v59\n"
    "v_mfma_f32_16x16x32_bf16 v[120:123], a[56:59], a[100:103], v[120:123]\n"
    "v_permlane32_swap_b32 v60, v62\n"
    "v_permlane32_swap_b32 v61, v63\n"
    "v_permlane16_swap_b32 v48, v50\n"
    "v_permlane16_swap_b32 v49, v51\n"
    "v_permlane16_swap_b32 v52, v54\n"
    "v_mfma_f32_16x16x32_bf16 v[124:127], a[60:63], a[100:103], v[124:127]\n"
    "v_permlane16_swap_b32 v53, v55\n"
    "v_permlane16_swap_b32 v56, v58\n"
    "v_permlane16_swap_b32 v57, v59\n"
    "v_permlane16_swap_b32 v60, v62\n"
    "v_permlane16_swap_b32 v61, v63\n"
    "v_pk_mul_f32 v[96:97], v[96:97], s[80:81]\n"
    "v_pk_mul_f32 v[98:99], v[98:99], s[80:81]\n"
    "v_pk_mul_f32 v[100:101], v[100:101], s[80:81]\n"
    "v_pk_mul_f32 v[102:103], v[102:103], s[80:81]\n"
    "v_pk_mul_f32 v[104:105], v[104:105], s[80:81]\n"
    "v_pk_mul_f32 v[106:107], v[106:107], s[80:81]\n"
    "v_pk_mul_f32 v[108:109], v[108:109], s[80:81]\n"
    "v_pk_mul_f32 v[110:111], v[110:111], s[80:81]\n"
    "v_pk_mul_f32 v[112:113], v[112:113], s[80:81]\n"
    "v_pk_mul_f32 v[114:115], v[114:115], s[80:81]\n"
    "v_pk_mul_f32 v[116:117], v[116:117], s[80:81]\n"
    "v_pk_mul_f32 v[118:119], v[118:119], s[80:81]\n"
    "v_pk_mul_f32 v[120:121], v[120:121], s[80:81]\n"
    "v_pk_mul_f32 v[122:123], v[122:123], s[80:81]\n"
    "v_pk_mul_f32 v[124:125], v[124:125], s[80:81]\n"
    "v_pk_mul_f32 v[126:127], v[126:127], s[80:81]\n"
    "v_cvt_pk_bf16_f32 v64, v96, v97\n"
    "v_cvt_pk_bf16_f32 v65, v98, v99\n"
    "v_cvt_pk_bf16_f32 v66, v100, v101\n"
    "v_cvt_pk_bf16_f32 v67, v102, v103\n"
    "v_cvt_pk_bf16_f32 v68, v104, v105\n"
    "v_cvt_pk_bf16_f32 v69, v106, v107\n"
    "v_cvt_pk_bf16_f32 v70, v108, v109\n"
    "v_cvt_pk_bf16_f32 v71, v110, v111\n"
    "v_cvt_pk_bf16_f32 v72, v112, v113\n"
    "v_cvt_pk_bf16_f32 v73, v114, v115\n"
    "v_cvt_pk_bf16_f32 v74, v116, v117\n"
    "v_cvt_pk_bf16_f32 v75, v118, v119\n"
    "v_cvt_pk_bf16_f32 v76, v120, v121\n"
    "v_cvt_pk_bf16_f32 v77, v122, v123\n"
    "v_cvt_pk_bf16_f32 v78, v124, v125\n"
    "v_cvt_pk_bf16_f32 v79, v126, v127\n"
    "v_permlane32_swap_b32 v64, v66\n"
    "v_permlane32_swap_b32 v65, v67\n"
    "v_permlane32_swap_b32 v68, v70\n"
    "v_permlane32_swap_b32 v69, v71\n"
    "v_permlane32_swap_b32 v72, v74\n"
    "v_permlane32_swap_b32 v73, v75\n"
    "v_permlane32_swap_b32 v76, v78\n"
    "v_permlane32_swap_b32 v77, v79\n"
    "v_permlane16_swap_b32 v64, v66\n"
    "v_permlane16_swap_b32 v65, v67\n"
    "v_permlane16_swap_b32 v68, v70\n"
    "v_permlane16_swap_b32 v69, v71\n"
    "v_permlane16_swap_b32 v72, v74\n"
    "v_permlane16_swap_b32 v73, v75\n"
    "v_permlane16_swap_b32 v76, v78\n"
    "v_permlane16_swap_b32 v77, v79\n"
    "s_sub_u32 s74, %[wid], 2\n"
    "s_mul_i32 s65, s74, 16384\n"
    "s_add_u32 s65, s65, %[lds]\n"
    "s_add_u32 s65, s65, 131072\n"
    "v_add_u32 v6, s65, v24\n"
    "ds_write_b128 v6, v[32:35] offset:0\n"
    "ds_write_b128 v6, v[36:39] offset:1024\n"
    "ds_write_b128 v6, v[40:43] offset:2048\n"
    "ds_write_b128 v6, v[44:47] offset:3072\n"
    "ds_write_b128 v6, v[48:51] offset:4096\n"
    "ds_write_b128 v6, v[52:55] offset:5120\n"
    "ds_write_b128 v6, v[56:59] offset:6144\n"
    "ds_write_b128 v6, v[60:63] offset:7168\n"
    "ds_write_b128 v6, v[64:67] offset:8192\n"
    "ds_write_b128 v6, v[68:71] offset:9216\n"
    "ds_write_b128 v6, v[72:75] offset:10240\n"
    "ds_write_b128 v6, v[76:79] offset:11264\n"
    "s_waitcnt lgkmcnt(0)\n"
    "s_barrier\n"
    "s_add_u32 m0, s64, 16384\n"
    "s_nop 0\n"
    "buffer_load_dwordx4 v26, s[60:63], s68 offen offset:0 lds\n"
    "s_add_u32 s74, s68, 31744\n"
    "buffer_load_dwordx4 v26, s[60:63], s74 offen offset:1024 lds\n"
    "s_add_u32 s74, s68, 63488\n"
    "buffer_load_dwordx4 v27, s[60:63], s74 offen offset:2048 lds\n"
    "s_add_u32 s74, s68, 95232\n"
    "buffer_load_dwordx4 v27, s[60:63], s74 offen offset:3072 lds\n"
    "s_add_u32 m0, s64, 20480\n"
    "s_add_u32 s74, s68, 131072\n"
    "buffer_load_dwordx4 v26, s[60:63], s74 offen offset:0 lds\n"
    "s_add_u32 s74, s68, 162816\n"
    "buffer_load_dwordx4 v26, s[60:63], s74 offen offset:1024 lds\n"
    "s_add_u32 s74, s68, 194560\n"
    "buffer_load_dwordx4 v27, s[60:63], s74 offen offset:2048 lds\n"
    "s_add_u32 s74, s68, 226304\n"
    "buffer_load_dwordx4 v27, s[60:63], s74 offen offset:3072 lds\n"
    "s_add_u32 s68, s68, 262144\n"
    "v_lshrrev_b32 v29, 4, v25\n"
    "v_lshlrev_b32 v29, 1, v29\n"
    "v_mov_b32 v30, 0x78\n"
    "v_lshrrev_b32 v29, v29, v30\n"
    "v_xor_b32 v29, v29, v25\n"
    "v_and_b32 v29, 3, v29\n"
    "v_lshlrev_b32 v29, 4, v29\n"
    "v_lshrrev_b32 v31, 2, v25\n"
    "v_lshl_add_u32 v28, v31, 10, v29\n"
    "v_lshrrev_b32 v29, 2, v25\n"
    "v_and_b32 v29, 3, v29\n"
    "v_lshlrev_b32 v29, 1, v29\n"
    "v_lshrrev_b32 v29, v29, v30\n"
    "v_lshrrev_b32 v31, 4, v25\n"
    "v_xor_b32 v29, v29, v31\n"
    "v_and_b32 v29, 3, v29\n"
    "v_lshlrev_b32 v29, 4, v29\n"
    "v_and_b32 v31, 15, v25\n"
    "v_lshl_add_u32 v29, v31, 6, v29\n"
    "v_add_u32 v6, s65, v29\n"
    "v_add_u32 v7, s64, v29\n"
    "s_mov_b32 s69, 0\n"
    "v_mov_b64 v[32:33], 0\n"
    "v_mov_b64 v[34:35], 0\n"
    "v_mov_b64 v[36:37], 0\n"
    "v_mov_b64 v[38:39], 0\n"
    "v_mov_b64 v[40:41], 0\n"
    "v_mov_b64 v[42:43], 0\n"
    "v_mov_b64 v[44:45], 0\n"
    "v_mov_b64 v[46:47], 0\n"
    "v_mov_b64 v[48:49], 0\n"
    "v_mov_b64 v[50:51], 0\n"
    "v_mov_b64 v[52:53], 0\n"
    "v_mov_b64 v[54:55], 0\n"
    "v_mov_b64 v[56:57], 0\n"
    "v_mov_b64 v[58:59], 0\n"
    "v_mov_b64 v[60:61], 0\n"
    "v_mov_b64 v[62:63], 0\n"
    "v_mov_b64 v[64:65], 0\n"
    "v_mov_b64 v[66:67], 0\n"
    "v_mov_b64 v[68:69], 0\n"
    "v_mov_b64 v[70:71], 0\n"
    "v_mov_b64 v[72:73], 0\n"
    "v_mov_b64 v[74:75], 0\n"
    "v_mov_b64 v[76:77], 0\n"
    "v_mov_b64 v[78:79], 0\n"
    "v_mov_b64 v[80:81], 0\n"
    "v_mov_b64 v[82:83], 0\n"
    "v_mov_b64 v[84:85], 0\n"
    "v_mov_b64 v[86:87], 0\n"
    "v_mov_b64 v[88:89], 0\n"
    "v_mov_b64 v[90:91], 0\n"
    "v_mov_b64 v[92:93], 0\n"
    "v_mov_b64 v[94:95], 0\n"
    "v_mov_b64 v[96:97], 0\n"
    "v_mov_b64 v[98:99], 0\n"
    "v_mov_b64 v[100:101], 0\n"
    "v_mov_b64 v[102:103], 0\n"
    "v_mov_b64 v[104:105], 0\n"
    "v_mov_b64 v[106:107], 0\n"
    "v_mov_b64 v[108:109], 0\n"
    "v_mov_b64 v[110:111], 0\n"
    "v_mov_b64 v[112:113], 0\n"
    "v_mov_b64 v[114:115], 0\n"
    "v_mov_b64 v[116:117], 0\n"
    "v_mov_b64 v[118:119], 0\n"
    "v_mov_b64 v[120:121], 0\n"
    "v_mov_b64 v[122:123], 0\n"
    "v_mov_b64 v[124:125], 0\n"
    "v_mov_b64 v[126:127], 0\n"
    "v_mov_b64 v[128:129], 0\n"
    "v_mov_b64 v[130:131], 0\n"
    "v_mov_b64 v[132:133], 0\n"
    "v_mov_b64 v[134:135], 0\n"
    "v_mov_b64 v[136:137], 0\n"
    "v_mov_b64 v[138:139], 0\n"
    "v_mov_b64 v[140:141], 0\n"
    "v_mov_b64 v[142:143], 0\n"
    "v_mov_b64 v[144:145], 0\n"
    "v_mov_b64 v[146:147], 0\n"
    "v_mov_b64 v[148:149], 0\n"
    "v_mov_b64 v[150:151], 0\n"
    "v_mov_b64 v[152:153], 0\n"
    "v_mov_b64 v[154:155], 0\n"
    "v_mov_b64 v[156:157], 0\n"
    "v_mov_b64 v[158:159], 0\n"
    "v_mov_b64 v[160:161], 0\n"
    "v_mov_b64 v[162:163], 0\n"
    "v_mov_b64 v[164:165], 0\n"
    "v_mov_b64 v[166:167], 0\n"
    "v_mov_b64 v[168:169], 0\n"
    "v_mov_b64 v[170:171], 0\n"
    "v_mov_b64 v[172:173], 0\n"
    "v_mov_b64 v[174:175], 0\n"
    "v_mov_b64 v[176:177], 0\n"
    "v_mov_b64 v[178:179], 0\n"
    "v_mov_b64 v[180:181], 0\n"
    "v_mov_b64 v[182:183], 0\n"
    "v_mov_b64 v[184:185], 0\n"
    "v_mov_b64 v[186:187], 0\n"
    "v_mov_b64 v[188:189], 0\n"
    "v_mov_b64 v[190:191], 0\n"
    "v_mov_b64 v[192:193], 0\n"
    "v_mov_b64 v[194:195], 0\n"
    "v_mov_b64 v[196:197], 0\n"
    "v_mov_b64 v[198:199], 0\n"
    "v_mov_b64 v[200:201], 0\n"
    "v_mov_b64 v[202:203], 0\n"
    "v_mov_b64 v[204:205], 0\n"
    "v_mov_b64 v[206:207], 0\n"
    "v_mov_b64 v[208:209], 0\n"
    "v_mov_b64 v[210:211], 0\n"
    "v_mov_b64 v[212:213], 0\n"
    "v_mov_b64 v[214:215], 0\n"
    "v_mov_b64 v[216:217], 0\n"
    "v_mov_b64 v[218:219], 0\n"
    "v_mov_b64 v[220:221], 0\n"
    "v_mov_b64 v[222:223], 0\n"
    "v_mov_b64 v[224:225], 0\n"
    "v_mov_b64 v[226:227], 0\n"
    "v_mov_b64 v[228:229], 0\n"
    "v_mov_b64 v[230:231], 0\n"
    "v_mov_b64 v[232:233], 0\n"
    "v_mov_b64 v[234:235], 0\n"
    "v_mov_b64 v[236:237], 0\n"
    "v_mov_b64 v[238:239], 0\n"
    "v_mov_b64 v[240:241], 0\n"
    "v_mov_b64 v[242:243], 0\n"
    "v_mov_b64 v[244:245], 0\n"
    "v_mov_b64 v[246:247], 0\n"
    "v_mov_b64 v[248:249], 0\n"
    "v_mov_b64 v[250:251], 0\n"
    "v_mov_b64 v[252:253], 0\n"
    "v_mov_b64 v[254:255], 0\n"
    "s_barrier\n"
    "s_add_u32 m0, s65, 0\n"
    "s_nop 0\n"
    "buffer_load_dwordx4 v28, s[52:55], s69 offen offset:0 lds\n"
    "s_add_u32 s76, s69, 15360\n"
    "buffer_load_dwordx4 v28, s[52:55], s76 offen offset:1024 lds\n"
    "s_add_u32 s76, s69, 30720\n"
    "buffer_load_dwordx4 v28, s[52:55], s76 offen offset:2048 lds\n"
    "s_add_u32 s76, s69, 46080\n"
    "buffer_load_dwordx4 v28, s[52:55], s76 offen offset:3072 lds\n"
    "s_add_u32 m0, s65, 4096\n"
    "s_add_u32 s76, s69, 65536\n"
    "buffer_load_dwordx4 v28, s[52:55], s76 offen offset:0 lds\n"
    "s_add_u32 s76, s69, 80896\n"
    "buffer_load_dwordx4 v28, s[52:55], s76 offen offset:1024 lds\n"
    "s_add_u32 s76, s69, 96256\n"
    "buffer_load_dwordx4 v28, s[52:55], s76 offen offset:2048 lds\n"
    "s_add_u32 s69, s69, 64\n"
    "s_add_u32 m0, s65, 8192\n"
    "s_nop 0\n"
    "buffer_load_dwordx4 v28, s[52:55], s69 offen offset:0 lds\n"
    "s_add_u32 s76, s69, 15360\n"
    "buffer_load_dwordx4 v28, s[52:55], s76 offen offset:1024 lds\n"
    "s_add_u32 s76, s69, 30720\n"
    "buffer_load_dwordx4 v28, s[52:55], s76 offen offset:2048 lds\n"
    "s_add_u32 s76, s69, 46080\n"
    "buffer_load_dwordx4 v28, s[52:55], s76 offen offset:3072 lds\n"
    "s_add_u32 m0, s65, 12288\n"
    "s_add_u32 s76, s69, 65536\n"
    "buffer_load_dwordx4 v28, s[52:55], s76 offen offset:0 lds\n"
    "s_add_u32 s76, s69, 80896\n"
    "buffer_load_dwordx4 v28, s[52:55], s76 offen offset:1024 lds\n"
    "s_add_u32 s76, s69, 96256\n"
    "buffer_load_dwordx4 v28, s[52:55], s76 offen offset:2048 lds\n"
    "s_add_u32 s69, s69, 64\n"
    "s_add_u32 m0, s64, 0\n"
    "s_nop 0\n"
    "buffer_load_dwordx4 v28, s[52:55], s69 offen offset:0 lds\n"
    "s_add_u32 s76, s69, 15360\n"
    "buffer_load_dwordx4 v28, s[52:55], s76 offen offset:1024 lds\n"
    "s_add_u32 s76, s69, 30720\n"
    "buffer_load_dwordx4 v28, s[52:55], s76 offen offset:2048 lds\n"
    "s_add_u32 s76, s69, 46080\n"
    "buffer_load_dwordx4 v28, s[52:55], s76 offen offset:3072 lds\n"
    "s_add_u32 m0, s64, 4096\n"
    "s_add_u32 s76, s69, 65536\n"
    "buffer_load_dwordx4 v28, s[52:55], s76 offen offset:0 lds\n"
    "s_add_u32 s76, s69, 80896\n"
    "buffer_load_dwordx4 v28, s[52:55], s76 offen offset:1024 lds\n"
    "s_add_u32 s76, s69, 96256\n"
    "buffer_load_dwordx4 v28, s[52:55], s76 offen offset:2048 lds\n"
    "s_add_u32 s69, s69, 64\n"
    "s_waitcnt vmcnt(14)\n"
    "ds_read_b64_tr_b16 a[0:1], v8 offset:24576\n"
    "ds_read_b64_tr_b16 a[2:3], v8 offset:25600\n"
    "ds_read_b64_tr_b16 a[4:5], v9 offset:24576\n"
    "ds_read_b64_tr_b16 a[6:7], v9 offset:25600\n"
    "ds_read_b64_tr_b16 a[8:9], v10 offset:24576\n"
    "ds_read_b64_tr_b16 a[10:11], v10 offset:25600\n"
    "ds_read_b64_tr_b16 a[12:13], v11 offset:24576\n"
    "ds_read_b64_tr_b16 a[14:15], v11 offset:25600\n"
    "ds_read_b64_tr_b16 a[16:17], v12 offset:24576\n"
    "ds_read_b64_tr_b16 a[18:19], v12 offset:25600\n"
    "ds_read_b64_tr_b16 a[20:21], v13 offset:24576\n"
    "ds_read_b64_tr_b16 a[22:23], v13 offset:25600\n"
    "ds_read_b64_tr_b16 a[24:25], v14 offset:24576\n"
    "ds_read_b64_tr_b16 a[26:27], v14 offset:25600\n"
    "ds_read_b64_tr_b16 a[28:29], v15 offset:24576\n"
    "ds_read_b64_tr_b16 a[30:31], v15 offset:25600\n"
    "ds_read_b128 a[64:67], v6 offset:0\n"
    "ds_read_b128 a[68:71], v6 offset:1024\n"
    "ds_read_b128 a[72:75], v6 offset:2048\n"
    "ds_read_b128 a[76:79], v6 offset:3072\n"
    "ds_read_b128 a[80:83], v6 offset:4096\n"
    "ds_read_b128 a[84:87], v6 offset:5120\n"
    "ds_read_b128 a[88:91], v6 offset:6144\n"
    "s_waitcnt lgkmcnt(0)\n"
    "s_waitcnt vmcnt(7)\n"
    "v_mfma_f32_16x16x32_bf16 v[32:35], a[0:3], a[64:67], v[32:35]\n"
    "s_add_u32 m0, s65, 0\n"
    "v_mfma_f32_16x16x32_bf16 v[64:67], a[0:3], a[68:71], v[64:67]\n"
    "ds_read_b64_tr_b16 a[32:33], v8 offset:8192\n"
    "v_mfma_f32_16x16x32_bf16 v[96:99], a[0:3], a[72:75], v[96:99]\n"
    "buffer_load_dwordx4 v28, s[52:55], s69 offen offset:0 lds\n"
    "ds_read_b64_tr_b16 a[34:35], v8 offset:9216\n"
    "v_mfma_f32_16x16x32_bf16 v[128:131], a[0:3], a[76:79], v[128:131]\n"
    "s_add_u32 s76, s69, 15360\n"
    "v_mfma_f32_16x16x32_bf16 v[160:163], a[0:3], a[80:83], v[160:163]\n"
    "ds_read_b64_tr_b16 a[36:37], v9 offset:8192\n"
    "v_mfma_f32_16x16x32_bf16 v[192:195], a[0:3], a[84:87], v[192:195]\n"
    "buffer_load_dwordx4 v28, s[52:55], s76 offen offset:1024 lds\n"
    "v_mfma_f32_16x16x32_bf16 v[224:227], a[0:3], a[88:91], v[224:227]\n"
    "s_add_u32 s76, s69, 30720\n"
    "v_mfma_f32_16x16x32_bf16 v[36:39], a[4:7], a[64:67], v[36:39]\n"
    "ds_read_b64_tr_b16 a[38:39], v9 offset:9216\n"
    "buffer_load_dwordx4 v28, s[52:55], s76 offen offset:2048 lds\n"
    "v_mfma_f32_16x16x32_bf16 v[68:71], a[4:7], a[68:71], v[68:71]\n"
    "ds_read_b64_tr_b16 a[40:41], v10 offset:8192\n"
    "v_mfma_f32_16x16x32_bf16 v[100:103], a[4:7], a[72:75], v[100:103]\n"
    "s_add_u32 s76, s69, 46080\n"
    "v_mfma_f32_16x16x32_bf16 v[132:135], a[4:7], a[76:79], v[132:135]\n"
    "buffer_load_dwordx4 v28, s[52:55], s76 offen offset:3072 lds\n"
    "v_mfma_f32_16x16x32_bf16 v[164:167], a[4:7], a[80:83], v[164:167]\n"
    "ds_read_b64_tr_b16 a[42:43], v10 offset:9216\n"
    "v_mfma_f32_16x16x32_bf16 v[196:199], a[4:7], a[84:87], v[196:199]\n"
    "s_add_u32 m0, s65, 4096\n"
    "ds_read_b64_tr_b16 a[44:45], v11 offset:8192\n"
    "v_mfma_f32_16x16x32_bf16 v[228:231], a[4:7], a[88:91], v[228:231]\n"
    "s_add_u32 s76, s69, 65536\n"
    "v_mfma_f32_16x16x32_bf16 v[40:43], a[8:11], a[64:67], v[40:43]\n"
    "buffer_load_dwordx4 v28, s[52:55], s76 offen offset:0 lds\n"
    "v_mfma_f32_16x16x32_bf16 v[72:75], a[8:11], a[68:71], v[72:75]\n"
    "ds_read_b64_tr_b16 a[46:47], v11 offset:9216\n"
    "v_mfma_f32_16x16x32_bf16 v[104:107], a[8:11], a[72:75], v[104:107]\n"
    "s_add_u32 s76, s69, 80896\n"
    "v_mfma_f32_16x16x32_bf16 v[136:139], a[8:11], a[76:79], v[136:139]\n"
    "ds_read_b64_tr_b16 a[48:49], v12 offset:8192\n"
    "buffer_load_dwordx4 v28, s[52:55], s76 offen offset:1024 lds\n"
    "v_mfma_f32_16x16x32_bf16 v[168:171], a[8:11], a[80:83], v[168:171]\n"
    "s_add_u32 s76, s69, 96256\n"
    "v_mfma_f32_16x16x32_bf16 v[200:203], a[8:11], a[84:87], v[200:203]\n"
    "ds_read_b64_tr_b16 a[50:51], v12 offset:9216\n"
    "v_mfma_f32_16x16x32_bf16 v[232:235], a[8:11], a[88:91], v[232:235]\n"
    "buffer_load_dwordx4 v28, s[52:55], s76 offen offset:2048 lds\n"
    "v_mfma_f32_16x16x32_bf16 v[44:47], a[12:15], a[64:67], v[44:47]\n"
    "ds_read_b64_tr_b16 a[52:53], v13 offset:8192\n"
    "v_mfma_f32_16x16x32_bf16 v[76:79], a[12:15], a[68:71], v[76:79]\n"
    "s_add_u32 s69, s69, 64\n"
    "s_add_u32 m0, s64, 24576\n"
    "v_mfma_f32_16x16x32_bf16 v[108:111], a[12:15], a[72:75], v[108:111]\n"
    "ds_read_b64_tr_b16 a[54:55], v13 offset:9216\n"
    "v_mfma_f32_16x16x32_bf16 v[140:143], a[12:15], a[76:79], v[140:143]\n"
    "buffer_load_dwordx4 v26, s[60:63], s68 offen offset:0 lds\n"
    "v_mfma_f32_16x16x32_bf16 v[172:175], a[12:15], a[80:83], v[172:175]\n"
    "ds_read_b64_tr_b16 a[56:57], v14 offset:8192\n"
    "v_mfma_f32_16x16x32_bf16 v[204:207], a[12:15], a[84:87], v[204:207]\n"
    "s_add_u32 s74, s68, 31744\n"
    "v_mfma_f32_16x16x32_bf16 v[236:239], a[12:15], a[88:91], v[236:239]\n"
    "buffer_load_dwordx4 v26, s[60:63], s74 offen offset:1024 lds\n"
    "ds_read_b64_tr_b16 a[58:59], v14 offset:9216\n"
    "v_mfma_f32_16x16x32_bf16 v[48:51], a[16:19], a[64:67], v[48:51]\n"
    "s_add_u32 s74, s68, 63488\n"
    "v_mfma_f32_16x16x32_bf16 v[80:83], a[16:19], a[68:71], v[80:83]\n"
    "ds_read_b64_tr_b16 a[60:61], v15 offset:8192\n"
    "v_mfma_f32_16x16x32_bf16 v[112:115], a[16:19], a[72:75], v[112:115]\n"
    "buffer_load_dwordx4 v27, s[60:63], s74 offen offset:2048 lds\n"
    "v_mfma_f32_16x16x32_bf16 v[144:147], a[16:19], a[76:79], v[144:147]\n"
    "s_add_u32 s74, s68, 95232\n"
    "v_mfma_f32_16x16x32_bf16 v[176:179], a[16:19], a[80:83], v[176:179]\n"
    "ds_read_b64_tr_b16 a[62:63], v15 offset:9216\n"
    "buffer_load_dwordx4 v27, s[60:63], s74 offen offset:3072 lds\n"
    "v_mfma_f32_16x16x32_bf16 v[208:211], a[16:19], a[84:87], v[208:211]\n"
    "ds_read_b128 a[92:95], v6 offset:8192\n"
    "v_mfma_f32_16x16x32_bf16 v[240:243], a[16:19], a[88:91], v[240:243]\n"
    "s_add_u32 m0, s64, 28672\n"
    "v_mfma_f32_16x16x32_bf16 v[52:55], a[20:23], a[64:67], v[52:55]\n"
    "s_add_u32 s74, s68, 131072\n"
    "v_mfma_f32_16x16x32_bf16 v[84:87], a[20:23], a[68:71], v[84:87]\n"
    "ds_read_b128 a[96:99], v6 offset:9216\n"
    "v_mfma_f32_16x16x32_bf16 v[116:119], a[20:23], a[72:75], v[116:119]\n"
    "buffer_load_dwordx4 v26, s[60:63], s74 offen offset:0 lds\n"
    "ds_read_b128 a[100:103], v6 offset:10240\n"
    "v_mfma_f32_16x16x32_bf16 v[148:151], a[20:23], a[76:79], v[148:151]\n"
    "s_add_u32 s74, s68, 162816\n"
    "v_mfma_f32_16x16x32_bf16 v[180:183], a[20:23], a[80:83], v[180:183]\n"
    "buffer_load_dwordx4 v26, s[60:63], s74 offen offset:1024 lds\n"
    "v_mfma_f32_16x16x32_bf16 v[212:215], a[20:23], a[84:87], v[212:215]\n"
    "ds_read_b128 a[104:107], v6 offset:11264\n"
    "v_mfma_f32_16x16x32_bf16 v[244:247], a[20:23], a[88:91], v[244:247]\n"
    "s_add_u32 s74, s68, 194560\n"
    "v_mfma_f32_16x16x32_bf16 v[56:59], a[24:27], a[64:67], v[56:59]\n"
    "ds_read_b128 a[108:111], v6 offset:12288\n"
    "v_mfma_f32_16x16x32_bf16 v[88:91], a[24:27], a[68:71], v[88:91]\n"
    "buffer_load_dwordx4 v27, s[60:63], s74 offen offset:2048 lds\n"
    "s_add_u32 s74, s68, 226304\n"
    "v_mfma_f32_16x16x32_bf16 v[120:123], a[24:27], a[72:75], v[120:123]\n"
    "ds_read_b128 a[112:115], v6 offset:13312\n"
    "v_mfma_f32_16x16x32_bf16 v[152:155], a[24:27], a[76:79], v[152:155]\n"
    "buffer_load_dwordx4 v27, s[60:63], s74 offen offset:3072 lds\n"
    "v_mfma_f32_16x16x32_bf16 v[184:187], a[24:27], a[80:83], v[184:187]\n"
    "ds_read_b128 a[116:119], v6 offset:14336\n"
    "v_mfma_f32_16x16x32_bf16 v[216:219], a[24:27], a[84:87], v[216:219]\n"
    "s_add_u32 s68, s68, 262144\n"
    "v_mfma_f32_16x16x32_bf16 v[248:251], a[24:27], a[88:91], v[248:251]\n"
    "v_mfma_f32_16x16x32_bf16 v[60:63], a[28:31], a[64:67], v[60:63]\n"
    "v_mfma_f32_16x16x32_bf16 v[92:95], a[28:31], a[68:71], v[92:95]\n"
    "v_mfma_f32_16x16x32_bf16 v[124:127], a[28:31], a[72:75], v[124:127]\n"
    "v_mfma_f32_16x16x32_bf16 v[156:159], a[28:31], a[76:79], v[156:159]\n"
    "v_mfma_f32_16x16x32_bf16 v[188:191], a[28:31], a[80:83], v[188:191]\n"
    "v_mfma_f32_16x16x32_bf16 v[220:223], a[28:31], a[84:87], v[220:223]\n"
    "v_mfma_f32_16x16x32_bf16 v[252:255], a[28:31], a[88:91], v[252:255]\n"
    "s_waitcnt lgkmcnt(0)\n"
    "s_sleep 20\n"
    "s_waitcnt vmcnt(15)\n"
    "v_mfma_f32_16x16x32_bf16 v[32:35], a[32:35], a[92:95], v[32:35]\n"
    "s_add_u32 m0, s65, 8192\n"
    "v_mfma_f32_16x16x32_bf16 v[64:67], a[32:35], a[96:99], v[64:67]\n"
    "ds_read_b64_tr_b16 a[0:1], v8 offset:16384\n"
    "v_mfma_f32_16x16x32_bf16 v[96:99], a[32:35], a[100:103], v[96:99]\n"
    "buffer_load_dwordx4 v28, s[52:55], s69 offen offset:0 lds\n"
    "ds_read_b64_tr_b16 a[2:3], v8 offset:17408\n"
    "v_mfma_f32_16x16x32_bf16 v[128:131], a[32:35], a[104:107], v[128:131]\n"
    "s_add_u32 s76, s69, 15360\n"
    "v_mfma_f32_16x16x32_bf16 v[160:163], a[32:35], a[108:111], v[160:163]\n"
    "ds_read_b64_tr_b16 a[4:5], v9 offset:16384\n"
    "v_mfma_f32_16x16x32_bf16 v[192:195], a[32:35], a[112:115], v[192:195]\n"
    "buffer_load_dwordx4 v28, s[52:55], s76 offen offset:1024 lds\n"
    "v_mfma_f32_16x16x32_bf16 v[224:227], a[32:35], a[116:119], v[224:227]\n"
    "s_add_u32 s76, s69, 30720\n"
    "v_mfma_f32_16x16x32_bf16 v[36:39], a[36:39], a[92:95], v[36:39]\n"
    "ds_read_b64_tr_b16 a[6:7], v9 offset:17408\n"
    "buffer_load_dwordx4 v28, s[52:55], s76 offen offset:2048 lds\n"
    "v_mfma_f32_16x16x32_bf16 v[68:71], a[36:39], a[96:99], v[68:71]\n"
    "ds_read_b64_tr_b16 a[8:9], v10 offset:16384\n"
    "v_mfma_f32_16x16x32_bf16 v[100:103], a[36:39], a[100:103], v[100:103]\n"
    "s_add_u32 s76, s69, 46080\n"
    "v_mfma_f32_16x16x32_bf16 v[132:135], a[36:39], a[104:107], v[132:135]\n"
    "buffer_load_dwordx4 v28, s[52:55], s76 offen offset:3072 lds\n"
    "v_mfma_f32_16x16x32_bf16 v[164:167], a[36:39], a[108:111], v[164:167]\n"
    "ds_read_b64_tr_b16 a[10:11], v10 offset:17408\n"
    "v_mfma_f32_16x16x32_bf16 v[196:199], a[36:39], a[112:115], v[196:199]\n"
    "s_add_u32 m0, s65, 12288\n"
    "ds_read_b64_tr_b16 a[12:13], v11 offset:16384\n"
    "v_mfma_f32_16x16x32_bf16 v[228:231], a[36:39], a[116:119], v[228:231]\n"
    "s_add_u32 s76, s69, 65536\n"
    "v_mfma_f32_16x16x32_bf16 v[40:43], a[40:43], a[92:95], v[40:43]\n"
    "buffer_load_dwordx4 v28, s[52:55], s76 offen offset:0 lds\n"
    "v_mfma_f32_16x16x32_bf16 v[72:75], a[40:43], a[96:99], v[72:75]\n"
    "ds_read_b64_tr_b16 a[14:15], v11 offset:17408\n"
    "v_mfma_f32_16x16x32_bf16 v[104:107], a[40:43], a[100:103], v[104:107]\n"
    "s_add_u32 s76, s69, 80896\n"
    "v_mfma_f32_16x16x32_bf16 v[136:139], a[40:43], a[104:107], v[136:139]\n"
    "ds_read_b64_tr_b16 a[16:17], v12 offset:16384\n"
    "buffer_load_dwordx4 v28, s[52:55], s76 offen offset:1024 lds\n"
    "v_mfma_f32_16x16x32_bf16 v[168:171], a[40:43], a[108:111], v[168:171]\n"
    "s_add_u32 s76, s69, 96256\n"
    "v_mfma_f32_16x16x32_bf16 v[200:203], a[40:43], a[112:115], v[200:203]\n"
    "ds_read_b64_tr_b16 a[18:19], v12 offset:17408\n"
    "v_mfma_f32_16x16x32_bf16 v[232:235], a[40:43], a[116:119], v[232:235]\n"
    "buffer_load_dwordx4 v28, s[52:55], s76 offen offset:2048 lds\n"
    "v_mfma_f32_16x16x32_bf16 v[44:47], a[44:47], a[92:95], v[44:47]\n"
    "ds_read_b64_tr_b16 a[20:21], v13 offset:16384\n"
    "v_mfma_f32_16x16x32_bf16 v[76:79], a[44:47], a[96:99], v[76:79]\n"
    "s_add_u32 s69, s69, 64\n"
    "s_add_u32 m0, s64, 8192\n"
    "v_mfma_f32_16x16x32_bf16 v[108:111], a[44:47], a[100:103], v[108:111]\n"
    "ds_read_b64_tr_b16 a[22:23], v13 offset:17408\n"
    "v_mfma_f32_16x16x32_bf16 v[140:143], a[44:47], a[104:107], v[140:143]\n"
    "buffer_load_dwordx4 v26, s[60:63], s68 offen offset:0 lds\n"
    "v_mfma_f32_16x16x32_bf16 v[172:175], a[44:47], a[108:111], v[172:175]\n"
    "ds_read_b64_tr_b16 a[24:25], v14 offset:16384\n"
    "v_mfma_f32_16x16x32_bf16 v[204:207], a[44:47], a[112:115], v[204:207]\n"
    "s_add_u32 s74, s68, 31744\n"
    "v_mfma_f32_16x16x32_bf16 v[236:239], a[44:47], a[116:119], v[236:239]\n"
    "buffer_load_dwordx4 v26, s[60:63], s74 offen offset:1024 lds\n"
    "ds_read_b64_tr_b16 a[26:27], v14 offset:17408\n"
    "v_mfma_f32_16x16x32_bf16 v[48:51], a[48:51], a[92:95], v[48:51]\n"
    "s_add_u32 s74, s68, 63488\n"
    "v_mfma_f32_16x16x32_bf16 v[80:83], a[48:51], a[96:99], v[80:83]\n"
    "ds_read_b64_tr_b16 a[28:29], v15 offset:16384\n"
    "v_mfma_f32_16x16x32_bf16 v[112:115], a[48:51], a[100:103], v[112:115]\n"
    "buffer_load_dwordx4 v27, s[60:63], s74 offen offset:2048 lds\n"
    "v_mfma_f32_16x16x32_bf16 v[144:147], a[48:51], a[104:107], v[144:147]\n"
    "s_add_u32 s74, s68, 95232\n"
    "v_mfma_f32_16x16x32_bf16 v[176:179], a[48:51], a[108:111], v[176:179]\n"
    "ds_read_b64_tr_b16 a[30:31], v15 offset:17408\n"
    "buffer_load_dwordx4 v27, s[60:63], s74 offen offset:3072 lds\n"
    "v_mfma_f32_16x16x32_bf16 v[208:211], a[48:51], a[112:115], v[208:211]\n"
    "ds_read_b128 a[64:67], v7 offset:0\n"
    "v_mfma_f32_16x16x32_bf16 v[240:243], a[48:51], a[116:119], v[240:243]\n"
    "s_add_u32 m0, s64, 12288\n"
    "v_mfma_f32_16x16x32_bf16 v[52:55], a[52:55], a[92:95], v[52:55]\n"
    "s_add_u32 s74, s68, 131072\n"
    "v_mfma_f32_16x16x32_bf16 v[84:87], a[52:55], a[96:99], v[84:87]\n"
    "ds_read_b128 a[68:71], v7 offset:1024\n"
    "v_mfma_f32_16x16x32_bf16 v[116:119], a[52:55], a[100:103], v[116:119]\n"
    "buffer_load_dwordx4 v26, s[60:63], s74 offen offset:0 lds\n"
    "ds_read_b128 a[72:75], v7 offset:2048\n"
    "v_mfma_f32_16x16x32_bf16 v[148:151], a[52:55], a[104:107], v[148:151]\n"
    "s_add_u32 s74, s68, 162816\n"
    "v_mfma_f32_16x16x32_bf16 v[180:183], a[52:55], a[108:111], v[180:183]\n"
    "buffer_load_dwordx4 v26, s[60:63], s74 offen offset:1024 lds\n"
    "v_mfma_f32_16x16x32_bf16 v[212:215], a[52:55], a[112:115], v[212:215]\n"
    "ds_read_b128 a[76:79], v7 offset:3072\n"
    "v_mfma_f32_16x16x32_bf16 v[244:247], a[52:55], a[116:119], v[244:247]\n"
    "s_add_u32 s74, s68, 194560\n"
    "v_mfma_f32_16x16x32_bf16 v[56:59], a[56:59], a[92:95], v[56:59]\n"
    "ds_read_b128 a[80:83], v7 offset:4096\n"
    "v_mfma_f32_16x16x32_bf16 v[88:91], a[56:59], a[96:99], v[88:91]\n"
    "buffer_load_dwordx4 v27, s[60:63], s74 offen offset:2048 lds\n"
    "s_add_u32 s74, s68, 226304\n"
    "v_mfma_f32_16x16x32_bf16 v[120:123], a[56:59], a[100:103], v[120:123]\n"
    "ds_read_b128 a[84:87], v7 offset:5120\n"
    "v_mfma_f32_16x16x32_bf16 v[152:155], a[56:59], a[104:107], v[152:155]\n"
    "buffer_load_dwordx4 v27, s[60:63], s74 offen offset:3072 lds\n"
    "v_mfma_f32_16x16x32_bf16 v[184:187], a[56:59], a[108:111], v[184:187]\n"
    "ds_read_b128 a[88:91], v7 offset:6144\n"
    "v_mfma_f32_16x16x32_bf16 v[216:219], a[56:59], a[112:115], v[216:219]\n"
    "s_add_u32 s68, s68, 262144\n"
    "v_mfma_f32_16x16x32_bf16 v[248:251], a[56:59], a[116:119], v[248:251]\n"
    "v_mfma_f32_16x16x32_bf16 v[60:63], a[60:63], a[92:95], v[60:63]\n"
    "v_mfma_f32_16x16x32_bf16 v[92:95], a[60:63], a[96:99], v[92:95]\n"
    "v_mfma_f32_16x16x32_bf16 v[124:127], a[60:63], a[100:103], v[124:127]\n"
    "v_mfma_f32_16x16x32_bf16 v[156:159], a[60:63], a[104:107], v[156:159]\n"
    "v_mfma_f32_16x16x32_bf16 v[188:191], a[60:63], a[108:111], v[188:191]\n"
    "v_mfma_f32_16x16x32_bf16 v[220:223], a[60:63], a[112:115], v[220:223]\n"
    "v_mfma_f32_16x16x32_bf16 v[252:255], a[60:63], a[116:119], v[252:255]\n"
    "s_waitcnt lgkmcnt(0)\n"
    "s_sleep 20\n"
    "s_waitcnt vmcnt(15)\n"
    "v_mfma_f32_16x16x32_bf16 v[32:35], a[0:3], a[64:67], v[32:35]\n"
    "s_add_u32 m0, s64, 0\n"
    "v_mfma_f32_16x16x32_bf16 v[64:67], a[0:3], a[68:71], v[64:67]\n"
    "ds_read_b64_tr_b16 a[32:33], v8 offset:24576\n"
    "v_mfma_f32_16x16x32_bf16 v[96:99], a[0:3], a[72:75], v[96:99]\n"
    "buffer_load_dwordx4 v28, s[52:55], s69 offen offset:0 lds\n"
    "ds_read_b64_tr_b16 a[34:35], v8 offset:25600\n"
    "v_mfma_f32_16x16x32_bf16 v[128:131], a[0:3], a[76:79], v[128:131]\n"
    "s_add_u32 s76, s69, 15360\n"
    "v_mfma_f32_16x16x32_bf16 v[160:163], a[0:3], a[80:83], v[160:163]\n"
    "ds_read_b64_tr_b16 a[36:37], v9 offset:24576\n"
    "v_mfma_f32_16x16x32_bf16 v[192:195], a[0:3], a[84:87], v[192:195]\n"
    "buffer_load_dwordx4 v28, s[52:55], s76 offen offset:1024 lds\n"
    "v_mfma_f32_16x16x32_bf16 v[224:227], a[0:3], a[88:91], v[224:227]\n"
    "s_add_u32 s76, s69, 30720\n"
    "v_mfma_f32_16x16x32_bf16 v[36:39], a[4:7], a[64:67], v[36:39]\n"
    "ds_read_b64_tr_b16 a[38:39], v9 offset:25600\n"
    "buffer_load_dwordx4 v28, s[52:55], s76 offen offset:2048 lds\n"
    "v_mfma_f32_16x16x32_bf16 v[68:71], a[4:7], a[68:71], v[68:71]\n"
    "ds_read_b64_tr_b16 a[40:41], v10 offset:24576\n"
    "v_mfma_f32_16x16x32_bf16 v[100:103], a[4:7], a[72:75], v[100:103]\n"
    "s_add_u32 s76, s69, 46080\n"
    "v_mfma_f32_16x16x32_bf16 v[132:135], a[4:7], a[76:79], v[132:135]\n"
    "buffer_load_dwordx4 v28, s[52:55], s76 offen offset:3072 lds\n"
    "v_mfma_f32_16x16x32_bf16 v[164:167], a[4:7], a[80:83], v[164:167]\n"
    "ds_read_b64_tr_b16 a[42:43], v10 offset:25600\n"
    "v_mfma_f32_16x16x32_bf16 v[196:199], a[4:7], a[84:87], v[196:199]\n"
    "s_add_u32 m0, s64, 4096\n"
    "ds_read_b64_tr_b16 a[44:45], v11 offset:24576\n"
    "v_mfma_f32_16x16x32_bf16 v[228:231], a[4:7], a[88:91], v[228:231]\n"
    "s_add_u32 s76, s69, 65536\n"
    "v_mfma_f32_16x16x32_bf16 v[40:43], a[8:11], a[64:67], v[40:43]\n"
    "buffer_load_dwordx4 v28, s[52:55], s76 offen offset:0 lds\n"
    "v_mfma_f32_16x16x32_bf16 v[72:75], a[8:11], a[68:71], v[72:75]\n"
    "ds_read_b64_tr_b16 a[46:47], v11 offset:25600\n"
    "v_mfma_f32_16x16x32_bf16 v[104:107], a[8:11], a[72:75], v[104:107]\n"
    "s_add_u32 s76, s69, 80896\n"
    "v_mfma_f32_16x16x32_bf16 v[136:139], a[8:11], a[76:79], v[136:139]\n"
    "ds_read_b64_tr_b16 a[48:49], v12 offset:24576\n"
    "buffer_load_dwordx4 v28, s[52:55], s76 offen offset:1024 lds\n"
    "v_mfma_f32_16x16x32_bf16 v[168:171], a[8:11], a[80:83], v[168:171]\n"
    "s_add_u32 s76, s69, 96256\n"
    "v_mfma_f32_16x16x32_bf16 v[200:203], a[8:11], a[84:87], v[200:203]\n"
    "ds_read_b64_tr_b16 a[50:51], v12 offset:25600\n"
    "v_mfma_f32_16x16x32_bf16 v[232:235], a[8:11], a[88:91], v[232:235]\n"
    "buffer_load_dwordx4 v28, s[52:55], s76 offen offset:2048 lds\n"
    "v_mfma_f32_16x16x32_bf16 v[44:47], a[12:15], a[64:67], v[44:47]\n"
    "ds_read_b64_tr_b16 a[52:53], v13 offset:24576\n"
    "v_mfma_f32_16x16x32_bf16 v[76:79], a[12:15], a[68:71], v[76:79]\n"
    "s_add_u32 s69, s69, 64\n"
    "s_add_u32 m0, s64, 16384\n"
    "v_mfma_f32_16x16x32_bf16 v[108:111], a[12:15], a[72:75], v[108:111]\n"
    "ds_read_b64_tr_b16 a[54:55], v13 offset:25600\n"
    "v_mfma_f32_16x16x32_bf16 v[140:143], a[12:15], a[76:79], v[140:143]\n"
    "buffer_load_dwordx4 v26, s[60:63], s68 offen offset:0 lds\n"
    "v_mfma_f32_16x16x32_bf16 v[172:175], a[12:15], a[80:83], v[172:175]\n"
    "ds_read_b64_tr_b16 a[56:57], v14 offset:24576\n"
    "v_mfma_f32_16x16x32_bf16 v[204:207], a[12:15], a[84:87], v[204:207]\n"
    "s_add_u32 s74, s68, 31744\n"
    "v_mfma_f32_16x16x32_bf16 v[236:239], a[12:15], a[88:91], v[236:239]\n"
    "buffer_load_dwordx4 v26, s[60:63], s74 offen offset:1024 lds\n"
    "ds_read_b64_tr_b16 a[58:59], v14 offset:25600\n"
    "v_mfma_f32_16x16x32_bf16 v[48:51], a[16:19], a[64:67], v[48:51]\n"
    "s_add_u32 s74, s68, 63488\n"
    "v_mfma_f32_16x16x32_bf16 v[80:83], a[16:19], a[68:71], v[80:83]\n"
    "ds_read_b64_tr_b16 a[60:61], v15 offset:24576\n"
    "v_mfma_f32_16x16x32_bf16 v[112:115], a[16:19], a[72:75], v[112:115]\n"
    "buffer_load_dwordx4 v27, s[60:63], s74 offen offset:2048 lds\n"
    "v_mfma_f32_16x16x32_bf16 v[144:147], a[16:19], a[76:79], v[144:147]\n"
    "s_add_u32 s74, s68, 95232\n"
    "v_mfma_f32_16x16x32_bf16 v[176:179], a[16:19], a[80:83], v[176:179]\n"
    "ds_read_b64_tr_b16 a[62:63], v15 offset:25600\n"
    "buffer_load_dwordx4 v27, s[60:63], s74 offen offset:3072 lds\n"
    "v_mfma_f32_16x16x32_bf16 v[208:211], a[16:19], a[84:87], v[208:211]\n"
    "ds_read_b128 a[92:95], v6 offset:0\n"
    "v_mfma_f32_16x16x32_bf16 v[240:243], a[16:19], a[88:91], v[240:243]\n"
    "s_add_u32 m0, s64, 20480\n"
    "v_mfma_f32_16x16x32_bf16 v[52:55], a[20:23], a[64:67], v[52:55]\n"
    "s_add_u32 s74, s68, 131072\n"
    "v_mfma_f32_16x16x32_bf16 v[84:87], a[20:23], a[68:71], v[84:87]\n"
    "ds_read_b128 a[96:99], v6 offset:1024\n"
    "v_mfma_f32_16x16x32_bf16 v[116:119], a[20:23], a[72:75], v[116:119]\n"
    "buffer_load_dwordx4 v26, s[60:63], s74 offen offset:0 lds\n"
    "ds_read_b128 a[100:103], v6 offset:2048\n"
    "v_mfma_f32_16x16x32_bf16 v[148:151], a[20:23], a[76:79], v[148:151]\n"
    "s_add_u32 s74, s68, 162816\n"
    "v_mfma_f32_16x16x32_bf16 v[180:183], a[20:23], a[80:83], v[180:183]\n"
    "buffer_load_dwordx4 v26, s[60:63], s74 offen offset:1024 lds\n"
    "v_mfma_f32_16x16x32_bf16 v[212:215], a[20:23], a[84:87], v[212:215]\n"
    "ds_read_b128 a[104:107], v6 offset:3072\n"
    "v_mfma_f32_16x16x32_bf16 v[244:247], a[20:23], a[88:91], v[244:247]\n"
    "s_add_u32 s74, s68, 194560\n"
    "v_mfma_f32_16x16x32_bf16 v[56:59], a[24:27], a[64:67], v[56:59]\n"
    "ds_read_b128 a[108:111], v6 offset:4096\n"
    "v_mfma_f32_16x16x32_bf16 v[88:91], a[24:27], a[68:71], v[88:91]\n"
    "buffer_load_dwordx4 v27, s[60:63], s74 offen offset:2048 lds\n"
    "s_add_u32 s74, s68, 226304\n"
    "v_mfma_f32_16x16x32_bf16 v[120:123], a[24:27], a[72:75], v[120:123]\n"
    "ds_read_b128 a[112:115], v6 offset:5120\n"
    "v_mfma_f32_16x16x32_bf16 v[152:155], a[24:27], a[76:79], v[152:155]\n"
    "buffer_load_dwordx4 v27, s[60:63], s74 offen offset:3072 lds\n"
    "v_mfma_f32_16x16x32_bf16 v[184:187], a[24:27], a[80:83], v[184:187]\n"
    "ds_read_b128 a[116:119], v6 offset:6144\n"
    "v_mfma_f32_16x16x32_bf16 v[216:219], a[24:27], a[84:87], v[216:219]\n"
    "s_add_u32 s68, s68, 262144\n"
    "v_mfma_f32_16x16x32_bf16 v[248:251], a[24:27], a[88:91], v[248:251]\n"
    "v_mfma_f32_16x16x32_bf16 v[60:63], a[28:31], a[64:67], v[60:63]\n"
    "v_mfma_f32_16x16x32_bf16 v[92:95], a[28:31], a[68:71], v[92:95]\n"
    "v_mfma_f32_16x16x32_bf16 v[124:127], a[28:31], a[72:75], v[124:127]\n"
    "v_mfma_f32_16x16x32_bf16 v[156:159], a[28:31], a[76:79], v[156:159]\n"
    "v_mfma_f32_16x16x32_bf16 v[188:191], a[28:31], a[80:83], v[188:191]\n"
    "v_mfma_f32_16x16x32_bf16 v[220:223], a[28:31], a[84:87], v[220:223]\n"
    "v_mfma_f32_16x16x32_bf16 v[252:255], a[28:31], a[88:91], v[252:255]\n"
    "s_waitcnt lgkmcnt(0)\n"
    "s_sleep 20\n"
    "s_waitcnt vmcnt(15)\n"
    "v_mfma_f32_16x16x32_bf16 v[32:35], a[32:35], a[92:95], v[32:35]\n"
    "s_add_u32 m0, s65, 0\n"
    "v_mfma_f32_16x16x32_bf16 v[64:67], a[32:35], a[96:99], v[64:67]\n"
    "ds_read_b64_tr_b16 a[0:1], v8 offset:8192\n"
    "v_mfma_f32_16x16x32_bf16 v[96:99], a[32:35], a[100:103], v[96:99]\n"
    "buffer_load_dwordx4 v28, s[52:55], s69 offen offset:0 lds\n"
    "ds_read_b64_tr_b16 a[2:3], v8 offset:9216\n"
    "v_mfma_f32_16x16x32_bf16 v[128:131], a[32:35], a[104:107], v[128:131]\n"
    "s_add_u32 s76, s69, 15360\n"
    "v_mfma_f32_16x16x32_bf16 v[160:163], a[32:35], a[108:111], v[160:163]\n"
    "ds_read_b64_tr_b16 a[4:5], v9 offset:8192\n"
    "v_mfma_f32_16x16x32_bf16 v[192:195], a[32:35], a[112:115], v[192:195]\n"
    "buffer_load_dwordx4 v28, s[52:55], s76 offen offset:1024 lds\n"
    "v_mfma_f32_16x16x32_bf16 v[224:227], a[32:35], a[116:119], v[224:227]\n"
    "s_add_u32 s76, s69, 30720\n"
    "v_mfma_f32_16x16x32_bf16 v[36:39], a[36:39], a[92:95], v[36:39]\n"
    "ds_read_b64_tr_b16 a[6:7], v9 offset:9216\n"
    "buffer_load_dwordx4 v28, s[52:55], s76 offen offset:2048 lds\n"
    "v_mfma_f32_16x16x32_bf16 v[68:71], a[36:39], a[96:99], v[68:71]\n"
    "ds_read_b64_tr_b16 a[8:9], v10 offset:8192\n"
    "v_mfma_f32_16x16x32_bf16 v[100:103], a[36:39], a[100:103], v[100:103]\n"
    "s_add_u32 s76, s69, 46080\n"
    "v_mfma_f32_16x16x32_bf16 v[132:135], a[36:39], a[104:107], v[132:135]\n"
    "buffer_load_dwordx4 v28, s[52:55], s76 offen offset:3072 lds\n"
    "v_mfma_f32_16x16x32_bf16 v[164:167], a[36:39], a[108:111], v[164:167]\n"
    "ds_read_b64_tr_b16 a[10:11], v10 offset:9216\n"
    "v_mfma_f32_16x16x32_bf16 v[196:199], a[36:39], a[112:115], v[196:199]\n"
    "s_add_u32 m0, s65, 4096\n"
    "ds_read_b64_tr_b16 a[12:13], v11 offset:8192\n"
    "v_mfma_f32_16x16x32_bf16 v[228:231], a[36:39], a[116:119], v[228:231]\n"
    "s_add_u32 s76, s69, 65536\n"
    "v_mfma_f32_16x16x32_bf16 v[40:43], a[40:43], a[92:95], v[40:43]\n"
    "buffer_load_dwordx4 v28, s[52:55], s76 offen offset:0 lds\n"
    "v_mfma_f32_16x16x32_bf16 v[72:75], a[40:43], a[96:99], v[72:75]\n"
    "ds_read_b64_tr_b16 a[14:15], v11 offset:9216\n"
    "v_mfma_f32_16x16x32_bf16 v[104:107], a[40:43], a[100:103], v[104:107]\n"
    "s_add_u32 s76, s69, 80896\n"
    "v_mfma_f32_16x16x32_bf16 v[136:139], a[40:43], a[104:107], v[136:139]\n"
    "ds_read_b64_tr_b16 a[16:17], v12 offset:8192\n"
    "buffer_load_dwordx4 v28, s[52:55], s76 offen offset:1024 lds\n"
    "v_mfma_f32_16x16x32_bf16 v[168:171], a[40:43], a[108:111], v[168:171]\n"
    "s_add_u32 s76, s69, 96256\n"
    "v_mfma_f32_16x16x32_bf16 v[200:203], a[40:43], a[112:115], v[200:203]\n"
    "ds_read_b64_tr_b16 a[18:19], v12 offset:9216\n"
    "v_mfma_f32_16x16x32_bf16 v[232:235], a[40:43], a[116:119], v[232:235]\n"
    "buffer_load_dwordx4 v28, s[52:55], s76 offen offset:2048 lds\n"
    "v_mfma_f32_16x16x32_bf16 v[44:47], a[44:47], a[92:95], v[44:47]\n"
    "ds_read_b64_tr_b16 a[20:21], v13 offset:8192\n"
    "v_mfma_f32_16x16x32_bf16 v[76:79], a[44:47], a[96:99], v[76:79]\n"
    "s_add_u32 s69, s69, 64\n"
    "s_add_u32 m0, s64, 24576\n"
    "v_mfma_f32_16x16x32_bf16 v[108:111], a[44:47], a[100:103], v[108:111]\n"
    "ds_read_b64_tr_b16 a[22:23], v13 offset:9216\n"
    "v_mfma_f32_16x16x32_bf16 v[140:143], a[44:47], a[104:107], v[140:143]\n"
    "buffer_load_dwordx4 v26, s[60:63], s68 offen offset:0 lds\n"
    "v_mfma_f32_16x16x32_bf16 v[172:175], a[44:47], a[108:111], v[172:175]\n"
    "ds_read_b64_tr_b16 a[24:25], v14 offset:8192\n"
    "v_mfma_f32_16x16x32_bf16 v[204:207], a[44:47], a[112:115], v[204:207]\n"
    "s_add_u32 s74, s68, 31744\n"
    "v_mfma_f32_16x16x32_bf16 v[236:239], a[44:47], a[116:119], v[236:239]\n"
    "buffer_load_dwordx4 v26, s[60:63], s74 offen offset:1024 lds\n"
    "ds_read_b64_tr_b16 a[26:27], v14 offset:9216\n"
    "v_mfma_f32_16x16x32_bf16 v[48:51], a[48:51], a[92:95], v[48:51]\n"
    "s_add_u32 s74, s68, 63488\n"
    "v_mfma_f32_16x16x32_bf16 v[80:83], a[48:51], a[96:99], v[80:83]\n"
    "ds_read_b64_tr_b16 a[28:29], v15 offset:8192\n"
    "v_mfma_f32_16x16x32_bf16 v[112:115], a[48:51], a[100:103], v[112:115]\n"
    "buffer_load_dwordx4 v27, s[60:63], s74 offen offset:2048 lds\n"
    "v_mfma_f32_16x16x32_bf16 v[144:147], a[48:51], a[104:107], v[144:147]\n"
    "s_add_u32 s74, s68, 95232\n"
    "v_mfma_f32_16x16x32_bf16 v[176:179], a[48:51], a[108:111], v[176:179]\n"
    "ds_read_b64_tr_b16 a[30:31], v15 offset:9216\n"
    "buffer_load_dwordx4 v27, s[60:63], s74 offen offset:3072 lds\n"
    "v_mfma_f32_16x16x32_bf16 v[208:211], a[48:51], a[112:115], v[208:211]\n"
    "ds_read_b128 a[64:67], v6 offset:8192\n"
    "v_mfma_f32_16x16x32_bf16 v[240:243], a[48:51], a[116:119], v[240:243]\n"
    "s_add_u32 m0, s64, 28672\n"
    "v_mfma_f32_16x16x32_bf16 v[52:55], a[52:55], a[92:95], v[52:55]\n"
    "s_add_u32 s74, s68, 131072\n"
    "v_mfma_f32_16x16x32_bf16 v[84:87], a[52:55], a[96:99], v[84:87]\n"
    "ds_read_b128 a[68:71], v6 offset:9216\n"
    "v_mfma_f32_16x16x32_bf16 v[116:119], a[52:55], a[100:103], v[116:119]\n"
    "buffer_load_dwordx4 v26, s[60:63], s74 offen offset:0 lds\n"
    "ds_read_b128 a[72:75], v6 offset:10240\n"
    "v_mfma_f32_16x16x32_bf16 v[148:151], a[52:55], a[104:107], v[148:151]\n"
    "s_add_u32 s74, s68, 162816\n"
    "v_mfma_f32_16x16x32_bf16 v[180:183], a[52:55], a[108:111], v[180:183]\n"
    "buffer_load_dwordx4 v26, s[60:63], s74 offen offset:1024 lds\n"
    "v_mfma_f32_16x16x32_bf16 v[212:215], a[52:55], a[112:115], v[212:215]\n"
    "ds_read_b128 a[76:79], v6 offset:11264\n"
    "v_mfma_f32_16x16x32_bf16 v[244:247], a[52:55], a[116:119], v[244:247]\n"
    "s_add_u32 s74, s68, 194560\n"
    "v_mfma_f32_16x16x32_bf16 v[56:59], a[56:59], a[92:95], v[56:59]\n"
    "ds_read_b128 a[80:83], v6 offset:12288\n"
    "v_mfma_f32_16x16x32_bf16 v[88:91], a[56:59], a[96:99], v[88:91]\n"
    "buffer_load_dwordx4 v27, s[60:63], s74 offen offset:2048 lds\n"
    "s_add_u32 s74, s68, 226304\n"
    "v_mfma_f32_16x16x32_bf16 v[120:123], a[56:59], a[100:103], v[120:123]\n"
    "ds_read_b128 a[84:87], v6 offset:13312\n"
    "v_mfma_f32_16x16x32_bf16 v[152:155], a[56:59], a[104:107], v[152:155]\n"
    "buffer_load_dwordx4 v27, s[60:63], s74 offen offset:3072 lds\n"
    "v_mfma_f32_16x16x32_bf16 v[184:187], a[56:59], a[108:111], v[184:187]\n"
    "ds_read_b128 a[88:91], v6 offset:14336\n"
    "v_mfma_f32_16x16x32_bf16 v[216:219], a[56:59], a[112:115], v[216:219]\n"
    "s_add_u32 s68, s68, 262144\n"
    "v_mfma_f32_16x16x32_bf16 v[248:251], a[56:59], a[116:119], v[248:251]\n"
    "v_mfma_f32_16x16x32_bf16 v[60:63], a[60:63], a[92:95], v[60:63]\n"
    "v_mfma_f32_16x16x32_bf16 v[92:95], a[60:63], a[96:99], v[92:95]\n"
    "v_mfma_f32_16x16x32_bf16 v[124:127], a[60:63], a[100:103], v[124:127]\n"
    "v_mfma_f32_16x16x32_bf16 v[156:159], a[60:63], a[104:107], v[156:159]\n"
    "v_mfma_f32_16x16x32_bf16 v[188:191], a[60:63], a[108:111], v[188:191]\n"
    "v_mfma_f32_16x16x32_bf16 v[220:223], a[60:63], a[112:115], v[220:223]\n"
    "v_mfma_f32_16x16x32_bf16 v[252:255], a[60:63], a[116:119], v[252:255]\n"
    "s_waitcnt lgkmcnt(0)\n"
    "s_sleep 20\n"
    "s_waitcnt vmcnt(15)\n"
    "v_mfma_f32_16x16x32_bf16 v[32:35], a[0:3], a[64:67], v[32:35]\n"
    "s_add_u32 m0, s65, 8192\n"
    "v_mfma_f32_16x16x32_bf16 v[64:67], a[0:3], a[68:71], v[64:67]\n"
    "ds_read_b64_tr_b16 a[32:33], v8 offset:16384\n"
    "v_mfma_f32_16x16x32_bf16 v[96:99], a[0:3], a[72:75], v[96:99]\n"
    "buffer_load_dwordx4 v28, s[52:55], s69 offen offset:0 lds\n"
    "ds_read_b64_tr_b16 a[34:35], v8 offset:17408\n"
    "v_mfma_f32_16x16x32_bf16 v[128:131], a[0:3], a[76:79], v[128:131]\n"
    "s_add_u32 s76, s69, 15360\n"
    "v_mfma_f32_16x16x32_bf16 v[160:163], a[0:3], a[80:83], v[160:163]\n"
    "ds_read_b64_tr_b16 a[36:37], v9 offset:16384\n"
    "v_mfma_f32_16x16x32_bf16 v[192:195], a[0:3], a[84:87], v[192:195]\n"
    "buffer_load_dwordx4 v28, s[52:55], s76 offen offset:1024 lds\n"
    "v_mfma_f32_16x16x32_bf16 v[224:227], a[0:3], a[88:91], v[224:227]\n"
    "s_add_u32 s76, s69, 30720\n"
    "v_mfma_f32_16x16x32_bf16 v[36:39], a[4:7], a[64:67], v[36:39]\n"
    "ds_read_b64_tr_b16 a[38:39], v9 offset:17408\n"
    "buffer_load_dwordx4 v28, s[52:55], s76 offen offset:2048 lds\n"
    "v_mfma_f32_16x16x32_bf16 v[68:71], a[4:7], a[68:71], v[68:71]\n"
    "ds_read_b64_tr_b16 a[40:41], v10 offset:16384\n"
    "v_mfma_f32_16x16x32_bf16 v[100:103], a[4:7], a[72:75], v[100:103]\n"
    "s_add_u32 s76, s69, 46080\n"
    "v_mfma_f32_16x16x32_bf16 v[132:135], a[4:7], a[76:79], v[132:135]\n"
    "buffer_load_dwordx4 v28, s[52:55], s76 offen offset:3072 lds\n"
    "v_mfma_f32_16x16x32_bf16 v[164:167], a[4:7], a[80:83], v[164:167]\n"
    "ds_read_b64_tr_b16 a[42:43], v10 offset:17408\n"
    "v_mfma_f32_16x16x32_bf16 v[196:199], a[4:7], a[84:87], v[196:199]\n"
    "s_add_u32 m0, s65, 12288\n"
    "ds_read_b64_tr_b16 a[44:45], v11 offset:16384\n"
    "v_mfma_f32_16x16x32_bf16 v[228:231], a[4:7], a[88:91], v[228:231]\n"
    "s_add_u32 s76, s69, 65536\n"
    "v_mfma_f32_16x16x32_bf16 v[40:43], a[8:11], a[64:67], v[40:43]\n"
    "buffer_load_dwordx4 v28, s[52:55], s76 offen offset:0 lds\n"
    "v_mfma_f32_16x16x32_bf16 v[72:75], a[8:11], a[68:71], v[72:75]\n"
    "ds_read_b64_tr_b16 a[46:47], v11 offset:17408\n"
    "v_mfma_f32_16x16x32_bf16 v[104:107], a[8:11], a[72:75], v[104:107]\n"
    "s_add_u32 s76, s69, 80896\n"
    "v_mfma_f32_16x16x32_bf16 v[136:139], a[8:11], a[76:79], v[136:139]\n"
    "ds_read_b64_tr_b16 a[48:49], v12 offset:16384\n"
    "buffer_load_dwordx4 v28, s[52:55], s76 offen offset:1024 lds\n"
    "v_mfma_f32_16x16x32_bf16 v[168:171], a[8:11], a[80:83], v[168:171]\n"
    "s_add_u32 s76, s69, 96256\n"
    "v_mfma_f32_16x16x32_bf16 v[200:203], a[8:11], a[84:87], v[200:203]\n"
    "ds_read_b64_tr_b16 a[50:51], v12 offset:17408\n"
    "v_mfma_f32_16x16x32_bf16 v[232:235], a[8:11], a[88:91], v[232:235]\n"
    "buffer_load_dwordx4 v28, s[52:55], s76 offen offset:2048 lds\n"
    "v_mfma_f32_16x16x32_bf16 v[44:47], a[12:15], a[64:67], v[44:47]\n"
    "ds_read_b64_tr_b16 a[52:53], v13 offset:16384\n"
    "v_mfma_f32_16x16x32_bf16 v[76:79], a[12:15], a[68:71], v[76:79]\n"
    "s_add_u32 s69, s69, 64\n"
    "s_add_u32 m0, s64, 8192\n"
    "v_mfma_f32_16x16x32_bf16 v[108:111], a[12:15], a[72:75], v[108:111]\n"
    "ds_read_b64_tr_b16 a[54:55], v13 offset:17408\n"
    "v_mfma_f32_16x16x32_bf16 v[140:143], a[12:15], a[76:79], v[140:143]\n"
    "buffer_load_dwordx4 v26, s[60:63], s68 offen offset:0 lds\n"
    "v_mfma_f32_16x16x32_bf16 v[172:175], a[12:15], a[80:83], v[172:175]\n"
    "ds_read_b64_tr_b16 a[56:57], v14 offset:16384\n"
    "v_mfma_f32_16x16x32_bf16 v[204:207], a[12:15], a[84:87], v[204:207]\n"
    "s_add_u32 s74, s68, 31744\n"
    "v_mfma_f32_16x16x32_bf16 v[236:239], a[12:15], a[88:91], v[236:239]\n"
    "buffer_load_dwordx4 v26, s[60:63], s74 offen offset:1024 lds\n"
    "ds_read_b64_tr_b16 a[58:59], v14 offset:17408\n"
    "v_mfma_f32_16x16x32_bf16 v[48:51], a[16:19], a[64:67], v[48:51]\n"
    "s_add_u32 s74, s68, 63488\n"
    "v_mfma_f32_16x16x32_bf16 v[80:83], a[16:19], a[68:71], v[80:83]\n"
    "ds_read_b64_tr_b16 a[60:61], v15 offset:16384\n"
    "v_mfma_f32_16x16x32_bf16 v[112:115], a[16:19], a[72:75], v[112:115]\n"
    "buffer_load_dwordx4 v27, s[60:63], s74 offen offset:2048 lds\n"
    "v_mfma_f32_16x16x32_bf16 v[144:147], a[16:19], a[76:79], v[144:147]\n"
    "s_add_u32 s74, s68, 95232\n"
    "v_mfma_f32_16x16x32_bf16 v[176:179], a[16:19], a[80:83], v[176:179]\n"
    "ds_read_b64_tr_b16 a[62:63], v15 offset:17408\n"
    "buffer_load_dwordx4 v27, s[60:63], s74 offen offset:3072 lds\n"
    "v_mfma_f32_16x16x32_bf16 v[208:211], a[16:19], a[84:87], v[208:211]\n"
    "ds_read_b128 a[92:95], v7 offset:0\n"
    "v_mfma_f32_16x16x32_bf16 v[240:243], a[16:19], a[88:91], v[240:243]\n"
    "s_add_u32 m0, s64, 12288\n"
    "v_mfma_f32_16x16x32_bf16 v[52:55], a[20:23], a[64:67], v[52:55]\n"
    "s_add_u32 s74, s68, 131072\n"
    "v_mfma_f32_16x16x32_bf16 v[84:87], a[20:23], a[68:71], v[84:87]\n"
    "ds_read_b128 a[96:99], v7 offset:1024\n"
    "v_mfma_f32_16x16x32_bf16 v[116:119], a[20:23], a[72:75], v[116:119]\n"
    "buffer_load_dwordx4 v26, s[60:63], s74 offen offset:0 lds\n"
    "ds_read_b128 a[100:103], v7 offset:2048\n"
    "v_mfma_f32_16x16x32_bf16 v[148:151], a[20:23], a[76:79], v[148:151]\n"
    "s_add_u32 s74, s68, 162816\n"
    "v_mfma_f32_16x16x32_bf16 v[180:183], a[20:23], a[80:83], v[180:183]\n"
    "buffer_load_dwordx4 v26, s[60:63], s74 offen offset:1024 lds\n"
    "v_mfma_f32_16x16x32_bf16 v[212:215], a[20:23], a[84:87], v[212:215]\n"
    "ds_read_b128 a[104:107], v7 offset:3072\n"
    "v_mfma_f32_16x16x32_bf16 v[244:247], a[20:23], a[88:91], v[244:247]\n"
    "s_add_u32 s74, s68, 194560\n"
    "v_mfma_f32_16x16x32_bf16 v[56:59], a[24:27], a[64:67], v[56:59]\n"
    "ds_read_b128 a[108:111], v7 offset:4096\n"
    "v_mfma_f32_16x16x32_bf16 v[88:91], a[24:27], a[68:71], v[88:91]\n"
    "buffer_load_dwordx4 v27, s[60:63], s74 offen offset:2048 lds\n"
    "s_add_u32 s74, s68, 226304\n"
    "v_mfma_f32_16x16x32_bf16 v[120:123], a[24:27], a[72:75], v[120:123]\n"
    "ds_read_b128 a[112:115], v7 offset:5120\n"
    "v_mfma_f32_16x16x32_bf16 v[152:155], a[24:27], a[76:79], v[152:155]\n"
    "buffer_load_dwordx4 v27, s[60:63], s74 offen offset:3072 lds\n"
    "v_mfma_f32_16x16x32_bf16 v[184:187], a[24:27], a[80:83], v[184:187]\n"
    "ds_read_b128 a[116:119], v7 offset:6144\n"
    "v_mfma_f32_16x16x32_bf16 v[216:219], a[24:27], a[84:87], v[216:219]\n"
    "s_add_u32 s68, s68, 262144\n"
    "v_mfma_f32_16x16x32_bf16 v[248:251], a[24:27], a[88:91], v[248:251]\n"
    "v_mfma_f32_16x16x32_bf16 v[60:63], a[28:31], a[64:67], v[60:63]\n"
    "v_mfma_f32_16x16x32_bf16 v[92:95], a[28:31], a[68:71], v[92:95]\n"
    "v_mfma_f32_16x16x32_bf16 v[124:127], a[28:31], a[72:75], v[124:127]\n"
    "v_mfma_f32_16x16x32_bf16 v[156:159], a[28:31], a[76:79], v[156:159]\n"
    "v_mfma_f32_16x16x32_bf16 v[188:191], a[28:31], a[80:83], v[188:191]\n"
    "v_mfma_f32_16x16x32_bf16 v[220:223], a[28:31], a[84:87], v[220:223]\n"
    "v_mfma_f32_16x16x32_bf16 v[252:255], a[28:31], a[88:91], v[252:255]\n"
    "s_waitcnt lgkmcnt(0)\n"
    "s_sleep 20\n"
    "s_waitcnt vmcnt(15)\n"
    "v_mfma_f32_16x16x32_bf16 v[32:35], a[32:35], a[92:95], v[32:35]\n"
    "s_add_u32 m0, s64, 0\n"
    "v_mfma_f32_16x16x32_bf16 v[64:67], a[32:35], a[96:99], v[64:67]\n"
    "ds_read_b64_tr_b16 a[0:1], v8 offset:24576\n"
    "v_mfma_f32_16x16x32_bf16 v[96:99], a[32:35], a[100:103], v[96:99]\n"
    "buffer_load_dwordx4 v28, s[52:55], s69 offen offset:0 lds\n"
    "ds_read_b64_tr_b16 a[2:3], v8 offset:25600\n"
    "v_mfma_f32_16x16x32_bf16 v[128:131], a[32:35], a[104:107], v[128:131]\n"
    "s_add_u32 s76, s69, 15360\n"
    "v_mfma_f32_16x16x32_bf16 v[160:163], a[32:35], a[108:111], v[160:163]\n"
    "ds_read_b64_tr_b16 a[4:5], v9 offset:24576\n"
    "v_mfma_f32_16x16x32_bf16 v[192:195], a[32:35], a[112:115], v[192:195]\n"
    "buffer_load_dwordx4 v28, s[52:55], s76 offen offset:1024 lds\n"
    "v_mfma_f32_16x16x32_bf16 v[224:227], a[32:35], a[116:119], v[224:227]\n"
    "s_add_u32 s76, s69, 30720\n"
    "v_mfma_f32_16x16x32_bf16 v[36:39], a[36:39], a[92:95], v[36:39]\n"
    "ds_read_b64_tr_b16 a[6:7], v9 offset:25600\n"
    "buffer_load_dwordx4 v28, s[52:55], s76 offen offset:2048 lds\n"
    "v_mfma_f32_16x16x32_bf16 v[68:71], a[36:39], a[96:99], v[68:71]\n"
    "ds_read_b64_tr_b16 a[8:9], v10 offset:24576\n"
    "v_mfma_f32_16x16x32_bf16 v[100:103], a[36:39], a[100:103], v[100:103]\n"
    "s_add_u32 s76, s69, 46080\n"
    "v_mfma_f32_16x16x32_bf16 v[132:135], a[36:39], a[104:107], v[132:135]\n"
    "buffer_load_dwordx4 v28, s[52:55], s76 offen offset:3072 lds\n"
    "v_mfma_f32_16x16x32_bf16 v[164:167], a[36:39], a[108:111], v[164:167]\n"
    "ds_read_b64_tr_b16 a[10:11], v10 offset:25600\n"
    "v_mfma_f32_16x16x32_bf16 v[196:199], a[36:39], a[112:115], v[196:199]\n"
    "s_add_u32 m0, s64, 4096\n"
    "ds_read_b64_tr_b16 a[12:13], v11 offset:24576\n"
    "v_mfma_f32_16x16x32_bf16 v[228:231], a[36:39], a[116:119], v[228:231]\n"
    "s_add_u32 s76, s69, 65536\n"
    "v_mfma_f32_16x16x32_bf16 v[40:43], a[40:43], a[92:95], v[40:43]\n"
    "buffer_load_dwordx4 v28, s[52:55], s76 offen offset:0 lds\n"
    "v_mfma_f32_16x16x32_bf16 v[72:75], a[40:43], a[96:99], v[72:75]\n"
    "ds_read_b64_tr_b16 a[14:15], v11 offset:25600\n"
    "v_mfma_f32_16x16x32_bf16 v[104:107], a[40:43], a[100:103], v[104:107]\n"
    "s_add_u32 s76, s69, 80896\n"
    "v_mfma_f32_16x16x32_bf16 v[136:139], a[40:43], a[104:107], v[136:139]\n"
    "ds_read_b64_tr_b16 a[16:17], v12 offset:24576\n"
    "buffer_load_dwordx4 v28, s[52:55], s76 offen offset:1024 lds\n"
    "v_mfma_f32_16x16x32_bf16 v[168:171], a[40:43], a[108:111], v[168:171]\n"
    "s_add_u32 s76, s69, 96256\n"
    "v_mfma_f32_16x16x32_bf16 v[200:203], a[40:43], a[112:115], v[200:203]\n"
    "ds_read_b64_tr_b16 a[18:19], v12 offset:25600\n"
    "v_mfma_f32_16x16x32_bf16 v[232:235], a[40:43], a[116:119], v[232:235]\n"
    "buffer_load_dwordx4 v28, s[52:55], s76 offen offset:2048 lds\n"
    "v_mfma_f32_16x16x32_bf16 v[44:47], a[44:47], a[92:95], v[44:47]\n"
    "ds_read_b64_tr_b16 a[20:21], v13 offset:24576\n"
    "v_mfma_f32_16x16x32_bf16 v[76:79], a[44:47], a[96:99], v[76:79]\n"
    "s_add_u32 s69, s69, 64\n"
    "s_add_u32 m0, s64, 16384\n"
    "v_mfma_f32_16x16x32_bf16 v[108:111], a[44:47], a[100:103], v[108:111]\n"
    "ds_read_b64_tr_b16 a[22:23], v13 offset:25600\n"
    "v_mfma_f32_16x16x32_bf16 v[140:143], a[44:47], a[104:107], v[140:143]\n"
    "buffer_load_dwordx4 v26, s[60:63], s68 offen offset:0 lds\n"
    "v_mfma_f32_16x16x32_bf16 v[172:175], a[44:47], a[108:111], v[172:175]\n"
    "ds_read_b64_tr_b16 a[24:25], v14 offset:24576\n"
    "v_mfma_f32_16x16x32_bf16 v[204:207], a[44:47], a[112:115], v[204:207]\n"
    "s_add_u32 s74, s68, 31744\n"
    "v_mfma_f32_16x16x32_bf16 v[236:239], a[44:47], a[116:119], v[236:239]\n"
    "buffer_load_dwordx4 v26, s[60:63], s74 offen offset:1024 lds\n"
    "ds_read_b64_tr_b16 a[26:27], v14 offset:25600\n"
    "v_mfma_f32_16x16x32_bf16 v[48:51], a[48:51], a[92:95], v[48:51]\n"
    "s_add_u32 s74, s68, 63488\n"
    "v_mfma_f32_16x16x32_bf16 v[80:83], a[48:51], a[96:99], v[80:83]\n"
    "ds_read_b64_tr_b16 a[28:29], v15 offset:24576\n"
    "v_mfma_f32_16x16x32_bf16 v[112:115], a[48:51], a[100:103], v[112:115]\n"
    "buffer_load_dwordx4 v27, s[60:63], s74 offen offset:2048 lds\n"
    "v_mfma_f32_16x16x32_bf16 v[144:147], a[48:51], a[104:107], v[144:147]\n"
    "s_add_u32 s74, s68, 95232\n"
    "v_mfma_f32_16x16x32_bf16 v[176:179], a[48:51], a[108:111], v[176:179]\n"
    "ds_read_b64_tr_b16 a[30:31], v15 offset:25600\n"
    "buffer_load_dwordx4 v27, s[60:63], s74 offen offset:3072 lds\n"
    "v_mfma_f32_16x16x32_bf16 v[208:211], a[48:51], a[112:115], v[208:211]\n"
    "ds_read_b128 a[64:67], v6 offset:0\n"
    "v_mfma_f32_16x16x32_bf16 v[240:243], a[48:51], a[116:119], v[240:243]\n"
    "s_add_u32 m0, s64, 20480\n"
    "v_mfma_f32_16x16x32_bf16 v[52:55], a[52:55], a[92:95], v[52:55]\n"
    "s_add_u32 s74, s68, 131072\n"
    "v_mfma_f32_16x16x32_bf16 v[84:87], a[52:55], a[96:99], v[84:87]\n"
    "ds_read_b128 a[68:71], v6 offset:1024\n"
    "v_mfma_f32_16x16x32_bf16 v[116:119], a[52:55], a[100:103], v[116:119]\n"
    "buffer_load_dwordx4 v26, s[60:63], s74 offen offset:0 lds\n"
    "ds_read_b128 a[72:75], v6 offset:2048\n"
    "v_mfma_f32_16x16x32_bf16 v[148:151], a[52:55], a[104:107], v[148:151]\n"
    "s_add_u32 s74, s68, 162816\n"
    "v_mfma_f32_16x16x32_bf16 v[180:183], a[52:55], a[108:111], v[180:183]\n"
    "buffer_load_dwordx4 v26, s[60:63], s74 offen offset:1024 lds\n"
    "v_mfma_f32_16x16x32_bf16 v[212:215], a[52:55], a[112:115], v[212:215]\n"
    "ds_read_b128 a[76:79], v6 offset:3072\n"
    "v_mfma_f32_16x16x32_bf16 v[244:247], a[52:55], a[116:119], v[244:247]\n"
    "s_add_u32 s74, s68, 194560\n"
    "v_mfma_f32_16x16x32_bf16 v[56:59], a[56:59], a[92:95], v[56:59]\n"
    "ds_read_b128 a[80:83], v6 offset:4096\n"
    "v_mfma_f32_16x16x32_bf16 v[88:91], a[56:59], a[96:99], v[88:91]\n"
    "buffer_load_dwordx4 v27, s[60:63], s74 offen offset:2048 lds\n"
    "s_add_u32 s74, s68, 226304\n"
    "v_mfma_f32_16x16x32_bf16 v[120:123], a[56:59], a[100:103], v[120:123]\n"
    "ds_read_b128 a[84:87], v6 offset:5120\n"
    "v_mfma_f32_16x16x32_bf16 v[152:155], a[56:59], a[104:107], v[152:155]\n"
    "buffer_load_dwordx4 v27, s[60:63], s74 offen offset:3072 lds\n"
    "v_mfma_f32_16x16x32_bf16 v[184:187], a[56:59], a[108:111], v[184:187]\n"
    "ds_read_b128 a[88:91], v6 offset:6144\n"
    "v_mfma_f32_16x16x32_bf16 v[216:219], a[56:59], a[112:115], v[216:219]\n"
    "s_add_u32 s68, s68, 262144\n"
    "v_mfma_f32_16x16x32_bf16 v[248:251], a[56:59], a[116:119], v[248:251]\n"
    "v_mfma_f32_16x16x32_bf16 v[60:63], a[60:63], a[92:95], v[60:63]\n"
    "v_mfma_f32_16x16x32_bf16 v[92:95], a[60:63], a[96:99], v[92:95]\n"
    "v_mfma_f32_16x16x32_bf16 v[124:127], a[60:63], a[100:103], v[124:127]\n"
    "v_mfma_f32_16x16x32_bf16 v[156:159], a[60:63], a[104:107], v[156:159]\n"
    "v_mfma_f32_16x16x32_bf16 v[188:191], a[60:63], a[108:111], v[188:191]\n"
    "v_mfma_f32_16x16x32_bf16 v[220:223], a[60:63], a[112:115], v[220:223]\n"
    "v_mfma_f32_16x16x32_bf16 v[252:255], a[60:63], a[116:119], v[252:255]\n"
    "s_waitcnt lgkmcnt(0)\n"
    "s_sleep 20\n"
    "s_waitcnt vmcnt(15)\n"
    "v_mfma_f32_16x16x32_bf16 v[32:35], a[0:3], a[64:67], v[32:35]\n"
    "s_add_u32 m0, s65, 0\n"
    "v_mfma_f32_16x16x32_bf16 v[64:67], a[0:3], a[68:71], v[64:67]\n"
    "ds_read_b64_tr_b16 a[32:33], v8 offset:8192\n"
    "v_mfma_f32_16x16x32_bf16 v[96:99], a[0:3], a[72:75], v[96:99]\n"
    "buffer_load_dwordx4 v28, s[52:55], s69 offen offset:0 lds\n"
    "ds_read_b64_tr_b16 a[34:35], v8 offset:9216\n"
    "v_mfma_f32_16x16x32_bf16 v[128:131], a[0:3], a[76:79], v[128:131]\n"
    "s_add_u32 s76, s69, 15360\n"
    "v_mfma_f32_16x16x32_bf16 v[160:163], a[0:3], a[80:83], v[160:163]\n"
    "ds_read_b64_tr_b16 a[36:37], v9 offset:8192\n"
    "v_mfma_f32_16x16x32_bf16 v[192:195], a[0:3], a[84:87], v[192:195]\n"
    "buffer_load_dwordx4 v28, s[52:55], s76 offen offset:1024 lds\n"
    "v_mfma_f32_16x16x32_bf16 v[224:227], a[0:3], a[88:91], v[224:227]\n"
    "s_add_u32 s76, s69, 30720\n"
    "v_mfma_f32_16x16x32_bf16 v[36:39], a[4:7], a[64:67], v[36:39]\n"
    "ds_read_b64_tr_b16 a[38:39], v9 offset:9216\n"
    "buffer_load_dwordx4 v28, s[52:55], s76 offen offset:2048 lds\n"
    "v_mfma_f32_16x16x32_bf16 v[68:71], a[4:7], a[68:71], v[68:71]\n"
    "ds_read_b64_tr_b16 a[40:41], v10 offset:8192\n"
    "v_mfma_f32_16x16x32_bf16 v[100:103], a[4:7], a[72:75], v[100:103]\n"
    "s_add_u32 s76, s69, 46080\n"
    "v_mfma_f32_16x16x32_bf16 v[132:135], a[4:7], a[76:79], v[132:135]\n"
    "buffer_load_dwordx4 v28, s[52:55], s76 offen offset:3072 lds\n"
    "v_mfma_f32_16x16x32_bf16 v[164:167], a[4:7], a[80:83], v[164:167]\n"
    "ds_read_b64_tr_b16 a[42:43], v10 offset:9216\n"
    "v_mfma_f32_16x16x32_bf16 v[196:199], a[4:7], a[84:87], v[196:199]\n"
    "s_add_u32 m0, s65, 4096\n"
    "ds_read_b64_tr_b16 a[44:45], v11 offset:8192\n"
    "v_mfma_f32_16x16x32_bf16 v[228:231], a[4:7], a[88:91], v[228:231]\n"
    "s_add_u32 s76, s69, 65536\n"
    "v_mfma_f32_16x16x32_bf16 v[40:43], a[8:11], a[64:67], v[40:43]\n"
    "buffer_load_dwordx4 v28, s[52:55], s76 offen offset:0 lds\n"
    "v_mfma_f32_16x16x32_bf16 v[72:75], a[8:11], a[68:71], v[72:75]\n"
    "ds_read_b64_tr_b16 a[46:47], v11 offset:9216\n"
    "v_mfma_f32_16x16x32_bf16 v[104:107], a[8:11], a[72:75], v[104:107]\n"
    "s_add_u32 s76, s69, 80896\n"
    "v_mfma_f32_16x16x32_bf16 v[136:139], a[8:11], a[76:79], v[136:139]\n"
    "ds_read_b64_tr_b16 a[48:49], v12 offset:8192\n"
    "buffer_load_dwordx4 v28, s[52:55], s76 offen offset:1024 lds\n"
    "v_mfma_f32_16x16x32_bf16 v[168:171], a[8:11], a[80:83], v[168:171]\n"
    "s_add_u32 s76, s69, 96256\n"
    "v_mfma_f32_16x16x32_bf16 v[200:203], a[8:11], a[84:87], v[200:203]\n"
    "ds_read_b64_tr_b16 a[50:51], v12 offset:9216\n"
    "v_mfma_f32_16x16x32_bf16 v[232:235], a[8:11], a[88:91], v[232:235]\n"
    "buffer_load_dwordx4 v28, s[52:55], s76 offen offset:2048 lds\n"
    "v_mfma_f32_16x16x32_bf16 v[44:47], a[12:15], a[64:67], v[44:47]\n"
    "ds_read_b64_tr_b16 a[52:53], v13 offset:8192\n"
    "v_mfma_f32_16x16x32_bf16 v[76:79], a[12:15], a[68:71], v[76:79]\n"
    "s_add_u32 s69, s69, 64\n"
    "s_add_u32 m0, s64, 24576\n"
    "v_mfma_f32_16x16x32_bf16 v[108:111], a[12:15], a[72:75], v[108:111]\n"
    "ds_read_b64_tr_b16 a[54:55], v13 offset:9216\n"
    "v_mfma_f32_16x16x32_bf16 v[140:143], a[12:15], a[76:79], v[140:143]\n"
    "buffer_load_dwordx4 v26, s[60:63], s68 offen offset:0 lds\n"
    "v_mfma_f32_16x16x32_bf16 v[172:175], a[12:15], a[80:83], v[172:175]\n"
    "ds_read_b64_tr_b16 a[56:57], v14 offset:8192\n"
    "v_mfma_f32_16x16x32_bf16 v[204:207], a[12:15], a[84:87], v[204:207]\n"
    "s_add_u32 s74, s68, 31744\n"
    "v_mfma_f32_16x16x32_bf16 v[236:239], a[12:15], a[88:91], v[236:239]\n"
    "buffer_load_dwordx4 v26, s[60:63], s74 offen offset:1024 lds\n"
    "ds_read_b64_tr_b16 a[58:59], v14 offset:9216\n"
    "v_mfma_f32_16x16x32_bf16 v[48:51], a[16:19], a[64:67], v[48:51]\n"
    "s_add_u32 s74, s68, 63488\n"
    "v_mfma_f32_16x16x32_bf16 v[80:83], a[16:19], a[68:71], v[80:83]\n"
    "ds_read_b64_tr_b16 a[60:61], v15 offset:8192\n"
    "v_mfma_f32_16x16x32_bf16 v[112:115], a[16:19], a[72:75], v[112:115]\n"
    "buffer_load_dwordx4 v27, s[60:63], s74 offen offset:2048 lds\n"
    "v_mfma_f32_16x16x32_bf16 v[144:147], a[16:19], a[76:79], v[144:147]\n"
    "s_add_u32 s74, s68, 95232\n"
    "v_mfma_f32_16x16x32_bf16 v[176:179], a[16:19], a[80:83], v[176:179]\n"
    "ds_read_b64_tr_b16 a[62:63], v15 offset:9216\n"
    "buffer_load_dwordx4 v27, s[60:63], s74 offen offset:3072 lds\n"
    "v_mfma_f32_16x16x32_bf16 v[208:211], a[16:19], a[84:87], v[208:211]\n"
    "ds_read_b128 a[92:95], v6 offset:8192\n"
    "v_mfma_f32_16x16x32_bf16 v[240:243], a[16:19], a[88:91], v[240:243]\n"
    "s_add_u32 m0, s64, 28672\n"
    "v_mfma_f32_16x16x32_bf16 v[52:55], a[20:23], a[64:67], v[52:55]\n"
    "s_add_u32 s74, s68, 131072\n"
    "v_mfma_f32_16x16x32_bf16 v[84:87], a[20:23], a[68:71], v[84:87]\n"
    "ds_read_b128 a[96:99], v6 offset:9216\n"
    "v_mfma_f32_16x16x32_bf16 v[116:119], a[20:23], a[72:75], v[116:119]\n"
    "buffer_load_dwordx4 v26, s[60:63], s74 offen offset:0 lds\n"
    "ds_read_b128 a[100:103], v6 offset:10240\n"
    "v_mfma_f32_16x16x32_bf16 v[148:151], a[20:23], a[76:79], v[148:151]\n"
    "s_add_u32 s74, s68, 162816\n"
    "v_mfma_f32_16x16x32_bf16 v[180:183], a[20:23], a[80:83], v[180:183]\n"
    "buffer_load_dwordx4 v26, s[60:63], s74 offen offset:1024 lds\n"
    "v_mfma_f32_16x16x32_bf16 v[212:215], a[20:23], a[84:87], v[212:215]\n"
    "ds_read_b128 a[104:107], v6 offset:11264\n"
    "v_mfma_f32_16x16x32_bf16 v[244:247], a[20:23], a[88:91], v[244:247]\n"
    "s_add_u32 s74, s68, 194560\n"
    "v_mfma_f32_16x16x32_bf16 v[56:59], a[24:27], a[64:67], v[56:59]\n"
    "ds_read_b128 a[108:111], v6 offset:12288\n"
    "v_mfma_f32_16x16x32_bf16 v[88:91], a[24:27], a[68:71], v[88:91]\n"
    "buffer_load_dwordx4 v27, s[60:63], s74 offen offset:2048 lds\n"
    "s_add_u32 s74, s68, 226304\n"
    "v_mfma_f32_16x16x32_bf16 v[120:123], a[24:27], a[72:75], v[120:123]\n"
    "ds_read_b128 a[112:115], v6 offset:13312\n"
    "v_mfma_f32_16x16x32_bf16 v[152:155], a[24:27], a[76:79], v[152:155]\n"
    "buffer_load_dwordx4 v27, s[60:63], s74 offen offset:3072 lds\n"
    "v_mfma_f32_16x16x32_bf16 v[184:187], a[24:27], a[80:83], v[184:187]\n"
    "ds_read_b128 a[116:119], v6 offset:14336\n"
    "v_mfma_f32_16x16x32_bf16 v[216:219], a[24:27], a[84:87], v[216:219]\n"
    "s_add_u32 s68, s68, 262144\n"
    "v_mfma_f32_16x16x32_bf16 v[248:251], a[24:27], a[88:91], v[248:251]\n"
    "v_mfma_f32_16x16x32_bf16 v[60:63], a[28:31], a[64:67], v[60:63]\n"
    "v_mfma_f32_16x16x32_bf16 v[92:95], a[28:31], a[68:71], v[92:95]\n"
    "v_mfma_f32_16x16x32_bf16 v[124:127], a[28:31], a[72:75], v[124:127]\n"
    "v_mfma_f32_16x16x32_bf16 v[156:159], a[28:31], a[76:79], v[156:159]\n"
    "v_mfma_f32_16x16x32_bf16 v[188:191], a[28:31], a[80:83], v[188:191]\n"
    "v_mfma_f32_16x16x32_bf16 v[220:223], a[28:31], a[84:87], v[220:223]\n"
    "v_mfma_f32_16x16x32_bf16 v[252:255], a[28:31], a[88:91], v[252:255]\n"
    "s_waitcnt lgkmcnt(0)\n"
    "s_sleep 20\n"
    "s_waitcnt vmcnt(15)\n"
    "v_mfma_f32_16x16x32_bf16 v[32:35], a[32:35], a[92:95], v[32:35]\n"
    "s_add_u32 m0, s65, 8192\n"
    "v_mfma_f32_16x16x32_bf16 v[64:67], a[32:35], a[96:99], v[64:67]\n"
    "ds_read_b64_tr_b16 a[0:1], v8 offset:16384\n"
    "v_mfma_f32_16x16x32_bf16 v[96:99], a[32:35], a[100:103], v[96:99]\n"
    "buffer_load_dwordx4 v28, s[52:55], s69 offen offset:0 lds\n"
    "ds_read_b64_tr_b16 a[2:3], v8 offset:17408\n"
    "v_mfma_f32_16x16x32_bf16 v[128:131], a[32:35], a[104:107], v[128:131]\n"
    "s_add_u32 s76, s69, 15360\n"
    "v_mfma_f32_16x16x32_bf16 v[160:163], a[32:35], a[108:111], v[160:163]\n"
    "ds_read_b64_tr_b16 a[4:5], v9 offset:16384\n"
    "v_mfma_f32_16x16x32_bf16 v[192:195], a[32:35], a[112:115], v[192:195]\n"
    "buffer_load_dwordx4 v28, s[52:55], s76 offen offset:1024 lds\n"
    "v_mfma_f32_16x16x32_bf16 v[224:227], a[32:35], a[116:119], v[224:227]\n"
    "s_add_u32 s76, s69, 30720\n"
    "v_mfma_f32_16x16x32_bf16 v[36:39], a[36:39], a[92:95], v[36:39]\n"
    "ds_read_b64_tr_b16 a[6:7], v9 offset:17408\n"
    "buffer_load_dwordx4 v28, s[52:55], s76 offen offset:2048 lds\n"
    "v_mfma_f32_16x16x32_bf16 v[68:71], a[36:39], a[96:99], v[68:71]\n"
    "ds_read_b64_tr_b16 a[8:9], v10 offset:16384\n"
    "v_mfma_f32_16x16x32_bf16 v[100:103], a[36:39], a[100:103], v[100:103]\n"
    "s_add_u32 s76, s69, 46080\n"
    "v_mfma_f32_16x16x32_bf16 v[132:135], a[36:39], a[104:107], v[132:135]\n"
    "buffer_load_dwordx4 v28, s[52:55], s76 offen offset:3072 lds\n"
    "v_mfma_f32_16x16x32_bf16 v[164:167], a[36:39], a[108:111], v[164:167]\n"
    "ds_read_b64_tr_b16 a[10:11], v10 offset:17408\n"
    "v_mfma_f32_16x16x32_bf16 v[196:199], a[36:39], a[112:115], v[196:199]\n"
    "s_add_u32 m0, s65, 12288\n"
    "ds_read_b64_tr_b16 a[12:13], v11 offset:16384\n"
    "v_mfma_f32_16x16x32_bf16 v[228:231], a[36:39], a[116:119], v[228:231]\n"
    "s_add_u32 s76, s69, 65536\n"
    "v_mfma_f32_16x16x32_bf16 v[40:43], a[40:43], a[92:95], v[40:43]\n"
    "buffer_load_dwordx4 v28, s[52:55], s76 offen offset:0 lds\n"
    "v_mfma_f32_16x16x32_bf16 v[72:75], a[40:43], a[96:99], v[72:75]\n"
    "ds_read_b64_tr_b16 a[14:15], v11 offset:17408\n"
    "v_mfma_f32_16x16x32_bf16 v[104:107], a[40:43], a[100:103], v[104:107]\n"
    "s_add_u32 s76, s69, 80896\n"
    "v_mfma_f32_16x16x32_bf16 v[136:139], a[40:43], a[104:107], v[136:139]\n"
    "ds_read_b64_tr_b16 a[16:17], v12 offset:16384\n"
    "buffer_load_dwordx4 v28, s[52:55], s76 offen offset:1024 lds\n"
    "v_mfma_f32_16x16x32_bf16 v[168:171], a[40:43], a[108:111], v[168:171]\n"
    "s_add_u32 s76, s69, 96256\n"
    "v_mfma_f32_16x16x32_bf16 v[200:203], a[40:43], a[112:115], v[200:203]\n"
    "ds_read_b64_tr_b16 a[18:19], v12 offset:17408\n"
    "v_mfma_f32_16x16x32_bf16 v[232:235], a[40:43], a[116:119], v[232:235]\n"
    "buffer_load_dwordx4 v28, s[52:55], s76 offen offset:2048 lds\n"
    "v_mfma_f32_16x16x32_bf16 v[44:47], a[44:47], a[92:95], v[44:47]\n"
    "ds_read_b64_tr_b16 a[20:21], v13 offset:16384\n"
    "v_mfma_f32_16x16x32_bf16 v[76:79], a[44:47], a[96:99], v[76:79]\n"
    "s_add_u32 s69, s69, 64\n"
    "s_add_u32 m0, s64, 8192\n"
    "v_mfma_f32_16x16x32_bf16 v[108:111], a[44:47], a[100:103], v[108:111]\n"
    "ds_read_b64_tr_b16 a[22:23], v13 offset:17408\n"
    "v_mfma_f32_16x16x32_bf16 v[140:143], a[44:47], a[104:107], v[140:143]\n"
    "buffer_load_dwordx4 v26, s[60:63], s68 offen offset:0 lds\n"
    "v_mfma_f32_16x16x32_bf16 v[172:175], a[44:47], a[108:111], v[172:175]\n"
    "ds_read_b64_tr_b16 a[24:25], v14 offset:16384\n"
    "v_mfma_f32_16x16x32_bf16 v[204:207], a[44:47], a[112:115], v[204:207]\n"
    "s_add_u32 s74, s68, 31744\n"
    "v_mfma_f32_16x16x32_bf16 v[236:239], a[44:47], a[116:119], v[236:239]\n"
    "buffer_load_dwordx4 v26, s[60:63], s74 offen offset:1024 lds\n"
    "ds_read_b64_tr_b16 a[26:27], v14 offset:17408\n"
    "v_mfma_f32_16x16x32_bf16 v[48:51], a[48:51], a[92:95], v[48:51]\n"
    "s_add_u32 s74, s68, 63488\n"
    "v_mfma_f32_16x16x32_bf16 v[80:83], a[48:51], a[96:99], v[80:83]\n"
    "ds_read_b64_tr_b16 a[28:29], v15 offset:16384\n"
    "v_mfma_f32_16x16x32_bf16 v[112:115], a[48:51], a[100:103], v[112:115]\n"
    "buffer_load_dwordx4 v27, s[60:63], s74 offen offset:2048 lds\n"
    "v_mfma_f32_16x16x32_bf16 v[144:147], a[48:51], a[104:107], v[144:147]\n"
    "s_add_u32 s74, s68, 95232\n"
    "v_mfma_f32_16x16x32_bf16 v[176:179], a[48:51], a[108:111], v[176:179]\n"
    "ds_read_b64_tr_b16 a[30:31], v15 offset:17408\n"
    "buffer_load_dwordx4 v27, s[60:63], s74 offen offset:3072 lds\n"
    "v_mfma_f32_16x16x32_bf16 v[208:211], a[48:51], a[112:115], v[208:211]\n"
    "ds_read_b128 a[64:67], v7 offset:0\n"
    "v_mfma_f32_16x16x32_bf16 v[240:243], a[48:51], a[116:119], v[240:243]\n"
    "s_add_u32 m0, s64, 12288\n"
    "v_mfma_f32_16x16x32_bf16 v[52:55], a[52:55], a[92:95], v[52:55]\n"
    "s_add_u32 s74, s68, 131072\n"
    "v_mfma_f32_16x16x32_bf16 v[84:87], a[52:55], a[96:99], v[84:87]\n"
    "ds_read_b128 a[68:71], v7 offset:1024\n"
    "v_mfma_f32_16x16x32_bf16 v[116:119], a[52:55], a[100:103], v[116:119]\n"
    "buffer_load_dwordx4 v26, s[60:63], s74 offen offset:0 lds\n"
    "ds_read_b128 a[72:75], v7 offset:2048\n"
    "v_mfma_f32_16x16x32_bf16 v[148:151], a[52:55], a[104:107], v[148:151]\n"
    "s_add_u32 s74, s68, 162816\n"
    "v_mfma_f32_16x16x32_bf16 v[180:183], a[52:55], a[108:111], v[180:183]\n"
    "buffer_load_dwordx4 v26, s[60:63], s74 offen offset:1024 lds\n"
    "v_mfma_f32_16x16x32_bf16 v[212:215], a[52:55], a[112:115], v[212:215]\n"
    "ds_read_b128 a[76:79], v7 offset:3072\n"
    "v_mfma_f32_16x16x32_bf16 v[244:247], a[52:55], a[116:119], v[244:247]\n"
    "s_add_u32 s74, s68, 194560\n"
    "v_mfma_f32_16x16x32_bf16 v[56:59], a[56:59], a[92:95], v[56:59]\n"
    "ds_read_b128 a[80:83], v7 offset:4096\n"
    "v_mfma_f32_16x16x32_bf16 v[88:91], a[56:59], a[96:99], v[88:91]\n"
    "buffer_load_dwordx4 v27, s[60:63], s74 offen offset:2048 lds\n"
    "s_add_u32 s74, s68, 226304\n"
    "v_mfma_f32_16x16x32_bf16 v[120:123], a[56:59], a[100:103], v[120:123]\n"
    "ds_read_b128 a[84:87], v7 offset:5120\n"
    "v_mfma_f32_16x16x32_bf16 v[152:155], a[56:59], a[104:107], v[152:155]\n"
    "buffer_load_dwordx4 v27, s[60:63], s74 offen offset:3072 lds\n"
    "v_mfma_f32_16x16x32_bf16 v[184:187], a[56:59], a[108:111], v[184:187]\n"
    "ds_read_b128 a[88:91], v7 offset:6144\n"
    "v_mfma_f32_16x16x32_bf16 v[216:219], a[56:59], a[112:115], v[216:219]\n"
    "s_add_u32 s68, s68, 262144\n"
    "v_mfma_f32_16x16x32_bf16 v[248:251], a[56:59], a[116:119], v[248:251]\n"
    "v_mfma_f32_16x16x32_bf16 v[60:63], a[60:63], a[92:95], v[60:63]\n"
    "v_mfma_f32_16x16x32_bf16 v[92:95], a[60:63], a[96:99], v[92:95]\n"
    "v_mfma_f32_16x16x32_bf16 v[124:127], a[60:63], a[100:103], v[124:127]\n"
    "v_mfma_f32_16x16x32_bf16 v[156:159], a[60:63], a[104:107], v[156:159]\n"
    "v_mfma_f32_16x16x32_bf16 v[188:191], a[60:63], a[108:111], v[188:191]\n"
    "v_mfma_f32_16x16x32_bf16 v[220:223], a[60:63], a[112:115], v[220:223]\n"
    "v_mfma_f32_16x16x32_bf16 v[252:255], a[60:63], a[116:119], v[252:255]\n"
    "s_waitcnt lgkmcnt(0)\n"
    "s_sleep 20\n"
    "s_waitcnt vmcnt(15)\n"
    "v_mfma_f32_16x16x32_bf16 v[32:35], a[0:3], a[64:67], v[32:35]\n"
    "s_add_u32 m0, s64, 0\n"
    "v_mfma_f32_16x16x32_bf16 v[64:67], a[0:3], a[68:71], v[64:67]\n"
    "ds_read_b64_tr_b16 a[32:33], v8 offset:24576\n"
    "v_mfma_f32_16x16x32_bf16 v[96:99], a[0:3], a[72:75], v[96:99]\n"
    "buffer_load_dwordx4 v28, s[52:55], s69 offen offset:0 lds\n"
    "ds_read_b64_tr_b16 a[34:35], v8 offset:25600\n"
    "v_mfma_f32_16x16x32_bf16 v[128:131], a[0:3], a[76:79], v[128:131]\n"
    "s_add_u32 s76, s69, 15360\n"
    "v_mfma_f32_16x16x32_bf16 v[160:163], a[0:3], a[80:83], v[160:163]\n"
    "ds_read_b64_tr_b16 a[36:37], v9 offset:24576\n"
    "v_mfma_f32_16x16x32_bf16 v[192:195], a[0:3], a[84:87], v[192:195]\n"
    "buffer_load_dwordx4 v28, s[52:55], s76 offen offset:1024 lds\n"
    "v_mfma_f32_16x16x32_bf16 v[224:227], a[0:3], a[88:91], v[224:227]\n"
    "s_add_u32 s76, s69, 30720\n"
    "v_mfma_f32_16x16x32_bf16 v[36:39], a[4:7], a[64:67], v[36:39]\n"
    "ds_read_b64_tr_b16 a[38:39], v9 offset:25600\n"
    "buffer_load_dwordx4 v28, s[52:55], s76 offen offset:2048 lds\n"
    "v_mfma_f32_16x16x32_bf16 v[68:71], a[4:7], a[68:71], v[68:71]\n"
    "ds_read_b64_tr_b16 a[40:41], v10 offset:24576\n"
    "v_mfma_f32_16x16x32_bf16 v[100:103], a[4:7], a[72:75], v[100:103]\n"
    "s_add_u32 s76, s69, 46080\n"
    "v_mfma_f32_16x16x32_bf16 v[132:135], a[4:7], a[76:79], v[132:135]\n"
    "buffer_load_dwordx4 v28, s[52:55], s76 offen offset:3072 lds\n"
    "v_mfma_f32_16x16x32_bf16 v[164:167], a[4:7], a[80:83], v[164:167]\n"
    "ds_read_b64_tr_b16 a[42:43], v10 offset:25600\n"
    "v_mfma_f32_16x16x32_bf16 v[196:199], a[4:7], a[84:87], v[196:199]\n"
    "s_add_u32 m0, s64, 4096\n"
    "ds_read_b64_tr_b16 a[44:45], v11 offset:24576\n"
    "v_mfma_f32_16x16x32_bf16 v[228:231], a[4:7], a[88:91], v[228:231]\n"
    "s_add_u32 s76, s69, 65536\n"
    "v_mfma_f32_16x16x32_bf16 v[40:43], a[8:11], a[64:67], v[40:43]\n"
    "buffer_load_dwordx4 v28, s[52:55], s76 offen offset:0 lds\n"
    "v_mfma_f32_16x16x32_bf16 v[72:75], a[8:11], a[68:71], v[72:75]\n"
    "ds_read_b64_tr_b16 a[46:47], v11 offset:25600\n"
    "v_mfma_f32_16x16x32_bf16 v[104:107], a[8:11], a[72:75], v[104:107]\n"
    "s_add_u32 s76, s69, 80896\n"
    "v_mfma_f32_16x16x32_bf16 v[136:139], a[8:11], a[76:79], v[136:139]\n"
    "ds_read_b64_tr_b16 a[48:49], v12 offset:24576\n"
    "buffer_load_dwordx4 v28, s[52:55], s76 offen offset:1024 lds\n"
    "v_mfma_f32_16x16x32_bf16 v[168:171], a[8:11], a[80:83], v[168:171]\n"
    "s_add_u32 s76, s69, 96256\n"
    "v_mfma_f32_16x16x32_bf16 v[200:203], a[8:11], a[84:87], v[200:203]\n"
    "ds_read_b64_tr_b16 a[50:51], v12 offset:25600\n"
    "v_mfma_f32_16x16x32_bf16 v[232:235], a[8:11], a[88:91], v[232:235]\n"
    "buffer_load_dwordx4 v28, s[52:55], s76 offen offset:2048 lds\n"
    "v_mfma_f32_16x16x32_bf16 v[44:47], a[12:15], a[64:67], v[44:47]\n"
    "ds_read_b64_tr_b16 a[52:53], v13 offset:24576\n"
    "v_mfma_f32_16x16x32_bf16 v[76:79], a[12:15], a[68:71], v[76:79]\n"
    "s_add_u32 s69, s69, 64\n"
    "s_add_u32 m0, s64, 16384\n"
    "v_mfma_f32_16x16x32_bf16 v[108:111], a[12:15], a[72:75], v[108:111]\n"
    "ds_read_b64_tr_b16 a[54:55], v13 offset:25600\n"
    "v_mfma_f32_16x16x32_bf16 v[140:143], a[12:15], a[76:79], v[140:143]\n"
    "buffer_load_dwordx4 v26, s[60:63], s68 offen offset:0 lds\n"
    "v_mfma_f32_16x16x32_bf16 v[172:175], a[12:15], a[80:83], v[172:175]\n"
    "ds_read_b64_tr_b16 a[56:57], v14 offset:24576\n"
    "v_mfma_f32_16x16x32_bf16 v[204:207], a[12:15], a[84:87], v[204:207]\n"
    "s_add_u32 s74, s68, 31744\n"
    "v_mfma_f32_16x16x32_bf16 v[236:239], a[12:15], a[88:91], v[236:239]\n"
    "buffer_load_dwordx4 v26, s[60:63], s74 offen offset:1024 lds\n"
    "ds_read_b64_tr_b16 a[58:59], v14 offset:25600\n"
    "v_mfma_f32_16x16x32_bf16 v[48:51], a[16:19], a[64:67], v[48:51]\n"
    "s_add_u32 s74, s68, 63488\n"
    "v_mfma_f32_16x16x32_bf16 v[80:83], a[16:19], a[68:71], v[80:83]\n"
    "ds_read_b64_tr_b16 a[60:61], v15 offset:24576\n"
    "v_mfma_f32_16x16x32_bf16 v[112:115], a[16:19], a[72:75], v[112:115]\n"
    "buffer_load_dwordx4 v27, s[60:63], s74 offen offset:2048 lds\n"
    "v_mfma_f32_16x16x32_bf16 v[144:147], a[16:19], a[76:79], v[144:147]\n"
    "s_add_u32 s74, s68, 95232\n"
    "v_mfma_f32_16x16x32_bf16 v[176:179], a[16:19], a[80:83], v[176:179]\n"
    "ds_read_b64_tr_b16 a[62:63], v15 offset:25600\n"
    "buffer_load_dwordx4 v27, s[60:63], s74 offen offset:3072 lds\n"
    "v_mfma_f32_16x16x32_bf16 v[208:211], a[16:19], a[84:87], v[208:211]\n"
    "ds_read_b128 a[92:95], v6 offset:0\n"
    "v_mfma_f32_16x16x32_bf16 v[240:243], a[16:19], a[88:91], v[240:243]\n"
    "s_add_u32 m0, s64, 20480\n"
    "v_mfma_f32_16x16x32_bf16 v[52:55], a[20:23], a[64:67], v[52:55]\n"
    "s_add_u32 s74, s68, 131072\n"
    "v_mfma_f32_16x16x32_bf16 v[84:87], a[20:23], a[68:71], v[84:87]\n"
    "ds_read_b128 a[96:99], v6 offset:1024\n"
    "v_mfma_f32_16x16x32_bf16 v[116:119], a[20:23], a[72:75], v[116:119]\n"
    "buffer_load_dwordx4 v26, s[60:63], s74 offen offset:0 lds\n"
    "ds_read_b128 a[100:103], v6 offset:2048\n"
    "v_mfma_f32_16x16x32_bf16 v[148:151], a[20:23], a[76:79], v[148:151]\n"
    "s_add_u32 s74, s68, 162816\n"
    "v_mfma_f32_16x16x32_bf16 v[180:183], a[20:23], a[80:83], v[180:183]\n"
    "buffer_load_dwordx4 v26, s[60:63], s74 offen offset:1024 lds\n"
    "v_mfma_f32_16x16x32_bf16 v[212:215], a[20:23], a[84:87], v[212:215]\n"
    "ds_read_b128 a[104:107], v6 offset:3072\n"
    "v_mfma_f32_16x16x32_bf16 v[244:247], a[20:23], a[88:91], v[244:247]\n"
    "s_add_u32 s74, s68, 194560\n"
    "v_mfma_f32_16x16x32_bf16 v[56:59], a[24:27], a[64:67], v[56:59]\n"
    "ds_read_b128 a[108:111], v6 offset:4096\n"
    "v_mfma_f32_16x16x32_bf16 v[88:91], a[24:27], a[68:71], v[88:91]\n"
    "buffer_load_dwordx4 v27, s[60:63], s74 offen offset:2048 lds\n"
    "s_add_u32 s74, s68, 226304\n"
    "v_mfma_f32_16x16x32_bf16 v[120:123], a[24:27], a[72:75], v[120:123]\n"
    "ds_read_b128 a[112:115], v6 offset:5120\n"
    "v_mfma_f32_16x16x32_bf16 v[152:155], a[24:27], a[76:79], v[152:155]\n"
    "buffer_load_dwordx4 v27, s[60:63], s74 offen offset:3072 lds\n"
    "v_mfma_f32_16x16x32_bf16 v[184:187], a[24:27], a[80:83], v[184:187]\n"
    "ds_read_b128 a[116:119], v6 offset:6144\n"
    "v_mfma_f32_16x16x32_bf16 v[216:219], a[24:27], a[84:87], v[216:219]\n"
    "s_add_u32 s68, s68, 262144\n"
    "v_mfma_f32_16x16x32_bf16 v[248:251], a[24:27], a[88:91], v[248:251]\n"
    "v_mfma_f32_16x16x32_bf16 v[60:63], a[28:31], a[64:67], v[60:63]\n"
    "v_mfma_f32_16x16x32_bf16 v[92:95], a[28:31], a[68:71], v[92:95]\n"
    "v_mfma_f32_16x16x32_bf16 v[124:127], a[28:31], a[72:75], v[124:127]\n"
    "v_mfma_f32_16x16x32_bf16 v[156:159], a[28:31], a[76:79], v[156:159]\n"
    "v_mfma_f32_16x16x32_bf16 v[188:191], a[28:31], a[80:83], v[188:191]\n"
    "v_mfma_f32_16x16x32_bf16 v[220:223], a[28:31], a[84:87], v[220:223]\n"
    "v_mfma_f32_16x16x32_bf16 v[252:255], a[28:31], a[88:91], v[252:255]\n"
    "s_waitcnt lgkmcnt(0)\n"
    "s_sleep 20\n"
    "s_waitcnt vmcnt(15)\n"
    "v_mfma_f32_16x16x32_bf16 v[32:35], a[32:35], a[92:95], v[32:35]\n"
    "s_add_u32 m0, s65, 0\n"
    "v_mfma_f32_16x16x32_bf16 v[64:67], a[32:35], a[96:99], v[64:67]\n"
    "ds_read_b64_tr_b16 a[0:1], v8 offset:8192\n"
    "v_mfma_f32_16x16x32_bf16 v[96:99], a[32:35], a[100:103], v[96:99]\n"
    "buffer_load_dwordx4 v28, s[52:55], s69 offen offset:0 lds\n"
    "ds_read_b64_tr_b16 a[2:3], v8 offset:9216\n"
    "v_mfma_f32_16x16x32_bf16 v[128:131], a[32:35], a[104:107], v[128:131]\n"
    "s_add_u32 s76, s69, 15360\n"
    "v_mfma_f32_16x16x32_bf16 v[160:163], a[32:35], a[108:111], v[160:163]\n"
    "ds_read_b64_tr_b16 a[4:5], v9 offset:8192\n"
    "v_mfma_f32_16x16x32_bf16 v[192:195], a[32:35], a[112:115], v[192:195]\n"
    "buffer_load_dwordx4 v28, s[52:55], s76 offen offset:1024 lds\n"
    "v_mfma_f32_16x16x32_bf16 v[224:227], a[32:35], a[116:119], v[224:227]\n"
    "s_add_u32 s76, s69, 30720\n"
    "v_mfma_f32_16x16x32_bf16 v[36:39], a[36:39], a[92:95], v[36:39]\n"
    "ds_read_b64_tr_b16 a[6:7], v9 offset:9216\n"
    "buffer_load_dwordx4 v28, s[52:55], s76 offen offset:2048 lds\n"
    "v_mfma_f32_16x16x32_bf16 v[68:71], a[36:39], a[96:99], v[68:71]\n"
    "ds_read_b64_tr_b16 a[8:9], v10 offset:8192\n"
    "v_mfma_f32_16x16x32_bf16 v[100:103], a[36:39], a[100:103], v[100:103]\n"
    "s_add_u32 s76, s69, 46080\n"
    "v_mfma_f32_16x16x32_bf16 v[132:135], a[36:39], a[104:107], v[132:135]\n"
    "buffer_load_dwordx4 v28, s[52:55], s76 offen offset:3072 lds\n"
    "v_mfma_f32_16x16x32_bf16 v[164:167], a[36:39], a[108:111], v[164:167]\n"
    "ds_read_b64_tr_b16 a[10:11], v10 offset:9216\n"
    "v_mfma_f32_16x16x32_bf16 v[196:199], a[36:39], a[112:115], v[196:199]\n"
    "s_add_u32 m0, s65, 4096\n"
    "ds_read_b64_tr_b16 a[12:13], v11 offset:8192\n"
    "v_mfma_f32_16x16x32_bf16 v[228:231], a[36:39], a[116:119], v[228:231]\n"
    "s_add_u32 s76, s69, 65536\n"
    "v_mfma_f32_16x16x32_bf16 v[40:43], a[40:43], a[92:95], v[40:43]\n"
    "buffer_load_dwordx4 v28, s[52:55], s76 offen offset:0 lds\n"
    "v_mfma_f32_16x16x32_bf16 v[72:75], a[40:43], a[96:99], v[72:75]\n"
    "ds_read_b64_tr_b16 a[14:15], v11 offset:9216\n"
    "v_mfma_f32_16x16x32_bf16 v[104:107], a[40:43], a[100:103], v[104:107]\n"
    "s_add_u32 s76, s69, 80896\n"
    "v_mfma_f32_16x16x32_bf16 v[136:139], a[40:43], a[104:107], v[136:139]\n"
    "ds_read_b64_tr_b16 a[16:17], v12 offset:8192\n"
    "buffer_load_dwordx4 v28, s[52:55], s76 offen offset:1024 lds\n"
    "v_mfma_f32_16x16x32_bf16 v[168:171], a[40:43], a[108:111], v[168:171]\n"
    "s_add_u32 s76, s69, 96256\n"
    "v_mfma_f32_16x16x32_bf16 v[200:203], a[40:43], a[112:115], v[200:203]\n"
    "ds_read_b64_tr_b16 a[18:19], v12 offset:9216\n"
    "v_mfma_f32_16x16x32_bf16 v[232:235], a[40:43], a[116:119], v[232:235]\n"
    "buffer_load_dwordx4 v28, s[52:55], s76 offen offset:2048 lds\n"
    "v_mfma_f32_16x16x32_bf16 v[44:47], a[44:47], a[92:95], v[44:47]\n"
    "ds_read_b64_tr_b16 a[20:21], v13 offset:8192\n"
    "v_mfma_f32_16x16x32_bf16 v[76:79], a[44:47], a[96:99], v[76:79]\n"
    "s_add_u32 s69, s69, 64\n"
    "s_add_u32 m0, s64, 24576\n"
    "v_mfma_f32_16x16x32_bf16 v[108:111], a[44:47], a[100:103], v[108:111]\n"
    "ds_read_b64_tr_b16 a[22:23], v13 offset:9216\n"
    "v_mfma_f32_16x16x32_bf16 v[140:143], a[44:47], a[104:107], v[140:143]\n"
    "buffer_load_dwordx4 v26, s[60:63], s68 offen offset:0 lds\n"
    "v_mfma_f32_16x16x32_bf16 v[172:175], a[44:47], a[108:111], v[172:175]\n"
    "ds_read_b64_tr_b16 a[24:25], v14 offset:8192\n"
    "v_mfma_f32_16x16x32_bf16 v[204:207], a[44:47], a[112:115], v[204:207]\n"
    "s_add_u32 s74, s68, 31744\n"
    "v_mfma_f32_16x16x32_bf16 v[236:239], a[44:47], a[116:119], v[236:239]\n"
    "buffer_load_dwordx4 v26, s[60:63], s74 offen offset:1024 lds\n"
    "ds_read_b64_tr_b16 a[26:27], v14 offset:9216\n"
    "v_mfma_f32_16x16x32_bf16 v[48:51], a[48:51], a[92:95], v[48:51]\n"
    "s_add_u32 s74, s68, 63488\n"
    "v_mfma_f32_16x16x32_bf16 v[80:83], a[48:51], a[96:99], v[80:83]\n"
    "ds_read_b64_tr_b16 a[28:29], v15 offset:8192\n"
    "v_mfma_f32_16x16x32_bf16 v[112:115], a[48:51], a[100:103], v[112:115]\n"
    "buffer_load_dwordx4 v27, s[60:63], s74 offen offset:2048 lds\n"
    "v_mfma_f32_16x16x32_bf16 v[144:147], a[48:51], a[104:107], v[144:147]\n"
    "s_add_u32 s74, s68, 95232\n"
    "v_mfma_f32_16x16x32_bf16 v[176:179], a[48:51], a[108:111], v[176:179]\n"
    "ds_read_b64_tr_b16 a[30:31], v15 offset:9216\n"
    "buffer_load_dwordx4 v27, s[60:63], s74 offen offset:3072 lds\n"
    "v_mfma_f32_16x16x32_bf16 v[208:211], a[48:51], a[112:115], v[208:211]\n"
    "ds_read_b128 a[64:67], v6 offset:8192\n"
    "v_mfma_f32_16x16x32_bf16 v[240:243], a[48:51], a[116:119], v[240:243]\n"
    "s_add_u32 m0, s64, 28672\n"
    "v_mfma_f32_16x16x32_bf16 v[52:55], a[52:55], a[92:95], v[52:55]\n"
    "s_add_u32 s74, s68, 131072\n"
    "v_mfma_f32_16x16x32_bf16 v[84:87], a[52:55], a[96:99], v[84:87]\n"
    "ds_read_b128 a[68:71], v6 offset:9216\n"
    "v_mfma_f32_16x16x32_bf16 v[116:119], a[52:55], a[100:103], v[116:119]\n"
    "buffer_load_dwordx4 v26, s[60:63], s74 offen offset:0 lds\n"
    "ds_read_b128 a[72:75], v6 offset:10240\n"
    "v_mfma_f32_16x16x32_bf16 v[148:151], a[52:55], a[104:107], v[148:151]\n"
    "s_add_u32 s74, s68, 162816\n"
    "v_mfma_f32_16x16x32_bf16 v[180:183], a[52:55], a[108:111], v[180:183]\n"
    "buffer_load_dwordx4 v26, s[60:63], s74 offen offset:1024 lds\n"
    "v_mfma_f32_16x16x32_bf16 v[212:215], a[52:55], a[112:115], v[212:215]\n"
    "ds_read_b128 a[76:79], v6 offset:11264\n"
    "v_mfma_f32_16x16x32_bf16 v[244:247], a[52:55], a[116:119], v[244:247]\n"
    "s_add_u32 s74, s68, 194560\n"
    "v_mfma_f32_16x16x32_bf16 v[56:59], a[56:59], a[92:95], v[56:59]\n"
    "ds_read_b128 a[80:83], v6 offset:12288\n"
    "v_mfma_f32_16x16x32_bf16 v[88:91], a[56:59], a[96:99], v[88:91]\n"
    "buffer_load_dwordx4 v27, s[60:63], s74 offen offset:2048 lds\n"
    "s_add_u32 s74, s68, 226304\n"
    "v_mfma_f32_16x16x32_bf16 v[120:123], a[56:59], a[100:103], v[120:123]\n"
    "ds_read_b128 a[84:87], v6 offset:13312\n"
    "v_mfma_f32_16x16x32_bf16 v[152:155], a[56:59], a[104:107], v[152:155]\n"
    "buffer_load_dwordx4 v27, s[60:63], s74 offen offset:3072 lds\n"
    "v_mfma_f32_16x16x32_bf16 v[184:187], a[56:59], a[108:111], v[184:187]\n"
    "ds_read_b128 a[88:91], v6 offset:14336\n"
    "v_mfma_f32_16x16x32_bf16 v[216:219], a[56:59], a[112:115], v[216:219]\n"
    "s_add_u32 s68, s68, 262144\n"
    "v_mfma_f32_16x16x32_bf16 v[248:251], a[56:59], a[116:119], v[248:251]\n"
    "v_mfma_f32_16x16x32_bf16 v[60:63], a[60:63], a[92:95], v[60:63]\n"
    "v_mfma_f32_16x16x32_bf16 v[92:95], a[60:63], a[96:99], v[92:95]\n"
    "v_mfma_f32_16x16x32_bf16 v[124:127], a[60:63], a[100:103], v[124:127]\n"
    "v_mfma_f32_16x16x32_bf16 v[156:159], a[60:63], a[104:107], v[156:159]\n"
    "v_mfma_f32_16x16x32_bf16 v[188:191], a[60:63], a[108:111], v[188:191]\n"
    "v_mfma_f32_16x16x32_bf16 v[220:223], a[60:63], a[112:115], v[220:223]\n"
    "v_mfma_f32_16x16x32_bf16 v[252:255], a[60:63], a[116:119], v[252:255]\n"
    "s_waitcnt lgkmcnt(0)\n"
    "s_sleep 20\n"
    "s_waitcnt vmcnt(15)\n"
    "v_mfma_f32_16x16x32_bf16 v[32:35], a[0:3], a[64:67], v[32:35]\n"
    "s_add_u32 m0, s65, 8192\n"
    "v_mfma_f32_16x16x32_bf16 v[64:67], a[0:3], a[68:71], v[64:67]\n"
    "ds_read_b64_tr_b16 a[32:33], v8 offset:16384\n"
    "v_mfma_f32_16x16x32_bf16 v[96:99], a[0:3], a[72:75], v[96:99]\n"
    "buffer_load_dwordx4 v28, s[52:55], s69 offen offset:0 lds\n"
    "ds_read_b64_tr_b16 a[34:35], v8 offset:17408\n"
    "v_mfma_f32_16x16x32_bf16 v[128:131], a[0:3], a[76:79], v[128:131]\n"
    "s_add_u32 s76, s69, 15360\n"
    "v_mfma_f32_16x16x32_bf16 v[160:163], a[0:3], a[80:83], v[160:163]\n"
    "ds_read_b64_tr_b16 a[36:37], v9 offset:16384\n"
    "v_mfma_f32_16x16x32_bf16 v[192:195], a[0:3], a[84:87], v[192:195]\n"
    "buffer_load_dwordx4 v28, s[52:55], s76 offen offset:1024 lds\n"
    "v_mfma_f32_16x16x32_bf16 v[224:227], a[0:3], a[88:91], v[224:227]\n"
    "s_add_u32 s76, s69, 30720\n"
    "v_mfma_f32_16x16x32_bf16 v[36:39], a[4:7], a[64:67], v[36:39]\n"
    "ds_read_b64_tr_b16 a[38:39], v9 offset:17408\n"
    "buffer_load_dwordx4 v28, s[52:55], s76 offen offset:2048 lds\n"
    "v_mfma_f32_16x16x32_bf16 v[68:71], a[4:7], a[68:71], v[68:71]\n"
    "ds_read_b64_tr_b16 a[40:41], v10 offset:16384\n"
    "v_mfma_f32_16x16x32_bf16 v[100:103], a[4:7], a[72:75], v[100:103]\n"
    "s_add_u32 s76, s69, 46080\n"
    "v_mfma_f32_16x16x32_bf16 v[132:135], a[4:7], a[76:79], v[132:135]\n"
    "buffer_load_dwordx4 v28, s[52:55], s76 offen offset:3072 lds\n"
    "v_mfma_f32_16x16x32_bf16 v[164:167], a[4:7], a[80:83], v[164:167]\n"
    "ds_read_b64_tr_b16 a[42:43], v10 offset:17408\n"
    "v_mfma_f32_16x16x32_bf16 v[196:199], a[4:7], a[84:87], v[196:199]\n"
    "s_add_u32 m0, s65, 12288\n"
    "ds_read_b64_tr_b16 a[44:45], v11 offset:16384\n"
    "v_mfma_f32_16x16x32_bf16 v[228:231], a[4:7], a[88:91], v[228:231]\n"
    "s_add_u32 s76, s69, 65536\n"
    "v_mfma_f32_16x16x32_bf16 v[40:43], a[8:11], a[64:67], v[40:43]\n"
    "buffer_load_dwordx4 v28, s[52:55], s76 offen offset:0 lds\n"
    "v_mfma_f32_16x16x32_bf16 v[72:75], a[8:11], a[68:71], v[72:75]\n"
    "ds_read_b64_tr_b16 a[46:47], v11 offset:17408\n"
    "v_mfma_f32_16x16x32_bf16 v[104:107], a[8:11], a[72:75], v[104:107]\n"
    "s_add_u32 s76, s69, 80896\n"
    "v_mfma_f32_16x16x32_bf16 v[136:139], a[8:11], a[76:79], v[136:139]\n"
    "ds_read_b64_tr_b16 a[48:49], v12 offset:16384\n"
    "buffer_load_dwordx4 v28, s[52:55], s76 offen offset:1024 lds\n"
    "v_mfma_f32_16x16x32_bf16 v[168:171], a[8:11], a[80:83], v[168:171]\n"
    "s_add_u32 s76, s69, 96256\n"
    "v_mfma_f32_16x16x32_bf16 v[200:203], a[8:11], a[84:87], v[200:203]\n"
    "ds_read_b64_tr_b16 a[50:51], v12 offset:17408\n"
    "v_mfma_f32_16x16x32_bf16 v[232:235], a[8:11], a[88:91], v[232:235]\n"
    "buffer_load_dwordx4 v28, s[52:55], s76 offen offset:2048 lds\n"
    "v_mfma_f32_16x16x32_bf16 v[44:47], a[12:15], a[64:67], v[44:47]\n"
    "ds_read_b64_tr_b16 a[52:53], v13 offset:16384\n"
    "v_mfma_f32_16x16x32_bf16 v[76:79], a[12:15], a[68:71], v[76:79]\n"
    "s_add_u32 s69, s69, 64\n"
    "s_add_u32 m0, s64, 8192\n"
    "v_mfma_f32_16x16x32_bf16 v[108:111], a[12:15], a[72:75], v[108:111]\n"
    "ds_read_b64_tr_b16 a[54:55], v13 offset:17408\n"
    "v_mfma_f32_16x16x32_bf16 v[140:143], a[12:15], a[76:79], v[140:143]\n"
    "buffer_load_dwordx4 v26, s[60:63], s68 offen offset:0 lds\n"
    "v_mfma_f32_16x16x32_bf16 v[172:175], a[12:15], a[80:83], v[172:175]\n"
    "ds_read_b64_tr_b16 a[56:57], v14 offset:16384\n"
    "v_mfma_f32_16x16x32_bf16 v[204:207], a[12:15], a[84:87], v[204:207]\n"
    "s_add_u32 s74, s68, 31744\n"
    "v_mfma_f32_16x16x32_bf16 v[236:239], a[12:15], a[88:91], v[236:239]\n"
    "buffer_load_dwordx4 v26, s[60:63], s74 offen offset:1024 lds\n"
    "ds_read_b64_tr_b16 a[58:59], v14 offset:17408\n"
    "v_mfma_f32_16x16x32_bf16 v[48:51], a[16:19], a[64:67], v[48:51]\n"
    "s_add_u32 s74, s68, 63488\n"
    "v_mfma_f32_16x16x32_bf16 v[80:83], a[16:19], a[68:71], v[80:83]\n"
    "ds_read_b64_tr_b16 a[60:61], v15 offset:16384\n"
    "v_mfma_f32_16x16x32_bf16 v[112:115], a[16:19], a[72:75], v[112:115]\n"
    "buffer_load_dwordx4 v27, s[60:63], s74 offen offset:2048 lds\n"
    "v_mfma_f32_16x16x32_bf16 v[144:147], a[16:19], a[76:79], v[144:147]\n"
    "s_add_u32 s74, s68, 95232\n"
    "v_mfma_f32_16x16x32_bf16 v[176:179], a[16:19], a[80:83], v[176:179]\n"
    "ds_read_b64_tr_b16 a[62:63], v15 offset:17408\n"
    "buffer_load_dwordx4 v27, s[60:63], s74 offen offset:3072 lds\n"
    "v_mfma_f32_16x16x32_bf16 v[208:211], a[16:19], a[84:87], v[208:211]\n"
    "ds_read_b128 a[92:95], v7 offset:0\n"
    "v_mfma_f32_16x16x32_bf16 v[240:243], a[16:19], a[88:91], v[240:243]\n"
    "s_add_u32 m0, s64, 12288\n"
    "v_mfma_f32_16x16x32_bf16 v[52:55], a[20:23], a[64:67], v[52:55]\n"
    "s_add_u32 s74, s68, 131072\n"
    "v_mfma_f32_16x16x32_bf16 v[84:87], a[20:23], a[68:71], v[84:87]\n"
    "ds_read_b128 a[96:99], v7 offset:1024\n"
    "v_mfma_f32_16x16x32_bf16 v[116:119], a[20:23], a[72:75], v[116:119]\n"
    "buffer_load_dwordx4 v26, s[60:63], s74 offen offset:0 lds\n"
    "ds_read_b128 a[100:103], v7 offset:2048\n"
    "v_mfma_f32_16x16x32_bf16 v[148:151], a[20:23], a[76:79], v[148:151]\n"
    "s_add_u32 s74, s68, 162816\n"
    "v_mfma_f32_16x16x32_bf16 v[180:183], a[20:23], a[80:83], v[180:183]\n"
    "buffer_load_dwordx4 v26, s[60:63], s74 offen offset:1024 lds\n"
    "v_mfma_f32_16x16x32_bf16 v[212:215], a[20:23], a[84:87], v[212:215]\n"
    "ds_read_b128 a[104:107], v7 offset:3072\n"
    "v_mfma_f32_16x16x32_bf16 v[244:247], a[20:23], a[88:91], v[244:247]\n"
    "s_add_u32 s74, s68, 194560\n"
    "v_mfma_f32_16x16x32_bf16 v[56:59], a[24:27], a[64:67], v[56:59]\n"
    "ds_read_b128 a[108:111], v7 offset:4096\n"
    "v_mfma_f32_16x16x32_bf16 v[88:91], a[24:27], a[68:71], v[88:91]\n"
    "buffer_load_dwordx4 v27, s[60:63], s74 offen offset:2048 lds\n"
    "s_add_u32 s74, s68, 226304\n"
    "v_mfma_f32_16x16x32_bf16 v[120:123], a[24:27], a[72:75], v[120:123]\n"
    "ds_read_b128 a[112:115], v7 offset:5120\n"
    "v_mfma_f32_16x16x32_bf16 v[152:155], a[24:27], a[76:79], v[152:155]\n"
    "buffer_load_dwordx4 v27, s[60:63], s74 offen offset:3072 lds\n"
    "v_mfma_f32_16x16x32_bf16 v[184:187], a[24:27], a[80:83], v[184:187]\n"
    "ds_read_b128 a[116:119], v7 offset:6144\n"
    "v_mfma_f32_16x16x32_bf16 v[216:219], a[24:27], a[84:87], v[216:219]\n"
    "s_add_u32 s68, s68, 262144\n"
    "v_mfma_f32_16x16x32_bf16 v[248:251], a[24:27], a[88:91], v[248:251]\n"
    "v_mfma_f32_16x16x32_bf16 v[60:63], a[28:31], a[64:67], v[60:63]\n"
    "v_mfma_f32_16x16x32_bf16 v[92:95], a[28:31], a[68:71], v[92:95]\n"
    "v_mfma_f32_16x16x32_bf16 v[124:127], a[28:31], a[72:75], v[124:127]\n"
    "v_mfma_f32_16x16x32_bf16 v[156:159], a[28:31], a[76:79], v[156:159]\n"
    "v_mfma_f32_16x16x32_bf16 v[188:191], a[28:31], a[80:83], v[188:191]\n"
    "v_mfma_f32_16x16x32_bf16 v[220:223], a[28:31], a[84:87], v[220:223]\n"
    "v_mfma_f32_16x16x32_bf16 v[252:255], a[28:31], a[88:91], v[252:255]\n"
    "s_waitcnt lgkmcnt(0)\n"
    "s_sleep 20\n"
    "s_waitcnt vmcnt(15)\n"
    "v_mfma_f32_16x16x32_bf16 v[32:35], a[32:35], a[92:95], v[32:35]\n"
    "s_add_u32 m0, s64, 0\n"
    "v_mfma_f32_16x16x32_bf16 v[64:67], a[32:35], a[96:99], v[64:67]\n"
    "ds_read_b64_tr_b16 a[0:1], v8 offset:24576\n"
    "v_mfma_f32_16x16x32_bf16 v[96:99], a[32:35], a[100:103], v[96:99]\n"
    "buffer_load_dwordx4 v28, s[52:55], s69 offen offset:0 lds\n"
    "ds_read_b64_tr_b16 a[2:3], v8 offset:25600\n"
    "v_mfma_f32_16x16x32_bf16 v[128:131], a[32:35], a[104:107], v[128:131]\n"
    "s_add_u32 s76, s69, 15360\n"
    "v_mfma_f32_16x16x32_bf16 v[160:163], a[32:35], a[108:111], v[160:163]\n"
    "ds_read_b64_tr_b16 a[4:5], v9 offset:24576\n"
    "v_mfma_f32_16x16x32_bf16 v[192:195], a[32:35], a[112:115], v[192:195]\n"
    "buffer_load_dwordx4 v28, s[52:55], s76 offen offset:1024 lds\n"
    "v_mfma_f32_16x16x32_bf16 v[224:227], a[32:35], a[116:119], v[224:227]\n"
    "s_add_u32 s76, s69, 30720\n"
    "v_mfma_f32_16x16x32_bf16 v[36:39], a[36:39], a[92:95], v[36:39]\n"
    "ds_read_b64_tr_b16 a[6:7], v9 offset:25600\n"
    "buffer_load_dwordx4 v28, s[52:55], s76 offen offset:2048 lds\n"
    "v_mfma_f32_16x16x32_bf16 v[68:71], a[36:39], a[96:99], v[68:71]\n"
    "ds_read_b64_tr_b16 a[8:9], v10 offset:24576\n"
    "v_mfma_f32_16x16x32_bf16 v[100:103], a[36:39], a[100:103], v[100:103]\n"
    "s_add_u32 s76, s69, 46080\n"
    "v_mfma_f32_16x16x32_bf16 v[132:135], a[36:39], a[104:107], v[132:135]\n"
    "buffer_load_dwordx4 v28, s[52:55], s76 offen offset:3072 lds\n"
    "v_mfma_f32_16x16x32_bf16 v[164:167], a[36:39], a[108:111], v[164:167]\n"
    "ds_read_b64_tr_b16 a[10:11], v10 offset:25600\n"
    "v_mfma_f32_16x16x32_bf16 v[196:199], a[36:39], a[112:115], v[196:199]\n"
    "s_add_u32 m0, s64, 4096\n"
    "ds_read_b64_tr_b16 a[12:13], v11 offset:24576\n"
    "v_mfma_f32_16x16x32_bf16 v[228:231], a[36:39], a[116:119], v[228:231]\n"
    "s_add_u32 s76, s69, 65536\n"
    "v_mfma_f32_16x16x32_bf16 v[40:43], a[40:43], a[92:95], v[40:43]\n"
    "buffer_load_dwordx4 v28, s[52:55], s76 offen offset:0 lds\n"
    "v_mfma_f32_16x16x32_bf16 v[72:75], a[40:43], a[96:99], v[72:75]\n"
    "ds_read_b64_tr_b16 a[14:15], v11 offset:25600\n"
    "v_mfma_f32_16x16x32_bf16 v[104:107], a[40:43], a[100:103], v[104:107]\n"
    "s_add_u32 s76, s69, 80896\n"
    "v_mfma_f32_16x16x32_bf16 v[136:139], a[40:43], a[104:107], v[136:139]\n"
    "ds_read_b64_tr_b16 a[16:17], v12 offset:24576\n"
    "buffer_load_dwordx4 v28, s[52:55], s76 offen offset:1024 lds\n"
    "v_mfma_f32_16x16x32_bf16 v[168:171], a[40:43], a[108:111], v[168:171]\n"
    "s_add_u32 s76, s69, 96256\n"
    "v_mfma_f32_16x16x32_bf16 v[200:203], a[40:43], a[112:115], v[200:203]\n"
    "ds_read_b64_tr_b16 a[18:19], v12 offset:25600\n"
    "v_mfma_f32_16x16x32_bf16 v[232:235], a[40:43], a[116:119], v[232:235]\n"
    "buffer_load_dwordx4 v28, s[52:55], s76 offen offset:2048 lds\n"
    "v_mfma_f32_16x16x32_bf16 v[44:47], a[44:47], a[92:95], v[44:47]\n"
    "ds_read_b64_tr_b16 a[20:21], v13 offset:24576\n"
    "v_mfma_f32_16x16x32_bf16 v[76:79], a[44:47], a[96:99], v[76:79]\n"
    "s_add_u32 s69, s69, 64\n"
    "s_add_u32 m0, s64, 16384\n"
    "v_mfma_f32_16x16x32_bf16 v[108:111], a[44:47], a[100:103], v[108:111]\n"
    "ds_read_b64_tr_b16 a[22:23], v13 offset:25600\n"
    "v_mfma_f32_16x16x32_bf16 v[140:143], a[44:47], a[104:107], v[140:143]\n"
    "buffer_load_dwordx4 v26, s[60:63], s68 offen offset:0 lds\n"
    "v_mfma_f32_16x16x32_bf16 v[172:175], a[44:47], a[108:111], v[172:175]\n"
    "ds_read_b64_tr_b16 a[24:25], v14 offset:24576\n"
    "v_mfma_f32_16x16x32_bf16 v[204:207], a[44:47], a[112:115], v[204:207]\n"
    "s_add_u32 s74, s68, 31744\n"
    "v_mfma_f32_16x16x32_bf16 v[236:239], a[44:47], a[116:119], v[236:239]\n"
    "buffer_load_dwordx4 v26, s[60:63], s74 offen offset:1024 lds\n"
    "ds_read_b64_tr_b16 a[26:27], v14 offset:25600\n"
    "v_mfma_f32_16x16x32_bf16 v[48:51], a[48:51], a[92:95], v[48:51]\n"
    "s_add_u32 s74, s68, 63488\n"
    "v_mfma_f32_16x16x32_bf16 v[80:83], a[48:51], a[96:99], v[80:83]\n"
    "ds_read_b64_tr_b16 a[28:29], v15 offset:24576\n"
    "v_mfma_f32_16x16x32_bf16 v[112:115], a[48:51], a[100:103], v[112:115]\n"
    "buffer_load_dwordx4 v27, s[60:63], s74 offen offset:2048 lds\n"
    "v_mfma_f32_16x16x32_bf16 v[144:147], a[48:51], a[104:107], v[144:147]\n"
    "s_add_u32 s74, s68, 95232\n"
    "v_mfma_f32_16x16x32_bf16 v[176:179], a[48:51], a[108:111], v[176:179]\n"
    "ds_read_b64_tr_b16 a[30:31], v15 offset:25600\n"
    "buffer_load_dwordx4 v27, s[60:63], s74 offen offset:3072 lds\n"
    "v_mfma_f32_16x16x32_bf16 v[208:211], a[48:51], a[112:115], v[208:211]\n"
    "ds_read_b128 a[64:67], v6 offset:0\n"
    "v_mfma_f32_16x16x32_bf16 v[240:243], a[48:51], a[116:119], v[240:243]\n"
    "s_add_u32 m0, s64, 20480\n"
    "v_mfma_f32_16x16x32_bf16 v[52:55], a[52:55], a[92:95], v[52:55]\n"
    "s_add_u32 s74, s68, 131072\n"
    "v_mfma_f32_16x16x32_bf16 v[84:87], a[52:55], a[96:99], v[84:87]\n"
    "ds_read_b128 a[68:71], v6 offset:1024\n"
    "v_mfma_f32_16x16x32_bf16 v[116:119], a[52:55], a[100:103], v[116:119]\n"
    "buffer_load_dwordx4 v26, s[60:63], s74 offen offset:0 lds\n"
    "ds_read_b128 a[72:75], v6 offset:2048\n"
    "v_mfma_f32_16x16x32_bf16 v[148:151], a[52:55], a[104:107], v[148:151]\n"
    "s_add_u32 s74, s68, 162816\n"
    "v_mfma_f32_16x16x32_bf16 v[180:183], a[52:55], a[108:111], v[180:183]\n"
    "buffer_load_dwordx4 v26, s[60:63], s74 offen offset:1024 lds\n"
    "v_mfma_f32_16x16x32_bf16 v[212:215], a[52:55], a[112:115], v[212:215]\n"
    "ds_read_b128 a[76:79], v6 offset:3072\n"
    "v_mfma_f32_16x16x32_bf16 v[244:247], a[52:55], a[116:119], v[244:247]\n"
    "s_add_u32 s74, s68, 194560\n"
    "v_mfma_f32_16x16x32_bf16 v[56:59], a[56:59], a[92:95], v[56:59]\n"
    "ds_read_b128 a[80:83], v6 offset:4096\n"
    "v_mfma_f32_16x16x32_bf16 v[88:91], a[56:59], a[96:99], v[88:91]\n"
    "buffer_load_dwordx4 v27, s[60:63], s74 offen offset:2048 lds\n"
    "s_add_u32 s74, s68, 226304\n"
    "v_mfma_f32_16x16x32_bf16 v[120:123], a[56:59], a[100:103], v[120:123]\n"
    "ds_read_b128 a[84:87], v6 offset:5120\n"
    "v_mfma_f32_16x16x32_bf16 v[152:155], a[56:59], a[104:107], v[152:155]\n"
    "buffer_load_dwordx4 v27, s[60:63], s74 offen offset:3072 lds\n"
    "v_mfma_f32_16x16x32_bf16 v[184:187], a[56:59], a[108:111], v[184:187]\n"
    "ds_read_b128 a[88:91], v6 offset:6144\n"
    "v_mfma_f32_16x16x32_bf16 v[216:219], a[56:59], a[112:115], v[216:219]\n"
    "s_add_u32 s68, s68, 262144\n"
    "v_mfma_f32_16x16x32_bf16 v[248:251], a[56:59], a[116:119], v[248:251]\n"
    "v_mfma_f32_16x16x32_bf16 v[60:63], a[60:63], a[92:95], v[60:63]\n"
    "v_mfma_f32_16x16x32_bf16 v[92:95], a[60:63], a[96:99], v[92:95]\n"
    "v_mfma_f32_16x16x32_bf16 v[124:127], a[60:63], a[100:103], v[124:127]\n"
    "v_mfma_f32_16x16x32_bf16 v[156:159], a[60:63], a[104:107], v[156:159]\n"
    "v_mfma_f32_16x16x32_bf16 v[188:191], a[60:63], a[108:111], v[188:191]\n"
    "v_mfma_f32_16x16x32_bf16 v[220:223], a[60:63], a[112:115], v[220:223]\n"
    "v_mfma_f32_16x16x32_bf16 v[252:255], a[60:63], a[116:119], v[252:255]\n"
    "s_waitcnt lgkmcnt(0)\n"
    "s_sleep 20\n"
    "s_waitcnt vmcnt(15)\n"
    "v_mfma_f32_16x16x32_bf16 v[32:35], a[0:3], a[64:67], v[32:35]\n"
    "s_add_u32 m0, s65, 0\n"
    "v_mfma_f32_16x16x32_bf16 v[64:67], a[0:3], a[68:71], v[64:67]\n"
    "ds_read_b64_tr_b16 a[32:33], v8 offset:8192\n"
    "v_mfma_f32_16x16x32_bf16 v[96:99], a[0:3], a[72:75], v[96:99]\n"
    "buffer_load_dwordx4 v28, s[52:55], s69 offen offset:0 lds\n"
    "ds_read_b64_tr_b16 a[34:35], v8 offset:9216\n"
    "v_mfma_f32_16x16x32_bf16 v[128:131], a[0:3], a[76:79], v[128:131]\n"
    "s_add_u32 s76, s69, 15360\n"
    "v_mfma_f32_16x16x32_bf16 v[160:163], a[0:3], a[80:83], v[160:163]\n"
    "ds_read_b64_tr_b16 a[36:37], v9 offset:8192\n"
    "v_mfma_f32_16x16x32_bf16 v[192:195], a[0:3], a[84:87], v[192:195]\n"
    "buffer_load_dwordx4 v28, s[52:55], s76 offen offset:1024 lds\n"
    "v_mfma_f32_16x16x32_bf16 v[224:227], a[0:3], a[88:91], v[224:227]\n"
    "s_add_u32 s76, s69, 30720\n"
    "v_mfma_f32_16x16x32_bf16 v[36:39], a[4:7], a[64:67], v[36:39]\n"
    "ds_read_b64_tr_b16 a[38:39], v9 offset:9216\n"
    "buffer_load_dwordx4 v28, s[52:55], s76 offen offset:2048 lds\n"
    "v_mfma_f32_16x16x32_bf16 v[68:71], a[4:7], a[68:71], v[68:71]\n"
    "ds_read_b64_tr_b16 a[40:41], v10 offset:8192\n"
    "v_mfma_f32_16x16x32_bf16 v[100:103], a[4:7], a[72:75], v[100:103]\n"
    "s_add_u32 s76, s69, 46080\n"
    "v_mfma_f32_16x16x32_bf16 v[132:135], a[4:7], a[76:79], v[132:135]\n"
    "buffer_load_dwordx4 v28, s[52:55], s76 offen offset:3072 lds\n"
    "v_mfma_f32_16x16x32_bf16 v[164:167], a[4:7], a[80:83], v[164:167]\n"
    "ds_read_b64_tr_b16 a[42:43], v10 offset:9216\n"
    "v_mfma_f32_16x16x32_bf16 v[196:199], a[4:7], a[84:87], v[196:199]\n"
    "s_add_u32 m0, s65, 4096\n"
    "ds_read_b64_tr_b16 a[44:45], v11 offset:8192\n"
    "v_mfma_f32_16x16x32_bf16 v[228:231], a[4:7], a[88:91], v[228:231]\n"
    "s_add_u32 s76, s69, 65536\n"
    "v_mfma_f32_16x16x32_bf16 v[40:43], a[8:11], a[64:67], v[40:43]\n"
    "buffer_load_dwordx4 v28, s[52:55], s76 offen offset:0 lds\n"
    "v_mfma_f32_16x16x32_bf16 v[72:75], a[8:11], a[68:71], v[72:75]\n"
    "ds_read_b64_tr_b16 a[46:47], v11 offset:9216\n"
    "v_mfma_f32_16x16x32_bf16 v[104:107], a[8:11], a[72:75], v[104:107]\n"
    "s_add_u32 s76, s69, 80896\n"
    "v_mfma_f32_16x16x32_bf16 v[136:139], a[8:11], a[76:79], v[136:139]\n"
    "ds_read_b64_tr_b16 a[48:49], v12 offset:8192\n"
    "buffer_load_dwordx4 v28, s[52:55], s76 offen offset:1024 lds\n"
    "v_mfma_f32_16x16x32_bf16 v[168:171], a[8:11], a[80:83], v[168:171]\n"
    "s_add_u32 s76, s69, 96256\n"
    "v_mfma_f32_16x16x32_bf16 v[200:203], a[8:11], a[84:87], v[200:203]\n"
    "ds_read_b64_tr_b16 a[50:51], v12 offset:9216\n"
    "v_mfma_f32_16x16x32_bf16 v[232:235], a[8:11], a[88:91], v[232:235]\n"
    "buffer_load_dwordx4 v28, s[52:55], s76 offen offset:2048 lds\n"
    "v_mfma_f32_16x16x32_bf16 v[44:47], a[12:15], a[64:67], v[44:47]\n"
    "ds_read_b64_tr_b16 a[52:53], v13 offset:8192\n"
    "v_mfma_f32_16x16x32_bf16 v[76:79], a[12:15], a[68:71], v[76:79]\n"
    "s_add_u32 s69, s69, 64\n"
    "s_add_u32 m0, s64, 24576\n"
    "v_mfma_f32_16x16x32_bf16 v[108:111], a[12:15], a[72:75], v[108:111]\n"
    "ds_read_b64_tr_b16 a[54:55], v13 offset:9216\n"
    "v_mfma_f32_16x16x32_bf16 v[140:143], a[12:15], a[76:79], v[140:143]\n"
    "buffer_load_dwordx4 v26, s[60:63], s68 offen offset:0 lds\n"
    "v_mfma_f32_16x16x32_bf16 v[172:175], a[12:15], a[80:83], v[172:175]\n"
    "ds_read_b64_tr_b16 a[56:57], v14 offset:8192\n"
    "v_mfma_f32_16x16x32_bf16 v[204:207], a[12:15], a[84:87], v[204:207]\n"
    "s_add_u32 s74, s68, 31744\n"
    "v_mfma_f32_16x16x32_bf16 v[236:239], a[12:15], a[88:91], v[236:239]\n"
    "buffer_load_dwordx4 v26, s[60:63], s74 offen offset:1024 lds\n"
    "ds_read_b64_tr_b16 a[58:59], v14 offset:9216\n"
    "v_mfma_f32_16x16x32_bf16 v[48:51], a[16:19], a[64:67], v[48:51]\n"
    "s_add_u32 s74, s68, 63488\n"
    "v_mfma_f32_16x16x32_bf16 v[80:83], a[16:19], a[68:71], v[80:83]\n"
    "ds_read_b64_tr_b16 a[60:61], v15 offset:8192\n"
    "v_mfma_f32_16x16x32_bf16 v[112:115], a[16:19], a[72:75], v[112:115]\n"
    "buffer_load_dwordx4 v27, s[60:63], s74 offen offset:2048 lds\n"
    "v_mfma_f32_16x16x32_bf16 v[144:147], a[16:19], a[76:79], v[144:147]\n"
    "s_add_u32 s74, s68, 95232\n"
    "v_mfma_f32_16x16x32_bf16 v[176:179], a[16:19], a[80:83], v[176:179]\n"
    "ds_read_b64_tr_b16 a[62:63], v15 offset:9216\n"
    "buffer_load_dwordx4 v27, s[60:63], s74 offen offset:3072 lds\n"
    "v_mfma_f32_16x16x32_bf16 v[208:211], a[16:19], a[84:87], v[208:211]\n"
    "ds_read_b128 a[92:95], v6 offset:8192\n"
    "v_mfma_f32_16x16x32_bf16 v[240:243], a[16:19], a[88:91], v[240:243]\n"
    "s_add_u32 m0, s64, 28672\n"
    "v_mfma_f32_16x16x32_bf16 v[52:55], a[20:23], a[64:67], v[52:55]\n"
    "s_add_u32 s74, s68, 131072\n"
    "v_mfma_f32_16x16x32_bf16 v[84:87], a[20:23], a[68:71], v[84:87]\n"
    "ds_read_b128 a[96:99], v6 offset:9216\n"
    "v_mfma_f32_16x16x32_bf16 v[116:119], a[20:23], a[72:75], v[116:119]\n"
    "buffer_load_dwordx4 v26, s[60:63], s74 offen offset:0 lds\n"
    "ds_read_b128 a[100:103], v6 offset:10240\n"
    "v_mfma_f32_16x16x32_bf16 v[148:151], a[20:23], a[76:79], v[148:151]\n"
    "s_add_u32 s74, s68, 162816\n"
    "v_mfma_f32_16x16x32_bf16 v[180:183], a[20:23], a[80:83], v[180:183]\n"
    "buffer_load_dwordx4 v26, s[60:63], s74 offen offset:1024 lds\n"
    "v_mfma_f32_16x16x32_bf16 v[212:215], a[20:23], a[84:87], v[212:215]\n"
    "ds_read_b128 a[104:107], v6 offset:11264\n"
    "v_mfma_f32_16x16x32_bf16 v[244:247], a[20:23], a[88:91], v[244:247]\n"
    "s_add_u32 s74, s68, 194560\n"
    "v_mfma_f32_16x16x32_bf16 v[56:59], a[24:27], a[64:67], v[56:59]\n"
    "ds_read_b128 a[108:111], v6 offset:12288\n"
    "v_mfma_f32_16x16x32_bf16 v[88:91], a[24:27], a[68:71], v[88:91]\n"
    "buffer_load_dwordx4 v27, s[60:63], s74 offen offset:2048 lds\n"
    "s_add_u32 s74, s68, 226304\n"
    "v_mfma_f32_16x16x32_bf16 v[120:123], a[24:27], a[72:75], v[120:123]\n"
    "ds_read_b128 a[112:115], v6 offset:13312\n"
    "v_mfma_f32_16x16x32_bf16 v[152:155], a[24:27], a[76:79], v[152:155]\n"
    "buffer_load_dwordx4 v27, s[60:63], s74 offen offset:3072 lds\n"
    "v_mfma_f32_16x16x32_bf16 v[184:187], a[24:27], a[80:83], v[184:187]\n"
    "ds_read_b128 a[116:119], v6 offset:14336\n"
    "v_mfma_f32_16x16x32_bf16 v[216:219], a[24:27], a[84:87], v[216:219]\n"
    "s_add_u32 s68, s68, 262144\n"
    "v_mfma_f32_16x16x32_bf16 v[248:251], a[24:27], a[88:91], v[248:251]\n"
    "v_mfma_f32_16x16x32_bf16 v[60:63], a[28:31], a[64:67], v[60:63]\n"
    "v_mfma_f32_16x16x32_bf16 v[92:95], a[28:31], a[68:71], v[92:95]\n"
    "v_mfma_f32_16x16x32_bf16 v[124:127], a[28:31], a[72:75], v[124:127]\n"
    "v_mfma_f32_16x16x32_bf16 v[156:159], a[28:31], a[76:79], v[156:159]\n"
    "v_mfma_f32_16x16x32_bf16 v[188:191], a[28:31], a[80:83], v[188:191]\n"
    "v_mfma_f32_16x16x32_bf16 v[220:223], a[28:31], a[84:87], v[220:223]\n"
    "v_mfma_f32_16x16x32_bf16 v[252:255], a[28:31], a[88:91], v[252:255]\n"
    "s_waitcnt lgkmcnt(0)\n"
    "s_sleep 20\n"
    "s_waitcnt vmcnt(15)\n"
    "v_mfma_f32_16x16x32_bf16 v[32:35], a[32:35], a[92:95], v[32:35]\n"
    "s_add_u32 m0, s64, 8192\n"
    "v_mfma_f32_16x16x32_bf16 v[64:67], a[32:35], a[96:99], v[64:67]\n"
    "v_mfma_f32_16x16x32_bf16 v[96:99], a[32:35], a[100:103], v[96:99]\n"
    "ds_read_b64_tr_b16 a[0:1], v8 offset:16384\n"
    "v_mfma_f32_16x16x32_bf16 v[128:131], a[32:35], a[104:107], v[128:131]\n"
    "ds_read_b64_tr_b16 a[2:3], v8 offset:17408\n"
    "v_mfma_f32_16x16x32_bf16 v[160:163], a[32:35], a[108:111], v[160:163]\n"
    "ds_read_b64_tr_b16 a[4:5], v9 offset:16384\n"
    "v_mfma_f32_16x16x32_bf16 v[192:195], a[32:35], a[112:115], v[192:195]\n"
    "v_mfma_f32_16x16x32_bf16 v[224:227], a[32:35], a[116:119], v[224:227]\n"
    "buffer_load_dwordx4 v20, s[44:47], s67 offen offset:0 nt lds\n"
    "v_mfma_f32_16x16x32_bf16 v[36:39], a[36:39], a[92:95], v[36:39]\n"
    "ds_read_b64_tr_b16 a[6:7], v9 offset:17408\n"
    "v_mfma_f32_16x16x32_bf16 v[68:71], a[36:39], a[96:99], v[68:71]\n"
    "v_mfma_f32_16x16x32_bf16 v[100:103], a[36:39], a[100:103], v[100:103]\n"
    "ds_read_b64_tr_b16 a[8:9], v10 offset:16384\n"
    "v_mfma_f32_16x16x32_bf16 v[132:135], a[36:39], a[104:107], v[132:135]\n"
    "buffer_load_dwordx4 v21, s[44:47], s67 offen offset:1024 nt lds\n"
    "v_mfma_f32_16x16x32_bf16 v[164:167], a[36:39], a[108:111], v[164:167]\n"
    "ds_read_b64_tr_b16 a[10:11], v10 offset:17408\n"
    "v_mfma_f32_16x16x32_bf16 v[196:199], a[36:39], a[112:115], v[196:199]\n"
    "v_mfma_f32_16x16x32_bf16 v[228:231], a[36:39], a[116:119], v[228:231]\n"
    "ds_read_b64_tr_b16 a[12:13], v11 offset:16384\n"
    "v_mfma_f32_16x16x32_bf16 v[40:43], a[40:43], a[92:95], v[40:43]\n"
    "buffer_load_dwordx4 v20, s[44:47], s67 offen offset:2048 nt lds\n"
    "v_mfma_f32_16x16x32_bf16 v[72:75], a[40:43], a[96:99], v[72:75]\n"
    "v_mfma_f32_16x16x32_bf16 v[104:107], a[40:43], a[100:103], v[104:107]\n"
    "ds_read_b64_tr_b16 a[14:15], v11 offset:17408\n"
    "v_mfma_f32_16x16x32_bf16 v[136:139], a[40:43], a[104:107], v[136:139]\n"
    "ds_read_b64_tr_b16 a[16:17], v12 offset:16384\n"
    "v_mfma_f32_16x16x32_bf16 v[168:171], a[40:43], a[108:111], v[168:171]\n"
    "buffer_load_dwordx4 v21, s[44:47], s67 offen offset:3072 nt lds\n"
    "v_mfma_f32_16x16x32_bf16 v[200:203], a[40:43], a[112:115], v[200:203]\n"
    "v_mfma_f32_16x16x32_bf16 v[232:235], a[40:43], a[116:119], v[232:235]\n"
    "ds_read_b64_tr_b16 a[18:19], v12 offset:17408\n"
    "v_mfma_f32_16x16x32_bf16 v[44:47], a[44:47], a[92:95], v[44:47]\n"
    "ds_read_b64_tr_b16 a[20:21], v13 offset:16384\n"
    "v_mfma_f32_16x16x32_bf16 v[76:79], a[44:47], a[96:99], v[76:79]\n"
    "v_mfma_f32_16x16x32_bf16 v[108:111], a[44:47], a[100:103], v[108:111]\n"
    "s_add_u32 m0, s64, 12288\n"
    "v_mfma_f32_16x16x32_bf16 v[140:143], a[44:47], a[104:107], v[140:143]\n"
    "ds_read_b64_tr_b16 a[22:23], v13 offset:17408\n"
    "v_mfma_f32_16x16x32_bf16 v[172:175], a[44:47], a[108:111], v[172:175]\n"
    "ds_read_b64_tr_b16 a[24:25], v14 offset:16384\n"
    "v_mfma_f32_16x16x32_bf16 v[204:207], a[44:47], a[112:115], v[204:207]\n"
    "v_mfma_f32_16x16x32_bf16 v[236:239], a[44:47], a[116:119], v[236:239]\n"
    "buffer_load_dwordx4 v22, s[44:47], s67 offen offset:0 nt lds\n"
    "v_mfma_f32_16x16x32_bf16 v[48:51], a[48:51], a[92:95], v[48:51]\n"
    "ds_read_b64_tr_b16 a[26:27], v14 offset:17408\n"
    "v_mfma_f32_16x16x32_bf16 v[80:83], a[48:51], a[96:99], v[80:83]\n"
    "v_mfma_f32_16x16x32_bf16 v[112:115], a[48:51], a[100:103], v[112:115]\n"
    "ds_read_b64_tr_b16 a[28:29], v15 offset:16384\n"
    "v_mfma_f32_16x16x32_bf16 v[144:147], a[48:51], a[104:107], v[144:147]\n"
    "buffer_load_dwordx4 v23, s[44:47], s67 offen offset:1024 nt lds\n"
    "v_mfma_f32_16x16x32_bf16 v[176:179], a[48:51], a[108:111], v[176:179]\n"
    "ds_read_b64_tr_b16 a[30:31], v15 offset:17408\n"
    "v_mfma_f32_16x16x32_bf16 v[208:211], a[48:51], a[112:115], v[208:211]\n"
    "v_mfma_f32_16x16x32_bf16 v[240:243], a[48:51], a[116:119], v[240:243]\n"
    "ds_read_b128 a[64:67], v7 offset:0\n"
    "v_mfma_f32_16x16x32_bf16 v[52:55], a[52:55], a[92:95], v[52:55]\n"
    "buffer_load_dwordx4 v22, s[44:47], s67 offen offset:2048 nt lds\n"
    "v_mfma_f32_16x16x32_bf16 v[84:87], a[52:55], a[96:99], v[84:87]\n"
    "v_mfma_f32_16x16x32_bf16 v[116:119], a[52:55], a[100:103], v[116:119]\n"
    "ds_read_b128 a[68:71], v7 offset:1024\n"
    "v_mfma_f32_16x16x32_bf16 v[148:151], a[52:55], a[104:107], v[148:151]\n"
    "ds_read_b128 a[72:75], v7 offset:2048\n"
    "v_mfma_f32_16x16x32_bf16 v[180:183], a[52:55], a[108:111], v[180:183]\n"
    "buffer_load_dwordx4 v23, s[44:47], s67 offen offset:3072 nt lds\n"
    "v_mfma_f32_16x16x32_bf16 v[212:215], a[52:55], a[112:115], v[212:215]\n"
    "v_mfma_f32_16x16x32_bf16 v[244:247], a[52:55], a[116:119], v[244:247]\n"
    "ds_read_b128 a[76:79], v7 offset:3072\n"
    "v_mfma_f32_16x16x32_bf16 v[56:59], a[56:59], a[92:95], v[56:59]\n"
    "ds_read_b128 a[80:83], v7 offset:4096\n"
    "v_mfma_f32_16x16x32_bf16 v[88:91], a[56:59], a[96:99], v[88:91]\n"
    "v_mfma_f32_16x16x32_bf16 v[120:123], a[56:59], a[100:103], v[120:123]\n"
    "s_add_u32 s67, s67, 8192\n"
    "v_mfma_f32_16x16x32_bf16 v[152:155], a[56:59], a[104:107], v[152:155]\n"
    "ds_read_b128 a[84:87], v7 offset:5120\n"
    "v_mfma_f32_16x16x32_bf16 v[184:187], a[56:59], a[108:111], v[184:187]\n"
    "ds_read_b128 a[88:91], v7 offset:6144\n"
    "v_mfma_f32_16x16x32_bf16 v[216:219], a[56:59], a[112:115], v[216:219]\n"
    "v_mfma_f32_16x16x32_bf16 v[248:251], a[56:59], a[116:119], v[248:251]\n"
    "v_mfma_f32_16x16x32_bf16 v[60:63], a[60:63], a[92:95], v[60:63]\n"
    "v_mfma_f32_16x16x32_bf16 v[92:95], a[60:63], a[96:99], v[92:95]\n"
    "v_mfma_f32_16x16x32_bf16 v[124:127], a[60:63], a[100:103], v[124:127]\n"
    "v_mfma_f32_16x16x32_bf16 v[156:159], a[60:63], a[104:107], v[156:159]\n"
    "v_mfma_f32_16x16x32_bf16 v[188:191], a[60:63], a[108:111], v[188:191]\n"
    "v_mfma_f32_16x16x32_bf16 v[220:223], a[60:63], a[112:115], v[220:223]\n"
    "v_mfma_f32_16x16x32_bf16 v[252:255], a[60:63], a[116:119], v[252:255]\n"
    "s_waitcnt lgkmcnt(0)\n"
    "s_waitcnt vmcnt(8)\n"
    "v_mfma_f32_16x16x32_bf16 v[32:35], a[0:3], a[64:67], v[32:35]\n"
    "s_add_u32 m0, s64, 0\n"
    "v_mfma_f32_16x16x32_bf16 v[64:67], a[0:3], a[68:71], v[64:67]\n"
    "ds_read_b64_tr_b16 a[32:33], v8 offset:24576\n"
    "v_mfma_f32_16x16x32_bf16 v[96:99], a[0:3], a[72:75], v[96:99]\n"
    "ds_read_b64_tr_b16 a[34:35], v8 offset:25600\n"
    "v_mfma_f32_16x16x32_bf16 v[128:131], a[0:3], a[76:79], v[128:131]\n"
    "buffer_load_dwordx4 v20, s[40:43], s66 offen offset:0 nt lds\n"
    "v_mfma_f32_16x16x32_bf16 v[160:163], a[0:3], a[80:83], v[160:163]\n"
    "ds_read_b64_tr_b16 a[36:37], v9 offset:24576\n"
    "v_mfma_f32_16x16x32_bf16 v[192:195], a[0:3], a[84:87], v[192:195]\n"
    "buffer_load_dwordx4 v21, s[40:43], s66 offen offset:1024 nt lds\n"
    "v_mfma_f32_16x16x32_bf16 v[224:227], a[0:3], a[88:91], v[224:227]\n"
    "ds_read_b64_tr_b16 a[38:39], v9 offset:25600\n"
    "v_mfma_f32_16x16x32_bf16 v[36:39], a[4:7], a[64:67], v[36:39]\n"
    "buffer_load_dwordx4 v20, s[40:43], s66 offen offset:2048 nt lds\n"
    "v_mfma_f32_16x16x32_bf16 v[68:71], a[4:7], a[68:71], v[68:71]\n"
    "ds_read_b64_tr_b16 a[40:41], v10 offset:24576\n"
    "v_mfma_f32_16x16x32_bf16 v[100:103], a[4:7], a[72:75], v[100:103]\n"
    "v_mfma_f32_16x16x32_bf16 v[132:135], a[4:7], a[76:79], v[132:135]\n"
    "buffer_load_dwordx4 v21, s[40:43], s66 offen offset:3072 nt lds\n"
    "v_mfma_f32_16x16x32_bf16 v[164:167], a[4:7], a[80:83], v[164:167]\n"
    "ds_read_b64_tr_b16 a[42:43], v10 offset:25600\n"
    "v_mfma_f32_16x16x32_bf16 v[196:199], a[4:7], a[84:87], v[196:199]\n"
    "s_add_u32 m0, s64, 4096\n"
    "v_mfma_f32_16x16x32_bf16 v[228:231], a[4:7], a[88:91], v[228:231]\n"
    "ds_read_b64_tr_b16 a[44:45], v11 offset:24576\n"
    "v_mfma_f32_16x16x32_bf16 v[40:43], a[8:11], a[64:67], v[40:43]\n"
    "buffer_load_dwordx4 v22, s[40:43], s66 offen offset:0 nt lds\n"
    "v_mfma_f32_16x16x32_bf16 v[72:75], a[8:11], a[68:71], v[72:75]\n"
    "ds_read_b64_tr_b16 a[46:47], v11 offset:25600\n"
    "v_mfma_f32_16x16x32_bf16 v[104:107], a[8:11], a[72:75], v[104:107]\n"
    "buffer_load_dwordx4 v23, s[40:43], s66 offen offset:1024 nt lds\n"
    "v_mfma_f32_16x16x32_bf16 v[136:139], a[8:11], a[76:79], v[136:139]\n"
    "ds_read_b64_tr_b16 a[48:49], v12 offset:24576\n"
    "v_mfma_f32_16x16x32_bf16 v[168:171], a[8:11], a[80:83], v[168:171]\n"
    "buffer_load_dwordx4 v22, s[40:43], s66 offen offset:2048 nt lds\n"
    "v_mfma_f32_16x16x32_bf16 v[200:203], a[8:11], a[84:87], v[200:203]\n"
    "ds_read_b64_tr_b16 a[50:51], v12 offset:25600\n"
    "v_mfma_f32_16x16x32_bf16 v[232:235], a[8:11], a[88:91], v[232:235]\n"
    "buffer_load_dwordx4 v23, s[40:43], s66 offen offset:3072 nt lds\n"
    "v_mfma_f32_16x16x32_bf16 v[44:47], a[12:15], a[64:67], v[44:47]\n"
    "ds_read_b64_tr_b16 a[52:53], v13 offset:24576\n"
    "v_mfma_f32_16x16x32_bf16 v[76:79], a[12:15], a[68:71], v[76:79]\n"
    "s_add_u32 s66, s66, 8192\n"
    "v_mfma_f32_16x16x32_bf16 v[108:111], a[12:15], a[72:75], v[108:111]\n"
    "ds_read_b64_tr_b16 a[54:55], v13 offset:25600\n"
    "v_mfma_f32_16x16x32_bf16 v[140:143], a[12:15], a[76:79], v[140:143]\n"
    "s_add_u32 m0, s64, 16384\n"
    "v_mfma_f32_16x16x32_bf16 v[172:175], a[12:15], a[80:83], v[172:175]\n"
    "ds_read_b64_tr_b16 a[56:57], v14 offset:24576\n"
    "v_mfma_f32_16x16x32_bf16 v[204:207], a[12:15], a[84:87], v[204:207]\n"
    "buffer_load_dwordx4 v20, s[40:43], s66 offen offset:0 nt lds\n"
    "v_mfma_f32_16x16x32_bf16 v[236:239], a[12:15], a[88:91], v[236:239]\n"
    "v_mfma_f32_16x16x32_bf16 v[48:51], a[16:19], a[64:67], v[48:51]\n"
    "ds_read_b64_tr_b16 a[58:59], v14 offset:25600\n"
    "v_mfma_f32_16x16x32_bf16 v[80:83], a[16:19], a[68:71], v[80:83]\n"
    "buffer_load_dwordx4 v21, s[40:43], s66 offen offset:1024 nt lds\n"
    "v_mfma_f32_16x16x32_bf16 v[112:115], a[16:19], a[72:75], v[112:115]\n"
    "ds_read_b64_tr_b16 a[60:61], v15 offset:24576\n"
    "v_mfma_f32_16x16x32_bf16 v[144:147], a[16:19], a[76:79], v[144:147]\n"
    "buffer_load_dwordx4 v20, s[40:43], s66 offen offset:2048 nt lds\n"
    "v_mfma_f32_16x16x32_bf16 v[176:179], a[16:19], a[80:83], v[176:179]\n"
    "ds_read_b64_tr_b16 a[62:63], v15 offset:25600\n"
    "v_mfma_f32_16x16x32_bf16 v[208:211], a[16:19], a[84:87], v[208:211]\n"
    "buffer_load_dwordx4 v21, s[40:43], s66 offen offset:3072 nt lds\n"
    "v_mfma_f32_16x16x32_bf16 v[240:243], a[16:19], a[88:91], v[240:243]\n"
    "ds_read_b128 a[92:95], v6 offset:0\n"
    "v_mfma_f32_16x16x32_bf16 v[52:55], a[20:23], a[64:67], v[52:55]\n"
    "s_add_u32 m0, s64, 20480\n"
    "v_mfma_f32_16x16x32_bf16 v[84:87], a[20:23], a[68:71], v[84:87]\n"
    "ds_read_b128 a[96:99], v6 offset:1024\n"
    "v_mfma_f32_16x16x32_bf16 v[116:119], a[20:23], a[72:75], v[116:119]\n"
    "buffer_load_dwordx4 v22, s[40:43], s66 offen offset:0 nt lds\n"
    "v_mfma_f32_16x16x32_bf16 v[148:151], a[20:23], a[76:79], v[148:151]\n"
    "ds_read_b128 a[100:103], v6 offset:2048\n"
    "v_mfma_f32_16x16x32_bf16 v[180:183], a[20:23], a[80:83], v[180:183]\n"
    "buffer_load_dwordx4 v23, s[40:43], s66 offen offset:1024 nt lds\n"
    "v_mfma_f32_16x16x32_bf16 v[212:215], a[20:23], a[84:87], v[212:215]\n"
    "ds_read_b128 a[104:107], v6 offset:3072\n"
    "v_mfma_f32_16x16x32_bf16 v[244:247], a[20:23], a[88:91], v[244:247]\n"
    "buffer_load_dwordx4 v22, s[40:43], s66 offen offset:2048 nt lds\n"
    "v_mfma_f32_16x16x32_bf16 v[56:59], a[24:27], a[64:67], v[56:59]\n"
    "ds_read_b128 a[108:111], v6 offset:4096\n"
    "v_mfma_f32_16x16x32_bf16 v[88:91], a[24:27], a[68:71], v[88:91]\n"
    "buffer_load_dwordx4 v23, s[40:43], s66 offen offset:3072 nt lds\n"
    "v_mfma_f32_16x16x32_bf16 v[120:123], a[24:27], a[72:75], v[120:123]\n"
    "ds_read_b128 a[112:115], v6 offset:5120\n"
    "v_mfma_f32_16x16x32_bf16 v[152:155], a[24:27], a[76:79], v[152:155]\n"
    "v_mfma_f32_16x16x32_bf16 v[184:187], a[24:27], a[80:83], v[184:187]\n"
    "s_add_u32 s66, s66, 8192\n"
    "v_mfma_f32_16x16x32_bf16 v[216:219], a[24:27], a[84:87], v[216:219]\n"
    "ds_read_b128 a[116:119], v6 offset:6144\n"
    "v_mfma_f32_16x16x32_bf16 v[248:251], a[24:27], a[88:91], v[248:251]\n"
    "v_mfma_f32_16x16x32_bf16 v[60:63], a[28:31], a[64:67], v[60:63]\n"
    "v_mfma_f32_16x16x32_bf16 v[92:95], a[28:31], a[68:71], v[92:95]\n"
    "v_mfma_f32_16x16x32_bf16 v[124:127], a[28:31], a[72:75], v[124:127]\n"
    "v_mfma_f32_16x16x32_bf16 v[156:159], a[28:31], a[76:79], v[156:159]\n"
    "v_mfma_f32_16x16x32_bf16 v[188:191], a[28:31], a[80:83], v[188:191]\n"
    "v_mfma_f32_16x16x32_bf16 v[220:223], a[28:31], a[84:87], v[220:223]\n"
    "v_mfma_f32_16x16x32_bf16 v[252:255], a[28:31], a[88:91], v[252:255]\n"
    "s_waitcnt lgkmcnt(0)\n"
    "s_waitcnt vmcnt(8)\n"
    "v_mfma_f32_16x16x32_bf16 v[32:35], a[32:35], a[92:95], v[32:35]\n"
    "v_mfma_f32_16x16x32_bf16 v[36:39], a[36:39], a[92:95], v[36:39]\n"
    "v_mfma_f32_16x16x32_bf16 v[40:43], a[40:43], a[92:95], v[40:43]\n"
    "v_mfma_f32_16x16x32_bf16 v[44:47], a[44:47], a[92:95], v[44:47]\n"
    "v_mfma_f32_16x16x32_bf16 v[48:51], a[48:51], a[92:95], v[48:51]\n"
    "v_mfma_f32_16x16x32_bf16 v[52:55], a[52:55], a[92:95], v[52:55]\n"
    "v_mfma_f32_16x16x32_bf16 v[56:59], a[56:59], a[92:95], v[56:59]\n"
    "v_mfma_f32_16x16x32_bf16 v[60:63], a[60:63], a[92:95], v[60:63]\n"
    "v_mfma_f32_16x16x32_bf16 v[64:67], a[32:35], a[96:99], v[64:67]\n"
    "v_pk_mul_f32 v[32:33], v[32:33], s[80:81]\n"
    "v_pk_mul_f32 v[34:35], v[34:35], s[80:81]\n"
    "v_pk_mul_f32 v[36:37], v[36:37], s[80:81]\n"
    "v_pk_mul_f32 v[38:39], v[38:39], s[80:81]\n"
    "v_pk_mul_f32 v[40:41], v[40:41], s[80:81]\n"
    "v_mfma_f32_16x16x32_bf16 v[68:71], a[36:39], a[96:99], v[68:71]\n"
    "v_pk_mul_f32 v[42:43], v[42:43], s[80:81]\n"
    "v_pk_mul_f32 v[44:45], v[44:45], s[80:81]\n"
    "v_pk_mul_f32 v[46:47], v[46:47], s[80:81]\n"
    "v_pk_mul_f32 v[48:49], v[48:49], s[80:81]\n"
    "v_pk_mul_f32 v[50:51], v[50:51], s[80:81]\n"
    "v_mfma_f32_16x16x32_bf16 v[72:75], a[40:43], a[96:99], v[72:75]\n"
    "v_pk_mul_f32 v[52:53], v[52:53], s[80:81]\n"
    "v_pk_mul_f32 v[54:55], v[54:55], s[80:81]\n"
    "v_pk_mul_f32 v[56:57], v[56:57], s[80:81]\n"
    "v_pk_mul_f32 v[58:59], v[58:59], s[80:81]\n"
    "v_pk_mul_f32 v[60:61], v[60:61], s[80:81]\n"
    "v_mfma_f32_16x16x32_bf16 v[76:79], a[44:47], a[96:99], v[76:79]\n"
    "v_pk_mul_f32 v[62:63], v[62:63], s[80:81]\n"
    "v_cvt_pk_bf16_f32 v32, v32, v33\n"
    "v_cvt_pk_bf16_f32 v33, v34, v35\n"
    "v_cvt_pk_bf16_f32 v34, v36, v37\n"
    "v_cvt_pk_bf16_f32 v35, v38, v39\n"
    "v_cvt_pk_bf16_f32 v36, v40, v41\n"
    "v_cvt_pk_bf16_f32 v37, v42, v43\n"
    "v_cvt_pk_bf16_f32 v38, v44, v45\n"
    "v_cvt_pk_bf16_f32 v39, v46, v47\n"
    "v_mfma_f32_16x16x32_bf16 v[80:83], a[48:51], a[96:99], v[80:83]\n"
    "v_cvt_pk_bf16_f32 v40, v48, v49\n"
    "v_cvt_pk_bf16_f32 v41, v50, v51\n"
    "v_cvt_pk_bf16_f32 v42, v52, v53\n"
    "v_cvt_pk_bf16_f32 v43, v54, v55\n"
    "v_cvt_pk_bf16_f32 v44, v56, v57\n"
    "v_cvt_pk_bf16_f32 v45, v58, v59\n"
    "v_cvt_pk_bf16_f32 v46, v60, v61\n"
    "v_cvt_pk_bf16_f32 v47, v62, v63\n"
    "v_permlane32_swap_b32 v32, v34\n"
    "v_mfma_f32_16x16x32_bf16 v[84:87], a[52:55], a[96:99], v[84:87]\n"
    "v_permlane32_swap_b32 v33, v35\n"
    "v_permlane32_swap_b32 v36, v38\n"
    "v_permlane32_swap_b32 v37, v39\n"
    "v_permlane32_swap_b32 v40, v42\n"
    "v_permlane32_swap_b32 v41, v43\n"
    "v_mfma_f32_16x16x32_bf16 v[88:91], a[56:59], a[96:99], v[88:91]\n"
    "v_permlane32_swap_b32 v44, v46\n"
    "v_permlane32_swap_b32 v45, v47\n"
    "v_permlane16_swap_b32 v32, v34\n"
    "v_permlane16_swap_b32 v33, v35\n"
    "v_permlane16_swap_b32 v36, v38\n"
    "v_mfma_f32_16x16x32_bf16 v[92:95], a[60:63], a[96:99], v[92:95]\n"
    "v_permlane16_swap_b32 v37, v39\n"
    "v_permlane16_swap_b32 v40, v42\n"
    "v_permlane16_swap_b32 v41, v43\n"
    "v_permlane16_swap_b32 v44, v46\n"
    "v_permlane16_swap_b32 v45, v47\n"
    "v_mfma_f32_16x16x32_bf16 v[96:99], a[32:35], a[100:103], v[96:99]\n"
    "v_pk_mul_f32 v[64:65], v[64:65], s[80:81]\n"
    "v_pk_mul_f32 v[66:67], v[66:67], s[80:81]\n"
    "v_pk_mul_f32 v[68:69], v[68:69], s[80:81]\n"
    "v_pk_mul_f32 v[70:71], v[70:71], s[80:81]\n"
    "v_pk_mul_f32 v[72:73], v[72:73], s[80:81]\n"
    "v_mfma_f32_16x16x32_bf16 v[100:103], a[36:39], a[100:103], v[100:103]\n"
    "v_pk_mul_f32 v[74:75], v[74:75], s[80:81]\n"
    "v_pk_mul_f32 v[76:77], v[76:77], s[80:81]\n"
    "v_pk_mul_f32 v[78:79], v[78:79], s[80:81]\n"
    "v_pk_mul_f32 v[80:81], v[80:81], s[80:81]\n"
    "v_pk_mul_f32 v[82:83], v[82:83], s[80:81]\n"
    "v_mfma_f32_16x16x32_bf16 v[104:107], a[40:43], a[100:103], v[104:107]\n"
    "v_pk_mul_f32 v[84:85], v[84:85], s[80:81]\n"
    "v_pk_mul_f32 v[86:87], v[86:87], s[80:81]\n"
    "v_pk_mul_f32 v[88:89], v[88:89], s[80:81]\n"
    "v_pk_mul_f32 v[90:91], v[90:91], s[80:81]\n"
    "v_pk_mul_f32 v[92:93], v[92:93], s[80:81]\n"
    "v_mfma_f32_16x16x32_bf16 v[108:111], a[44:47], a[100:103], v[108:111]\n"
    "v_pk_mul_f32 v[94:95], v[94:95], s[80:81]\n"
    "v_cvt_pk_bf16_f32 v48, v64, v65\n"
    "v_cvt_pk_bf16_f32 v49, v66, v67\n"
    "v_cvt_pk_bf16_f32 v50, v68, v69\n"
    "v_cvt_pk_bf16_f32 v51, v70, v71\n"
    "v_cvt_pk_bf16_f32 v52, v72, v73\n"
    "v_cvt_pk_bf16_f32 v53, v74, v75\n"
    "v_cvt_pk_bf16_f32 v54, v76, v77\n"
    "v_cvt_pk_bf16_f32 v55, v78, v79\n"
    "v_mfma_f32_16x16x32_bf16 v[112:115], a[48:51], a[100:103], v[112:115]\n"
    "v_cvt_pk_bf16_f32 v56, v80, v81\n"
    "v_cvt_pk_bf16_f32 v57, v82, v83\n"
    "v_cvt_pk_bf16_f32 v58, v84, v85\n"
    "v_cvt_pk_bf16_f32 v59, v86, v87\n"
    "v_cvt_pk_bf16_f32 v60, v88, v89\n"
    "v_cvt_pk_bf16_f32 v61, v90, v91\n"
    "v_cvt_pk_bf16_f32 v62, v92, v93\n"
    "v_cvt_pk_bf16_f32 v63, v94, v95\n"
    "v_permlane32_swap_b32 v48, v50\n"
    "v_mfma_f32_16x16x32_bf16 v[116:119], a[52:55], a[100:103], v[116:119]\n"
    "v_permlane32_swap_b32 v49, v51\n"
    "v_permlane32_swap_b32 v52, v54\n"
    "v_permlane32_swap_b32 v53, v55\n"
    "v_permlane32_swap_b32 v56, v58\n"
    "v_permlane32_swap_b32 v57, v59\n"
    "v_mfma_f32_16x16x32_bf16 v[120:123], a[56:59], a[100:103], v[120:123]\n"
    "v_permlane32_swap_b32 v60, v62\n"
    "v_permlane32_swap_b32 v61, v63\n"
    "v_permlane16_swap_b32 v48, v50\n"
    "v_permlane16_swap_b32 v49, v51\n"
    "v_permlane16_swap_b32 v52, v54\n"
    "v_mfma_f32_16x16x32_bf16 v[124:127], a[60:63], a[100:103], v[124:127]\n"
    "v_permlane16_swap_b32 v53, v55\n"
    "v_permlane16_swap_b32 v56, v58\n"
    "v_permlane16_swap_b32 v57, v59\n"
    "v_permlane16_swap_b32 v60, v62\n"
    "v_permlane16_swap_b32 v61, v63\n"
    "v_mfma_f32_16x16x32_bf16 v[128:131], a[32:35], a[104:107], v[128:131]\n"
    "v_pk_mul_f32 v[96:97], v[96:97], s[80:81]\n"
    "v_pk_mul_f32 v[98:99], v[98:99], s[80:81]\n"
    "v_pk_mul_f32 v[100:101], v[100:101], s[80:81]\n"
    "v_pk_mul_f32 v[102:103], v[102:103], s[80:81]\n"
    "v_pk_mul_f32 v[104:105], v[104:105], s[80:81]\n"
    "v_mfma_f32_16x16x32_bf16 v[132:135], a[36:39], a[104:107], v[132:135]\n"
    "v_pk_mul_f32 v[106:107], v[106:107], s[80:81]\n"
    "v_pk_mul_f32 v[108:109], v[108:109], s[80:81]\n"
    "v_pk_mul_f32 v[110:111], v[110:111], s[80:81]\n"
    "v_pk_mul_f32 v[112:113], v[112:113], s[80:81]\n"
    "v_pk_mul_f32 v[114:115], v[114:115], s[80:81]\n"
    "v_mfma_f32_16x16x32_bf16 v[136:139], a[40:43], a[104:107], v[136:139]\n"
    "v_pk_mul_f32 v[116:117], v[116:117], s[80:81]\n"
    "v_pk_mul_f32 v[118:119], v[118:119], s[80:81]\n"
    "v_pk_mul_f32 v[120:121], v[120:121], s[80:81]\n"
    "v_pk_mul_f32 v[122:123], v[122:123], s[80:81]\n"
    "v_pk_mul_f32 v[124:125], v[124:125], s[80:81]\n"
    "v_mfma_f32_16x16x32_bf16 v[140:143], a[44:47], a[104:107], v[140:143]\n"
    "v_pk_mul_f32 v[126:127], v[126:127], s[80:81]\n"
    "v_cvt_pk_bf16_f32 v64, v96, v97\n"
    "v_cvt_pk_bf16_f32 v65, v98, v99\n"
    "v_cvt_pk_bf16_f32 v66, v100, v101\n"
    "v_cvt_pk_bf16_f32 v67, v102, v103\n"
    "v_cvt_pk_bf16_f32 v68, v104, v105\n"
    "v_cvt_pk_bf16_f32 v69, v106, v107\n"
    "v_cvt_pk_bf16_f32 v70, v108, v109\n"
    "v_cvt_pk_bf16_f32 v71, v110, v111\n"
    "v_mfma_f32_16x16x32_bf16 v[144:147], a[48:51], a[104:107], v[144:147]\n"
    "v_cvt_pk_bf16_f32 v72, v112, v113\n"
    "v_cvt_pk_bf16_f32 v73, v114, v115\n"
    "v_cvt_pk_bf16_f32 v74, v116, v117\n"
    "v_cvt_pk_bf16_f32 v75, v118, v119\n"
    "v_cvt_pk_bf16_f32 v76, v120, v121\n"
    "v_cvt_pk_bf16_f32 v77, v122, v123\n"
    "v_cvt_pk_bf16_f32 v78, v124, v125\n"
    "v_cvt_pk_bf16_f32 v79, v126, v127\n"
    "v_permlane32_swap_b32 v64, v66\n"
    "v_mfma_f32_16x16x32_bf16 v[148:151], a[52:55], a[104:107], v[148:151]\n"
    "v_permlane32_swap_b32 v65, v67\n"
    "v_permlane32_swap_b32 v68, v70\n"
    "v_permlane32_swap_b32 v69, v71\n"
    "v_permlane32_swap_b32 v72, v74\n"
    "v_permlane32_swap_b32 v73, v75\n"
    "v_mfma_f32_16x16x32_bf16 v[152:155], a[56:59], a[104:107], v[152:155]\n"
    "v_permlane32_swap_b32 v76, v78\n"
    "v_permlane32_swap_b32 v77, v79\n"
    "v_permlane16_swap_b32 v64, v66\n"
    "v_permlane16_swap_b32 v65, v67\n"
    "v_permlane16_swap_b32 v68, v70\n"
    "v_mfma_f32_16x16x32_bf16 v[156:159], a[60:63], a[104:107], v[156:159]\n"
    "v_permlane16_swap_b32 v69, v71\n"
    "v_permlane16_swap_b32 v72, v74\n"
    "v_permlane16_swap_b32 v73, v75\n"
    "v_permlane16_swap_b32 v76, v78\n"
    "v_permlane16_swap_b32 v77, v79\n"
    "v_mfma_f32_16x16x32_bf16 v[160:163], a[32:35], a[108:111], v[160:163]\n"
    "v_pk_mul_f32 v[128:129], v[128:129], s[80:81]\n"
    "v_pk_mul_f32 v[130:131], v[130:131], s[80:81]\n"
    "v_pk_mul_f32 v[132:133], v[132:133], s[80:81]\n"
    "v_pk_mul_f32 v[134:135], v[134:135], s[80:81]\n"
    "v_pk_mul_f32 v[136:137], v[136:137], s[80:81]\n"
    "v_mfma_f32_16x16x32_bf16 v[164:167], a[36:39], a[108:111], v[164:167]\n"
    "v_pk_mul_f32 v[138:139], v[138:139], s[80:81]\n"
    "v_pk_mul_f32 v[140:141], v[140:141], s[80:81]\n"
    "v_pk_mul_f32 v[142:143], v[142:143], s[80:81]\n"
    "v_pk_mul_f32 v[144:145], v[144:145], s[80:81]\n"
    "v_pk_mul_f32 v[146:147], v[146:147], s[80:81]\n"
    "v_mfma_f32_16x16x32_bf16 v[168:171], a[40:43], a[108:111], v[168:171]\n"
    "v_pk_mul_f32 v[148:149], v[148:149], s[80:81]\n"
    "v_pk_mul_f32 v[150:151], v[150:151], s[80:81]\n"
    "v_pk_mul_f32 v[152:153], v[152:153], s[80:81]\n"
    "v_pk_mul_f32 v[154:155], v[154:155], s[80:81]\n"
    "v_pk_mul_f32 v[156:157], v[156:157], s[80:81]\n"
    "v_mfma_f32_16x16x32_bf16 v[172:175], a[44:47], a[108:111], v[172:175]\n"
    "v_pk_mul_f32 v[158:159], v[158:159], s[80:81]\n"
    "v_cvt_pk_bf16_f32 v80, v128, v129\n"
    "v_cvt_pk_bf16_f32 v81, v130, v131\n"
    "v_cvt_pk_bf16_f32 v82, v132, v133\n"
    "v_cvt_pk_bf16_f32 v83, v134, v135\n"
    "v_cvt_pk_bf16_f32 v84, v136, v137\n"
    "v_cvt_pk_bf16_f32 v85, v138, v139\n"
    "v_cvt_pk_bf16_f32 v86, v140, v141\n"
    "v_cvt_pk_bf16_f32 v87, v142, v143\n"
    "v_mfma_f32_16x16x32_bf16 v[176:179], a[48:51], a[108:111], v[176:179]\n"
    "v_cvt_pk_bf16_f32 v88, v144, v145\n"
    "v_cvt_pk_bf16_f32 v89, v146, v147\n"
    "v_cvt_pk_bf16_f32 v90, v148, v149\n"
    "v_cvt_pk_bf16_f32 v91, v150, v151\n"
    "v_cvt_pk_bf16_f32 v92, v152, v153\n"
    "v_cvt_pk_bf16_f32 v93, v154, v155\n"
    "v_cvt_pk_bf16_f32 v94, v156, v157\n"
    "v_cvt_pk_bf16_f32 v95, v158, v159\n"
    "v_permlane32_swap_b32 v80, v82\n"
    "v_mfma_f32_16x16x32_bf16 v[180:183], a[52:55], a[108:111], v[180:183]\n"
    "v_permlane32_swap_b32 v81, v83\n"
    "v_permlane32_swap_b32 v84, v86\n"
    "v_permlane32_swap_b32 v85, v87\n"
    "v_permlane32_swap_b32 v88, v90\n"
    "v_permlane32_swap_b32 v89, v91\n"
    "v_mfma_f32_16x16x32_bf16 v[184:187], a[56:59], a[108:111], v[184:187]\n"
    "v_permlane32_swap_b32 v92, v94\n"
    "v_permlane32_swap_b32 v93, v95\n"
    "v_permlane16_swap_b32 v80, v82\n"
    "v_permlane16_swap_b32 v81, v83\n"
    "v_permlane16_swap_b32 v84, v86\n"
    "v_mfma_f32_16x16x32_bf16 v[188:191], a[60:63], a[108:111], v[188:191]\n"
    "v_permlane16_swap_b32 v85, v87\n"
    "v_permlane16_swap_b32 v88, v90\n"
    "v_permlane16_swap_b32 v89, v91\n"
    "v_permlane16_swap_b32 v92, v94\n"
    "v_permlane16_swap_b32 v93, v95\n"
    "v_mfma_f32_16x16x32_bf16 v[192:195], a[32:35], a[112:115], v[192:195]\n"
    "v_pk_mul_f32 v[160:161], v[160:161], s[80:81]\n"
    "v_pk_mul_f32 v[162:163], v[162:163], s[80:81]\n"
    "v_pk_mul_f32 v[164:165], v[164:165], s[80:81]\n"
    "v_pk_mul_f32 v[166:167], v[166:167], s[80:81]\n"
    "v_pk_mul_f32 v[168:169], v[168:169], s[80:81]\n"
    "v_mfma_f32_16x16x32_bf16 v[196:199], a[36:39], a[112:115], v[196:199]\n"
    "v_pk_mul_f32 v[170:171], v[170:171], s[80:81]\n"
    "v_pk_mul_f32 v[172:173], v[172:173], s[80:81]\n"
    "v_pk_mul_f32 v[174:175], v[174:175], s[80:81]\n"
    "v_pk_mul_f32 v[176:177], v[176:177], s[80:81]\n"
    "v_pk_mul_f32 v[178:179], v[178:179], s[80:81]\n"
    "v_mfma_f32_16x16x32_bf16 v[200:203], a[40:43], a[112:115], v[200:203]\n"
    "v_pk_mul_f32 v[180:181], v[180:181], s[80:81]\n"
    "v_pk_mul_f32 v[182:183], v[182:183], s[80:81]\n"
    "v_pk_mul_f32 v[184:185], v[184:185], s[80:81]\n"
    "v_pk_mul_f32 v[186:187], v[186:187], s[80:81]\n"
    "v_pk_mul_f32 v[188:189], v[188:189], s[80:81]\n"
    "v_mfma_f32_16x16x32_bf16 v[204:207], a[44:47], a[112:115], v[204:207]\n"
    "v_pk_mul_f32 v[190:191], v[190:191], s[80:81]\n"
    "v_cvt_pk_bf16_f32 v96, v160, v161\n"
    "v_cvt_pk_bf16_f32 v97, v162, v163\n"
    "v_cvt_pk_bf16_f32 v98, v164, v165\n"
    "v_cvt_pk_bf16_f32 v99, v166, v167\n"
    "v_cvt_pk_bf16_f32 v100, v168, v169\n"
    "v_cvt_pk_bf16_f32 v101, v170, v171\n"
    "v_cvt_pk_bf16_f32 v102, v172, v173\n"
    "v_cvt_pk_bf16_f32 v103, v174, v175\n"
    "v_mfma_f32_16x16x32_bf16 v[208:211], a[48:51], a[112:115], v[208:211]\n"
    "v_cvt_pk_bf16_f32 v104, v176, v177\n"
    "v_cvt_pk_bf16_f32 v105, v178, v179\n"
    "v_cvt_pk_bf16_f32 v106, v180, v181\n"
    "v_cvt_pk_bf16_f32 v107, v182, v183\n"
    "v_cvt_pk_bf16_f32 v108, v184, v185\n"
    "v_cvt_pk_bf16_f32 v109, v186, v187\n"
    "v_cvt_pk_bf16_f32 v110, v188, v189\n"
    "v_cvt_pk_bf16_f32 v111, v190, v191\n"
    "v_permlane32_swap_b32 v96, v98\n"
    "v_mfma_f32_16x16x32_bf16 v[212:215], a[52:55], a[112:115], v[212:215]\n"
    "v_permlane32_swap_b32 v97, v99\n"
    "v_permlane32_swap_b32 v100, v102\n"
    "v_permlane32_swap_b32 v101, v103\n"
    "v_permlane32_swap_b32 v104, v106\n"
    "v_permlane32_swap_b32 v105, v107\n"
    "v_mfma_f32_16x16x32_bf16 v[216:219], a[56:59], a[112:115], v[216:219]\n"
    "v_permlane32_swap_b32 v108, v110\n"
    "v_permlane32_swap_b32 v109, v111\n"
    "v_permlane16_swap_b32 v96, v98\n"
    "v_permlane16_swap_b32 v97, v99\n"
    "v_permlane16_swap_b32 v100, v102\n"
    "v_mfma_f32_16x16x32_bf16 v[220:223], a[60:63], a[112:115], v[220:223]\n"
    "v_permlane16_swap_b32 v101, v103\n"
    "v_permlane16_swap_b32 v104, v106\n"
    "v_permlane16_swap_b32 v105, v107\n"
    "v_permlane16_swap_b32 v108, v110\n"
    "v_permlane16_swap_b32 v109, v111\n"
    "v_mfma_f32_16x16x32_bf16 v[224:227], a[32:35], a[116:119], v[224:227]\n"
    "v_pk_mul_f32 v[192:193], v[192:193], s[80:81]\n"
    "v_pk_mul_f32 v[194:195], v[194:195], s[80:81]\n"
    "v_pk_mul_f32 v[196:197], v[196:197], s[80:81]\n"
    "v_pk_mul_f32 v[198:199], v[198:199], s[80:81]\n"
    "v_pk_mul_f32 v[200:201], v[200:201], s[80:81]\n"
    "v_mfma_f32_16x16x32_bf16 v[228:231], a[36:39], a[116:119], v[228:231]\n"
    "v_pk_mul_f32 v[202:203], v[202:203], s[80:81]\n"
    "v_pk_mul_f32 v[204:205], v[204:205], s[80:81]\n"
    "v_pk_mul_f32 v[206:207], v[206:207], s[80:81]\n"
    "v_pk_mul_f32 v[208:209], v[208:209], s[80:81]\n"
    "v_pk_mul_f32 v[210:211], v[210:211], s[80:81]\n"
    "v_mfma_f32_16x16x32_bf16 v[232:235], a[40:43], a[116:119], v[232:235]\n"
    "v_pk_mul_f32 v[212:213], v[212:213], s[80:81]\n"
    "v_pk_mul_f32 v[214:215], v[214:215], s[80:81]\n"
    "v_pk_mul_f32 v[216:217], v[216:217], s[80:81]\n"
    "v_pk_mul_f32 v[218:219], v[218:219], s[80:81]\n"
    "v_pk_mul_f32 v[220:221], v[220:221], s[80:81]\n"
    "v_mfma_f32_16x16x32_bf16 v[236:239], a[44:47], a[116:119], v[236:239]\n"
    "v_pk_mul_f32 v[222:223], v[222:223], s[80:81]\n"
    "v_cvt_pk_bf16_f32 v112, v192, v193\n"
    "v_cvt_pk_bf16_f32 v113, v194, v195\n"
    "v_cvt_pk_bf16_f32 v114, v196, v197\n"
    "v_cvt_pk_bf16_f32 v115, v198, v199\n"
    "v_cvt_pk_bf16_f32 v116, v200, v201\n"
    "v_cvt_pk_bf16_f32 v117, v202, v203\n"
    "v_cvt_pk_bf16_f32 v118, v204, v205\n"
    "v_cvt_pk_bf16_f32 v119, v206, v207\n"
    "v_mfma_f32_16x16x32_bf16 v[240:243], a[48:51], a[116:119], v[240:243]\n"
    "v_cvt_pk_bf16_f32 v120, v208, v209\n"
    "v_cvt_pk_bf16_f32 v121, v210, v211\n"
    "v_cvt_pk_bf16_f32 v122, v212, v213\n"
    "v_cvt_pk_bf16_f32 v123, v214, v215\n"
    "v_cvt_pk_bf16_f32 v124, v216, v217\n"
    "v_cvt_pk_bf16_f32 v125, v218, v219\n"
    "v_cvt_pk_bf16_f32 v126, v220, v221\n"
    "v_cvt_pk_bf16_f32 v127, v222, v223\n"
    "v_permlane32_swap_b32 v112, v114\n"
    "v_mfma_f32_16x16x32_bf16 v[244:247], a[52:55], a[116:119], v[244:247]\n"
    "v_permlane32_swap_b32 v113, v115\n"
    "v_permlane32_swap_b32 v116, v118\n"
    "v_permlane32_swap_b32 v117, v119\n"
    "v_permlane32_swap_b32 v120, v122\n"
    "v_permlane32_swap_b32 v121, v123\n"
    "v_mfma_f32_16x16x32_bf16 v[248:251], a[56:59], a[116:119], v[248:251]\n"
    "v_permlane32_swap_b32 v124, v126\n"
    "v_permlane32_swap_b32 v125, v127\n"
    "v_permlane16_swap_b32 v112, v114\n"
    "v_permlane16_swap_b32 v113, v115\n"
    "v_permlane16_swap_b32 v116, v118\n"
    "v_mfma_f32_16x16x32_bf16 v[252:255], a[60:63], a[116:119], v[252:255]\n"
    "v_permlane16_swap_b32 v117, v119\n"
    "v_permlane16_swap_b32 v120, v122\n"
    "v_permlane16_swap_b32 v121, v123\n"
    "v_permlane16_swap_b32 v124, v126\n"
    "v_permlane16_swap_b32 v125, v127\n"
    "v_pk_mul_f32 v[224:225], v[224:225], s[80:81]\n"
    "v_pk_mul_f32 v[226:227], v[226:227], s[80:81]\n"
    "v_pk_mul_f32 v[228:229], v[228:229], s[80:81]\n"
    "v_pk_mul_f32 v[230:231], v[230:231], s[80:81]\n"
    "v_pk_mul_f32 v[232:233], v[232:233], s[80:81]\n"
    "v_pk_mul_f32 v[234:235], v[234:235], s[80:81]\n"
    "v_pk_mul_f32 v[236:237], v[236:237], s[80:81]\n"
    "v_pk_mul_f32 v[238:239], v[238:239], s[80:81]\n"
    "v_pk_mul_f32 v[240:241], v[240:241], s[80:81]\n"
    "v_pk_mul_f32 v[242:243], v[242:243], s[80:81]\n"
    "v_pk_mul_f32 v[244:245], v[244:245], s[80:81]\n"
    "v_pk_mul_f32 v[246:247], v[246:247], s[80:81]\n"
    "v_pk_mul_f32 v[248:249], v[248:249], s[80:81]\n"
    "v_pk_mul_f32 v[250:251], v[250:251], s[80:81]\n"
    "v_pk_mul_f32 v[252:253], v[252:253], s[80:81]\n"
    "v_pk_mul_f32 v[254:255], v[254:255], s[80:81]\n"
    "v_cvt_pk_bf16_f32 v128, v224, v225\n"
    "v_cvt_pk_bf16_f32 v129, v226, v227\n"
    "v_cvt_pk_bf16_f32 v130, v228, v229\n"
    "v_cvt_pk_bf16_f32 v131, v230, v231\n"
    "v_cvt_pk_bf16_f32 v132, v232, v233\n"
    "v_cvt_pk_bf16_f32 v133, v234, v235\n"
    "v_cvt_pk_bf16_f32 v134, v236, v237\n"
    "v_cvt_pk_bf16_f32 v135, v238, v239\n"
    "v_cvt_pk_bf16_f32 v136, v240, v241\n"
    "v_cvt_pk_bf16_f32 v137, v242, v243\n"
    "v_cvt_pk_bf16_f32 v138, v244, v245\n"
    "v_cvt_pk_bf16_f32 v139, v246, v247\n"
    "v_cvt_pk_bf16_f32 v140, v248, v249\n"
    "v_cvt_pk_bf16_f32 v141, v250, v251\n"
    "v_cvt_pk_bf16_f32 v142, v252, v253\n"
    "v_cvt_pk_bf16_f32 v143, v254, v255\n"
    "v_permlane32_swap_b32 v128, v130\n"
    "v_permlane32_swap_b32 v129, v131\n"
    "v_permlane32_swap_b32 v132, v134\n"
    "v_permlane32_swap_b32 v133, v135\n"
    "v_permlane32_swap_b32 v136, v138\n"
    "v_permlane32_swap_b32 v137, v139\n"
    "v_permlane32_swap_b32 v140, v142\n"
    "v_permlane32_swap_b32 v141, v143\n"
    "v_permlane16_swap_b32 v128, v130\n"
    "v_permlane16_swap_b32 v129, v131\n"
    "v_permlane16_swap_b32 v132, v134\n"
    "v_permlane16_swap_b32 v133, v135\n"
    "v_permlane16_swap_b32 v136, v138\n"
    "v_permlane16_swap_b32 v137, v139\n"
    "v_permlane16_swap_b32 v140, v142\n"
    "v_permlane16_swap_b32 v141, v143\n"
    "ds_read_b128 a[224:227], v16 offset:0\n"
    "ds_read_b128 a[240:243], v16 offset:4096\n"
    "ds_read_b128 a[228:231], v17 offset:0\n"
    "ds_read_b128 a[244:247], v17 offset:4096\n"
    "ds_read_b128 a[232:235], v18 offset:0\n"
    "ds_read_b128 a[248:251], v18 offset:4096\n"
    "ds_read_b128 a[236:239], v19 offset:0\n"
    "ds_read_b128 a[252:255], v19 offset:4096\n"
    "s_add_u32 m0, s64, 24576\n"
    "s_nop 0\n"
    "buffer_load_dwordx4 v20, s[44:47], s67 offen offset:0 nt lds\n"
    "buffer_load_dwordx4 v21, s[44:47], s67 offen offset:1024 nt lds\n"
    "buffer_load_dwordx4 v20, s[44:47], s67 offen offset:2048 nt lds\n"
    "buffer_load_dwordx4 v21, s[44:47], s67 offen offset:3072 nt lds\n"
    "s_add_u32 m0, s64, 28672\n"
    "s_nop 0\n"
    "buffer_load_dwordx4 v22, s[44:47], s67 offen offset:0 nt lds\n"
    "buffer_load_dwordx4 v23, s[44:47], s67 offen offset:1024 nt lds\n"
    "buffer_load_dwordx4 v22, s[44:47], s67 offen offset:2048 nt lds\n"
    "buffer_load_dwordx4 v23, s[44:47], s67 offen offset:3072 nt lds\n"
    "s_add_u32 s67, s67, 8192\n"
    "s_waitcnt lgkmcnt(0)\n"
    "v_mov_b32 v6, 1\n"
    "s_lshl_b32 s74, %[wid], 2\n"
    "s_add_u32 s74, s74, %[lds]\n"
    "s_add_u32 s74, s74, 146432\n"
    "v_mov_b32 v7, s74\n"
    "ds_write_b32 v7, v6\n"
    "v_lshrrev_b32 v26, 4, v25\n"
    "v_lshlrev_b32 v26, 10, v26\n"
    "v_sub_u32 v8, v8, v26\n"
    "v_sub_u32 v9, v9, v26\n"
    "v_sub_u32 v10, v10, v26\n"
    "v_sub_u32 v11, v11, v26\n"
    "v_sub_u32 v12, v12, v26\n"
    "v_sub_u32 v13, v13, v26\n"
    "v_sub_u32 v14, v14, v26\n"
    "v_sub_u32 v15, v15, v26\n"
    "v_mov_b32 v228, 0x3f803f80\n"
    "v_mov_b32 v229, v228\n"
    "v_mov_b32 v230, v228\n"
    "v_mov_b32 v231, v228\n"
    "s_mov_b32 s88, 31744\n"
    "s_mov_b64 s[60:61], %[pto]\n"
    "s_and_b32 s61, s61, 0xffff\n"
    "s_mov_b32 s62, 8192\n"
    "s_mov_b32 s63, 0x20000\n"
    "s_mov_b32 s86, 0\n"
    "s_lshl_b32 s73, %[wid], 14\n"
    "s_add_u32 s73, s73, 7168\n"
    "s_cmp_ge_u32 %[wid], 2\n"
    "s_cselect_b32 s73, s88, s73\n"
    "s_add_u32 s73, s73, %[lds]\n"
    "s_add_u32 s73, s73, 131072\n"
    "v_mov_b32 v27, v24\n"
    ".LW_att_%=:\n"
    "v_mfma_f32_16x16x32_bf16 v[144:147], a[224:227], v[32:35], 0\n"
    "v_mfma_f32_16x16x32_bf16 v[148:151], a[240:243], v[32:35], 0\n"
    "v_mfma_f32_16x16x32_bf16 v[144:147], a[228:231], v[36:39], v[144:147]\n"
    "v_mfma_f32_16x16x32_bf16 v[148:151], a[244:247], v[36:39], v[148:151]\n"
    "v_mfma_f32_16x16x32_bf16 v[144:147], a[232:235], v[40:43], v[144:147]\n"
    "v_mfma_f32_16x16x32_bf16 v[148:151], a[248:251], v[40:43], v[148:151]\n"
    "v_mfma_f32_16x16x32_bf16 v[144:147], a[236:239], v[44:47], v[144:147]\n"
    "v_mfma_f32_16x16x32_bf16 v[148:151], a[252:255], v[44:47], v[148:151]\n"
    "s_waitcnt vmcnt(16)\n"
    "v_mfma_f32_16x16x32_bf16 v[152:155], a[224:227], v[48:51], 0\n"
    "s_nop 4\n"
    "v_exp_f32 v144, v144\n"
    "ds_read_b64_tr_b16 v[196:197], v8 offset:8192\n"
    "ds_read_b64_tr_b16 v[198:199], v8 offset:12288\n"
    "v_mfma_f32_16x16x32_bf16 v[156:159], a[240:243], v[48:51], 0\n"
    "v_exp_f32 v145, v145\n"
    "ds_read_b64_tr_b16 v[200:201], v9 offset:8192\n"
    "v_exp_f32 v146, v146\n"
    "v_mfma_f32_16x16x32_bf16 v[152:155], a[228:231], v[52:55], v[152:155]\n"
    "ds_read_b64_tr_b16 v[202:203], v9 offset:12288\n"
    "v_cvt_pk_bf16_f32 v164, v144, v145\n"
    "ds_read_b64_tr_b16 v[204:205], v10 offset:8192\n"
    "ds_read_b64_tr_b16 v[206:207], v10 offset:12288\n"
    "v_mfma_f32_16x16x32_bf16 v[156:159], a[244:247], v[52:55], v[156:159]\n"
    "v_exp_f32 v147, v147\n"
    "ds_read_b64_tr_b16 v[208:209], v11 offset:8192\n"
    "v_exp_f32 v148, v148\n"
    "v_mfma_f32_16x16x32_bf16 v[152:155], a[232:235], v[56:59], v[152:155]\n"
    "ds_read_b64_tr_b16 v[210:211], v11 offset:12288\n"
    "v_cvt_pk_bf16_f32 v165, v146, v147\n"
    "ds_read_b64_tr_b16 v[212:213], v12 offset:8192\n"
    "ds_read_b64_tr_b16 v[214:215], v12 offset:12288\n"
    "v_mfma_f32_16x16x32_bf16 v[156:159], a[248:251], v[56:59], v[156:159]\n"
    "v_exp_f32 v149, v149\n"
    "ds_read_b64_tr_b16 v[216:217], v13 offset:8192\n"
    "v_exp_f32 v150, v150\n"
    "v_mfma_f32_16x16x32_bf16 v[152:155], a[236:239], v[60:63], v[152:155]\n"
    "ds_read_b64_tr_b16 v[218:219], v13 offset:12288\n"
    "v_exp_f32 v151, v151\n"
    "ds_read_b64_tr_b16 v[220:221], v14 offset:8192\n"
    "ds_read_b64_tr_b16 v[222:223], v14 offset:12288\n"
    "v_mfma_f32_16x16x32_bf16 v[156:159], a[252:255], v[60:63], v[156:159]\n"
    "v_cvt_pk_bf16_f32 v166, v148, v149\n"
    "ds_read_b64_tr_b16 v[224:225], v15 offset:8192\n"
    "v_cvt_pk_bf16_f32 v167, v150, v151\n"
    "ds_read_b64_tr_b16 v[226:227], v15 offset:12288\n"
    "v_mfma_f32_16x16x32_bf16 v[144:147], a[224:227], v[64:67], 0\n"
    "v_exp_f32 v152, v152\n"
    "v_mfma_f32_16x16x32_bf16 v[148:151], a[240:243], v[64:67], 0\n"
    "s_add_u32 m0, s64, 0\n"
    "s_waitcnt lgkmcnt(14)\n"
    "v_mfma_f32_16x16x32_bf16 a[0:3], v[196:199], v[164:167], 0\n"
    "v_exp_f32 v153, v153\n"
    "v_mfma_f32_16x16x32_bf16 v[144:147], a[228:231], v[68:71], v[144:147]\n"
    "buffer_load_dwordx4 v20, s[40:43], s66 offen offset:0 nt lds\n"
    "v_exp_f32 v154, v154\n"
    "s_waitcnt lgkmcnt(12)\n"
    "v_mfma_f32_16x16x32_bf16 a[4:7], v[200:203], v[164:167], 0\n"
    "buffer_load_dwordx4 v21, s[40:43], s66 offen offset:1024 nt lds\n"
    "v_mfma_f32_16x16x32_bf16 v[148:151], a[244:247], v[68:71], v[148:151]\n"
    "v_cvt_pk_bf16_f32 v160, v152, v153\n"
    "buffer_load_dwordx4 v20, s[40:43], s66 offen offset:2048 nt lds\n"
    "s_waitcnt lgkmcnt(10)\n"
    "v_mfma_f32_16x16x32_bf16 a[8:11], v[204:207], v[164:167], 0\n"
    "v_exp_f32 v155, v155\n"
    "v_mfma_f32_16x16x32_bf16 v[144:147], a[232:235], v[72:75], v[144:147]\n"
    "buffer_load_dwordx4 v21, s[40:43], s66 offen offset:3072 nt lds\n"
    "s_waitcnt lgkmcnt(8)\n"
    "v_mfma_f32_16x16x32_bf16 a[12:15], v[208:211], v[164:167], 0\n"
    "v_exp_f32 v156, v156\n"
    "v_mfma_f32_16x16x32_bf16 v[148:151], a[248:251], v[72:75], v[148:151]\n"
    "s_add_u32 m0, s64, 4096\n"
    "v_cvt_pk_bf16_f32 v161, v154, v155\n"
    "s_waitcnt lgkmcnt(6)\n"
    "v_mfma_f32_16x16x32_bf16 a[16:19], v[212:215], v[164:167], 0\n"
    "buffer_load_dwordx4 v22, s[40:43], s66 offen offset:0 nt lds\n"
    "v_exp_f32 v157, v157\n"
    "v_mfma_f32_16x16x32_bf16 v[144:147], a[236:239], v[76:79], v[144:147]\n"
    "buffer_load_dwordx4 v23, s[40:43], s66 offen offset:1024 nt lds\n"
    "s_waitcnt lgkmcnt(4)\n"
    "v_mfma_f32_16x16x32_bf16 a[20:23], v[216:219], v[164:167], 0\n"
    "v_exp_f32 v158, v158\n"
    "v_mfma_f32_16x16x32_bf16 v[148:151], a[252:255], v[76:79], v[148:151]\n"
    "buffer_load_dwordx4 v22, s[40:43], s66 offen offset:2048 nt lds\n"
    "s_waitcnt lgkmcnt(2)\n"
    "v_mfma_f32_16x16x32_bf16 a[24:27], v[220:223], v[164:167], 0\n"
    "v_exp_f32 v159, v159\n"
    "s_waitcnt lgkmcnt(0)\n"
    "v_mfma_f32_16x16x32_bf16 a[28:31], v[224:227], v[164:167], 0\n"
    "buffer_load_dwordx4 v23, s[40:43], s66 offen offset:3072 nt lds\n"
    "v_cvt_pk_bf16_f32 v162, v156, v157\n"
    "v_mfma_f32_16x16x32_bf16 v[168:171], v[228:231], v[164:167], 0\n"
    "s_add_u32 s66, s66, 8192\n"
    "v_cvt_pk_bf16_f32 v163, v158, v159\n"
    "v_mfma_f32_16x16x32_bf16 v[152:155], a[224:227], v[80:83], 0\n"
    "v_exp_f32 v144, v144\n"
    "v_mfma_f32_16x16x32_bf16 v[156:159], a[240:243], v[80:83], 0\n"
    "v_mfma_f32_16x16x32_bf16 a[32:35], v[196:199], v[160:163], 0\n"
    "v_exp_f32 v145, v145\n"
    "v_mfma_f32_16x16x32_bf16 v[152:155], a[228:231], v[84:87], v[152:155]\n"
    "v_mfma_f32_16x16x32_bf16 a[36:39], v[200:203], v[160:163], 0\n"
    "v_exp_f32 v146, v146\n"
    "v_mfma_f32_16x16x32_bf16 v[156:159], a[244:247], v[84:87], v[156:159]\n"
    "v_cvt_pk_bf16_f32 v164, v144, v145\n"
    "v_mfma_f32_16x16x32_bf16 a[40:43], v[204:207], v[160:163], 0\n"
    "v_exp_f32 v147, v147\n"
    "v_mfma_f32_16x16x32_bf16 v[152:155], a[232:235], v[88:91], v[152:155]\n"
    "v_mfma_f32_16x16x32_bf16 a[44:47], v[208:211], v[160:163], 0\n"
    "v_exp_f32 v148, v148\n"
    "v_mfma_f32_16x16x32_bf16 v[156:159], a[248:251], v[88:91], v[156:159]\n"
    "v_cvt_pk_bf16_f32 v165, v146, v147\n"
    "v_mfma_f32_16x16x32_bf16 a[48:51], v[212:215], v[160:163], 0\n"
    "v_mfma_f32_16x16x32_bf16 v[152:155], a[236:239], v[92:95], v[152:155]\n"
    "v_exp_f32 v149, v149\n"
    "v_mfma_f32_16x16x32_bf16 a[52:55], v[216:219], v[160:163], 0\n"
    "v_exp_f32 v150, v150\n"
    "v_mfma_f32_16x16x32_bf16 v[156:159], a[252:255], v[92:95], v[156:159]\n"
    "v_mfma_f32_16x16x32_bf16 a[56:59], v[220:223], v[160:163], 0\n"
    "v_exp_f32 v151, v151\n"
    "v_mfma_f32_16x16x32_bf16 a[60:63], v[224:227], v[160:163], 0\n"
    "v_cvt_pk_bf16_f32 v166, v148, v149\n"
    "v_mfma_f32_16x16x32_bf16 v[172:175], v[228:231], v[160:163], 0\n"
    "v_cvt_pk_bf16_f32 v167, v150, v151\n"
    "v_mfma_f32_16x16x32_bf16 v[144:147], a[224:227], v[96:99], 0\n"
    "v_exp_f32 v152, v152\n"
    "v_mfma_f32_16x16x32_bf16 v[148:151], a[240:243], v[96:99], 0\n"
    "s_add_u32 m0, s64, 8192\n"
    "v_mfma_f32_16x16x32_bf16 a[64:67], v[196:199], v[164:167], 0\n"
    "v_exp_f32 v153, v153\n"
    "v_mfma_f32_16x16x32_bf16 v[144:147], a[228:231], v[100:103], v[144:147]\n"
    "buffer_load_dwordx4 v20, s[44:47], s67 offen offset:0 nt lds\n"
    "v_exp_f32 v154, v154\n"
    "v_mfma_f32_16x16x32_bf16 a[68:71], v[200:203], v[164:167], 0\n"
    "buffer_load_dwordx4 v21, s[44:47], s67 offen offset:1024 nt lds\n"
    "v_mfma_f32_16x16x32_bf16 v[148:151], a[244:247], v[100:103], v[148:151]\n"
    "v_cvt_pk_bf16_f32 v160, v152, v153\n"
    "buffer_load_dwordx4 v20, s[44:47], s67 offen offset:2048 nt lds\n"
    "v_mfma_f32_16x16x32_bf16 a[72:75], v[204:207], v[164:167], 0\n"
    "v_exp_f32 v155, v155\n"
    "v_mfma_f32_16x16x32_bf16 v[144:147], a[232:235], v[104:107], v[144:147]\n"
    "buffer_load_dwordx4 v21, s[44:47], s67 offen offset:3072 nt lds\n"
    "v_mfma_f32_16x16x32_bf16 a[76:79], v[208:211], v[164:167], 0\n"
    "v_exp_f32 v156, v156\n"
    "v_mfma_f32_16x16x32_bf16 v[148:151], a[248:251], v[104:107], v[148:151]\n"
    "s_add_u32 m0, s64, 12288\n"
    "v_cvt_pk_bf16_f32 v161, v154, v155\n"
    "v_mfma_f32_16x16x32_bf16 a[80:83], v[212:215], v[164:167], 0\n"
    "buffer_load_dwordx4 v22, s[44:47], s67 offen offset:0 nt lds\n"
    "v_exp_f32 v157, v157\n"
    "v_mfma_f32_16x16x32_bf16 v[144:147], a[236:239], v[108:111], v[144:147]\n"
    "buffer_load_dwordx4 v23, s[44:47], s67 offen offset:1024 nt lds\n"
    "v_mfma_f32_16x16x32_bf16 a[84:87], v[216:219], v[164:167], 0\n"
    "v_exp_f32 v158, v158\n"
    "v_mfma_f32_16x16x32_bf16 v[148:151], a[252:255], v[108:111], v[148:151]\n"
    "buffer_load_dwordx4 v22, s[44:47], s67 offen offset:2048 nt lds\n"
    "v_mfma_f32_16x16x32_bf16 a[88:91], v[220:223], v[164:167], 0\n"
    "v_exp_f32 v159, v159\n"
    "v_mfma_f32_16x16x32_bf16 a[92:95], v[224:227], v[164:167], 0\n"
    "buffer_load_dwordx4 v23, s[44:47], s67 offen offset:3072 nt lds\n"
    "v_cvt_pk_bf16_f32 v162, v156, v157\n"
    "v_mfma_f32_16x16x32_bf16 v[176:179], v[228:231], v[164:167], 0\n"
    "s_add_u32 s67, s67, 8192\n"
    "v_cvt_pk_bf16_f32 v163, v158, v159\n"
    "v_mfma_f32_16x16x32_bf16 v[152:155], a[224:227], v[112:115], 0\n"
    "v_exp_f32 v144, v144\n"
    "v_mfma_f32_16x16x32_bf16 v[156:159], a[240:243], v[112:115], 0\n"
    "s_mov_b32 m0, s73\n"
    "v_mfma_f32_16x16x32_bf16 a[96:99], v[196:199], v[160:163], 0\n"
    "v_exp_f32 v145, v145\n"
    "v_mfma_f32_16x16x32_bf16 v[152:155], a[228:231], v[116:119], v[152:155]\n"
    "v_mfma_f32_16x16x32_bf16 a[100:103], v[200:203], v[160:163], 0\n"
    "v_exp_f32 v146, v146\n"
    "v_mfma_f32_16x16x32_bf16 v[156:159], a[244:247], v[116:119], v[156:159]\n"
    "v_cvt_pk_bf16_f32 v164, v144, v145\n"
    "v_mfma_f32_16x16x32_bf16 a[104:107], v[204:207], v[160:163], 0\n"
    "v_exp_f32 v147, v147\n"
    "v_mfma_f32_16x16x32_bf16 v[152:155], a[232:235], v[120:123], v[152:155]\n"
    "buffer_load_dwordx4 v27, s[60:63], s86 offen lds\n"
    "v_mfma_f32_16x16x32_bf16 a[108:111], v[208:211], v[160:163], 0\n"
    "v_exp_f32 v148, v148\n"
    "v_mfma_f32_16x16x32_bf16 v[156:159], a[248:251], v[120:123], v[156:159]\n"
    "v_cvt_pk_bf16_f32 v165, v146, v147\n"
    "v_mfma_f32_16x16x32_bf16 a[112:115], v[212:215], v[160:163], 0\n"
    "v_mfma_f32_16x16x32_bf16 v[152:155], a[236:239], v[124:127], v[152:155]\n"
    "v_exp_f32 v149, v149\n"
    "v_mfma_f32_16x16x32_bf16 a[116:119], v[216:219], v[160:163], 0\n"
    "v_exp_f32 v150, v150\n"
    "v_mfma_f32_16x16x32_bf16 v[156:159], a[252:255], v[124:127], v[156:159]\n"
    "s_add_u32 s86, s86, 1024\n"
    "v_mfma_f32_16x16x32_bf16 a[120:123], v[220:223], v[160:163], 0\n"
    "v_exp_f32 v151, v151\n"
    "v_mfma_f32_16x16x32_bf16 a[124:127], v[224:227], v[160:163], 0\n"
    "v_cvt_pk_bf16_f32 v166, v148, v149\n"
    "v_mfma_f32_16x16x32_bf16 v[180:183], v[228:231], v[160:163], 0\n"
    "v_cvt_pk_bf16_f32 v167, v150, v151\n"
    "s_waitcnt vmcnt(25)\n"
    "v_mfma_f32_16x16x32_bf16 v[144:147], a[224:227], v[128:131], 0\n"
    "ds_read_b128 a[224:227], v16 offset:16384\n"
    "v_exp_f32 v152, v152\n"
    "v_mfma_f32_16x16x32_bf16 v[148:151], a[240:243], v[128:131], 0\n"
    "ds_read_b128 a[240:243], v16 offset:20480\n"
    "v_mfma_f32_16x16x32_bf16 a[128:131], v[196:199], v[164:167], 0\n"
    "v_exp_f32 v153, v153\n"
    "v_mfma_f32_16x16x32_bf16 v[144:147], a[228:231], v[132:135], v[144:147]\n"
    "ds_read_b128 a[228:231], v17 offset:16384\n"
    "v_mfma_f32_16x16x32_bf16 a[132:135], v[200:203], v[164:167], 0\n"
    "v_exp_f32 v154, v154\n"
    "v_mfma_f32_16x16x32_bf16 v[148:151], a[244:247], v[132:135], v[148:151]\n"
    "ds_read_b128 a[244:247], v17 offset:20480\n"
    "v_cvt_pk_bf16_f32 v160, v152, v153\n"
    "v_mfma_f32_16x16x32_bf16 a[136:139], v[204:207], v[164:167], 0\n"
    "v_exp_f32 v155, v155\n"
    "v_mfma_f32_16x16x32_bf16 v[144:147], a[232:235], v[136:139], v[144:147]\n"
    "ds_read_b128 a[232:235], v18 offset:16384\n"
    "v_mfma_f32_16x16x32_bf16 a[140:143], v[208:211], v[164:167], 0\n"
    "v_exp_f32 v156, v156\n"
    "v_mfma_f32_16x16x32_bf16 v[148:151], a[248:251], v[136:139], v[148:151]\n"
    "ds_read_b128 a[248:251], v18 offset:20480\n"
    "v_cvt_pk_bf16_f32 v161, v154, v155\n"
    "v_mfma_f32_16x16x32_bf16 a[144:147], v[212:215], v[164:167], 0\n"
    "v_mfma_f32_16x16x32_bf16 v[144:147], a[236:239], v[140:143], v[144:147]\n"
    "ds_read_b128 a[236:239], v19 offset:16384\n"
    "v_exp_f32 v157, v157\n"
    "v_mfma_f32_16x16x32_bf16 a[148:151], v[216:219], v[164:167], 0\n"
    "v_exp_f32 v158, v158\n"
    "v_mfma_f32_16x16x32_bf16 v[148:151], a[252:255], v[140:143], v[148:151]\n"
    "ds_read_b128 a[252:255], v19 offset:20480\n"
    "v_mfma_f32_16x16x32_bf16 a[152:155], v[220:223], v[164:167], 0\n"
    "v_exp_f32 v159, v159\n"
    "v_mfma_f32_16x16x32_bf16 a[156:159], v[224:227], v[164:167], 0\n"
    "v_cvt_pk_bf16_f32 v162, v156, v157\n"
    "v_mfma_f32_16x16x32_bf16 v[184:187], v[228:231], v[164:167], 0\n"
    "v_cvt_pk_bf16_f32 v163, v158, v159\n"
    "s_waitcnt lgkmcnt(7)\n"
    "v_mfma_f32_16x16x32_bf16 v[152:155], a[224:227], v[32:35], 0\n"
    "v_exp_f32 v144, v144\n"
    "s_waitcnt lgkmcnt(6)\n"
    "v_mfma_f32_16x16x32_bf16 v[156:159], a[240:243], v[32:35], 0\n"
    "v_mfma_f32_16x16x32_bf16 a[160:163], v[196:199], v[160:163], 0\n"
    "v_exp_f32 v145, v145\n"
    "s_waitcnt lgkmcnt(5)\n"
    "v_mfma_f32_16x16x32_bf16 v[152:155], a[228:231], v[36:39], v[152:155]\n"
    "v_mfma_f32_16x16x32_bf16 a[164:167], v[200:203], v[160:163], 0\n"
    "v_exp_f32 v146, v146\n"
    "s_waitcnt lgkmcnt(4)\n"
    "v_mfma_f32_16x16x32_bf16 v[156:159], a[244:247], v[36:39], v[156:159]\n"
    "v_cvt_pk_bf16_f32 v164, v144, v145\n"
    "v_mfma_f32_16x16x32_bf16 a[168:171], v[204:207], v[160:163], 0\n"
    "v_exp_f32 v147, v147\n"
    "s_waitcnt lgkmcnt(3)\n"
    "v_mfma_f32_16x16x32_bf16 v[152:155], a[232:235], v[40:43], v[152:155]\n"
    "v_mfma_f32_16x16x32_bf16 a[172:175], v[208:211], v[160:163], 0\n"
    "v_exp_f32 v148, v148\n"
    "s_waitcnt lgkmcnt(2)\n"
    "v_mfma_f32_16x16x32_bf16 v[156:159], a[248:251], v[40:43], v[156:159]\n"
    "v_cvt_pk_bf16_f32 v165, v146, v147\n"
    "v_mfma_f32_16x16x32_bf16 a[176:179], v[212:215], v[160:163], 0\n"
    "s_waitcnt lgkmcnt(1)\n"
    "v_mfma_f32_16x16x32_bf16 v[152:155], a[236:239], v[44:47], v[152:155]\n"
    "v_exp_f32 v149, v149\n"
    "v_mfma_f32_16x16x32_bf16 a[180:183], v[216:219], v[160:163], 0\n"
    "v_exp_f32 v150, v150\n"
    "s_waitcnt lgkmcnt(0)\n"
    "v_mfma_f32_16x16x32_bf16 v[156:159], a[252:255], v[44:47], v[156:159]\n"
    "v_mfma_f32_16x16x32_bf16 a[184:187], v[220:223], v[160:163], 0\n"
    "v_exp_f32 v151, v151\n"
    "v_mfma_f32_16x16x32_bf16 a[188:191], v[224:227], v[160:163], 0\n"
    "v_cvt_pk_bf16_f32 v166, v148, v149\n"
    "v_mfma_f32_16x16x32_bf16 v[188:191], v[228:231], v[160:163], 0\n"
    "v_cvt_pk_bf16_f32 v167, v150, v151\n"
    "s_waitcnt vmcnt(17)\n"
    "v_mfma_f32_16x16x32_bf16 v[144:147], a[224:227], v[48:51], 0\n"
    "v_exp_f32 v152, v152\n"
    "v_mfma_f32_16x16x32_bf16 v[148:151], a[240:243], v[48:51], 0\n"
    "v_mfma_f32_16x16x32_bf16 a[192:195], v[196:199], v[164:167], 0\n"
    "ds_read_b64_tr_b16 v[196:197], v8 offset:24576\n"
    "ds_read_b64_tr_b16 v[198:199], v8 offset:28672\n"
    "v_exp_f32 v153, v153\n"
    "v_mfma_f32_16x16x32_bf16 v[144:147], a[228:231], v[52:55], v[144:147]\n"
    "v_mfma_f32_16x16x32_bf16 a[196:199], v[200:203], v[164:167], 0\n"
    "ds_read_b64_tr_b16 v[200:201], v9 offset:24576\n"
    "ds_read_b64_tr_b16 v[202:203], v9 offset:28672\n"
    "v_exp_f32 v154, v154\n"
    "v_mfma_f32_16x16x32_bf16 v[148:151], a[244:247], v[52:55], v[148:151]\n"
    "v_cvt_pk_bf16_f32 v160, v152, v153\n"
    "v_mfma_f32_16x16x32_bf16 a[200:203], v[204:207], v[164:167], 0\n"
    "ds_read_b64_tr_b16 v[204:205], v10 offset:24576\n"
    "ds_read_b64_tr_b16 v[206:207], v10 offset:28672\n"
    "v_exp_f32 v155, v155\n"
    "v_mfma_f32_16x16x32_bf16 v[144:147], a[232:235], v[56:59], v[144:147]\n"
    "v_mfma_f32_16x16x32_bf16 a[204:207], v[208:211], v[164:167], 0\n"
    "ds_read_b64_tr_b16 v[208:209], v11 offset:24576\n"
    "ds_read_b64_tr_b16 v[210:211], v11 offset:28672\n"
    "v_exp_f32 v156, v156\n"
    "v_mfma_f32_16x16x32_bf16 v[148:151], a[248:251], v[56:59], v[148:151]\n"
    "v_cvt_pk_bf16_f32 v161, v154, v155\n"
    "v_mfma_f32_16x16x32_bf16 a[208:211], v[212:215], v[164:167], 0\n"
    "ds_read_b64_tr_b16 v[212:213], v12 offset:24576\n"
    "ds_read_b64_tr_b16 v[214:215], v12 offset:28672\n"
    "v_mfma_f32_16x16x32_bf16 v[144:147], a[236:239], v[60:63], v[144:147]\n"
    "v_exp_f32 v157, v157\n"
    "v_mfma_f32_16x16x32_bf16 a[212:215], v[216:219], v[164:167], 0\n"
    "ds_read_b64_tr_b16 v[216:217], v13 offset:24576\n"
    "ds_read_b64_tr_b16 v[218:219], v13 offset:28672\n"
    "v_exp_f32 v158, v158\n"
    "v_mfma_f32_16x16x32_bf16 v[148:151], a[252:255], v[60:63], v[148:151]\n"
    "v_mfma_f32_16x16x32_bf16 a[216:219], v[220:223], v[164:167], 0\n"
    "ds_read_b64_tr_b16 v[220:221], v14 offset:24576\n"
    "ds_read_b64_tr_b16 v[222:223], v14 offset:28672\n"
    "v_exp_f32 v159, v159\n"
    "v_mfma_f32_16x16x32_bf16 a[220:223], v[224:227], v[164:167], 0\n"
    "ds_read_b64_tr_b16 v[224:225], v15 offset:24576\n"
    "ds_read_b64_tr_b16 v[226:227], v15 offset:28672\n"
    "v_cvt_pk_bf16_f32 v162, v156, v157\n"
    "v_mfma_f32_16x16x32_bf16 v[192:195], v[228:231], v[164:167], 0\n"
    "v_cvt_pk_bf16_f32 v163, v158, v159\n"
    "v_mfma_f32_16x16x32_bf16 v[152:155], a[224:227], v[64:67], 0\n"
    "v_exp_f32 v144, v144\n"
    "v_mfma_f32_16x16x32_bf16 v[156:159], a[240:243], v[64:67], 0\n"
    "s_add_u32 m0, s64, 16384\n"
    "s_waitcnt lgkmcnt(14)\n"
    "v_mfma_f32_16x16x32_bf16 a[0:3], v[196:199], v[160:163], a[0:3]\n"
    "v_exp_f32 v145, v145\n"
    "v_mfma_f32_16x16x32_bf16 v[152:155], a[228:231], v[68:71], v[152:155]\n"
    "buffer_load_dwordx4 v20, s[40:43], s66 offen offset:0 nt lds\n"
    "v_exp_f32 v146, v146\n"
    "s_waitcnt lgkmcnt(12)\n"
    "v_mfma_f32_16x16x32_bf16 a[4:7], v[200:203], v[160:163], a[4:7]\n"
    "buffer_load_dwordx4 v21, s[40:43], s66 offen offset:1024 nt lds\n"
    "v_mfma_f32_16x16x32_bf16 v[156:159], a[244:247], v[68:71], v[156:159]\n"
    "v_cvt_pk_bf16_f32 v164, v144, v145\n"
    "buffer_load_dwordx4 v20, s[40:43], s66 offen offset:2048 nt lds\n"
    "s_waitcnt lgkmcnt(10)\n"
    "v_mfma_f32_16x16x32_bf16 a[8:11], v[204:207], v[160:163], a[8:11]\n"
    "v_exp_f32 v147, v147\n"
    "v_mfma_f32_16x16x32_bf16 v[152:155], a[232:235], v[72:75], v[152:155]\n"
    "buffer_load_dwordx4 v21, s[40:43], s66 offen offset:3072 nt lds\n"
    "s_waitcnt lgkmcnt(8)\n"
    "v_mfma_f32_16x16x32_bf16 a[12:15], v[208:211], v[160:163], a[12:15]\n"
    "v_exp_f32 v148, v148\n"
    "v_mfma_f32_16x16x32_bf16 v[156:159], a[248:251], v[72:75], v[156:159]\n"
    "s_add_u32 m0, s64, 20480\n"
    "v_cvt_pk_bf16_f32 v165, v146, v147\n"
    "s_waitcnt lgkmcnt(6)\n"
    "v_mfma_f32_16x16x32_bf16 a[16:19], v[212:215], v[160:163], a[16:19]\n"
    "buffer_load_dwordx4 v22, s[40:43], s66 offen offset:0 nt lds\n"
    "v_exp_f32 v149, v149\n"
    "v_mfma_f32_16x16x32_bf16 v[152:155], a[236:239], v[76:79], v[152:155]\n"
    "buffer_load_dwordx4 v23, s[40:43], s66 offen offset:1024 nt lds\n"
    "s_waitcnt lgkmcnt(4)\n"
    "v_mfma_f32_16x16x32_bf16 a[20:23], v[216:219], v[160:163], a[20:23]\n"
    "v_exp_f32 v150, v150\n"
    "v_mfma_f32_16x16x32_bf16 v[156:159], a[252:255], v[76:79], v[156:159]\n"
    "buffer_load_dwordx4 v22, s[40:43], s66 offen offset:2048 nt lds\n"
    "s_waitcnt lgkmcnt(2)\n"
    "v_mfma_f32_16x16x32_bf16 a[24:27], v[220:223], v[160:163], a[24:27]\n"
    "v_exp_f32 v151, v151\n"
    "s_waitcnt lgkmcnt(0)\n"
    "v_mfma_f32_16x16x32_bf16 a[28:31], v[224:227], v[160:163], a[28:31]\n"
    "buffer_load_dwordx4 v23, s[40:43], s66 offen offset:3072 nt lds\n"
    "v_cvt_pk_bf16_f32 v166, v148, v149\n"
    "v_mfma_f32_16x16x32_bf16 v[168:171], v[228:231], v[160:163], v[168:171]\n"
    "s_add_u32 s66, s66, 8192\n"
    "v_cvt_pk_bf16_f32 v167, v150, v151\n"
    "v_mfma_f32_16x16x32_bf16 v[144:147], a[224:227], v[80:83], 0\n"
    "v_exp_f32 v152, v152\n"
    "v_mfma_f32_16x16x32_bf16 v[148:151], a[240:243], v[80:83], 0\n"
    "v_mfma_f32_16x16x32_bf16 a[32:35], v[196:199], v[164:167], a[32:35]\n"
    "v_exp_f32 v153, v153\n"
    "v_mfma_f32_16x16x32_bf16 v[144:147], a[228:231], v[84:87], v[144:147]\n"
    "v_mfma_f32_16x16x32_bf16 a[36:39], v[200:203], v[164:167], a[36:39]\n"
    "v_exp_f32 v154, v154\n"
    "v_mfma_f32_16x16x32_bf16 v[148:151], a[244:247], v[84:87], v[148:151]\n"
    "v_cvt_pk_bf16_f32 v160, v152, v153\n"
    "v_mfma_f32_16x16x32_bf16 a[40:43], v[204:207], v[164:167], a[40:43]\n"
    "v_exp_f32 v155, v155\n"
    "v_mfma_f32_16x16x32_bf16 v[144:147], a[232:235], v[88:91], v[144:147]\n"
    "v_mfma_f32_16x16x32_bf16 a[44:47], v[208:211], v[164:167], a[44:47]\n"
    "v_exp_f32 v156, v156\n"
    "v_mfma_f32_16x16x32_bf16 v[148:151], a[248:251], v[88:91], v[148:151]\n"
    "v_cvt_pk_bf16_f32 v161, v154, v155\n"
    "v_mfma_f32_16x16x32_bf16 a[48:51], v[212:215], v[164:167], a[48:51]\n"
    "v_mfma_f32_16x16x32_bf16 v[144:147], a[236:239], v[92:95], v[144:147]\n"
    "v_exp_f32 v157, v157\n"
    "v_mfma_f32_16x16x32_bf16 a[52:55], v[216:219], v[164:167], a[52:55]\n"
    "v_exp_f32 v158, v158\n"
    "v_mfma_f32_16x16x32_bf16 v[148:151], a[252:255], v[92:95], v[148:151]\n"
    "v_mfma_f32_16x16x32_bf16 a[56:59], v[220:223], v[164:167], a[56:59]\n"
    "v_exp_f32 v159, v159\n"
    "v_mfma_f32_16x16x32_bf16 a[60:63], v[224:227], v[164:167], a[60:63]\n"
    "v_cvt_pk_bf16_f32 v162, v156, v157\n"
    "v_mfma_f32_16x16x32_bf16 v[172:175], v[228:231], v[164:167], v[172:175]\n"
    "v_cvt_pk_bf16_f32 v163, v158, v159\n"
    "v_mfma_f32_16x16x32_bf16 v[152:155], a[224:227], v[96:99], 0\n"
    "v_exp_f32 v144, v144\n"
    "v_mfma_f32_16x16x32_bf16 v[156:159], a[240:243], v[96:99], 0\n"
    "s_add_u32 m0, s64, 24576\n"
    "v_mfma_f32_16x16x32_bf16 a[64:67], v[196:199], v[160:163], a[64:67]\n"
    "v_exp_f32 v145, v145\n"
    "v_mfma_f32_16x16x32_bf16 v[152:155], a[228:231], v[100:103], v[152:155]\n"
    "buffer_load_dwordx4 v20, s[44:47], s67 offen offset:0 nt lds\n"
    "v_exp_f32 v146, v146\n"
    "v_mfma_f32_16x16x32_bf16 a[68:71], v[200:203], v[160:163], a[68:71]\n"
    "buffer_load_dwordx4 v21, s[44:47], s67 offen offset:1024 nt lds\n"
    "v_mfma_f32_16x16x32_bf16 v[156:159], a[244:247], v[100:103], v[156:159]\n"
    "v_cvt_pk_bf16_f32 v164, v144, v145\n"
    "buffer_load_dwordx4 v20, s[44:47], s67 offen offset:2048 nt lds\n"
    "v_mfma_f32_16x16x32_bf16 a[72:75], v[204:207], v[160:163], a[72:75]\n"
    "v_exp_f32 v147, v147\n"
    "v_mfma_f32_16x16x32_bf16 v[152:155], a[232:235], v[104:107], v[152:155]\n"
    "buffer_load_dwordx4 v21, s[44:47], s67 offen offset:3072 nt lds\n"
    "v_mfma_f32_16x16x32_bf16 a[76:79], v[208:211], v[160:163], a[76:79]\n"
    "v_exp_f32 v148, v148\n"
    "v_mfma_f32_16x16x32_bf16 v[156:159], a[248:251], v[104:107], v[156:159]\n"
    "s_add_u32 m0, s64, 28672\n"
    "v_cvt_pk_bf16_f32 v165, v146, v147\n"
    "v_mfma_f32_16x16x32_bf16 a[80:83], v[212:215], v[160:163], a[80:83]\n"
    "buffer_load_dwordx4 v22, s[44:47], s67 offen offset:0 nt lds\n"
    "v_exp_f32 v149, v149\n"
    "v_mfma_f32_16x16x32_bf16 v[152:155], a[236:239], v[108:111], v[152:155]\n"
    "buffer_load_dwordx4 v23, s[44:47], s67 offen offset:1024 nt lds\n"
    "v_mfma_f32_16x16x32_bf16 a[84:87], v[216:219], v[160:163], a[84:87]\n"
    "v_exp_f32 v150, v150\n"
    "v_mfma_f32_16x16x32_bf16 v[156:159], a[252:255], v[108:111], v[156:159]\n"
    "buffer_load_dwordx4 v22, s[44:47], s67 offen offset:2048 nt lds\n"
    "v_mfma_f32_16x16x32_bf16 a[88:91], v[220:223], v[160:163], a[88:91]\n"
    "v_exp_f32 v151, v151\n"
    "v_mfma_f32_16x16x32_bf16 a[92:95], v[224:227], v[160:163], a[92:95]\n"
    "buffer_load_dwordx4 v23, s[44:47], s67 offen offset:3072 nt lds\n"
    "v_cvt_pk_bf16_f32 v166, v148, v149\n"
    "v_mfma_f32_16x16x32_bf16 v[176:179], v[228:231], v[160:163], v[176:179]\n"
    "s_add_u32 s67, s67, 8192\n"
    "v_cvt_pk_bf16_f32 v167, v150, v151\n"
    "v_mfma_f32_16x16x32_bf16 v[144:147], a[224:227], v[112:115], 0\n"
    "v_exp_f32 v152, v152\n"
    "v_mfma_f32_16x16x32_bf16 v[148:151], a[240:243], v[112:115], 0\n"
    "s_mov_b32 m0, s73\n"
    "v_mfma_f32_16x16x32_bf16 a[96:99], v[196:199], v[164:167], a[96:99]\n"
    "v_exp_f32 v153, v153\n"
    "v_mfma_f32_16x16x32_bf16 v[144:147], a[228:231], v[116:119], v[144:147]\n"
    "v_mfma_f32_16x16x32_bf16 a[100:103], v[200:203], v[164:167], a[100:103]\n"
    "v_exp_f32 v154, v154\n"
    "v_mfma_f32_16x16x32_bf16 v[148:151], a[244:247], v[116:119], v[148:151]\n"
    "v_cvt_pk_bf16_f32 v160, v152, v153\n"
    "v_mfma_f32_16x16x32_bf16 a[104:107], v[204:207], v[164:167], a[104:107]\n"
    "v_exp_f32 v155, v155\n"
    "v_mfma_f32_16x16x32_bf16 v[144:147], a[232:235], v[120:123], v[144:147]\n"
    "buffer_load_dwordx4 v27, s[60:63], s86 offen lds\n"
    "v_mfma_f32_16x16x32_bf16 a[108:111], v[208:211], v[164:167], a[108:111]\n"
    "v_exp_f32 v156, v156\n"
    "v_mfma_f32_16x16x32_bf16 v[148:151], a[248:251], v[120:123], v[148:151]\n"
    "v_cvt_pk_bf16_f32 v161, v154, v155\n"
    "v_mfma_f32_16x16x32_bf16 a[112:115], v[212:215], v[164:167], a[112:115]\n"
    "v_mfma_f32_16x16x32_bf16 v[144:147], a[236:239], v[124:127], v[144:147]\n"
    "v_exp_f32 v157, v157\n"
    "v_mfma_f32_16x16x32_bf16 a[116:119], v[216:219], v[164:167], a[116:119]\n"
    "v_exp_f32 v158, v158\n"
    "v_mfma_f32_16x16x32_bf16 v[148:151], a[252:255], v[124:127], v[148:151]\n"
    "s_add_u32 s86, s86, 1024\n"
    "v_mfma_f32_16x16x32_bf16 a[120:123], v[220:223], v[164:167], a[120:123]\n"
    "v_exp_f32 v159, v159\n"
    "v_mfma_f32_16x16x32_bf16 a[124:127], v[224:227], v[164:167], a[124:127]\n"
    "v_cvt_pk_bf16_f32 v162, v156, v157\n"
    "v_mfma_f32_16x16x32_bf16 v[180:183], v[228:231], v[164:167], v[180:183]\n"
    "v_cvt_pk_bf16_f32 v163, v158, v159\n"
    "s_waitcnt vmcnt(26)\n"
    "v_mfma_f32_16x16x32_bf16 v[152:155], a[224:227], v[128:131], 0\n"
    "ds_read_b128 a[224:227], v16 offset:0\n"
    "v_exp_f32 v144, v144\n"
    "v_mfma_f32_16x16x32_bf16 v[156:159], a[240:243], v[128:131], 0\n"
    "ds_read_b128 a[240:243], v16 offset:4096\n"
    "v_mfma_f32_16x16x32_bf16 a[128:131], v[196:199], v[160:163], a[128:131]\n"
    "v_exp_f32 v145, v145\n"
    "v_mfma_f32_16x16x32_bf16 v[152:155], a[228:231], v[132:135], v[152:155]\n"
    "ds_read_b128 a[228:231], v17 offset:0\n"
    "v_mfma_f32_16x16x32_bf16 a[132:135], v[200:203], v[160:163], a[132:135]\n"
    "v_exp_f32 v146, v146\n"
    "v_mfma_f32_16x16x32_bf16 v[156:159], a[244:247], v[132:135], v[156:159]\n"
    "ds_read_b128 a[244:247], v17 offset:4096\n"
    "v_cvt_pk_bf16_f32 v164, v144, v145\n"
    "v_mfma_f32_16x16x32_bf16 a[136:139], v[204:207], v[160:163], a[136:139]\n"
    "v_exp_f32 v147, v147\n"
    "v_mfma_f32_16x16x32_bf16 v[152:155], a[232:235], v[136:139], v[152:155]\n"
    "ds_read_b128 a[232:235], v18 offset:0\n"
    "v_mfma_f32_16x16x32_bf16 a[140:143], v[208:211], v[160:163], a[140:143]\n"
    "v_exp_f32 v148, v148\n"
    "v_mfma_f32_16x16x32_bf16 v[156:159], a[248:251], v[136:139], v[156:159]\n"
    "ds_read_b128 a[248:251], v18 offset:4096\n"
    "v_cvt_pk_bf16_f32 v165, v146, v147\n"
    "v_mfma_f32_16x16x32_bf16 a[144:147], v[212:215], v[160:163], a[144:147]\n"
    "v_mfma_f32_16x16x32_bf16 v[152:155], a[236:239], v[140:143], v[152:155]\n"
    "ds_read_b128 a[236:239], v19 offset:0\n"
    "v_exp_f32 v149, v149\n"
    "v_mfma_f32_16x16x32_bf16 a[148:151], v[216:219], v[160:163], a[148:151]\n"
    "v_exp_f32 v150, v150\n"
    "v_mfma_f32_16x16x32_bf16 v[156:159], a[252:255], v[140:143], v[156:159]\n"
    "ds_read_b128 a[252:255], v19 offset:4096\n"
    "v_mfma_f32_16x16x32_bf16 a[152:155], v[220:223], v[160:163], a[152:155]\n"
    "v_exp_f32 v151, v151\n"
    "v_mfma_f32_16x16x32_bf16 a[156:159], v[224:227], v[160:163], a[156:159]\n"
    "v_cvt_pk_bf16_f32 v166, v148, v149\n"
    "v_mfma_f32_16x16x32_bf16 v[184:187], v[228:231], v[160:163], v[184:187]\n"
    "v_cvt_pk_bf16_f32 v167, v150, v151\n"
    "s_mov_b32 s70, 0\n"
    ".LW_a_%=:\n"
    "s_waitcnt lgkmcnt(7)\n"
    "v_mfma_f32_16x16x32_bf16 v[144:147], a[224:227], v[32:35], 0\n"
    "v_exp_f32 v152, v152\n"
    "s_waitcnt lgkmcnt(6)\n"
    "v_mfma_f32_16x16x32_bf16 v[148:151], a[240:243], v[32:35], 0\n"
    "v_mfma_f32_16x16x32_bf16 a[160:163], v[196:199], v[164:167], a[160:163]\n"
    "v_exp_f32 v153, v153\n"
    "s_waitcnt lgkmcnt(5)\n"
    "v_mfma_f32_16x16x32_bf16 v[144:147], a[228:231], v[36:39], v[144:147]\n"
    "v_mfma_f32_16x16x32_bf16 a[164:167], v[200:203], v[164:167], a[164:167]\n"
    "v_exp_f32 v154, v154\n"
    "s_waitcnt lgkmcnt(4)\n"
    "v_mfma_f32_16x16x32_bf16 v[148:151], a[244:247], v[36:39], v[148:151]\n"
    "v_cvt_pk_bf16_f32 v160, v152, v153\n"
    "v_mfma_f32_16x16x32_bf16 a[168:171], v[204:207], v[164:167], a[168:171]\n"
    "v_exp_f32 v155, v155\n"
    "s_waitcnt lgkmcnt(3)\n"
    "v_mfma_f32_16x16x32_bf16 v[144:147], a[232:235], v[40:43], v[144:147]\n"
    "v_mfma_f32_16x16x32_bf16 a[172:175], v[208:211], v[164:167], a[172:175]\n"
    "v_exp_f32 v156, v156\n"
    "s_waitcnt lgkmcnt(2)\n"
    "v_mfma_f32_16x16x32_bf16 v[148:151], a[248:251], v[40:43], v[148:151]\n"
    "v_cvt_pk_bf16_f32 v161, v154, v155\n"
    "v_mfma_f32_16x16x32_bf16 a[176:179], v[212:215], v[164:167], a[176:179]\n"
    "s_waitcnt lgkmcnt(1)\n"
    "v_mfma_f32_16x16x32_bf16 v[144:147], a[236:239], v[44:47], v[144:147]\n"
    "v_exp_f32 v157, v157\n"
    "v_mfma_f32_16x16x32_bf16 a[180:183], v[216:219], v[164:167], a[180:183]\n"
    "v_exp_f32 v158, v158\n"
    "s_waitcnt lgkmcnt(0)\n"
    "v_mfma_f32_16x16x32_bf16 v[148:151], a[252:255], v[44:47], v[148:151]\n"
    "v_mfma_f32_16x16x32_bf16 a[184:187], v[220:223], v[164:167], a[184:187]\n"
    "v_exp_f32 v159, v159\n"
    "v_mfma_f32_16x16x32_bf16 a[188:191], v[224:227], v[164:167], a[188:191]\n"
    "v_cvt_pk_bf16_f32 v162, v156, v157\n"
    "v_mfma_f32_16x16x32_bf16 v[188:191], v[228:231], v[164:167], v[188:191]\n"
    "v_cvt_pk_bf16_f32 v163, v158, v159\n"
    "s_waitcnt vmcnt(18)\n"
    "v_mfma_f32_16x16x32_bf16 v[152:155], a[224:227], v[48:51], 0\n"
    "v_exp_f32 v144, v144\n"
    "v_mfma_f32_16x16x32_bf16 v[156:159], a[240:243], v[48:51], 0\n"
    "v_mfma_f32_16x16x32_bf16 a[192:195], v[196:199], v[160:163], a[192:195]\n"
    "ds_read_b64_tr_b16 v[196:197], v8 offset:8192\n"
    "ds_read_b64_tr_b16 v[198:199], v8 offset:12288\n"
    "v_exp_f32 v145, v145\n"
    "v_mfma_f32_16x16x32_bf16 v[152:155], a[228:231], v[52:55], v[152:155]\n"
    "v_mfma_f32_16x16x32_bf16 a[196:199], v[200:203], v[160:163], a[196:199]\n"
    "ds_read_b64_tr_b16 v[200:201], v9 offset:8192\n"
    "ds_read_b64_tr_b16 v[202:203], v9 offset:12288\n"
    "v_exp_f32 v146, v146\n"
    "v_mfma_f32_16x16x32_bf16 v[156:159], a[244:247], v[52:55], v[156:159]\n"
    "v_cvt_pk_bf16_f32 v164, v144, v145\n"
    "v_mfma_f32_16x16x32_bf16 a[200:203], v[204:207], v[160:163], a[200:203]\n"
    "ds_read_b64_tr_b16 v[204:205], v10 offset:8192\n"
    "ds_read_b64_tr_b16 v[206:207], v10 offset:12288\n"
    "v_exp_f32 v147, v147\n"
    "v_mfma_f32_16x16x32_bf16 v[152:155], a[232:235], v[56:59], v[152:155]\n"
    "v_mfma_f32_16x16x32_bf16 a[204:207], v[208:211], v[160:163], a[204:207]\n"
    "ds_read_b64_tr_b16 v[208:209], v11 offset:8192\n"
    "ds_read_b64_tr_b16 v[210:211], v11 offset:12288\n"
    "v_exp_f32 v148, v148\n"
    "v_mfma_f32_16x16x32_bf16 v[156:159], a[248:251], v[56:59], v[156:159]\n"
    "v_cvt_pk_bf16_f32 v165, v146, v147\n"
    "v_mfma_f32_16x16x32_bf16 a[208:211], v[212:215], v[160:163], a[208:211]\n"
    "ds_read_b64_tr_b16 v[212:213], v12 offset:8192\n"
    "ds_read_b64_tr_b16 v[214:215], v12 offset:12288\n"
    "v_mfma_f32_16x16x32_bf16 v[152:155], a[236:239], v[60:63], v[152:155]\n"
    "v_exp_f32 v149, v149\n"
    "v_mfma_f32_16x16x32_bf16 a[212:215], v[216:219], v[160:163], a[212:215]\n"
    "ds_read_b64_tr_b16 v[216:217], v13 offset:8192\n"
    "ds_read_b64_tr_b16 v[218:219], v13 offset:12288\n"
    "v_exp_f32 v150, v150\n"
    "v_mfma_f32_16x16x32_bf16 v[156:159], a[252:255], v[60:63], v[156:159]\n"
    "v_mfma_f32_16x16x32_bf16 a[216:219], v[220:223], v[160:163], a[216:219]\n"
    "ds_read_b64_tr_b16 v[220:221], v14 offset:8192\n"
    "ds_read_b64_tr_b16 v[222:223], v14 offset:12288\n"
    "v_exp_f32 v151, v151\n"
    "v_mfma_f32_16x16x32_bf16 a[220:223], v[224:227], v[160:163], a[220:223]\n"
    "ds_read_b64_tr_b16 v[224:225], v15 offset:8192\n"
    "ds_read_b64_tr_b16 v[226:227], v15 offset:12288\n"
    "v_cvt_pk_bf16_f32 v166, v148, v149\n"
    "v_mfma_f32_16x16x32_bf16 v[192:195], v[228:231], v[160:163], v[192:195]\n"
    "v_cvt_pk_bf16_f32 v167, v150, v151\n"
    "v_mfma_f32_16x16x32_bf16 v[144:147], a[224:227], v[64:67], 0\n"
    "v_exp_f32 v152, v152\n"
    "v_mfma_f32_16x16x32_bf16 v[148:151], a[240:243], v[64:67], 0\n"
    "s_add_u32 m0, s64, 0\n"
    "s_waitcnt lgkmcnt(14)\n"
    "v_mfma_f32_16x16x32_bf16 a[0:3], v[196:199], v[164:167], a[0:3]\n"
    "v_exp_f32 v153, v153\n"
    "v_mfma_f32_16x16x32_bf16 v[144:147], a[228:231], v[68:71], v[144:147]\n"
    "buffer_load_dwordx4 v20, s[40:43], s66 offen offset:0 nt lds\n"
    "v_exp_f32 v154, v154\n"
    "s_waitcnt lgkmcnt(12)\n"
    "v_mfma_f32_16x16x32_bf16 a[4:7], v[200:203], v[164:167], a[4:7]\n"
    "buffer_load_dwordx4 v21, s[40:43], s66 offen offset:1024 nt lds\n"
    "v_mfma_f32_16x16x32_bf16 v[148:151], a[244:247], v[68:71], v[148:151]\n"
    "v_cvt_pk_bf16_f32 v160, v152, v153\n"
    "buffer_load_dwordx4 v20, s[40:43], s66 offen offset:2048 nt lds\n"
    "s_waitcnt lgkmcnt(10)\n"
    "v_mfma_f32_16x16x32_bf16 a[8:11], v[204:207], v[164:167], a[8:11]\n"
    "v_exp_f32 v155, v155\n"
    "v_mfma_f32_16x16x32_bf16 v[144:147], a[232:235], v[72:75], v[144:147]\n"
    "buffer_load_dwordx4 v21, s[40:43], s66 offen offset:3072 nt lds\n"
    "s_waitcnt lgkmcnt(8)\n"
    "v_mfma_f32_16x16x32_bf16 a[12:15], v[208:211], v[164:167], a[12:15]\n"
    "v_exp_f32 v156, v156\n"
    "v_mfma_f32_16x16x32_bf16 v[148:151], a[248:251], v[72:75], v[148:151]\n"
    "s_add_u32 m0, s64, 4096\n"
    "v_cvt_pk_bf16_f32 v161, v154, v155\n"
    "s_waitcnt lgkmcnt(6)\n"
    "v_mfma_f32_16x16x32_bf16 a[16:19], v[212:215], v[164:167], a[16:19]\n"
    "buffer_load_dwordx4 v22, s[40:43], s66 offen offset:0 nt lds\n"
    "v_exp_f32 v157, v157\n"
    "v_mfma_f32_16x16x32_bf16 v[144:147], a[236:239], v[76:79], v[144:147]\n"
    "buffer_load_dwordx4 v23, s[40:43], s66 offen offset:1024 nt lds\n"
    "s_waitcnt lgkmcnt(4)\n"
    "v_mfma_f32_16x16x32_bf16 a[20:23], v[216:219], v[164:167], a[20:23]\n"
    "v_exp_f32 v158, v158\n"
    "v_mfma_f32_16x16x32_bf16 v[148:151], a[252:255], v[76:79], v[148:151]\n"
    "buffer_load_dwordx4 v22, s[40:43], s66 offen offset:2048 nt lds\n"
    "s_waitcnt lgkmcnt(2)\n"
    "v_mfma_f32_16x16x32_bf16 a[24:27], v[220:223], v[164:167], a[24:27]\n"
    "v_exp_f32 v159, v159\n"
    "s_waitcnt lgkmcnt(0)\n"
    "v_mfma_f32_16x16x32_bf16 a[28:31], v[224:227], v[164:167], a[28:31]\n"
    "buffer_load_dwordx4 v23, s[40:43], s66 offen offset:3072 nt lds\n"
    "v_cvt_pk_bf16_f32 v162, v156, v157\n"
    "v_mfma_f32_16x16x32_bf16 v[168:171], v[228:231], v[164:167], v[168:171]\n"
    "s_add_u32 s66, s66, 8192\n"
    "v_cvt_pk_bf16_f32 v163, v158, v159\n"
    "v_mfma_f32_16x16x32_bf16 v[152:155], a[224:227], v[80:83], 0\n"
    "v_exp_f32 v144, v144\n"
    "v_mfma_f32_16x16x32_bf16 v[156:159], a[240:243], v[80:83], 0\n"
    "v_mfma_f32_16x16x32_bf16 a[32:35], v[196:199], v[160:163], a[32:35]\n"
    "v_exp_f32 v145, v145\n"
    "v_mfma_f32_16x16x32_bf16 v[152:155], a[228:231], v[84:87], v[152:155]\n"
    "v_mfma_f32_16x16x32_bf16 a[36:39], v[200:203], v[160:163], a[36:39]\n"
    "v_exp_f32 v146, v146\n"
    "v_mfma_f32_16x16x32_bf16 v[156:159], a[244:247], v[84:87], v[156:159]\n"
    "v_cvt_pk_bf16_f32 v164, v144, v145\n"
    "v_mfma_f32_16x16x32_bf16 a[40:43], v[204:207], v[160:163], a[40:43]\n"
    "v_exp_f32 v147, v147\n"
    "v_mfma_f32_16x16x32_bf16 v[152:155], a[232:235], v[88:91], v[152:155]\n"
    "v_mfma_f32_16x16x32_bf16 a[44:47], v[208:211], v[160:163], a[44:47]\n"
    "v_exp_f32 v148, v148\n"
    "v_mfma_f32_16x16x32_bf16 v[156:159], a[248:251], v[88:91], v[156:159]\n"
    "v_cvt_pk_bf16_f32 v165, v146, v147\n"
    "v_mfma_f32_16x16x32_bf16 a[48:51], v[212:215], v[160:163], a[48:51]\n"
    "v_mfma_f32_16x16x32_bf16 v[152:155], a[236:239], v[92:95], v[152:155]\n"
    "v_exp_f32 v149, v149\n"
    "v_mfma_f32_16x16x32_bf16 a[52:55], v[216:219], v[160:163], a[52:55]\n"
    "v_exp_f32 v150, v150\n"
    "v_mfma_f32_16x16x32_bf16 v[156:159], a[252:255], v[92:95], v[156:159]\n"
    "v_mfma_f32_16x16x32_bf16 a[56:59], v[220:223], v[160:163], a[56:59]\n"
    "v_exp_f32 v151, v151\n"
    "v_mfma_f32_16x16x32_bf16 a[60:63], v[224:227], v[160:163], a[60:63]\n"
    "v_cvt_pk_bf16_f32 v166, v148, v149\n"
    "v_mfma_f32_16x16x32_bf16 v[172:175], v[228:231], v[160:163], v[172:175]\n"
    "v_cvt_pk_bf16_f32 v167, v150, v151\n"
    "v_mfma_f32_16x16x32_bf16 v[144:147], a[224:227], v[96:99], 0\n"
    "v_exp_f32 v152, v152\n"
    "v_mfma_f32_16x16x32_bf16 v[148:151], a[240:243], v[96:99], 0\n"
    "s_add_u32 m0, s64, 8192\n"
    "v_mfma_f32_16x16x32_bf16 a[64:67], v[196:199], v[164:167], a[64:67]\n"
    "v_exp_f32 v153, v153\n"
    "v_mfma_f32_16x16x32_bf16 v[144:147], a[228:231], v[100:103], v[144:147]\n"
    "buffer_load_dwordx4 v20, s[44:47], s67 offen offset:0 nt lds\n"
    "v_exp_f32 v154, v154\n"
    "v_mfma_f32_16x16x32_bf16 a[68:71], v[200:203], v[164:167], a[68:71]\n"
    "buffer_load_dwordx4 v21, s[44:47], s67 offen offset:1024 nt lds\n"
    "v_mfma_f32_16x16x32_bf16 v[148:151], a[244:247], v[100:103], v[148:151]\n"
    "v_cvt_pk_bf16_f32 v160, v152, v153\n"
    "buffer_load_dwordx4 v20, s[44:47], s67 offen offset:2048 nt lds\n"
    "v_mfma_f32_16x16x32_bf16 a[72:75], v[204:207], v[164:167], a[72:75]\n"
    "v_exp_f32 v155, v155\n"
    "v_mfma_f32_16x16x32_bf16 v[144:147], a[232:235], v[104:107], v[144:147]\n"
    "buffer_load_dwordx4 v21, s[44:47], s67 offen offset:3072 nt lds\n"
    "v_mfma_f32_16x16x32_bf16 a[76:79], v[208:211], v[164:167], a[76:79]\n"
    "v_exp_f32 v156, v156\n"
    "v_mfma_f32_16x16x32_bf16 v[148:151], a[248:251], v[104:107], v[148:151]\n"
    "s_add_u32 m0, s64, 12288\n"
    "v_cvt_pk_bf16_f32 v161, v154, v155\n"
    "v_mfma_f32_16x16x32_bf16 a[80:83], v[212:215], v[164:167], a[80:83]\n"
    "buffer_load_dwordx4 v22, s[44:47], s67 offen offset:0 nt lds\n"
    "v_exp_f32 v157, v157\n"
    "v_mfma_f32_16x16x32_bf16 v[144:147], a[236:239], v[108:111], v[144:147]\n"
    "buffer_load_dwordx4 v23, s[44:47], s67 offen offset:1024 nt lds\n"
    "v_mfma_f32_16x16x32_bf16 a[84:87], v[216:219], v[164:167], a[84:87]\n"
    "v_exp_f32 v158, v158\n"
    "v_mfma_f32_16x16x32_bf16 v[148:151], a[252:255], v[108:111], v[148:151]\n"
    "buffer_load_dwordx4 v22, s[44:47], s67 offen offset:2048 nt lds\n"
    "v_mfma_f32_16x16x32_bf16 a[88:91], v[220:223], v[164:167], a[88:91]\n"
    "v_exp_f32 v159, v159\n"
    "v_mfma_f32_16x16x32_bf16 a[92:95], v[224:227], v[164:167], a[92:95]\n"
    "buffer_load_dwordx4 v23, s[44:47], s67 offen offset:3072 nt lds\n"
    "v_cvt_pk_bf16_f32 v162, v156, v157\n"
    "v_mfma_f32_16x16x32_bf16 v[176:179], v[228:231], v[164:167], v[176:179]\n"
    "s_add_u32 s67, s67, 8192\n"
    "v_cvt_pk_bf16_f32 v163, v158, v159\n"
    "v_mfma_f32_16x16x32_bf16 v[152:155], a[224:227], v[112:115], 0\n"
    "v_exp_f32 v144, v144\n"
    "v_mfma_f32_16x16x32_bf16 v[156:159], a[240:243], v[112:115], 0\n"
    "s_mov_b32 m0, s73\n"
    "v_mfma_f32_16x16x32_bf16 a[96:99], v[196:199], v[160:163], a[96:99]\n"
    "v_exp_f32 v145, v145\n"
    "v_mfma_f32_16x16x32_bf16 v[152:155], a[228:231], v[116:119], v[152:155]\n"
    "v_mfma_f32_16x16x32_bf16 a[100:103], v[200:203], v[160:163], a[100:103]\n"
    "v_exp_f32 v146, v146\n"
    "v_mfma_f32_16x16x32_bf16 v[156:159], a[244:247], v[116:119], v[156:159]\n"
    "v_cvt_pk_bf16_f32 v164, v144, v145\n"
    "v_mfma_f32_16x16x32_bf16 a[104:107], v[204:207], v[160:163], a[104:107]\n"
    "v_exp_f32 v147, v147\n"
    "v_mfma_f32_16x16x32_bf16 v[152:155], a[232:235], v[120:123], v[152:155]\n"
    "buffer_load_dwordx4 v27, s[60:63], s86 offen lds\n"
    "v_mfma_f32_16x16x32_bf16 a[108:111], v[208:211], v[160:163], a[108:111]\n"
    "v_exp_f32 v148, v148\n"
    "v_mfma_f32_16x16x32_bf16 v[156:159], a[248:251], v[120:123], v[156:159]\n"
    "v_cvt_pk_bf16_f32 v165, v146, v147\n"
    "v_mfma_f32_16x16x32_bf16 a[112:115], v[212:215], v[160:163], a[112:115]\n"
    "v_mfma_f32_16x16x32_bf16 v[152:155], a[236:239], v[124:127], v[152:155]\n"
    "v_exp_f32 v149, v149\n"
    "v_mfma_f32_16x16x32_bf16 a[116:119], v[216:219], v[160:163], a[116:119]\n"
    "v_exp_f32 v150, v150\n"
    "v_mfma_f32_16x16x32_bf16 v[156:159], a[252:255], v[124:127], v[156:159]\n"
    "s_add_u32 s86, s86, 1024\n"
    "v_mfma_f32_16x16x32_bf16 a[120:123], v[220:223], v[160:163], a[120:123]\n"
    "v_exp_f32 v151, v151\n"
    "v_mfma_f32_16x16x32_bf16 a[124:127], v[224:227], v[160:163], a[124:127]\n"
    "v_cvt_pk_bf16_f32 v166, v148, v149\n"
    "v_mfma_f32_16x16x32_bf16 v[180:183], v[228:231], v[160:163], v[180:183]\n"
    "v_cvt_pk_bf16_f32 v167, v150, v151\n"
    "s_waitcnt vmcnt(26)\n"
    "v_mfma_f32_16x16x32_bf16 v[144:147], a[224:227], v[128:131], 0\n"
    "ds_read_b128 a[224:227], v16 offset:16384\n"
    "v_exp_f32 v152, v152\n"
    "v_mfma_f32_16x16x32_bf16 v[148:151], a[240:243], v[128:131], 0\n"
    "ds_read_b128 a[240:243], v16 offset:20480\n"
    "v_mfma_f32_16x16x32_bf16 a[128:131], v[196:199], v[164:167], a[128:131]\n"
    "v_exp_f32 v153, v153\n"
    "v_mfma_f32_16x16x32_bf16 v[144:147], a[228:231], v[132:135], v[144:147]\n"
    "ds_read_b128 a[228:231], v17 offset:16384\n"
    "v_mfma_f32_16x16x32_bf16 a[132:135], v[200:203], v[164:167], a[132:135]\n"
    "v_exp_f32 v154, v154\n"
    "v_mfma_f32_16x16x32_bf16 v[148:151], a[244:247], v[132:135], v[148:151]\n"
    "ds_read_b128 a[244:247], v17 offset:20480\n"
    "v_cvt_pk_bf16_f32 v160, v152, v153\n"
    "v_mfma_f32_16x16x32_bf16 a[136:139], v[204:207], v[164:167], a[136:139]\n"
    "v_exp_f32 v155, v155\n"
    "v_mfma_f32_16x16x32_bf16 v[144:147], a[232:235], v[136:139], v[144:147]\n"
    "ds_read_b128 a[232:235], v18 offset:16384\n"
    "v_mfma_f32_16x16x32_bf16 a[140:143], v[208:211], v[164:167], a[140:143]\n"
    "v_exp_f32 v156, v156\n"
    "v_mfma_f32_16x16x32_bf16 v[148:151], a[248:251], v[136:139], v[148:151]\n"
    "ds_read_b128 a[248:251], v18 offset:20480\n"
    "v_cvt_pk_bf16_f32 v161, v154, v155\n"
    "v_mfma_f32_16x16x32_bf16 a[144:147], v[212:215], v[164:167], a[144:147]\n"
    "v_mfma_f32_16x16x32_bf16 v[144:147], a[236:239], v[140:143], v[144:147]\n"
    "ds_read_b128 a[236:239], v19 offset:16384\n"
    "v_exp_f32 v157, v157\n"
    "v_mfma_f32_16x16x32_bf16 a[148:151], v[216:219], v[164:167], a[148:151]\n"
    "v_exp_f32 v158, v158\n"
    "v_mfma_f32_16x16x32_bf16 v[148:151], a[252:255], v[140:143], v[148:151]\n"
    "ds_read_b128 a[252:255], v19 offset:20480\n"
    "v_mfma_f32_16x16x32_bf16 a[152:155], v[220:223], v[164:167], a[152:155]\n"
    "v_exp_f32 v159, v159\n"
    "v_mfma_f32_16x16x32_bf16 a[156:159], v[224:227], v[164:167], a[156:159]\n"
    "v_cvt_pk_bf16_f32 v162, v156, v157\n"
    "v_mfma_f32_16x16x32_bf16 v[184:187], v[228:231], v[164:167], v[184:187]\n"
    "v_cvt_pk_bf16_f32 v163, v158, v159\n"
    "s_waitcnt lgkmcnt(7)\n"
    "v_mfma_f32_16x16x32_bf16 v[152:155], a[224:227], v[32:35], 0\n"
    "v_exp_f32 v144, v144\n"
    "s_waitcnt lgkmcnt(6)\n"
    "v_mfma_f32_16x16x32_bf16 v[156:159], a[240:243], v[32:35], 0\n"
    "v_mfma_f32_16x16x32_bf16 a[160:163], v[196:199], v[160:163], a[160:163]\n"
    "v_exp_f32 v145, v145\n"
    "s_waitcnt lgkmcnt(5)\n"
    "v_mfma_f32_16x16x32_bf16 v[152:155], a[228:231], v[36:39], v[152:155]\n"
    "v_mfma_f32_16x16x32_bf16 a[164:167], v[200:203], v[160:163], a[164:167]\n"
    "v_exp_f32 v146, v146\n"
    "s_waitcnt lgkmcnt(4)\n"
    "v_mfma_f32_16x16x32_bf16 v[156:159], a[244:247], v[36:39], v[156:159]\n"
    "v_cvt_pk_bf16_f32 v164, v144, v145\n"
    "v_mfma_f32_16x16x32_bf16 a[168:171], v[204:207], v[160:163], a[168:171]\n"
    "v_exp_f32 v147, v147\n"
    "s_waitcnt lgkmcnt(3)\n"
    "v_mfma_f32_16x16x32_bf16 v[152:155], a[232:235], v[40:43], v[152:155]\n"
    "v_mfma_f32_16x16x32_bf16 a[172:175], v[208:211], v[160:163], a[172:175]\n"
    "v_exp_f32 v148, v148\n"
    "s_waitcnt lgkmcnt(2)\n"
    "v_mfma_f32_16x16x32_bf16 v[156:159], a[248:251], v[40:43], v[156:159]\n"
    "v_cvt_pk_bf16_f32 v165, v146, v147\n"
    "v_mfma_f32_16x16x32_bf16 a[176:179], v[212:215], v[160:163], a[176:179]\n"
    "s_waitcnt lgkmcnt(1)\n"
    "v_mfma_f32_16x16x32_bf16 v[152:155], a[236:239], v[44:47], v[152:155]\n"
    "v_exp_f32 v149, v149\n"
    "v_mfma_f32_16x16x32_bf16 a[180:183], v[216:219], v[160:163], a[180:183]\n"
    "v_exp_f32 v150, v150\n"
    "s_waitcnt lgkmcnt(0)\n"
    "v_mfma_f32_16x16x32_bf16 v[156:159], a[252:255], v[44:47], v[156:159]\n"
    "v_mfma_f32_16x16x32_bf16 a[184:187], v[220:223], v[160:163], a[184:187]\n"
    "v_exp_f32 v151, v151\n"
    "v_mfma_f32_16x16x32_bf16 a[188:191], v[224:227], v[160:163], a[188:191]\n"
    "v_cvt_pk_bf16_f32 v166, v148, v149\n"
    "v_mfma_f32_16x16x32_bf16 v[188:191], v[228:231], v[160:163], v[188:191]\n"
    "v_cvt_pk_bf16_f32 v167, v150, v151\n"
    "s_waitcnt vmcnt(18)\n"
    "v_mfma_f32_16x16x32_bf16 v[144:147], a[224:227], v[48:51], 0\n"
    "v_exp_f32 v152, v152\n"
    "v_mfma_f32_16x16x32_bf16 v[148:151], a[240:243], v[48:51], 0\n"
    "v_mfma_f32_16x16x32_bf16 a[192:195], v[196:199], v[164:167], a[192:195]\n"
    "ds_read_b64_tr_b16 v[196:197], v8 offset:24576\n"
    "ds_read_b64_tr_b16 v[198:199], v8 offset:28672\n"
    "v_exp_f32 v153, v153\n"
    "v_mfma_f32_16x16x32_bf16 v[144:147], a[228:231], v[52:55], v[144:147]\n"
    "v_mfma_f32_16x16x32_bf16 a[196:199], v[200:203], v[164:167], a[196:199]\n"
    "ds_read_b64_tr_b16 v[200:201], v9 offset:24576\n"
    "ds_read_b64_tr_b16 v[202:203], v9 offset:28672\n"
    "v_exp_f32 v154, v154\n"
    "v_mfma_f32_16x16x32_bf16 v[148:151], a[244:247], v[52:55], v[148:151]\n"
    "v_cvt_pk_bf16_f32 v160, v152, v153\n"
    "v_mfma_f32_16x16x32_bf16 a[200:203], v[204:207], v[164:167], a[200:203]\n"
    "ds_read_b64_tr_b16 v[204:205], v10 offset:24576\n"
    "ds_read_b64_tr_b16 v[206:207], v10 offset:28672\n"
    "v_exp_f32 v155, v155\n"
    "v_mfma_f32_16x16x32_bf16 v[144:147], a[232:235], v[56:59], v[144:147]\n"
    "v_mfma_f32_16x16x32_bf16 a[204:207], v[208:211], v[164:167], a[204:207]\n"
    "ds_read_b64_tr_b16 v[208:209], v11 offset:24576\n"
    "ds_read_b64_tr_b16 v[210:211], v11 offset:28672\n"
    "v_exp_f32 v156, v156\n"
    "v_mfma_f32_16x16x32_bf16 v[148:151], a[248:251], v[56:59], v[148:151]\n"
    "v_cvt_pk_bf16_f32 v161, v154, v155\n"
    "v_mfma_f32_16x16x32_bf16 a[208:211], v[212:215], v[164:167], a[208:211]\n"
    "ds_read_b64_tr_b16 v[212:213], v12 offset:24576\n"
    "ds_read_b64_tr_b16 v[214:215], v12 offset:28672\n"
    "v_mfma_f32_16x16x32_bf16 v[144:147], a[236:239], v[60:63], v[144:147]\n"
    "v_exp_f32 v157, v157\n"
    "v_mfma_f32_16x16x32_bf16 a[212:215], v[216:219], v[164:167], a[212:215]\n"
    "ds_read_b64_tr_b16 v[216:217], v13 offset:24576\n"
    "ds_read_b64_tr_b16 v[218:219], v13 offset:28672\n"
    "v_exp_f32 v158, v158\n"
    "v_mfma_f32_16x16x32_bf16 v[148:151], a[252:255], v[60:63], v[148:151]\n"
    "v_mfma_f32_16x16x32_bf16 a[216:219], v[220:223], v[164:167], a[216:219]\n"
    "ds_read_b64_tr_b16 v[220:221], v14 offset:24576\n"
    "ds_read_b64_tr_b16 v[222:223], v14 offset:28672\n"
    "v_exp_f32 v159, v159\n"
    "v_mfma_f32_16x16x32_bf16 a[220:223], v[224:227], v[164:167], a[220:223]\n"
    "ds_read_b64_tr_b16 v[224:225], v15 offset:24576\n"
    "ds_read_b64_tr_b16 v[226:227], v15 offset:28672\n"
    "v_cvt_pk_bf16_f32 v162, v156, v157\n"
    "v_mfma_f32_16x16x32_bf16 v[192:195], v[228:231], v[164:167], v[192:195]\n"
    "v_cvt_pk_bf16_f32 v163, v158, v159\n"
    "v_mfma_f32_16x16x32_bf16 v[152:155], a[224:227], v[64:67], 0\n"
    "v_exp_f32 v144, v144\n"
    "v_mfma_f32_16x16x32_bf16 v[156:159], a[240:243], v[64:67], 0\n"
    "s_add_u32 m0, s64, 16384\n"
    "s_waitcnt lgkmcnt(14)\n"
    "v_mfma_f32_16x16x32_bf16 a[0:3], v[196:199], v[160:163], a[0:3]\n"
    "v_exp_f32 v145, v145\n"
    "v_mfma_f32_16x16x32_bf16 v[152:155], a[228:231], v[68:71], v[152:155]\n"
    "buffer_load_dwordx4 v20, s[40:43], s66 offen offset:0 nt lds\n"
    "v_exp_f32 v146, v146\n"
    "s_waitcnt lgkmcnt(12)\n"
    "v_mfma_f32_16x16x32_bf16 a[4:7], v[200:203], v[160:163], a[4:7]\n"
    "buffer_load_dwordx4 v21, s[40:43], s66 offen offset:1024 nt lds\n"
    "v_mfma_f32_16x16x32_bf16 v[156:159], a[244:247], v[68:71], v[156:159]\n"
    "v_cvt_pk_bf16_f32 v164, v144, v145\n"
    "buffer_load_dwordx4 v20, s[40:43], s66 offen offset:2048 nt lds\n"
    "s_waitcnt lgkmcnt(10)\n"
    "v_mfma_f32_16x16x32_bf16 a[8:11], v[204:207], v[160:163], a[8:11]\n"
    "v_exp_f32 v147, v147\n"
    "v_mfma_f32_16x16x32_bf16 v[152:155], a[232:235], v[72:75], v[152:155]\n"
    "buffer_load_dwordx4 v21, s[40:43], s66 offen offset:3072 nt lds\n"
    "s_waitcnt lgkmcnt(8)\n"
    "v_mfma_f32_16x16x32_bf16 a[12:15], v[208:211], v[160:163], a[12:15]\n"
    "v_exp_f32 v148, v148\n"
    "v_mfma_f32_16x16x32_bf16 v[156:159], a[248:251], v[72:75], v[156:159]\n"
    "s_add_u32 m0, s64, 20480\n"
    "v_cvt_pk_bf16_f32 v165, v146, v147\n"
    "s_waitcnt lgkmcnt(6)\n"
    "v_mfma_f32_16x16x32_bf16 a[16:19], v[212:215], v[160:163], a[16:19]\n"
    "buffer_load_dwordx4 v22, s[40:43], s66 offen offset:0 nt lds\n"
    "v_exp_f32 v149, v149\n"
    "v_mfma_f32_16x16x32_bf16 v[152:155], a[236:239], v[76:79], v[152:155]\n"
    "buffer_load_dwordx4 v23, s[40:43], s66 offen offset:1024 nt lds\n"
    "s_waitcnt lgkmcnt(4)\n"
    "v_mfma_f32_16x16x32_bf16 a[20:23], v[216:219], v[160:163], a[20:23]\n"
    "v_exp_f32 v150, v150\n"
    "v_mfma_f32_16x16x32_bf16 v[156:159], a[252:255], v[76:79], v[156:159]\n"
    "buffer_load_dwordx4 v22, s[40:43], s66 offen offset:2048 nt lds\n"
    "s_waitcnt lgkmcnt(2)\n"
    "v_mfma_f32_16x16x32_bf16 a[24:27], v[220:223], v[160:163], a[24:27]\n"
    "v_exp_f32 v151, v151\n"
    "s_waitcnt lgkmcnt(0)\n"
    "v_mfma_f32_16x16x32_bf16 a[28:31], v[224:227], v[160:163], a[28:31]\n"
    "buffer_load_dwordx4 v23, s[40:43], s66 offen offset:3072 nt lds\n"
    "v_cvt_pk_bf16_f32 v166, v148, v149\n"
    "v_mfma_f32_16x16x32_bf16 v[168:171], v[228:231], v[160:163], v[168:171]\n"
    "s_add_u32 s66, s66, 8192\n"
    "v_cvt_pk_bf16_f32 v167, v150, v151\n"
    "v_mfma_f32_16x16x32_bf16 v[144:147], a[224:227], v[80:83], 0\n"
    "v_exp_f32 v152, v152\n"
    "v_mfma_f32_16x16x32_bf16 v[148:151], a[240:243], v[80:83], 0\n"
    "v_mfma_f32_16x16x32_bf16 a[32:35], v[196:199], v[164:167], a[32:35]\n"
    "v_exp_f32 v153, v153\n"
    "v_mfma_f32_16x16x32_bf16 v[144:147], a[228:231], v[84:87], v[144:147]\n"
    "v_mfma_f32_16x16x32_bf16 a[36:39], v[200:203], v[164:167], a[36:39]\n"
    "v_exp_f32 v154, v154\n"
    "v_mfma_f32_16x16x32_bf16 v[148:151], a[244:247], v[84:87], v[148:151]\n"
    "v_cvt_pk_bf16_f32 v160, v152, v153\n"
    "v_mfma_f32_16x16x32_bf16 a[40:43], v[204:207], v[164:167], a[40:43]\n"
    "v_exp_f32 v155, v155\n"
    "v_mfma_f32_16x16x32_bf16 v[144:147], a[232:235], v[88:91], v[144:147]\n"
    "v_mfma_f32_16x16x32_bf16 a[44:47], v[208:211], v[164:167], a[44:47]\n"
    "v_exp_f32 v156, v156\n"
    "v_mfma_f32_16x16x32_bf16 v[148:151], a[248:251], v[88:91], v[148:151]\n"
    "v_cvt_pk_bf16_f32 v161, v154, v155\n"
    "v_mfma_f32_16x16x32_bf16 a[48:51], v[212:215], v[164:167], a[48:51]\n"
    "v_mfma_f32_16x16x32_bf16 v[144:147], a[236:239], v[92:95], v[144:147]\n"
    "v_exp_f32 v157, v157\n"
    "v_mfma_f32_16x16x32_bf16 a[52:55], v[216:219], v[164:167], a[52:55]\n"
    "v_exp_f32 v158, v158\n"
    "v_mfma_f32_16x16x32_bf16 v[148:151], a[252:255], v[92:95], v[148:151]\n"
    "v_mfma_f32_16x16x32_bf16 a[56:59], v[220:223], v[164:167], a[56:59]\n"
    "v_exp_f32 v159, v159\n"
    "v_mfma_f32_16x16x32_bf16 a[60:63], v[224:227], v[164:167], a[60:63]\n"
    "v_cvt_pk_bf16_f32 v162, v156, v157\n"
    "v_mfma_f32_16x16x32_bf16 v[172:175], v[228:231], v[164:167], v[172:175]\n"
    "v_cvt_pk_bf16_f32 v163, v158, v159\n"
    "v_mfma_f32_16x16x32_bf16 v[152:155], a[224:227], v[96:99], 0\n"
    "v_exp_f32 v144, v144\n"
    "v_mfma_f32_16x16x32_bf16 v[156:159], a[240:243], v[96:99], 0\n"
    "s_add_u32 m0, s64, 24576\n"
    "v_mfma_f32_16x16x32_bf16 a[64:67], v[196:199], v[160:163], a[64:67]\n"
    "v_exp_f32 v145, v145\n"
    "v_mfma_f32_16x16x32_bf16 v[152:155], a[228:231], v[100:103], v[152:155]\n"
    "buffer_load_dwordx4 v20, s[44:47], s67 offen offset:0 nt lds\n"
    "v_exp_f32 v146, v146\n"
    "v_mfma_f32_16x16x32_bf16 a[68:71], v[200:203], v[160:163], a[68:71]\n"
    "buffer_load_dwordx4 v21, s[44:47], s67 offen offset:1024 nt lds\n"
    "v_mfma_f32_16x16x32_bf16 v[156:159], a[244:247], v[100:103], v[156:159]\n"
    "v_cvt_pk_bf16_f32 v164, v144, v145\n"
    "buffer_load_dwordx4 v20, s[44:47], s67 offen offset:2048 nt lds\n"
    "v_mfma_f32_16x16x32_bf16 a[72:75], v[204:207], v[160:163], a[72:75]\n"
    "v_exp_f32 v147, v147\n"
    "v_mfma_f32_16x16x32_bf16 v[152:155], a[232:235], v[104:107], v[152:155]\n"
    "buffer_load_dwordx4 v21, s[44:47], s67 offen offset:3072 nt lds\n"
    "v_mfma_f32_16x16x32_bf16 a[76:79], v[208:211], v[160:163], a[76:79]\n"
    "v_exp_f32 v148, v148\n"
    "v_mfma_f32_16x16x32_bf16 v[156:159], a[248:251], v[104:107], v[156:159]\n"
    "s_add_u32 m0, s64, 28672\n"
    "v_cvt_pk_bf16_f32 v165, v146, v147\n"
    "v_mfma_f32_16x16x32_bf16 a[80:83], v[212:215], v[160:163], a[80:83]\n"
    "buffer_load_dwordx4 v22, s[44:47], s67 offen offset:0 nt lds\n"
    "v_exp_f32 v149, v149\n"
    "v_mfma_f32_16x16x32_bf16 v[152:155], a[236:239], v[108:111], v[152:155]\n"
    "buffer_load_dwordx4 v23, s[44:47], s67 offen offset:1024 nt lds\n"
    "v_mfma_f32_16x16x32_bf16 a[84:87], v[216:219], v[160:163], a[84:87]\n"
    "v_exp_f32 v150, v150\n"
    "v_mfma_f32_16x16x32_bf16 v[156:159], a[252:255], v[108:111], v[156:159]\n"
    "buffer_load_dwordx4 v22, s[44:47], s67 offen offset:2048 nt lds\n"
    "v_mfma_f32_16x16x32_bf16 a[88:91], v[220:223], v[160:163], a[88:91]\n"
    "v_exp_f32 v151, v151\n"
    "v_mfma_f32_16x16x32_bf16 a[92:95], v[224:227], v[160:163], a[92:95]\n"
    "buffer_load_dwordx4 v23, s[44:47], s67 offen offset:3072 nt lds\n"
    "v_cvt_pk_bf16_f32 v166, v148, v149\n"
    "v_mfma_f32_16x16x32_bf16 v[176:179], v[228:231], v[160:163], v[176:179]\n"
    "s_add_u32 s67, s67, 8192\n"
    "v_cvt_pk_bf16_f32 v167, v150, v151\n"
    "v_mfma_f32_16x16x32_bf16 v[144:147], a[224:227], v[112:115], 0\n"
    "v_exp_f32 v152, v152\n"
    "v_mfma_f32_16x16x32_bf16 v[148:151], a[240:243], v[112:115], 0\n"
    "s_mov_b32 m0, s73\n"
    "v_mfma_f32_16x16x32_bf16 a[96:99], v[196:199], v[164:167], a[96:99]\n"
    "v_exp_f32 v153, v153\n"
    "v_mfma_f32_16x16x32_bf16 v[144:147], a[228:231], v[116:119], v[144:147]\n"
    "v_mfma_f32_16x16x32_bf16 a[100:103], v[200:203], v[164:167], a[100:103]\n"
    "v_exp_f32 v154, v154\n"
    "v_mfma_f32_16x16x32_bf16 v[148:151], a[244:247], v[116:119], v[148:151]\n"
    "v_cvt_pk_bf16_f32 v160, v152, v153\n"
    "v_mfma_f32_16x16x32_bf16 a[104:107], v[204:207], v[164:167], a[104:107]\n"
    "v_exp_f32 v155, v155\n"
    "v_mfma_f32_16x16x32_bf16 v[144:147], a[232:235], v[120:123], v[144:147]\n"
    "buffer_load_dwordx4 v27, s[60:63], s86 offen lds\n"
    "v_mfma_f32_16x16x32_bf16 a[108:111], v[208:211], v[164:167], a[108:111]\n"
    "v_exp_f32 v156, v156\n"
    "v_mfma_f32_16x16x32_bf16 v[148:151], a[248:251], v[120:123], v[148:151]\n"
    "v_cvt_pk_bf16_f32 v161, v154, v155\n"
    "v_mfma_f32_16x16x32_bf16 a[112:115], v[212:215], v[164:167], a[112:115]\n"
    "v_mfma_f32_16x16x32_bf16 v[144:147], a[236:239], v[124:127], v[144:147]\n"
    "v_exp_f32 v157, v157\n"
    "v_mfma_f32_16x16x32_bf16 a[116:119], v[216:219], v[164:167], a[116:119]\n"
    "v_exp_f32 v158, v158\n"
    "v_mfma_f32_16x16x32_bf16 v[148:151], a[252:255], v[124:127], v[148:151]\n"
    "s_add_u32 s86, s86, 1024\n"
    "v_mfma_f32_16x16x32_bf16 a[120:123], v[220:223], v[164:167], a[120:123]\n"
    "v_exp_f32 v159, v159\n"
    "v_mfma_f32_16x16x32_bf16 a[124:127], v[224:227], v[164:167], a[124:127]\n"
    "v_cvt_pk_bf16_f32 v162, v156, v157\n"
    "v_mfma_f32_16x16x32_bf16 v[180:183], v[228:231], v[164:167], v[180:183]\n"
    "v_cvt_pk_bf16_f32 v163, v158, v159\n"
    "s_waitcnt vmcnt(26)\n"
    "v_mfma_f32_16x16x32_bf16 v[152:155], a[224:227], v[128:131], 0\n"
    "ds_read_b128 a[224:227], v16 offset:0\n"
    "v_exp_f32 v144, v144\n"
    "v_mfma_f32_16x16x32_bf16 v[156:159], a[240:243], v[128:131], 0\n"
    "ds_read_b128 a[240:243], v16 offset:4096\n"
    "v_mfma_f32_16x16x32_bf16 a[128:131], v[196:199], v[160:163], a[128:131]\n"
    "v_exp_f32 v145, v145\n"
    "v_mfma_f32_16x16x32_bf16 v[152:155], a[228:231], v[132:135], v[152:155]\n"
    "ds_read_b128 a[228:231], v17 offset:0\n"
    "v_mfma_f32_16x16x32_bf16 a[132:135], v[200:203], v[160:163], a[132:135]\n"
    "v_exp_f32 v146, v146\n"
    "v_mfma_f32_16x16x32_bf16 v[156:159], a[244:247], v[132:135], v[156:159]\n"
    "ds_read_b128 a[244:247], v17 offset:4096\n"
    "v_cvt_pk_bf16_f32 v164, v144, v145\n"
    "v_mfma_f32_16x16x32_bf16 a[136:139], v[204:207], v[160:163], a[136:139]\n"
    "v_exp_f32 v147, v147\n"
    "v_mfma_f32_16x16x32_bf16 v[152:155], a[232:235], v[136:139], v[152:155]\n"
    "ds_read_b128 a[232:235], v18 offset:0\n"
    "v_mfma_f32_16x16x32_bf16 a[140:143], v[208:211], v[160:163], a[140:143]\n"
    "v_exp_f32 v148, v148\n"
    "v_mfma_f32_16x16x32_bf16 v[156:159], a[248:251], v[136:139], v[156:159]\n"
    "ds_read_b128 a[248:251], v18 offset:4096\n"
    "v_cvt_pk_bf16_f32 v165, v146, v147\n"
    "v_mfma_f32_16x16x32_bf16 a[144:147], v[212:215], v[160:163], a[144:147]\n"
    "v_mfma_f32_16x16x32_bf16 v[152:155], a[236:239], v[140:143], v[152:155]\n"
    "ds_read_b128 a[236:239], v19 offset:0\n"
    "v_exp_f32 v149, v149\n"
    "v_mfma_f32_16x16x32_bf16 a[148:151], v[216:219], v[160:163], a[148:151]\n"
    "v_exp_f32 v150, v150\n"
    "v_mfma_f32_16x16x32_bf16 v[156:159], a[252:255], v[140:143], v[156:159]\n"
    "ds_read_b128 a[252:255], v19 offset:4096\n"
    "v_mfma_f32_16x16x32_bf16 a[152:155], v[220:223], v[160:163], a[152:155]\n"
    "v_exp_f32 v151, v151\n"
    "v_mfma_f32_16x16x32_bf16 a[156:159], v[224:227], v[160:163], a[156:159]\n"
    "v_cvt_pk_bf16_f32 v166, v148, v149\n"
    "v_mfma_f32_16x16x32_bf16 v[184:187], v[228:231], v[160:163], v[184:187]\n"
    "v_cvt_pk_bf16_f32 v167, v150, v151\n"
    "s_add_u32 s70, s70, 1\n"
    "s_cmp_lt_u32 s70, 6\n"
    "s_cbranch_scc1 .LW_a_%=\n"
    "s_waitcnt lgkmcnt(7)\n"
    "v_mfma_f32_16x16x32_bf16 v[144:147], a[224:227], v[32:35], 0\n"
    "v_exp_f32 v152, v152\n"
    "s_waitcnt lgkmcnt(6)\n"
    "v_mfma_f32_16x16x32_bf16 v[148:151], a[240:243], v[32:35], 0\n"
    "v_mfma_f32_16x16x32_bf16 a[160:163], v[196:199], v[164:167], a[160:163]\n"
    "v_exp_f32 v153, v153\n"
    "s_waitcnt lgkmcnt(5)\n"
    "v_mfma_f32_16x16x32_bf16 v[144:147], a[228:231], v[36:39], v[144:147]\n"
    "v_mfma_f32_16x16x32_bf16 a[164:167], v[200:203], v[164:167], a[164:167]\n"
    "v_exp_f32 v154, v154\n"
    "s_waitcnt lgkmcnt(4)\n"
    "v_mfma_f32_16x16x32_bf16 v[148:151], a[244:247], v[36:39], v[148:151]\n"
    "v_cvt_pk_bf16_f32 v160, v152, v153\n"
    "v_mfma_f32_16x16x32_bf16 a[168:171], v[204:207], v[164:167], a[168:171]\n"
    "v_exp_f32 v155, v155\n"
    "s_waitcnt lgkmcnt(3)\n"
    "v_mfma_f32_16x16x32_bf16 v[144:147], a[232:235], v[40:43], v[144:147]\n"
    "v_mfma_f32_16x16x32_bf16 a[172:175], v[208:211], v[164:167], a[172:175]\n"
    "v_exp_f32 v156, v156\n"
    "s_waitcnt lgkmcnt(2)\n"
    "v_mfma_f32_16x16x32_bf16 v[148:151], a[248:251], v[40:43], v[148:151]\n"
    "v_cvt_pk_bf16_f32 v161, v154, v155\n"
    "v_mfma_f32_16x16x32_bf16 a[176:179], v[212:215], v[164:167], a[176:179]\n"
    "s_waitcnt lgkmcnt(1)\n"
    "v_mfma_f32_16x16x32_bf16 v[144:147], a[236:239], v[44:47], v[144:147]\n"
    "v_exp_f32 v157, v157\n"
    "v_mfma_f32_16x16x32_bf16 a[180:183], v[216:219], v[164:167], a[180:183]\n"
    "v_exp_f32 v158, v158\n"
    "s_waitcnt lgkmcnt(0)\n"
    "v_mfma_f32_16x16x32_bf16 v[148:151], a[252:255], v[44:47], v[148:151]\n"
    "v_mfma_f32_16x16x32_bf16 a[184:187], v[220:223], v[164:167], a[184:187]\n"
    "v_exp_f32 v159, v159\n"
    "v_mfma_f32_16x16x32_bf16 a[188:191], v[224:227], v[164:167], a[188:191]\n"
    "v_cvt_pk_bf16_f32 v162, v156, v157\n"
    "v_mfma_f32_16x16x32_bf16 v[188:191], v[228:231], v[164:167], v[188:191]\n"
    "v_cvt_pk_bf16_f32 v163, v158, v159\n"
    "s_waitcnt vmcnt(18)\n"
    "v_mfma_f32_16x16x32_bf16 v[152:155], a[224:227], v[48:51], 0\n"
    "v_exp_f32 v144, v144\n"
    "v_mfma_f32_16x16x32_bf16 v[156:159], a[240:243], v[48:51], 0\n"
    "v_mfma_f32_16x16x32_bf16 a[192:195], v[196:199], v[160:163], a[192:195]\n"
    "ds_read_b64_tr_b16 v[196:197], v8 offset:8192\n"
    "ds_read_b64_tr_b16 v[198:199], v8 offset:12288\n"
    "v_exp_f32 v145, v145\n"
    "v_mfma_f32_16x16x32_bf16 v[152:155], a[228:231], v[52:55], v[152:155]\n"
    "v_mfma_f32_16x16x32_bf16 a[196:199], v[200:203], v[160:163], a[196:199]\n"
    "ds_read_b64_tr_b16 v[200:201], v9 offset:8192\n"
    "ds_read_b64_tr_b16 v[202:203], v9 offset:12288\n"
    "v_exp_f32 v146, v146\n"
    "v_mfma_f32_16x16x32_bf16 v[156:159], a[244:247], v[52:55], v[156:159]\n"
    "v_cvt_pk_bf16_f32 v164, v144, v145\n"
    "v_mfma_f32_16x16x32_bf16 a[200:203], v[204:207], v[160:163], a[200:203]\n"
    "ds_read_b64_tr_b16 v[204:205], v10 offset:8192\n"
    "ds_read_b64_tr_b16 v[206:207], v10 offset:12288\n"
    "v_exp_f32 v147, v147\n"
    "v_mfma_f32_16x16x32_bf16 v[152:155], a[232:235], v[56:59], v[152:155]\n"
    "v_mfma_f32_16x16x32_bf16 a[204:207], v[208:211], v[160:163], a[204:207]\n"
    "ds_read_b64_tr_b16 v[208:209], v11 offset:8192\n"
    "ds_read_b64_tr_b16 v[210:211], v11 offset:12288\n"
    "v_exp_f32 v148, v148\n"
    "v_mfma_f32_16x16x32_bf16 v[156:159], a[248:251], v[56:59], v[156:159]\n"
    "v_cvt_pk_bf16_f32 v165, v146, v147\n"
    "v_mfma_f32_16x16x32_bf16 a[208:211], v[212:215], v[160:163], a[208:211]\n"
    "ds_read_b64_tr_b16 v[212:213], v12 offset:8192\n"
    "ds_read_b64_tr_b16 v[214:215], v12 offset:12288\n"
    "v_mfma_f32_16x16x32_bf16 v[152:155], a[236:239], v[60:63], v[152:155]\n"
    "v_exp_f32 v149, v149\n"
    "v_mfma_f32_16x16x32_bf16 a[212:215], v[216:219], v[160:163], a[212:215]\n"
    "ds_read_b64_tr_b16 v[216:217], v13 offset:8192\n"
    "ds_read_b64_tr_b16 v[218:219], v13 offset:12288\n"
    "v_exp_f32 v150, v150\n"
    "v_mfma_f32_16x16x32_bf16 v[156:159], a[252:255], v[60:63], v[156:159]\n"
    "v_mfma_f32_16x16x32_bf16 a[216:219], v[220:223], v[160:163], a[216:219]\n"
    "ds_read_b64_tr_b16 v[220:221], v14 offset:8192\n"
    "ds_read_b64_tr_b16 v[222:223], v14 offset:12288\n"
    "v_exp_f32 v151, v151\n"
    "v_mfma_f32_16x16x32_bf16 a[220:223], v[224:227], v[160:163], a[220:223]\n"
    "ds_read_b64_tr_b16 v[224:225], v15 offset:8192\n"
    "ds_read_b64_tr_b16 v[226:227], v15 offset:12288\n"
    "v_cvt_pk_bf16_f32 v166, v148, v149\n"
    "v_mfma_f32_16x16x32_bf16 v[192:195], v[228:231], v[160:163], v[192:195]\n"
    "v_cvt_pk_bf16_f32 v167, v150, v151\n"
    "v_mfma_f32_16x16x32_bf16 v[144:147], a[224:227], v[64:67], 0\n"
    "v_exp_f32 v152, v152\n"
    "v_mfma_f32_16x16x32_bf16 v[148:151], a[240:243], v[64:67], 0\n"
    "s_waitcnt lgkmcnt(14)\n"
    "v_mfma_f32_16x16x32_bf16 a[0:3], v[196:199], v[164:167], a[0:3]\n"
    "v_exp_f32 v153, v153\n"
    "v_mfma_f32_16x16x32_bf16 v[144:147], a[228:231], v[68:71], v[144:147]\n"
    "s_waitcnt lgkmcnt(12)\n"
    "v_mfma_f32_16x16x32_bf16 a[4:7], v[200:203], v[164:167], a[4:7]\n"
    "v_exp_f32 v154, v154\n"
    "v_mfma_f32_16x16x32_bf16 v[148:151], a[244:247], v[68:71], v[148:151]\n"
    "v_cvt_pk_bf16_f32 v160, v152, v153\n"
    "s_waitcnt lgkmcnt(10)\n"
    "v_mfma_f32_16x16x32_bf16 a[8:11], v[204:207], v[164:167], a[8:11]\n"
    "v_exp_f32 v155, v155\n"
    "v_mfma_f32_16x16x32_bf16 v[144:147], a[232:235], v[72:75], v[144:147]\n"
    "s_waitcnt lgkmcnt(8)\n"
    "v_mfma_f32_16x16x32_bf16 a[12:15], v[208:211], v[164:167], a[12:15]\n"
    "v_exp_f32 v156, v156\n"
    "v_mfma_f32_16x16x32_bf16 v[148:151], a[248:251], v[72:75], v[148:151]\n"
    "v_cvt_pk_bf16_f32 v161, v154, v155\n"
    "s_waitcnt lgkmcnt(6)\n"
    "v_mfma_f32_16x16x32_bf16 a[16:19], v[212:215], v[164:167], a[16:19]\n"
    "v_mfma_f32_16x16x32_bf16 v[144:147], a[236:239], v[76:79], v[144:147]\n"
    "v_exp_f32 v157, v157\n"
    "s_waitcnt lgkmcnt(4)\n"
    "v_mfma_f32_16x16x32_bf16 a[20:23], v[216:219], v[164:167], a[20:23]\n"
    "v_exp_f32 v158, v158\n"
    "v_mfma_f32_16x16x32_bf16 v[148:151], a[252:255], v[76:79], v[148:151]\n"
    "s_waitcnt lgkmcnt(2)\n"
    "v_mfma_f32_16x16x32_bf16 a[24:27], v[220:223], v[164:167], a[24:27]\n"
    "v_exp_f32 v159, v159\n"
    "s_waitcnt lgkmcnt(0)\n"
    "v_mfma_f32_16x16x32_bf16 a[28:31], v[224:227], v[164:167], a[28:31]\n"
    "v_cvt_pk_bf16_f32 v162, v156, v157\n"
    "v_mfma_f32_16x16x32_bf16 v[168:171], v[228:231], v[164:167], v[168:171]\n"
    "v_cvt_pk_bf16_f32 v163, v158, v159\n"
    "v_mfma_f32_16x16x32_bf16 v[152:155], a[224:227], v[80:83], 0\n"
    "v_exp_f32 v144, v144\n"
    "v_mfma_f32_16x16x32_bf16 v[156:159], a[240:243], v[80:83], 0\n"
    "v_mfma_f32_16x16x32_bf16 a[32:35], v[196:199], v[160:163], a[32:35]\n"
    "v_exp_f32 v145, v145\n"
    "v_mfma_f32_16x16x32_bf16 v[152:155], a[228:231], v[84:87], v[152:155]\n"
    "v_mfma_f32_16x16x32_bf16 a[36:39], v[200:203], v[160:163], a[36:39]\n"
    "v_exp_f32 v146, v146\n"
    "v_mfma_f32_16x16x32_bf16 v[156:159], a[244:247], v[84:87], v[156:159]\n"
    "v_cvt_pk_bf16_f32 v164, v144, v145\n"
    "v_mfma_f32_16x16x32_bf16 a[40:43], v[204:207], v[160:163], a[40:43]\n"
    "v_exp_f32 v147, v147\n"
    "v_mfma_f32_16x16x32_bf16 v[152:155], a[232:235], v[88:91], v[152:155]\n"
    "v_mfma_f32_16x16x32_bf16 a[44:47], v[208:211], v[160:163], a[44:47]\n"
    "v_exp_f32 v148, v148\n"
    "v_mfma_f32_16x16x32_bf16 v[156:159], a[248:251], v[88:91], v[156:159]\n"
    "v_cvt_pk_bf16_f32 v165, v146, v147\n"
    "v_mfma_f32_16x16x32_bf16 a[48:51], v[212:215], v[160:163], a[48:51]\n"
    "v_mfma_f32_16x16x32_bf16 v[152:155], a[236:239], v[92:95], v[152:155]\n"
    "v_exp_f32 v149, v149\n"
    "v_mfma_f32_16x16x32_bf16 a[52:55], v[216:219], v[160:163], a[52:55]\n"
    "v_exp_f32 v150, v150\n"
    "v_mfma_f32_16x16x32_bf16 v[156:159], a[252:255], v[92:95], v[156:159]\n"
    "v_mfma_f32_16x16x32_bf16 a[56:59], v[220:223], v[160:163], a[56:59]\n"
    "v_exp_f32 v151, v151\n"
    "v_mfma_f32_16x16x32_bf16 a[60:63], v[224:227], v[160:163], a[60:63]\n"
    "v_cvt_pk_bf16_f32 v166, v148, v149\n"
    "v_mfma_f32_16x16x32_bf16 v[172:175], v[228:231], v[160:163], v[172:175]\n"
    "v_cvt_pk_bf16_f32 v167, v150, v151\n"
    "v_mfma_f32_16x16x32_bf16 v[144:147], a[224:227], v[96:99], 0\n"
    "v_exp_f32 v152, v152\n"
    "v_mfma_f32_16x16x32_bf16 v[148:151], a[240:243], v[96:99], 0\n"
    "v_mfma_f32_16x16x32_bf16 a[64:67], v[196:199], v[164:167], a[64:67]\n"
    "v_exp_f32 v153, v153\n"
    "v_mfma_f32_16x16x32_bf16 v[144:147], a[228:231], v[100:103], v[144:147]\n"
    "v_mfma_f32_16x16x32_bf16 a[68:71], v[200:203], v[164:167], a[68:71]\n"
    "v_exp_f32 v154, v154\n"
    "v_mfma_f32_16x16x32_bf16 v[148:151], a[244:247], v[100:103], v[148:151]\n"
    "v_cvt_pk_bf16_f32 v160, v152, v153\n"
    "v_mfma_f32_16x16x32_bf16 a[72:75], v[204:207], v[164:167], a[72:75]\n"
    "v_exp_f32 v155, v155\n"
    "v_mfma_f32_16x16x32_bf16 v[144:147], a[232:235], v[104:107], v[144:147]\n"
    "v_mfma_f32_16x16x32_bf16 a[76:79], v[208:211], v[164:167], a[76:79]\n"
    "v_exp_f32 v156, v156\n"
    "v_mfma_f32_16x16x32_bf16 v[148:151], a[248:251], v[104:107], v[148:151]\n"
    "v_cvt_pk_bf16_f32 v161, v154, v155\n"
    "v_mfma_f32_16x16x32_bf16 a[80:83], v[212:215], v[164:167], a[80:83]\n"
    "v_mfma_f32_16x16x32_bf16 v[144:147], a[236:239], v[108:111], v[144:147]\n"
    "v_exp_f32 v157, v157\n"
    "v_mfma_f32_16x16x32_bf16 a[84:87], v[216:219], v[164:167], a[84:87]\n"
    "v_exp_f32 v158, v158\n"
    "v_mfma_f32_16x16x32_bf16 v[148:151], a[252:255], v[108:111], v[148:151]\n"
    "v_mfma_f32_16x16x32_bf16 a[88:91], v[220:223], v[164:167], a[88:91]\n"
    "v_exp_f32 v159, v159\n"
    "v_mfma_f32_16x16x32_bf16 a[92:95], v[224:227], v[164:167], a[92:95]\n"
    "v_cvt_pk_bf16_f32 v162, v156, v157\n"
    "v_mfma_f32_16x16x32_bf16 v[176:179], v[228:231], v[164:167], v[176:179]\n"
    "v_cvt_pk_bf16_f32 v163, v158, v159\n"
    "v_mfma_f32_16x16x32_bf16 v[152:155], a[224:227], v[112:115], 0\n"
    "v_exp_f32 v144, v144\n"
    "v_mfma_f32_16x16x32_bf16 v[156:159], a[240:243], v[112:115], 0\n"
    "v_mfma_f32_16x16x32_bf16 a[96:99], v[196:199], v[160:163], a[96:99]\n"
    "v_exp_f32 v145, v145\n"
    "v_mfma_f32_16x16x32_bf16 v[152:155], a[228:231], v[116:119], v[152:155]\n"
    "v_mfma_f32_16x16x32_bf16 a[100:103], v[200:203], v[160:163], a[100:103]\n"
    "v_exp_f32 v146, v146\n"
    "v_mfma_f32_16x16x32_bf16 v[156:159], a[244:247], v[116:119], v[156:159]\n"
    "v_cvt_pk_bf16_f32 v164, v144, v145\n"
    "v_mfma_f32_16x16x32_bf16 a[104:107], v[204:207], v[160:163], a[104:107]\n"
    "v_exp_f32 v147, v147\n"
    "v_mfma_f32_16x16x32_bf16 v[152:155], a[232:235], v[120:123], v[152:155]\n"
    "v_mfma_f32_16x16x32_bf16 a[108:111], v[208:211], v[160:163], a[108:111]\n"
    "v_exp_f32 v148, v148\n"
    "v_mfma_f32_16x16x32_bf16 v[156:159], a[248:251], v[120:123], v[156:159]\n"
    "v_cvt_pk_bf16_f32 v165, v146, v147\n"
    "v_mfma_f32_16x16x32_bf16 a[112:115], v[212:215], v[160:163], a[112:115]\n"
    "v_mfma_f32_16x16x32_bf16 v[152:155], a[236:239], v[124:127], v[152:155]\n"
    "v_exp_f32 v149, v149\n"
    "v_mfma_f32_16x16x32_bf16 a[116:119], v[216:219], v[160:163], a[116:119]\n"
    "v_exp_f32 v150, v150\n"
    "v_mfma_f32_16x16x32_bf16 v[156:159], a[252:255], v[124:127], v[156:159]\n"
    "v_mfma_f32_16x16x32_bf16 a[120:123], v[220:223], v[160:163], a[120:123]\n"
    "v_exp_f32 v151, v151\n"
    "v_mfma_f32_16x16x32_bf16 a[124:127], v[224:227], v[160:163], a[124:127]\n"
    "v_cvt_pk_bf16_f32 v166, v148, v149\n"
    "v_mfma_f32_16x16x32_bf16 v[180:183], v[228:231], v[160:163], v[180:183]\n"
    "v_cvt_pk_bf16_f32 v167, v150, v151\n"
    "s_waitcnt vmcnt(9)\n"
    "v_mfma_f32_16x16x32_bf16 v[144:147], a[224:227], v[128:131], 0\n"
    "ds_read_b128 a[224:227], v16 offset:16384\n"
    "v_exp_f32 v152, v152\n"
    "v_mfma_f32_16x16x32_bf16 v[148:151], a[240:243], v[128:131], 0\n"
    "ds_read_b128 a[240:243], v16 offset:20480\n"
    "v_mfma_f32_16x16x32_bf16 a[128:131], v[196:199], v[164:167], a[128:131]\n"
    "v_exp_f32 v153, v153\n"
    "v_mfma_f32_16x16x32_bf16 v[144:147], a[228:231], v[132:135], v[144:147]\n"
    "ds_read_b128 a[228:231], v17 offset:16384\n"
    "v_mfma_f32_16x16x32_bf16 a[132:135], v[200:203], v[164:167], a[132:135]\n"
    "v_exp_f32 v154, v154\n"
    "v_mfma_f32_16x16x32_bf16 v[148:151], a[244:247], v[132:135], v[148:151]\n"
    "ds_read_b128 a[244:247], v17 offset:20480\n"
    "v_cvt_pk_bf16_f32 v160, v152, v153\n"
    "v_mfma_f32_16x16x32_bf16 a[136:139], v[204:207], v[164:167], a[136:139]\n"
    "v_exp_f32 v155, v155\n"
    "v_mfma_f32_16x16x32_bf16 v[144:147], a[232:235], v[136:139], v[144:147]\n"
    "ds_read_b128 a[232:235], v18 offset:16384\n"
    "v_mfma_f32_16x16x32_bf16 a[140:143], v[208:211], v[164:167], a[140:143]\n"
    "v_exp_f32 v156, v156\n"
    "v_mfma_f32_16x16x32_bf16 v[148:151], a[248:251], v[136:139], v[148:151]\n"
    "ds_read_b128 a[248:251], v18 offset:20480\n"
    "v_cvt_pk_bf16_f32 v161, v154, v155\n"
    "v_mfma_f32_16x16x32_bf16 a[144:147], v[212:215], v[164:167], a[144:147]\n"
    "v_mfma_f32_16x16x32_bf16 v[144:147], a[236:239], v[140:143], v[144:147]\n"
    "ds_read_b128 a[236:239], v19 offset:16384\n"
    "v_exp_f32 v157, v157\n"
    "v_mfma_f32_16x16x32_bf16 a[148:151], v[216:219], v[164:167], a[148:151]\n"
    "v_exp_f32 v158, v158\n"
    "v_mfma_f32_16x16x32_bf16 v[148:151], a[252:255], v[140:143], v[148:151]\n"
    "ds_read_b128 a[252:255], v19 offset:20480\n"
    "v_mfma_f32_16x16x32_bf16 a[152:155], v[220:223], v[164:167], a[152:155]\n"
    "v_exp_f32 v159, v159\n"
    "v_mfma_f32_16x16x32_bf16 a[156:159], v[224:227], v[164:167], a[156:159]\n"
    "v_cvt_pk_bf16_f32 v162, v156, v157\n"
    "v_mfma_f32_16x16x32_bf16 v[184:187], v[228:231], v[164:167], v[184:187]\n"
    "v_cvt_pk_bf16_f32 v163, v158, v159\n"
    "s_waitcnt lgkmcnt(7)\n"
    "v_mfma_f32_16x16x32_bf16 v[152:155], a[224:227], v[32:35], 0\n"
    "v_exp_f32 v144, v144\n"
    "s_waitcnt lgkmcnt(6)\n"
    "v_mfma_f32_16x16x32_bf16 v[156:159], a[240:243], v[32:35], 0\n"
    "v_mfma_f32_16x16x32_bf16 a[160:163], v[196:199], v[160:163], a[160:163]\n"
    "v_exp_f32 v145, v145\n"
    "s_waitcnt lgkmcnt(5)\n"
    "v_mfma_f32_16x16x32_bf16 v[152:155], a[228:231], v[36:39], v[152:155]\n"
    "v_mfma_f32_16x16x32_bf16 a[164:167], v[200:203], v[160:163], a[164:167]\n"
    "v_exp_f32 v146, v146\n"
    "s_waitcnt lgkmcnt(4)\n"
    "v_mfma_f32_16x16x32_bf16 v[156:159], a[244:247], v[36:39], v[156:159]\n"
    "v_cvt_pk_bf16_f32 v164, v144, v145\n"
    "v_mfma_f32_16x16x32_bf16 a[168:171], v[204:207], v[160:163], a[168:171]\n"
    "v_exp_f32 v147, v147\n"
    "s_waitcnt lgkmcnt(3)\n"
    "v_mfma_f32_16x16x32_bf16 v[152:155], a[232:235], v[40:43], v[152:155]\n"
    "v_mfma_f32_16x16x32_bf16 a[172:175], v[208:211], v[160:163], a[172:175]\n"
    "v_exp_f32 v148, v148\n"
    "s_waitcnt lgkmcnt(2)\n"
    "v_mfma_f32_16x16x32_bf16 v[156:159], a[248:251], v[40:43], v[156:159]\n"
    "v_cvt_pk_bf16_f32 v165, v146, v147\n"
    "v_mfma_f32_16x16x32_bf16 a[176:179], v[212:215], v[160:163], a[176:179]\n"
    "s_waitcnt lgkmcnt(1)\n"
    "v_mfma_f32_16x16x32_bf16 v[152:155], a[236:239], v[44:47], v[152:155]\n"
    "v_exp_f32 v149, v149\n"
    "v_mfma_f32_16x16x32_bf16 a[180:183], v[216:219], v[160:163], a[180:183]\n"
    "v_exp_f32 v150, v150\n"
    "s_waitcnt lgkmcnt(0)\n"
    "v_mfma_f32_16x16x32_bf16 v[156:159], a[252:255], v[44:47], v[156:159]\n"
    "v_mfma_f32_16x16x32_bf16 a[184:187], v[220:223], v[160:163], a[184:187]\n"
    "v_exp_f32 v151, v151\n"
    "v_mfma_f32_16x16x32_bf16 a[188:191], v[224:227], v[160:163], a[188:191]\n"
    "v_cvt_pk_bf16_f32 v166, v148, v149\n"
    "v_mfma_f32_16x16x32_bf16 v[188:191], v[228:231], v[160:163], v[188:191]\n"
    "v_cvt_pk_bf16_f32 v167, v150, v151\n"
    "s_waitcnt vmcnt(1)\n"
    "v_mfma_f32_16x16x32_bf16 v[144:147], a[224:227], v[48:51], 0\n"
    "v_exp_f32 v152, v152\n"
    "v_mfma_f32_16x16x32_bf16 v[148:151], a[240:243], v[48:51], 0\n"
    "v_mfma_f32_16x16x32_bf16 a[192:195], v[196:199], v[164:167], a[192:195]\n"
    "ds_read_b64_tr_b16 v[196:197], v8 offset:24576\n"
    "ds_read_b64_tr_b16 v[198:199], v8 offset:28672\n"
    "v_exp_f32 v153, v153\n"
    "v_mfma_f32_16x16x32_bf16 v[144:147], a[228:231], v[52:55], v[144:147]\n"
    "v_mfma_f32_16x16x32_bf16 a[196:199], v[200:203], v[164:167], a[196:199]\n"
    "ds_read_b64_tr_b16 v[200:201], v9 offset:24576\n"
    "ds_read_b64_tr_b16 v[202:203], v9 offset:28672\n"
    "v_exp_f32 v154, v154\n"
    "v_mfma_f32_16x16x32_bf16 v[148:151], a[244:247], v[52:55], v[148:151]\n"
    "v_cvt_pk_bf16_f32 v160, v152, v153\n"
    "v_mfma_f32_16x16x32_bf16 a[200:203], v[204:207], v[164:167], a[200:203]\n"
    "ds_read_b64_tr_b16 v[204:205], v10 offset:24576\n"
    "ds_read_b64_tr_b16 v[206:207], v10 offset:28672\n"
    "v_exp_f32 v155, v155\n"
    "v_mfma_f32_16x16x32_bf16 v[144:147], a[232:235], v[56:59], v[144:147]\n"
    "v_mfma_f32_16x16x32_bf16 a[204:207], v[208:211], v[164:167], a[204:207]\n"
    "ds_read_b64_tr_b16 v[208:209], v11 offset:24576\n"
    "ds_read_b64_tr_b16 v[210:211], v11 offset:28672\n"
    "v_exp_f32 v156, v156\n"
    "v_mfma_f32_16x16x32_bf16 v[148:151], a[248:251], v[56:59], v[148:151]\n"
    "v_cvt_pk_bf16_f32 v161, v154, v155\n"
    "v_mfma_f32_16x16x32_bf16 a[208:211], v[212:215], v[164:167], a[208:211]\n"
    "ds_read_b64_tr_b16 v[212:213], v12 offset:24576\n"
    "ds_read_b64_tr_b16 v[214:215], v12 offset:28672\n"
    "v_mfma_f32_16x16x32_bf16 v[144:147], a[236:239], v[60:63], v[144:147]\n"
    "v_exp_f32 v157, v157\n"
    "v_mfma_f32_16x16x32_bf16 a[212:215], v[216:219], v[164:167], a[212:215]\n"
    "ds_read_b64_tr_b16 v[216:217], v13 offset:24576\n"
    "ds_read_b64_tr_b16 v[218:219], v13 offset:28672\n"
    "v_exp_f32 v158, v158\n"
    "v_mfma_f32_16x16x32_bf16 v[148:151], a[252:255], v[60:63], v[148:151]\n"
    "v_mfma_f32_16x16x32_bf16 a[216:219], v[220:223], v[164:167], a[216:219]\n"
    "ds_read_b64_tr_b16 v[220:221], v14 offset:24576\n"
    "ds_read_b64_tr_b16 v[222:223], v14 offset:28672\n"
    "v_exp_f32 v159, v159\n"
    "v_mfma_f32_16x16x32_bf16 a[220:223], v[224:227], v[164:167], a[220:223]\n"
    "ds_read_b64_tr_b16 v[224:225], v15 offset:24576\n"
    "ds_read_b64_tr_b16 v[226:227], v15 offset:28672\n"
    "v_cvt_pk_bf16_f32 v162, v156, v157\n"
    "v_mfma_f32_16x16x32_bf16 v[192:195], v[228:231], v[164:167], v[192:195]\n"
    "v_cvt_pk_bf16_f32 v163, v158, v159\n"
    "v_mfma_f32_16x16x32_bf16 v[152:155], a[224:227], v[64:67], 0\n"
    "v_exp_f32 v144, v144\n"
    "s_mul_i32 s77, %[wid], 384\n"
    "s_mul_i32 s69, %[wid], 43008\n"
    "s_lshl_b32 s66, %[wid], 14\n"
    "v_mfma_f32_16x16x32_bf16 v[156:159], a[240:243], v[64:67], 0\n"
    "s_lshl_b32 s74, %[wid], 7\n"
    "s_add_u32 s74, s74, 512\n"
    "s_mul_i32 s76, %[wid], 14336\n"
    "v_exp_f32 v145, v145\n"
    "s_waitcnt lgkmcnt(14)\n"
    "v_mfma_f32_16x16x32_bf16 a[0:3], v[196:199], v[160:163], a[0:3]\n"
    "s_add_u32 s76, s76, 57344\n"
    "s_and_b32 s78, %[wid], 1\n"
    "s_lshl_b32 s78, s78, 14\n"
    "s_add_u32 s78, s78, 4096\n"
    "v_mfma_f32_16x16x32_bf16 v[152:155], a[228:231], v[68:71], v[152:155]\n"
    "s_cmp_ge_u32 %[wid], 2\n"
    "s_cselect_b32 s77, s74, s77\n"
    "s_cselect_b32 s69, s76, s69\n"
    "s_cselect_b32 s66, s78, s66\n"
    "v_exp_f32 v146, v146\n"
    "s_add_u32 s66, s66, %[lds]\n"
    "s_waitcnt lgkmcnt(12)\n"
    "v_mfma_f32_16x16x32_bf16 a[4:7], v[200:203], v[160:163], a[4:7]\n"
    "s_add_u32 s66, s66, 131072\n"
    "s_lshl_b32 s67, %[wid], 15\n"
    "s_add_u32 s67, s67, %[lds]\n"
    "s_add_u32 s67, s67, 28672\n"
    "v_cvt_pk_bf16_f32 v164, v144, v145\n"
    "v_mfma_f32_16x16x32_bf16 v[156:159], a[244:247], v[68:71], v[156:159]\n"
    "s_mov_b64 s[44:45], %[wop]\n"
    "s_add_u32 s44, s44, s77\n"
    "s_addc_u32 s45, s45, 0\n"
    "s_and_b32 s45, s45, 0xffff\n"
    "s_sub_u32 s46, 524288, s77\n"
    "s_waitcnt lgkmcnt(10)\n"
    "v_mfma_f32_16x16x32_bf16 a[8:11], v[204:207], v[160:163], a[8:11]\n"
    "s_mov_b32 s47, 0x20000\n"
    "s_mov_b64 s[40:41], %[pp]\n"
    "v_exp_f32 v147, v147\n"
    "s_and_b32 s41, s41, 0xffff\n"
    "v_mfma_f32_16x16x32_bf16 v[152:155], a[232:235], v[72:75], v[152:155]\n"
    "s_mov_b32 s42, 114688\n"
    "s_mov_b32 s43, 0x20000\n"
    "v_lshrrev_b32 v28, 4, v25\n"
    "v_lshlrev_b32 v28, 1, v28\n"
    "v_exp_f32 v148, v148\n"
    "s_waitcnt lgkmcnt(8)\n"
    "v_mfma_f32_16x16x32_bf16 a[12:15], v[208:211], v[160:163], a[12:15]\n"
    "v_and_b32 v29, 7, v25\n"
    "v_xor_b32 v28, v28, v29\n"
    "v_lshlrev_b32 v28, 4, v28\n"
    "v_lshrrev_b32 v29, 3, v25\n"
    "v_lshl_add_u32 v28, v29, 10, v28\n"
    "v_mfma_f32_16x16x32_bf16 v[156:159], a[248:251], v[72:75], v[156:159]\n"
    "v_cvt_pk_bf16_f32 v165, v146, v147\n"
    "v_add_u32 v16, 0, v28\n"
    "v_add_u32 v17, 7168, v28\n"
    "v_add_u32 v18, 14336, v28\n"
    "v_add_u32 v19, 21504, v28\n"
    "s_waitcnt lgkmcnt(6)\n"
    "v_mfma_f32_16x16x32_bf16 a[16:19], v[212:215], v[160:163], a[16:19]\n"
    "v_and_b32 v21, 15, v25\n"
    "v_lshrrev_b32 v22, 4, v25\n"
    "v_exp_f32 v149, v149\n"
    "v_lshrrev_b32 v23, 2, v21\n"
    "v_mfma_f32_16x16x32_bf16 v[152:155], a[236:239], v[76:79], v[152:155]\n"
    "v_and_b32 v26, 3, v21\n"
    "v_lshl_add_u32 v28, v22, 2, v23\n"
    "v_lshrrev_b32 v29, 1, v28\n"
    "v_and_b32 v29, 3, v29\n"
    "v_exp_f32 v150, v150\n"
    "s_waitcnt lgkmcnt(4)\n"
    "v_mfma_f32_16x16x32_bf16 a[20:23], v[216:219], v[160:163], a[20:23]\n"
    "v_lshlrev_b32 v29, 1, v29\n"
    "v_lshrrev_b32 v31, 1, v26\n"
    "v_xor_b32 v29, v29, v31\n"
    "v_lshlrev_b32 v29, 4, v29\n"
    "v_mfma_f32_16x16x32_bf16 v[156:159], a[252:255], v[76:79], v[156:159]\n"
    "v_lshl_add_u32 v29, v28, 7, v29\n"
    "v_exp_f32 v151, v151\n"
    "v_and_b32 v31, 1, v26\n"
    "v_lshl_add_u32 v29, v31, 3, v29\n"
    "s_waitcnt lgkmcnt(2)\n"
    "v_mfma_f32_16x16x32_bf16 a[24:27], v[220:223], v[160:163], a[24:27]\n"
    "v_xor_b32 v12, 0, v29\n"
    "v_add_u32 v8, s66, v12\n"
    "v_add_u32 v12, s67, v12\n"
    "v_xor_b32 v13, 32, v29\n"
    "v_cvt_pk_bf16_f32 v166, v148, v149\n"
    "v_add_u32 v9, s66, v13\n"
    "s_waitcnt lgkmcnt(0)\n"
    "v_mfma_f32_16x16x32_bf16 a[28:31], v[224:227], v[160:163], a[28:31]\n"
    "v_add_u32 v13, s67, v13\n"
    "v_xor_b32 v14, 64, v29\n"
    "v_add_u32 v10, s66, v14\n"
    "v_add_u32 v14, s67, v14\n"
    "v_cvt_pk_bf16_f32 v167, v150, v151\n"
    "v_mfma_f32_16x16x32_bf16 v[168:171], v[228:231], v[160:163], v[168:171]\n"
    "v_xor_b32 v15, 96, v29\n"
    "v_add_u32 v11, s66, v15\n"
    "v_add_u32 v15, s67, v15\n"
    "v_add_u32 v20, %[lds], v24\n"
    "s_mov_b32 s64, 0\n"
    "v_mfma_f32_16x16x32_bf16 v[144:147], a[224:227], v[80:83], 0\n"
    "v_exp_f32 v152, v152\n"
    "s_nop 0\n"
    "v_rcp_f32 v232, v168\n"
    "v_cmp_nle_f32 vcc, 0x2b800000, v168\n"
    "v_cmp_nge_f32 s[94:95], s93, v168\n"
    "v_mfma_f32_16x16x32_bf16 v[148:151], a[240:243], v[80:83], 0\n"
    "v_mov_b32 v233, v232\n"
    "v_accvgpr_read_b32 v240, a0\n"
    "v_accvgpr_read_b32 v241, a1\n"
    "v_exp_f32 v153, v153\n"
    "v_accvgpr_read_b32 v242, a2\n"
    "v_accvgpr_read_b32 v243, a3\n"
    "v_mfma_f32_16x16x32_bf16 a[32:35], v[196:199], v[164:167], a[32:35]\n"
    "v_accvgpr_read_b32 v244, a4\n"
    "v_accvgpr_read_b32 v245, a5\n"
    "v_accvgpr_read_b32 v246, a6\n"
    "v_accvgpr_read_b32 v247, a7\n"
    "v_exp_f32 v154, v154\n"
    "v_mfma_f32_16x16x32_bf16 v[144:147], a[228:231], v[84:87], v[144:147]\n"
    "v_accvgpr_read_b32 v248, a8\n"
    "v_accvgpr_read_b32 v249, a9\n"
    "v_accvgpr_read_b32 v250, a10\n"
    "v_accvgpr_read_b32 v251, a11\n"
    "v_accvgpr_read_b32 v252, a12\n"
    "v_accvgpr_read_b32 v253, a13\n"
    "v_mfma_f32_16x16x32_bf16 a[36:39], v[200:203], v[164:167], a[36:39]\n"
    "v_cvt_pk_bf16_f32 v160, v152, v153\n"
    "v_accvgpr_read_b32 v254, a14\n"
    "v_accvgpr_read_b32 v255, a15\n"
    "s_or_b64 s[82:83], s[82:83], vcc\n"
    "v_pk_mul_f32 v[240:241], v[240:241], v[232:233]\n"
    "v_mfma_f32_16x16x32_bf16 v[148:151], a[244:247], v[84:87], v[148:151]\n"
    "v_pk_mul_f32 v[242:243], v[242:243], v[232:233]\n"
    "v_pk_mul_f32 v[244:245], v[244:245], v[232:233]\n"
    "v_exp_f32 v155, v155\n"
    "v_mfma_f32_16x16x32_bf16 a[40:43], v[204:207], v[164:167], a[40:43]\n"
    "v_pk_mul_f32 v[246:247], v[246:247], v[232:233]\n"
    "v_pk_mul_f32 v[248:249], v[248:249], v[232:233]\n"
    "v_pk_mul_f32 v[250:251], v[250:251], v[232:233]\n"
    "v_pk_mul_f32 v[252:253], v[252:253], v[232:233]\n"
    "v_mfma_f32_16x16x32_bf16 v[144:147], a[232:235], v[88:91], v[144:147]\n"
    "v_pk_mul_f32 v[254:255], v[254:255], v[232:233]\n"
    "v_cvt_pk_bf16_f32 v32, v240, v241\n"
    "v_exp_f32 v156, v156\n"
    "v_mfma_f32_16x16x32_bf16 a[44:47], v[208:211], v[164:167], a[44:47]\n"
    "v_cvt_pk_bf16_f32 v33, v242, v243\n"
    "v_cvt_pk_bf16_f32 v34, v244, v245\n"
    "v_cvt_pk_bf16_f32 v35, v246, v247\n"
    "v_cvt_pk_bf16_f32 v36, v248, v249\n"
    "v_cvt_pk_bf16_f32 v37, v250, v251\n"
    "v_cvt_pk_bf16_f32 v161, v154, v155\n"
    "v_cvt_pk_bf16_f32 v38, v252, v253\n"
    "v_mfma_f32_16x16x32_bf16 v[148:151], a[248:251], v[88:91], v[148:151]\n"
    "v_cvt_pk_bf16_f32 v39, v254, v255\n"
    "v_accvgpr_read_b32 v240, a16\n"
    "v_accvgpr_read_b32 v241, a17\n"
    "v_accvgpr_read_b32 v242, a18\n"
    "v_accvgpr_read_b32 v243, a19\n"
    "v_mfma_f32_16x16x32_bf16 a[48:51], v[212:215], v[164:167], a[48:51]\n"
    "v_exp_f32 v157, v157\n"
    "v_accvgpr_read_b32 v244, a20\n"
    "v_accvgpr_read_b32 v245, a21\n"
    "v_accvgpr_read_b32 v246, a22\n"
    "v_accvgpr_read_b32 v247, a23\n"
    "v_accvgpr_read_b32 v248, a24\n"
    "v_mfma_f32_16x16x32_bf16 v[144:147], a[236:239], v[92:95], v[144:147]\n"
    "v_accvgpr_read_b32 v249, a25\n"
    "v_exp_f32 v158, v158\n"
    "v_accvgpr_read_b32 v250, a26\n"
    "v_accvgpr_read_b32 v251, a27\n"
    "v_accvgpr_read_b32 v252, a28\n"
    "v_accvgpr_read_b32 v253, a29\n"
    "v_mfma_f32_16x16x32_bf16 a[52:55], v[216:219], v[164:167], a[52:55]\n"
    "v_accvgpr_read_b32 v254, a30\n"
    "v_accvgpr_read_b32 v255, a31\n"
    "v_exp_f32 v159, v159\n"
    "s_or_b64 s[82:83], s[82:83], s[94:95]\n"
    "v_mfma_f32_16x16x32_bf16 v[148:151], a[252:255], v[92:95], v[148:151]\n"
    "v_pk_mul_f32 v[240:241], v[240:241], v[232:233]\n"
    "v_pk_mul_f32 v[242:243], v[242:243], v[232:233]\n"
    "v_pk_mul_f32 v[244:245], v[244:245], v[232:233]\n"
    "v_pk_mul_f32 v[246:247], v[246:247], v[232:233]\n"
    "v_mfma_f32_16x16x32_bf16 a[56:59], v[220:223], v[164:167], a[56:59]\n"
    "v_pk_mul_f32 v[248:249], v[248:249], v[232:233]\n"
    "v_cvt_pk_bf16_f32 v162, v156, v157\n"
    "v_pk_mul_f32 v[250:251], v[250:251], v[232:233]\n"
    "v_mfma_f32_16x16x32_bf16 a[60:63], v[224:227], v[164:167], a[60:63]\n"
    "v_pk_mul_f32 v[252:253], v[252:253], v[232:233]\n"
    "v_pk_mul_f32 v[254:255], v[254:255], v[232:233]\n"
    "v_cvt_pk_bf16_f32 v40, v240, v241\n"
    "v_cvt_pk_bf16_f32 v41, v242, v243\n"
    "v_cvt_pk_bf16_f32 v42, v244, v245\n"
    "v_mfma_f32_16x16x32_bf16 v[172:175], v[228:231], v[164:167], v[172:175]\n"
    "v_cvt_pk_bf16_f32 v163, v158, v159\n"
    "v_cvt_pk_bf16_f32 v43, v246, v247\n"
    "v_cvt_pk_bf16_f32 v44, v248, v249\n"
    "v_cvt_pk_bf16_f32 v45, v250, v251\n"
    "v_cvt_pk_bf16_f32 v46, v252, v253\n"
    "v_cvt_pk_bf16_f32 v47, v254, v255\n"
    "v_mfma_f32_16x16x32_bf16 v[152:155], a[224:227], v[96:99], 0\n"
    "v_exp_f32 v144, v144\n"
    "v_rcp_f32 v234, v172\n"
    "v_cmp_nle_f32 vcc, 0x2b800000, v172\n"
    "v_cmp_nge_f32 s[94:95], s93, v172\n"
    "v_mfma_f32_16x16x32_bf16 v[156:159], a[240:243], v[96:99], 0\n"
    "v_mov_b32 v235, v234\n"
    "v_accvgpr_read_b32 v240, a32\n"
    "v_accvgpr_read_b32 v241, a33\n"
    "v_exp_f32 v145, v145\n"
    "v_accvgpr_read_b32 v242, a34\n"
    "v_accvgpr_read_b32 v243, a35\n"
    "v_mfma_f32_16x16x32_bf16 a[64:67], v[196:199], v[160:163], a[64:67]\n"
    "v_accvgpr_read_b32 v244, a36\n"
    "v_accvgpr_read_b32 v245, a37\n"
    "v_accvgpr_read_b32 v246, a38\n"
    "v_accvgpr_read_b32 v247, a39\n"
    "v_exp_f32 v146, v146\n"
    "v_mfma_f32_16x16x32_bf16 v[152:155], a[228:231], v[100:103], v[152:155]\n"
    "v_accvgpr_read_b32 v248, a40\n"
    "v_accvgpr_read_b32 v249, a41\n"
    "v_accvgpr_read_b32 v250, a42\n"
    "v_accvgpr_read_b32 v251, a43\n"
    "v_accvgpr_read_b32 v252, a44\n"
    "v_accvgpr_read_b32 v253, a45\n"
    "v_mfma_f32_16x16x32_bf16 a[68:71], v[200:203], v[160:163], a[68:71]\n"
    "v_cvt_pk_bf16_f32 v164, v144, v145\n"
    "v_accvgpr_read_b32 v254, a46\n"
    "v_accvgpr_read_b32 v255, a47\n"
    "s_or_b64 s[82:83], s[82:83], vcc\n"
    "v_pk_mul_f32 v[240:241], v[240:241], v[234:235]\n"
    "v_mfma_f32_16x16x32_bf16 v[156:159], a[244:247], v[100:103], v[156:159]\n"
    "v_pk_mul_f32 v[242:243], v[242:243], v[234:235]\n"
    "v_pk_mul_f32 v[244:245], v[244:245], v[234:235]\n"
    "v_exp_f32 v147, v147\n"
    "v_mfma_f32_16x16x32_bf16 a[72:75], v[204:207], v[160:163], a[72:75]\n"
    "v_pk_mul_f32 v[246:247], v[246:247], v[234:235]\n"
    "v_pk_mul_f32 v[248:249], v[248:249], v[234:235]\n"
    "v_pk_mul_f32 v[250:251], v[250:251], v[234:235]\n"
    "v_pk_mul_f32 v[252:253], v[252:253], v[234:235]\n"
    "v_mfma_f32_16x16x32_bf16 v[152:155], a[232:235], v[104:107], v[152:155]\n"
    "v_pk_mul_f32 v[254:255], v[254:255], v[234:235]\n"
    "v_cvt_pk_bf16_f32 v48, v240, v241\n"
    "v_exp_f32 v148, v148\n"
    "v_mfma_f32_16x16x32_bf16 a[76:79], v[208:211], v[160:163], a[76:79]\n"
    "v_cvt_pk_bf16_f32 v49, v242, v243\n"
    "v_cvt_pk_bf16_f32 v50, v244, v245\n"
    "v_cvt_pk_bf16_f32 v51, v246, v247\n"
    "v_cvt_pk_bf16_f32 v52, v248, v249\n"
    "v_cvt_pk_bf16_f32 v53, v250, v251\n"
    "v_cvt_pk_bf16_f32 v165, v146, v147\n"
    "v_cvt_pk_bf16_f32 v54, v252, v253\n"
    "v_mfma_f32_16x16x32_bf16 v[156:159], a[248:251], v[104:107], v[156:159]\n"
    "v_cvt_pk_bf16_f32 v55, v254, v255\n"
    "v_accvgpr_read_b32 v240, a48\n"
    "v_accvgpr_read_b32 v241, a49\n"
    "v_accvgpr_read_b32 v242, a50\n"
    "v_accvgpr_read_b32 v243, a51\n"
    "v_mfma_f32_16x16x32_bf16 a[80:83], v[212:215], v[160:163], a[80:83]\n"
    "v_exp_f32 v149, v149\n"
    "v_accvgpr_read_b32 v244, a52\n"
    "v_accvgpr_read_b32 v245, a53\n"
    "v_accvgpr_read_b32 v246, a54\n"
    "v_accvgpr_read_b32 v247, a55\n"
    "v_accvgpr_read_b32 v248, a56\n"
    "v_mfma_f32_16x16x32_bf16 v[152:155], a[236:239], v[108:111], v[152:155]\n"
    "v_accvgpr_read_b32 v249, a57\n"
    "v_exp_f32 v150, v150\n"
    "v_accvgpr_read_b32 v250, a58\n"
    "v_accvgpr_read_b32 v251, a59\n"
    "v_accvgpr_read_b32 v252, a60\n"
    "v_accvgpr_read_b32 v253, a61\n"
    "v_mfma_f32_16x16x32_bf16 a[84:87], v[216:219], v[160:163], a[84:87]\n"
    "v_accvgpr_read_b32 v254, a62\n"
    "v_accvgpr_read_b32 v255, a63\n"
    "v_exp_f32 v151, v151\n"
    "s_or_b64 s[82:83], s[82:83], s[94:95]\n"
    "v_mfma_f32_16x16x32_bf16 v[156:159], a[252:255], v[108:111], v[156:159]\n"
    "v_pk_mul_f32 v[240:241], v[240:241], v[234:235]\n"
    "v_pk_mul_f32 v[242:243], v[242:243], v[234:235]\n"
    "v_pk_mul_f32 v[244:245], v[244:245], v[234:235]\n"
    "v_pk_mul_f32 v[246:247], v[246:247], v[234:235]\n"
    "v_mfma_f32_16x16x32_bf16 a[88:91], v[220:223], v[160:163], a[88:91]\n"
    "v_pk_mul_f32 v[248:249], v[248:249], v[234:235]\n"
    "v_cvt_pk_bf16_f32 v166, v148, v149\n"
    "v_pk_mul_f32 v[250:251], v[250:251], v[234:235]\n"
    "v_mfma_f32_16x16x32_bf16 a[92:95], v[224:227], v[160:163], a[92:95]\n"
    "v_pk_mul_f32 v[252:253], v[252:253], v[234:235]\n"
    "v_pk_mul_f32 v[254:255], v[254:255], v[234:235]\n"
    "v_cvt_pk_bf16_f32 v56, v240, v241\n"
    "v_cvt_pk_bf16_f32 v57, v242, v243\n"
    "v_cvt_pk_bf16_f32 v58, v244, v245\n"
    "v_mfma_f32_16x16x32_bf16 v[176:179], v[228:231], v[160:163], v[176:179]\n"
    "v_cvt_pk_bf16_f32 v167, v150, v151\n"
    "v_cvt_pk_bf16_f32 v59, v246, v247\n"
    "v_cvt_pk_bf16_f32 v60, v248, v249\n"
    "v_cvt_pk_bf16_f32 v61, v250, v251\n"
    "v_cvt_pk_bf16_f32 v62, v252, v253\n"
    "v_cvt_pk_bf16_f32 v63, v254, v255\n"
    "v_mfma_f32_16x16x32_bf16 v[144:147], a[224:227], v[112:115], 0\n"
    "v_exp_f32 v152, v152\n"
    "v_rcp_f32 v232, v176\n"
    "v_cmp_nle_f32 vcc, 0x2b800000, v176\n"
    "v_cmp_nge_f32 s[94:95], s93, v176\n"
    "v_mfma_f32_16x16x32_bf16 v[148:151], a[240:243], v[112:115], 0\n"
    "v_mov_b32 v233, v232\n"
    "v_accvgpr_read_b32 v240, a64\n"
    "v_accvgpr_read_b32 v241, a65\n"
    "v_exp_f32 v153, v153\n"
    "v_accvgpr_read_b32 v242, a66\n"
    "v_accvgpr_read_b32 v243, a67\n"
    "v_mfma_f32_16x16x32_bf16 a[96:99], v[196:199], v[164:167], a[96:99]\n"
    "v_accvgpr_read_b32 v244, a68\n"
    "v_accvgpr_read_b32 v245, a69\n"
    "v_accvgpr_read_b32 v246, a70\n"
    "v_accvgpr_read_b32 v247, a71\n"
    "v_exp_f32 v154, v154\n"
    "v_mfma_f32_16x16x32_bf16 v[144:147], a[228:231], v[116:119], v[144:147]\n"
    "v_accvgpr_read_b32 v248, a72\n"
    "v_accvgpr_read_b32 v249, a73\n"
    "v_accvgpr_read_b32 v250, a74\n"
    "v_accvgpr_read_b32 v251, a75\n"
    "v_accvgpr_read_b32 v252, a76\n"
    "v_accvgpr_read_b32 v253, a77\n"
    "v_mfma_f32_16x16x32_bf16 a[100:103], v[200:203], v[164:167], a[100:103]\n"
    "v_cvt_pk_bf16_f32 v160, v152, v153\n"
    "v_accvgpr_read_b32 v254, a78\n"
    "v_accvgpr_read_b32 v255, a79\n"
    "s_or_b64 s[82:83], s[82:83], vcc\n"
    "v_pk_mul_f32 v[240:241], v[240:241], v[232:233]\n"
    "v_mfma_f32_16x16x32_bf16 v[148:151], a[244:247], v[116:119], v[148:151]\n"
    "v_pk_mul_f32 v[242:243], v[242:243], v[232:233]\n"
    "v_pk_mul_f32 v[244:245], v[244:245], v[232:233]\n"
    "v_exp_f32 v155, v155\n"
    "v_mfma_f32_16x16x32_bf16 a[104:107], v[204:207], v[164:167], a[104:107]\n"
    "v_pk_mul_f32 v[246:247], v[246:247], v[232:233]\n"
    "v_pk_mul_f32 v[248:249], v[248:249], v[232:233]\n"
    "v_pk_mul_f32 v[250:251], v[250:251], v[232:233]\n"
    "v_pk_mul_f32 v[252:253], v[252:253], v[232:233]\n"
    "v_mfma_f32_16x16x32_bf16 v[144:147], a[232:235], v[120:123], v[144:147]\n"
    "v_pk_mul_f32 v[254:255], v[254:255], v[232:233]\n"
    "v_cvt_pk_bf16_f32 v64, v240, v241\n"
    "v_exp_f32 v156, v156\n"
    "v_mfma_f32_16x16x32_bf16 a[108:111], v[208:211], v[164:167], a[108:111]\n"
    "v_cvt_pk_bf16_f32 v65, v242, v243\n"
    "v_cvt_pk_bf16_f32 v66, v244, v245\n"
    "v_cvt_pk_bf16_f32 v67, v246, v247\n"
    "v_cvt_pk_bf16_f32 v68, v248, v249\n"
    "v_cvt_pk_bf16_f32 v69, v250, v251\n"
    "v_cvt_pk_bf16_f32 v161, v154, v155\n"
    "v_cvt_pk_bf16_f32 v70, v252, v253\n"
    "v_mfma_f32_16x16x32_bf16 v[148:151], a[248:251], v[120:123], v[148:151]\n"
    "v_cvt_pk_bf16_f32 v71, v254, v255\n"
    "v_accvgpr_read_b32 v240, a80\n"
    "v_accvgpr_read_b32 v241, a81\n"
    "v_accvgpr_read_b32 v242, a82\n"
    "v_accvgpr_read_b32 v243, a83\n"
    "v_mfma_f32_16x16x32_bf16 a[112:115], v[212:215], v[164:167], a[112:115]\n"
    "v_exp_f32 v157, v157\n"
    "v_accvgpr_read_b32 v244, a84\n"
    "v_accvgpr_read_b32 v245, a85\n"
    "v_accvgpr_read_b32 v246, a86\n"
    "v_accvgpr_read_b32 v247, a87\n"
    "v_accvgpr_read_b32 v248, a88\n"
    "v_mfma_f32_16x16x32_bf16 v[144:147], a[236:239], v[124:127], v[144:147]\n"
    "v_accvgpr_read_b32 v249, a89\n"
    "v_exp_f32 v158, v158\n"
    "v_accvgpr_read_b32 v250, a90\n"
    "v_accvgpr_read_b32 v251, a91\n"
    "v_accvgpr_read_b32 v252, a92\n"
    "v_accvgpr_read_b32 v253, a93\n"
    "v_mfma_f32_16x16x32_bf16 a[116:119], v[216:219], v[164:167], a[116:119]\n"
    "v_accvgpr_read_b32 v254, a94\n"
    "v_accvgpr_read_b32 v255, a95\n"
    "v_exp_f32 v159, v159\n"
    "s_or_b64 s[82:83], s[82:83], s[94:95]\n"
    "v_mfma_f32_16x16x32_bf16 v[148:151], a[252:255], v[124:127], v[148:151]\n"
    "v_pk_mul_f32 v[240:241], v[240:241], v[232:233]\n"
    "v_pk_mul_f32 v[242:243], v[242:243], v[232:233]\n"
    "v_pk_mul_f32 v[244:245], v[244:245], v[232:233]\n"
    "v_pk_mul_f32 v[246:247], v[246:247], v[232:233]\n"
    "v_mfma_f32_16x16x32_bf16 a[120:123], v[220:223], v[164:167], a[120:123]\n"
    "v_pk_mul_f32 v[248:249], v[248:249], v[232:233]\n"
    "v_cvt_pk_bf16_f32 v162, v156, v157\n"
    "v_pk_mul_f32 v[250:251], v[250:251], v[232:233]\n"
    "v_mfma_f32_16x16x32_bf16 a[124:127], v[224:227], v[164:167], a[124:127]\n"
    "v_pk_mul_f32 v[252:253], v[252:253], v[232:233]\n"
    "v_pk_mul_f32 v[254:255], v[254:255], v[232:233]\n"
    "v_cvt_pk_bf16_f32 v72, v240, v241\n"
    "v_cvt_pk_bf16_f32 v73, v242, v243\n"
    "v_cvt_pk_bf16_f32 v74, v244, v245\n"
    "v_mfma_f32_16x16x32_bf16 v[180:183], v[228:231], v[164:167], v[180:183]\n"
    "v_cvt_pk_bf16_f32 v163, v158, v159\n"
    "v_cvt_pk_bf16_f32 v75, v246, v247\n"
    "v_cvt_pk_bf16_f32 v76, v248, v249\n"
    "v_cvt_pk_bf16_f32 v77, v250, v251\n"
    "v_cvt_pk_bf16_f32 v78, v252, v253\n"
    "v_cvt_pk_bf16_f32 v79, v254, v255\n"
    "v_mfma_f32_16x16x32_bf16 v[152:155], a[224:227], v[128:131], 0\n"
    "v_exp_f32 v144, v144\n"
    "v_rcp_f32 v234, v180\n"
    "v_cmp_nle_f32 vcc, 0x2b800000, v180\n"
    "v_cmp_nge_f32 s[94:95], s93, v180\n"
    "v_mfma_f32_16x16x32_bf16 v[156:159], a[240:243], v[128:131], 0\n"
    "v_mov_b32 v235, v234\n"
    "v_accvgpr_read_b32 v240, a96\n"
    "v_accvgpr_read_b32 v241, a97\n"
    "v_exp_f32 v145, v145\n"
    "v_accvgpr_read_b32 v242, a98\n"
    "v_accvgpr_read_b32 v243, a99\n"
    "v_mfma_f32_16x16x32_bf16 a[128:131], v[196:199], v[160:163], a[128:131]\n"
    "v_accvgpr_read_b32 v244, a100\n"
    "v_accvgpr_read_b32 v245, a101\n"
    "v_accvgpr_read_b32 v246, a102\n"
    "v_accvgpr_read_b32 v247, a103\n"
    "v_exp_f32 v146, v146\n"
    "v_mfma_f32_16x16x32_bf16 v[152:155], a[228:231], v[132:135], v[152:155]\n"
    "v_accvgpr_read_b32 v248, a104\n"
    "v_accvgpr_read_b32 v249, a105\n"
    "v_accvgpr_read_b32 v250, a106\n"
    "v_accvgpr_read_b32 v251, a107\n"
    "v_accvgpr_read_b32 v252, a108\n"
    "v_accvgpr_read_b32 v253, a109\n"
    "v_mfma_f32_16x16x32_bf16 a[132:135], v[200:203], v[160:163], a[132:135]\n"
    "v_cvt_pk_bf16_f32 v164, v144, v145\n"
    "v_accvgpr_read_b32 v254, a110\n"
    "v_accvgpr_read_b32 v255, a111\n"
    "s_or_b64 s[82:83], s[82:83], vcc\n"
    "v_pk_mul_f32 v[240:241], v[240:241], v[234:235]\n"
    "v_mfma_f32_16x16x32_bf16 v[156:159], a[244:247], v[132:135], v[156:159]\n"
    "v_pk_mul_f32 v[242:243], v[242:243], v[234:235]\n"
    "v_pk_mul_f32 v[244:245], v[244:245], v[234:235]\n"
    "v_exp_f32 v147, v147\n"
    "v_mfma_f32_16x16x32_bf16 a[136:139], v[204:207], v[160:163], a[136:139]\n"
    "v_pk_mul_f32 v[246:247], v[246:247], v[234:235]\n"
    "v_pk_mul_f32 v[248:249], v[248:249], v[234:235]\n"
    "v_pk_mul_f32 v[250:251], v[250:251], v[234:235]\n"
    "v_pk_mul_f32 v[252:253], v[252:253], v[234:235]\n"
    "v_mfma_f32_16x16x32_bf16 v[152:155], a[232:235], v[136:139], v[152:155]\n"
    "v_pk_mul_f32 v[254:255], v[254:255], v[234:235]\n"
    "v_cvt_pk_bf16_f32 v80, v240, v241\n"
    "v_exp_f32 v148, v148\n"
    "v_mfma_f32_16x16x32_bf16 a[140:143], v[208:211], v[160:163], a[140:143]\n"
    "v_cvt_pk_bf16_f32 v81, v242, v243\n"
    "v_cvt_pk_bf16_f32 v82, v244, v245\n"
    "v_cvt_pk_bf16_f32 v83, v246, v247\n"
    "v_cvt_pk_bf16_f32 v84, v248, v249\n"
    "v_cvt_pk_bf16_f32 v85, v250, v251\n"
    "v_cvt_pk_bf16_f32 v165, v146, v147\n"
    "v_cvt_pk_bf16_f32 v86, v252, v253\n"
    "v_mfma_f32_16x16x32_bf16 v[156:159], a[248:251], v[136:139], v[156:159]\n"
    "v_cvt_pk_bf16_f32 v87, v254, v255\n"
    "v_accvgpr_read_b32 v240, a112\n"
    "v_accvgpr_read_b32 v241, a113\n"
    "v_accvgpr_read_b32 v242, a114\n"
    "v_accvgpr_read_b32 v243, a115\n"
    "v_mfma_f32_16x16x32_bf16 a[144:147], v[212:215], v[160:163], a[144:147]\n"
    "v_exp_f32 v149, v149\n"
    "v_accvgpr_read_b32 v244, a116\n"
    "v_accvgpr_read_b32 v245, a117\n"
    "v_accvgpr_read_b32 v246, a118\n"
    "v_accvgpr_read_b32 v247, a119\n"
    "v_accvgpr_read_b32 v248, a120\n"
    "v_mfma_f32_16x16x32_bf16 v[152:155], a[236:239], v[140:143], v[152:155]\n"
    "v_accvgpr_read_b32 v249, a121\n"
    "v_exp_f32 v150, v150\n"
    "v_accvgpr_read_b32 v250, a122\n"
    "v_accvgpr_read_b32 v251, a123\n"
    "v_accvgpr_read_b32 v252, a124\n"
    "v_accvgpr_read_b32 v253, a125\n"
    "v_mfma_f32_16x16x32_bf16 a[148:151], v[216:219], v[160:163], a[148:151]\n"
    "v_accvgpr_read_b32 v254, a126\n"
    "v_accvgpr_read_b32 v255, a127\n"
    "v_exp_f32 v151, v151\n"
    "s_or_b64 s[82:83], s[82:83], s[94:95]\n"
    "v_mfma_f32_16x16x32_bf16 v[156:159], a[252:255], v[140:143], v[156:159]\n"
    "v_pk_mul_f32 v[240:241], v[240:241], v[234:235]\n"
    "v_pk_mul_f32 v[242:243], v[242:243], v[234:235]\n"
    "v_pk_mul_f32 v[244:245], v[244:245], v[234:235]\n"
    "v_pk_mul_f32 v[246:247], v[246:247], v[234:235]\n"
    "v_mfma_f32_16x16x32_bf16 a[152:155], v[220:223], v[160:163], a[152:155]\n"
    "v_pk_mul_f32 v[248:249], v[248:249], v[234:235]\n"
    "v_cvt_pk_bf16_f32 v166, v148, v149\n"
    "v_pk_mul_f32 v[250:251], v[250:251], v[234:235]\n"
    "v_mfma_f32_16x16x32_bf16 a[156:159], v[224:227], v[160:163], a[156:159]\n"
    "v_pk_mul_f32 v[252:253], v[252:253], v[234:235]\n"
    "v_pk_mul_f32 v[254:255], v[254:255], v[234:235]\n"
    "v_cvt_pk_bf16_f32 v88, v240, v241\n"
    "v_cvt_pk_bf16_f32 v89, v242, v243\n"
    "v_cvt_pk_bf16_f32 v90, v244, v245\n"
    "v_mfma_f32_16x16x32_bf16 v[184:187], v[228:231], v[160:163], v[184:187]\n"
    "v_cvt_pk_bf16_f32 v167, v150, v151\n"
    "v_cvt_pk_bf16_f32 v91, v246, v247\n"
    "v_cvt_pk_bf16_f32 v92, v248, v249\n"
    "v_cvt_pk_bf16_f32 v93, v250, v251\n"
    "v_cvt_pk_bf16_f32 v94, v252, v253\n"
    "v_cvt_pk_bf16_f32 v95, v254, v255\n"
    "v_mfma_f32_16x16x32_bf16 a[160:163], v[196:199], v[164:167], a[160:163]\n"
    "v_exp_f32 v152, v152\n"
    "v_exp_f32 v153, v153\n"
    "v_exp_f32 v154, v154\n"
    "v_cvt_pk_bf16_f32 v160, v152, v153\n"
    "v_exp_f32 v155, v155\n"
    "v_exp_f32 v156, v156\n"
    "v_cvt_pk_bf16_f32 v161, v154, v155\n"
    "v_mfma_f32_16x16x32_bf16 a[164:167], v[200:203], v[164:167], a[164:167]\n"
    "v_exp_f32 v157, v157\n"
    "v_exp_f32 v158, v158\n"
    "v_exp_f32 v159, v159\n"
    "v_cvt_pk_bf16_f32 v162, v156, v157\n"
    "v_cvt_pk_bf16_f32 v163, v158, v159\n"
    "v_rcp_f32 v232, v184\n"
    "v_cmp_nle_f32 vcc, 0x2b800000, v184\n"
    "v_cmp_nge_f32 s[94:95], s93, v184\n"
    "v_mfma_f32_16x16x32_bf16 a[168:171], v[204:207], v[164:167], a[168:171]\n"
    "v_mov_b32 v233, v232\n"
    "v_accvgpr_read_b32 v240, a128\n"
    "v_accvgpr_read_b32 v241, a129\n"
    "v_accvgpr_read_b32 v242, a130\n"
    "v_accvgpr_read_b32 v243, a131\n"
    "v_accvgpr_read_b32 v244, a132\n"
    "v_accvgpr_read_b32 v245, a133\n"
    "v_accvgpr_read_b32 v246, a134\n"
    "v_accvgpr_read_b32 v247, a135\n"
    "v_accvgpr_read_b32 v248, a136\n"
    "v_accvgpr_read_b32 v249, a137\n"
    "v_accvgpr_read_b32 v250, a138\n"
    "v_mfma_f32_16x16x32_bf16 a[172:175], v[208:211], v[164:167], a[172:175]\n"
    "v_accvgpr_read_b32 v251, a139\n"
    "v_accvgpr_read_b32 v252, a140\n"
    "v_accvgpr_read_b32 v253, a141\n"
    "v_accvgpr_read_b32 v254, a142\n"
    "v_accvgpr_read_b32 v255, a143\n"
    "s_or_b64 s[82:83], s[82:83], vcc\n"
    "v_pk_mul_f32 v[240:241], v[240:241], v[232:233]\n"
    "v_pk_mul_f32 v[242:243], v[242:243], v[232:233]\n"
    "v_pk_mul_f32 v[244:245], v[244:245], v[232:233]\n"
    "v_mfma_f32_16x16x32_bf16 a[176:179], v[212:215], v[164:167], a[176:179]\n"
    "v_pk_mul_f32 v[246:247], v[246:247], v[232:233]\n"
    "v_pk_mul_f32 v[248:249], v[248:249], v[232:233]\n"
    "v_pk_mul_f32 v[250:251], v[250:251], v[232:233]\n"
    "v_pk_mul_f32 v[252:253], v[252:253], v[232:233]\n"
    "v_pk_mul_f32 v[254:255], v[254:255], v[232:233]\n"
    "v_cvt_pk_bf16_f32 v96, v240, v241\n"
    "v_mfma_f32_16x16x32_bf16 a[180:183], v[216:219], v[164:167], a[180:183]\n"
    "v_cvt_pk_bf16_f32 v97, v242, v243\n"
    "v_cvt_pk_bf16_f32 v98, v244, v245\n"
    "v_cvt_pk_bf16_f32 v99, v246, v247\n"
    "v_cvt_pk_bf16_f32 v100, v248, v249\n"
    "v_cvt_pk_bf16_f32 v101, v250, v251\n"
    "v_cvt_pk_bf16_f32 v102, v252, v253\n"
    "v_cvt_pk_bf16_f32 v103, v254, v255\n"
    "v_accvgpr_read_b32 v240, a144\n"
    "v_accvgpr_read_b32 v241, a145\n"
    "v_accvgpr_read_b32 v242, a146\n"
    "v_accvgpr_read_b32 v243, a147\n"
    "v_accvgpr_read_b32 v244, a148\n"
    "v_mfma_f32_16x16x32_bf16 a[184:187], v[220:223], v[164:167], a[184:187]\n"
    "v_accvgpr_read_b32 v245, a149\n"
    "v_accvgpr_read_b32 v246, a150\n"
    "v_accvgpr_read_b32 v247, a151\n"
    "v_accvgpr_read_b32 v248, a152\n"
    "v_accvgpr_read_b32 v249, a153\n"
    "v_accvgpr_read_b32 v250, a154\n"
    "v_accvgpr_read_b32 v251, a155\n"
    "v_accvgpr_read_b32 v252, a156\n"
    "v_accvgpr_read_b32 v253, a157\n"
    "v_accvgpr_read_b32 v254, a158\n"
    "v_accvgpr_read_b32 v255, a159\n"
    "s_or_b64 s[82:83], s[82:83], s[94:95]\n"
    "v_mfma_f32_16x16x32_bf16 a[188:191], v[224:227], v[164:167], a[188:191]\n"
    "v_pk_mul_f32 v[240:241], v[240:241], v[232:233]\n"
    "v_pk_mul_f32 v[242:243], v[242:243], v[232:233]\n"
    "v_pk_mul_f32 v[244:245], v[244:245], v[232:233]\n"
    "v_pk_mul_f32 v[246:247], v[246:247], v[232:233]\n"
    "v_pk_mul_f32 v[248:249], v[248:249], v[232:233]\n"
    "v_pk_mul_f32 v[250:251], v[250:251], v[232:233]\n"
    "v_mfma_f32_16x16x32_bf16 v[188:191], v[228:231], v[164:167], v[188:191]\n"
    "v_pk_mul_f32 v[252:253], v[252:253], v[232:233]\n"
    "v_pk_mul_f32 v[254:255], v[254:255], v[232:233]\n"
    "v_cvt_pk_bf16_f32 v104, v240, v241\n"
    "v_cvt_pk_bf16_f32 v105, v242, v243\n"
    "v_cvt_pk_bf16_f32 v106, v244, v245\n"
    "v_cvt_pk_bf16_f32 v107, v246, v247\n"
    "v_cvt_pk_bf16_f32 v108, v248, v249\n"
    "v_cvt_pk_bf16_f32 v109, v250, v251\n"
    "v_cvt_pk_bf16_f32 v110, v252, v253\n"
    "v_cvt_pk_bf16_f32 v111, v254, v255\n"
    "v_mfma_f32_16x16x32_bf16 a[192:195], v[196:199], v[160:163], a[192:195]\n"
    "v_rcp_f32 v234, v188\n"
    "v_cmp_nle_f32 vcc, 0x2b800000, v188\n"
    "v_cmp_nge_f32 s[94:95], s93, v188\n"
    "v_mov_b32 v235, v234\n"
    "v_accvgpr_read_b32 v240, a160\n"
    "v_accvgpr_read_b32 v241, a161\n"
    "v_accvgpr_read_b32 v242, a162\n"
    "v_accvgpr_read_b32 v243, a163\n"
    "v_accvgpr_read_b32 v244, a164\n"
    "v_mfma_f32_16x16x32_bf16 a[196:199], v[200:203], v[160:163], a[196:199]\n"
    "v_accvgpr_read_b32 v245, a165\n"
    "v_accvgpr_read_b32 v246, a166\n"
    "v_accvgpr_read_b32 v247, a167\n"
    "v_accvgpr_read_b32 v248, a168\n"
    "v_accvgpr_read_b32 v249, a169\n"
    "v_accvgpr_read_b32 v250, a170\n"
    "v_accvgpr_read_b32 v251, a171\n"
    "v_accvgpr_read_b32 v252, a172\n"
    "v_accvgpr_read_b32 v253, a173\n"
    "v_mfma_f32_16x16x32_bf16 a[200:203], v[204:207], v[160:163], a[200:203]\n"
    "v_accvgpr_read_b32 v254, a174\n"
    "v_accvgpr_read_b32 v255, a175\n"
    "s_or_b64 s[82:83], s[82:83], vcc\n"
    "v_pk_mul_f32 v[240:241], v[240:241], v[234:235]\n"
    "v_pk_mul_f32 v[242:243], v[242:243], v[234:235]\n"
    "v_pk_mul_f32 v[244:245], v[244:245], v[234:235]\n"
    "v_pk_mul_f32 v[246:247], v[246:247], v[234:235]\n"
    "v_mfma_f32_16x16x32_bf16 a[204:207], v[208:211], v[160:163], a[204:207]\n"
    "v_pk_mul_f32 v[248:249], v[248:249], v[234:235]\n"
    "v_pk_mul_f32 v[250:251], v[250:251], v[234:235]\n"
    "v_pk_mul_f32 v[252:253], v[252:253], v[234:235]\n"
    "v_pk_mul_f32 v[254:255], v[254:255], v[234:235]\n"
    "v_cvt_pk_bf16_f32 v112, v240, v241\n"
    "v_mfma_f32_16x16x32_bf16 a[208:211], v[212:215], v[160:163], a[208:211]\n"
    "v_cvt_pk_bf16_f32 v113, v242, v243\n"
    "v_cvt_pk_bf16_f32 v114, v244, v245\n"
    "v_cvt_pk_bf16_f32 v115, v246, v247\n"
    "v_cvt_pk_bf16_f32 v116, v248, v249\n"
    "v_cvt_pk_bf16_f32 v117, v250, v251\n"
    "v_cvt_pk_bf16_f32 v118, v252, v253\n"
    "v_cvt_pk_bf16_f32 v119, v254, v255\n"
    "v_accvgpr_read_b32 v240, a176\n"
    "v_accvgpr_read_b32 v241, a177\n"
    "v_mfma_f32_16x16x32_bf16 a[212:215], v[216:219], v[160:163], a[212:215]\n"
    "v_accvgpr_read_b32 v242, a178\n"
    "v_accvgpr_read_b32 v243, a179\n"
    "v_accvgpr_read_b32 v244, a180\n"
    "v_accvgpr_read_b32 v245, a181\n"
    "v_accvgpr_read_b32 v246, a182\n"
    "v_accvgpr_read_b32 v247, a183\n"
    "v_accvgpr_read_b32 v248, a184\n"
    "v_accvgpr_read_b32 v249, a185\n"
    "v_accvgpr_read_b32 v250, a186\n"
    "v_accvgpr_read_b32 v251, a187\n"
    "v_mfma_f32_16x16x32_bf16 a[216:219], v[220:223], v[160:163], a[216:219]\n"
    "v_accvgpr_read_b32 v252, a188\n"
    "v_accvgpr_read_b32 v253, a189\n"
    "v_accvgpr_read_b32 v254, a190\n"
    "v_accvgpr_read_b32 v255, a191\n"
    "s_or_b64 s[82:83], s[82:83], s[94:95]\n"
    "v_pk_mul_f32 v[240:241], v[240:241], v[234:235]\n"
    "v_pk_mul_f32 v[242:243], v[242:243], v[234:235]\n"
    "v_mfma_f32_16x16x32_bf16 a[220:223], v[224:227], v[160:163], a[220:223]\n"
    "v_pk_mul_f32 v[244:245], v[244:245], v[234:235]\n"
    "v_pk_mul_f32 v[246:247], v[246:247], v[234:235]\n"
    "v_pk_mul_f32 v[248:249], v[248:249], v[234:235]\n"
    "v_pk_mul_f32 v[250:251], v[250:251], v[234:235]\n"
    "v_pk_mul_f32 v[252:253], v[252:253], v[234:235]\n"
    "v_mfma_f32_16x16x32_bf16 v[192:195], v[228:231], v[160:163], v[192:195]\n"
    "v_pk_mul_f32 v[254:255], v[254:255], v[234:235]\n"
    "v_cvt_pk_bf16_f32 v120, v240, v241\n"
    "v_cvt_pk_bf16_f32 v121, v242, v243\n"
    "v_cvt_pk_bf16_f32 v122, v244, v245\n"
    "v_cvt_pk_bf16_f32 v123, v246, v247\n"
    "v_cvt_pk_bf16_f32 v124, v248, v249\n"
    "v_cvt_pk_bf16_f32 v125, v250, v251\n"
    "v_cvt_pk_bf16_f32 v126, v252, v253\n"
    "v_cvt_pk_bf16_f32 v127, v254, v255\n"
    "v_rcp_f32 v232, v192\n"
    "v_cmp_nle_f32 vcc, 0x2b800000, v192\n"
    "v_cmp_nge_f32 s[94:95], s93, v192\n"
    "v_mov_b32 v233, v232\n"
    "v_accvgpr_read_b32 v240, a192\n"
    "v_accvgpr_read_b32 v241, a193\n"
    "v_accvgpr_read_b32 v242, a194\n"
    "v_accvgpr_read_b32 v243, a195\n"
    "v_accvgpr_read_b32 v244, a196\n"
    "v_accvgpr_read_b32 v245, a197\n"
    "v_accvgpr_read_b32 v246, a198\n"
    "v_accvgpr_read_b32 v247, a199\n"
    "v_accvgpr_read_b32 v248, a200\n"
    "v_accvgpr_read_b32 v249, a201\n"
    "v_accvgpr_read_b32 v250, a202\n"
    "v_accvgpr_read_b32 v251, a203\n"
    "v_accvgpr_read_b32 v252, a204\n"
    "v_accvgpr_read_b32 v253, a205\n"
    "v_accvgpr_read_b32 v254, a206\n"
    "v_accvgpr_read_b32 v255, a207\n"
    "s_or_b64 s[82:83], s[82:83], vcc\n"
    "v_pk_mul_f32 v[240:241], v[240:241], v[232:233]\n"
    "v_pk_mul_f32 v[242:243], v[242:243], v[232:233]\n"
    "v_pk_mul_f32 v[244:245], v[244:245], v[232:233]\n"
    "v_pk_mul_f32 v[246:247], v[246:247], v[232:233]\n"
    "v_pk_mul_f32 v[248:249], v[248:249], v[232:233]\n"
    "v_pk_mul_f32 v[250:251], v[250:251], v[232:233]\n"
    "v_pk_mul_f32 v[252:253], v[252:253], v[232:233]\n"
    "v_pk_mul_f32 v[254:255], v[254:255], v[232:233]\n"
    "v_cvt_pk_bf16_f32 v128, v240, v241\n"
    "v_cvt_pk_bf16_f32 v129, v242, v243\n"
    "v_cvt_pk_bf16_f32 v130, v244, v245\n"
    "v_cvt_pk_bf16_f32 v131, v246, v247\n"
    "v_cvt_pk_bf16_f32 v132, v248, v249\n"
    "v_cvt_pk_bf16_f32 v133, v250, v251\n"
    "v_cvt_pk_bf16_f32 v134, v252, v253\n"
    "v_cvt_pk_bf16_f32 v135, v254, v255\n"
    "v_accvgpr_read_b32 v240, a208\n"
    "v_accvgpr_read_b32 v241, a209\n"
    "v_accvgpr_read_b32 v242, a210\n"
    "v_accvgpr_read_b32 v243, a211\n"
    "v_accvgpr_read_b32 v244, a212\n"
    "v_accvgpr_read_b32 v245, a213\n"
    "v_accvgpr_read_b32 v246, a214\n"
    "v_accvgpr_read_b32 v247, a215\n"
    "v_accvgpr_read_b32 v248, a216\n"
    "v_accvgpr_read_b32 v249, a217\n"
    "v_accvgpr_read_b32 v250, a218\n"
    "v_accvgpr_read_b32 v251, a219\n"
    "v_accvgpr_read_b32 v252, a220\n"
    "v_accvgpr_read_b32 v253, a221\n"
    "v_accvgpr_read_b32 v254, a222\n"
    "v_accvgpr_read_b32 v255, a223\n"
    "s_or_b64 s[82:83], s[82:83], s[94:95]\n"
    "v_pk_mul_f32 v[240:241], v[240:241], v[232:233]\n"
    "v_pk_mul_f32 v[242:243], v[242:243], v[232:233]\n"
    "v_pk_mul_f32 v[244:245], v[244:245], v[232:233]\n"
    "v_pk_mul_f32 v[246:247], v[246:247], v[232:233]\n"
    "v_pk_mul_f32 v[248:249], v[248:249], v[232:233]\n"
    "v_pk_mul_f32 v[250:251], v[250:251], v[232:233]\n"
    "v_pk_mul_f32 v[252:253], v[252:253], v[232:233]\n"
    "v_pk_mul_f32 v[254:255], v[254:255], v[232:233]\n"
    "v_cvt_pk_bf16_f32 v136, v240, v241\n"
    "v_cvt_pk_bf16_f32 v137, v242, v243\n"
    "v_cvt_pk_bf16_f32 v138, v244, v245\n"
    "v_cvt_pk_bf16_f32 v139, v246, v247\n"
    "v_cvt_pk_bf16_f32 v140, v248, v249\n"
    "v_cvt_pk_bf16_f32 v141, v250, v251\n"
    "v_cvt_pk_bf16_f32 v142, v252, v253\n"
    "v_cvt_pk_bf16_f32 v143, v254, v255\n"
    "s_cmp_lg_u64 s[82:83], 0\n"
    "s_cselect_b32 s75, 1, 0\n"
    "s_lshl_b32 s74, %[wid], 15\n"
    "s_add_u32 s74, s74, %[lds]\n"
    "v_add_u32 v28, s74, v24\n"
    "ds_write_b128 v28, v[32:35] offset:0\n"
    "ds_write_b128 v28, v[36:39] offset:1024\n"
    "ds_write_b128 v28, v[40:43] offset:2048\n"
    "ds_write_b128 v28, v[44:47] offset:3072\n"
    "ds_write_b128 v28, v[48:51] offset:4096\n"
    "ds_write_b128 v28, v[52:55] offset:5120\n"
    "ds_write_b128 v28, v[56:59] offset:6144\n"
    "ds_write_b128 v28, v[60:63] offset:7168\n"
    "ds_write_b128 v28, v[64:67] offset:8192\n"
    "ds_write_b128 v28, v[68:71] offset:9216\n"
    "ds_write_b128 v28, v[72:75] offset:10240\n"
    "ds_write_b128 v28, v[76:79] offset:11264\n"
    "ds_write_b128 v28, v[80:83] offset:12288\n"
    "ds_write_b128 v28, v[84:87] offset:13312\n"
    "ds_write_b128 v28, v[88:91] offset:14336\n"
    "ds_write_b128 v28, v[92:95] offset:15360\n"
    "ds_write_b128 v28, v[96:99] offset:16384\n"
    "ds_write_b128 v28, v[100:103] offset:17408\n"
    "ds_write_b128 v28, v[104:107] offset:18432\n"
    "ds_write_b128 v28, v[108:111] offset:19456\n"
    "ds_write_b128 v28, v[112:115] offset:20480\n"
    "ds_write_b128 v28, v[116:119] offset:21504\n"
    "ds_write_b128 v28, v[120:123] offset:22528\n"
    "ds_write_b128 v28, v[124:127] offset:23552\n"
    "ds_write_b128 v28, v[128:131] offset:24576\n"
    "ds_write_b128 v28, v[132:135] offset:25600\n"
    "ds_write_b128 v28, v[136:139] offset:26624\n"
    "ds_write_b128 v28, v[140:143] offset:27648\n"
    "s_waitcnt lgkmcnt(0)\n"
    "s_cmp_ge_u32 %[wid], 2\n"
    "s_cbranch_scc1 .LX_late_%=\n"
    "v_mov_b32 v6, 1\n"
    "s_lshl_b32 s74, %[wid], 2\n"
    "s_add_u32 s74, s74, %[lds]\n"
    "s_add_u32 s74, s74, 146432\n"
    "v_mov_b32 v7, s74\n"
    "ds_write_b32 v7, v6\n"
    "v_mov_b32 v7, %[lds]\n"
    "v_add_u32 v7, 146432, v7\n"
    "s_mov_b32 s70, 0\n"
    "s_waitcnt lgkmcnt(0)\n"
    "v_mov_b64 v[32:33], 0\n"
    "v_mov_b64 v[34:35], 0\n"
    "v_mov_b64 v[36:37], 0\n"
    "v_mov_b64 v[38:39], 0\n"
    "v_mov_b64 v[40:41], 0\n"
    "v_mov_b64 v[42:43], 0\n"
    "v_mov_b64 v[44:45], 0\n"
    "v_mov_b64 v[46:47], 0\n"
    "v_mov_b64 v[48:49], 0\n"
    "v_mov_b64 v[50:51], 0\n"
    "v_mov_b64 v[52:53], 0\n"
    "v_mov_b64 v[54:55], 0\n"
    "v_mov_b64 v[56:57], 0\n"
    "v_mov_b64 v[58:59], 0\n"
    "v_mov_b64 v[60:61], 0\n"
    "v_mov_b64 v[62:63], 0\n"
    "v_mov_b64 v[64:65], 0\n"
    "v_mov_b64 v[66:67], 0\n"
    "v_mov_b64 v[68:69], 0\n"
    "v_mov_b64 v[70:71], 0\n"
    "v_mov_b64 v[72:73], 0\n"
    "v_mov_b64 v[74:75], 0\n"
    "v_mov_b64 v[76:77], 0\n"
    "v_mov_b64 v[78:79], 0\n"
    "v_mov_b64 v[80:81], 0\n"
    "v_mov_b64 v[82:83], 0\n"
    "v_mov_b64 v[84:85], 0\n"
    "v_mov_b64 v[86:87], 0\n"
    "v_mov_b64 v[88:89], 0\n"
    "v_mov_b64 v[90:91], 0\n"
    "v_mov_b64 v[92:93], 0\n"
    "v_mov_b64 v[94:95], 0\n"
    "v_mov_b64 v[96:97], 0\n"
    "v_mov_b64 v[98:99], 0\n"
    "v_mov_b64 v[100:101], 0\n"
    "v_mov_b64 v[102:103], 0\n"
    "v_mov_b64 v[104:105], 0\n"
    "v_mov_b64 v[106:107], 0\n"
    "v_mov_b64 v[108:109], 0\n"
    "v_mov_b64 v[110:111], 0\n"
    "v_mov_b64 v[112:113], 0\n"
    "v_mov_b64 v[114:115], 0\n"
    "v_mov_b64 v[116:117], 0\n"
    "v_mov_b64 v[118:119], 0\n"
    "v_mov_b64 v[120:121], 0\n"
    "v_mov_b64 v[122:123], 0\n"
    "v_mov_b64 v[124:125], 0\n"
    "v_mov_b64 v[126:127], 0\n"
    "v_mov_b64 v[128:129], 0\n"
    "v_mov_b64 v[130:131], 0\n"
    "v_mov_b64 v[132:133], 0\n"
    "v_mov_b64 v[134:135], 0\n"
    "v_mov_b64 v[136:137], 0\n"
    "v_mov_b64 v[138:139], 0\n"
    "v_mov_b64 v[140:141], 0\n"
    "v_mov_b64 v[142:143], 0\n"
    "v_mov_b64 v[144:145], 0\n"
    "v_mov_b64 v[146:147], 0\n"
    "v_mov_b64 v[148:149], 0\n"
    "v_mov_b64 v[150:151], 0\n"
    "v_mov_b64 v[152:153], 0\n"
    "v_mov_b64 v[154:155], 0\n"
    "v_mov_b64 v[156:157], 0\n"
    "v_mov_b64 v[158:159], 0\n"
    "v_mov_b64 v[160:161], 0\n"
    "v_mov_b64 v[162:163], 0\n"
    "v_mov_b64 v[164:165], 0\n"
    "v_mov_b64 v[166:167], 0\n"
    "v_mov_b64 v[168:169], 0\n"
    "v_mov_b64 v[170:171], 0\n"
    "v_mov_b64 v[172:173], 0\n"
    "v_mov_b64 v[174:175], 0\n"
    "v_mov_b64 v[176:177], 0\n"
    "v_mov_b64 v[178:179], 0\n"
    "v_mov_b64 v[180:181], 0\n"
    "v_mov_b64 v[182:183], 0\n"
    "v_mov_b64 v[184:185], 0\n"
    "v_mov_b64 v[186:187], 0\n"
    "v_mov_b64 v[188:189], 0\n"
    "v_mov_b64 v[190:191], 0\n"
    "v_mov_b64 v[192:193], 0\n"
    "v_mov_b64 v[194:195], 0\n"
    "v_mov_b64 v[196:197], 0\n"
    "v_mov_b64 v[198:199], 0\n"
    "v_mov_b64 v[200:201], 0\n"
    "v_mov_b64 v[202:203], 0\n"
    "v_mov_b64 v[204:205], 0\n"
    "v_mov_b64 v[206:207], 0\n"
    "v_mov_b64 v[208:209], 0\n"
    "v_mov_b64 v[210:211], 0\n"
    "v_mov_b64 v[212:213], 0\n"
    "v_mov_b64 v[214:215], 0\n"
    "v_mov_b64 v[216:217], 0\n"
    "v_mov_b64 v[218:219], 0\n"
    "v_mov_b64 v[220:221], 0\n"
    "v_mov_b64 v[222:223], 0\n"
    "v_mov_b64 v[224:225], 0\n"
    "v_mov_b64 v[226:227], 0\n"
    "v_mov_b64 v[228:229], 0\n"
    "v_mov_b64 v[230:231], 0\n"
    "v_mov_b64 v[232:233], 0\n"
    "v_mov_b64 v[234:235], 0\n"
    "v_mov_b64 v[236:237], 0\n"
    "v_mov_b64 v[238:239], 0\n"
    "v_mov_b64 v[240:241], 0\n"
    "v_mov_b64 v[242:243], 0\n"
    "v_mov_b64 v[244:245], 0\n"
    "v_mov_b64 v[246:247], 0\n"
    "v_mov_b64 v[248:249], 0\n"
    "v_mov_b64 v[250:251], 0\n"
    "v_mov_b64 v[252:253], 0\n"
    "v_mov_b64 v[254:255], 0\n"
    "v_accvgpr_write_b32 a0, 0\n"
    "v_accvgpr_write_b32 a1, 0\n"
    "v_accvgpr_write_b32 a2, 0\n"
    "v_accvgpr_write_b32 a3, 0\n"
    "v_accvgpr_write_b32 a4, 0\n"
    "v_accvgpr_write_b32 a5, 0\n"
    "v_accvgpr_write_b32 a6, 0\n"
    "v_accvgpr_write_b32 a7, 0\n"
    "v_accvgpr_write_b32 a8, 0\n"
    "v_accvgpr_write_b32 a9, 0\n"
    "v_accvgpr_write_b32 a10, 0\n"
    "v_accvgpr_write_b32 a11, 0\n"
    "v_accvgpr_write_b32 a12, 0\n"
    "v_accvgpr_write_b32 a13, 0\n"
    "v_accvgpr_write_b32 a14, 0\n"
    "v_accvgpr_write_b32 a15, 0\n"
    "v_accvgpr_write_b32 a16, 0\n"
    "v_accvgpr_write_b32 a17, 0\n"
    "v_accvgpr_write_b32 a18, 0\n"
    "v_accvgpr_write_b32 a19, 0\n"
    "v_accvgpr_write_b32 a20, 0\n"
    "v_accvgpr_write_b32 a21, 0\n"
    "v_accvgpr_write_b32 a22, 0\n"
    "v_accvgpr_write_b32 a23, 0\n"
    "v_accvgpr_write_b32 a24, 0\n"
    "v_accvgpr_write_b32 a25, 0\n"
    "v_accvgpr_write_b32 a26, 0\n"
    "v_accvgpr_write_b32 a27, 0\n"
    "v_accvgpr_write_b32 a28, 0\n"
    "v_accvgpr_write_b32 a29, 0\n"
    "v_accvgpr_write_b32 a30, 0\n"
    "v_accvgpr_write_b32 a31, 0\n"
    "v_accvgpr_write_b32 a32, 0\n"
    "v_accvgpr_write_b32 a33, 0\n"
    "v_accvgpr_write_b32 a34, 0\n"
    "v_accvgpr_write_b32 a35, 0\n"
    "v_accvgpr_write_b32 a36, 0\n"
    "v_accvgpr_write_b32 a37, 0\n"
    "v_accvgpr_write_b32 a38, 0\n"
    "v_accvgpr_write_b32 a39, 0\n"
    "v_accvgpr_write_b32 a40, 0\n"
    "v_accvgpr_write_b32 a41, 0\n"
    "v_accvgpr_write_b32 a42, 0\n"
    "v_accvgpr_write_b32 a43, 0\n"
    "v_accvgpr_write_b32 a44, 0\n"
    "v_accvgpr_write_b32 a45, 0\n"
    "v_accvgpr_write_b32 a46, 0\n"
    "v_accvgpr_write_b32 a47, 0\n"
    "v_accvgpr_write_b32 a48, 0\n"
    "v_accvgpr_write_b32 a49, 0\n"
    "v_accvgpr_write_b32 a50, 0\n"
    "v_accvgpr_write_b32 a51, 0\n"
    "v_accvgpr_write_b32 a52, 0\n"
    "v_accvgpr_write_b32 a53, 0\n"
    "v_accvgpr_write_b32 a54, 0\n"
    "v_accvgpr_write_b32 a55, 0\n"
    "v_accvgpr_write_b32 a56, 0\n"
    "v_accvgpr_write_b32 a57, 0\n"
    "v_accvgpr_write_b32 a58, 0\n"
    "v_accvgpr_write_b32 a59, 0\n"
    "v_accvgpr_write_b32 a60, 0\n"
    "v_accvgpr_write_b32 a61, 0\n"
    "v_accvgpr_write_b32 a62, 0\n"
    "v_accvgpr_write_b32 a63, 0\n"
    "v_accvgpr_write_b32 a64, 0\n"
    "v_accvgpr_write_b32 a65, 0\n"
    "v_accvgpr_write_b32 a66, 0\n"
    "v_accvgpr_write_b32 a67, 0\n"
    "v_accvgpr_write_b32 a68, 0\n"
    "v_accvgpr_write_b32 a69, 0\n"
    "v_accvgpr_write_b32 a70, 0\n"
    "v_accvgpr_write_b32 a71, 0\n"
    "v_accvgpr_write_b32 a72, 0\n"
    "v_accvgpr_write_b32 a73, 0\n"
    "v_accvgpr_write_b32 a74, 0\n"
    "v_accvgpr_write_b32 a75, 0\n"
    "v_accvgpr_write_b32 a76, 0\n"
    "v_accvgpr_write_b32 a77, 0\n"
    "v_accvgpr_write_b32 a78, 0\n"
    "v_accvgpr_write_b32 a79, 0\n"
    "v_accvgpr_write_b32 a80, 0\n"
    "v_accvgpr_write_b32 a81, 0\n"
    "v_accvgpr_write_b32 a82, 0\n"
    "v_accvgpr_write_b32 a83, 0\n"
    "v_accvgpr_write_b32 a84, 0\n"
    "v_accvgpr_write_b32 a85, 0\n"
    "v_accvgpr_write_b32 a86, 0\n"
    "v_accvgpr_write_b32 a87, 0\n"
    "v_accvgpr_write_b32 a88, 0\n"
    "v_accvgpr_write_b32 a89, 0\n"
    "v_accvgpr_write_b32 a90, 0\n"
    "v_accvgpr_write_b32 a91, 0\n"
    "v_accvgpr_write_b32 a92, 0\n"
    "v_accvgpr_write_b32 a93, 0\n"
    "v_accvgpr_write_b32 a94, 0\n"
    "v_accvgpr_write_b32 a95, 0\n"
    "v_accvgpr_write_b32 a96, 0\n"
    "v_accvgpr_write_b32 a97, 0\n"
    "v_accvgpr_write_b32 a98, 0\n"
    "v_accvgpr_write_b32 a99, 0\n"
    "v_accvgpr_write_b32 a100, 0\n"
    "v_accvgpr_write_b32 a101, 0\n"
    "v_accvgpr_write_b32 a102, 0\n"
    "v_accvgpr_write_b32 a103, 0\n"
    "v_accvgpr_write_b32 a104, 0\n"
    "v_accvgpr_write_b32 a105, 0\n"
    "v_accvgpr_write_b32 a106, 0\n"
    "v_accvgpr_write_b32 a107, 0\n"
    "v_accvgpr_write_b32 a108, 0\n"
    "v_accvgpr_write_b32 a109, 0\n"
    "v_accvgpr_write_b32 a110, 0\n"
    "v_accvgpr_write_b32 a111, 0\n"
    ".LX_spin_%=:\n"
    "ds_read_b128 v[28:31], v7\n"
    "s_waitcnt lgkmcnt(0)\n"
    "v_and_b32 v28, v28, v29\n"
    "v_and_b32 v30, v30, v31\n"
    "v_and_b32 v28, v28, v30\n"
    "v_cmp_eq_u32 vcc, 1, v28\n"
    "s_add_u32 s70, s70, 1\n"
    "s_nop 2\n"
    "s_cmp_eq_u64 vcc, exec\n"
    "s_cbranch_scc1 .LX_go_%=\n"
    "s_sleep 8\n"
    "s_cmp_lt_u32 s70, 0x100000\n"
    "s_cbranch_scc1 .LX_spin_%=\n"
    ".LX_go_%=:\n"
    "s_add_u32 s74, s64, 0\n"
    "s_add_u32 m0, s66, 0\n"
    "s_nop 0\n"
    "buffer_load_dwordx4 v16, s[44:47], s74 offen offset:0 lds\n"
    "buffer_load_dwordx4 v17, s[44:47], s74 offen offset:1024 lds\n"
    "buffer_load_dwordx4 v18, s[44:47], s74 offen offset:2048 lds\n"
    "buffer_load_dwordx4 v19, s[44:47], s74 offen offset:3072 lds\n"
    "s_add_u32 s74, s64, 128\n"
    "s_add_u32 m0, s66, 8192\n"
    "s_nop 0\n"
    "buffer_load_dwordx4 v16, s[44:47], s74 offen offset:0 lds\n"
    "buffer_load_dwordx4 v17, s[44:47], s74 offen offset:1024 lds\n"
    "buffer_load_dwordx4 v18, s[44:47], s74 offen offset:2048 lds\n"
    "buffer_load_dwordx4 v19, s[44:47], s74 offen offset:3072 lds\n"
    "s_add_u32 s74, s64, 256\n"
    "s_mov_b32 m0, s67\n"
    "s_nop 0\n"
    "buffer_load_dwordx4 v16, s[44:47], s74 offen offset:0 lds\n"
    "buffer_load_dwordx4 v17, s[44:47], s74 offen offset:1024 lds\n"
    "buffer_load_dwordx4 v18, s[44:47], s74 offen offset:2048 lds\n"
    "buffer_load_dwordx4 v19, s[44:47], s74 offen offset:3072 lds\n"
    "s_add_u32 s64, s64, 32768\n"
    "s_mov_b32 s73, 0\n"
    "s_mov_b32 s79, 29696\n"
    ".LX_trip_%=:\n"
    "s_cmp_eq_u32 s73, 0\n"
    "s_cbranch_scc1 .LX_nb_%=\n"
    "s_barrier\n"
    ".LX_nb_%=:\n"
    "s_waitcnt vmcnt(8)\n"
    "ds_read_b64_tr_b16 a[168:169], v8 offset:0\n"
    "ds_read_b64_tr_b16 a[170:171], v8 offset:2048\n"
    "ds_read_b64_tr_b16 a[172:173], v9 offset:0\n"
    "ds_read_b64_tr_b16 a[174:175], v9 offset:2048\n"
    "ds_read_b64_tr_b16 a[176:177], v10 offset:0\n"
    "ds_read_b64_tr_b16 a[178:179], v10 offset:2048\n"
    "ds_read_b64_tr_b16 a[180:181], v11 offset:0\n"
    "ds_read_b64_tr_b16 a[182:183], v11 offset:2048\n"
    "ds_read_b128 a[112:115], v20 offset:0\n"
    "ds_read_b128 a[116:119], v20 offset:4096\n"
    "ds_read_b128 a[120:123], v20 offset:8192\n"
    "ds_read_b128 a[124:127], v20 offset:12288\n"
    "ds_read_b128 a[128:131], v20 offset:16384\n"
    "ds_read_b128 a[132:135], v20 offset:20480\n"
    "ds_read_b128 a[136:139], v20 offset:24576\n"
    "s_waitcnt lgkmcnt(0)\n"
    "s_mov_b32 s70, 0\n"
    ".LX_loop_%=:\n"
    "s_waitcnt vmcnt(4)\n"
    "v_mfma_f32_16x16x32_bf16 v[32:35], a[168:171], a[112:115], v[32:35]\n"
    "ds_read_b64_tr_b16 a[184:185], v8 offset:8192\n"
    "v_mfma_f32_16x16x32_bf16 v[60:63], a[172:175], a[112:115], v[60:63]\n"
    "v_mfma_f32_16x16x32_bf16 v[88:91], a[176:179], a[112:115], v[88:91]\n"
    "ds_read_b64_tr_b16 a[186:187], v8 offset:10240\n"
    "v_mfma_f32_16x16x32_bf16 v[116:119], a[180:183], a[112:115], v[116:119]\n"
    "ds_read_b64_tr_b16 a[188:189], v9 offset:8192\n"
    "v_mfma_f32_16x16x32_bf16 v[36:39], a[168:171], a[116:119], v[36:39]\n"
    "v_mfma_f32_16x16x32_bf16 v[64:67], a[172:175], a[116:119], v[64:67]\n"
    "ds_read_b64_tr_b16 a[190:191], v9 offset:10240\n"
    "v_mfma_f32_16x16x32_bf16 v[92:95], a[176:179], a[116:119], v[92:95]\n"
    "v_mfma_f32_16x16x32_bf16 v[120:123], a[180:183], a[116:119], v[120:123]\n"
    "ds_read_b64_tr_b16 a[192:193], v10 offset:8192\n"
    "v_mfma_f32_16x16x32_bf16 v[40:43], a[168:171], a[120:123], v[40:43]\n"
    "ds_read_b64_tr_b16 a[194:195], v10 offset:10240\n"
    "v_mfma_f32_16x16x32_bf16 v[68:71], a[172:175], a[120:123], v[68:71]\n"
    "v_mfma_f32_16x16x32_bf16 v[96:99], a[176:179], a[120:123], v[96:99]\n"
    "ds_read_b64_tr_b16 a[196:197], v11 offset:8192\n"
    "v_mfma_f32_16x16x32_bf16 v[124:127], a[180:183], a[120:123], v[124:127]\n"
    "ds_read_b64_tr_b16 a[198:199], v11 offset:10240\n"
    "v_mfma_f32_16x16x32_bf16 v[44:47], a[168:171], a[124:127], v[44:47]\n"
    "v_mfma_f32_16x16x32_bf16 v[72:75], a[172:175], a[124:127], v[72:75]\n"
    "s_add_u32 s74, s64, 0\n"
    "v_mfma_f32_16x16x32_bf16 v[100:103], a[176:179], a[124:127], v[100:103]\n"
    "v_mfma_f32_16x16x32_bf16 v[128:131], a[180:183], a[124:127], v[128:131]\n"
    "s_add_u32 m0, s66, 0\n"
    "v_mfma_f32_16x16x32_bf16 v[48:51], a[168:171], a[128:131], v[48:51]\n"
    "buffer_load_dwordx4 v16, s[44:47], s74 offen offset:0 lds\n"
    "v_mfma_f32_16x16x32_bf16 v[76:79], a[172:175], a[128:131], v[76:79]\n"
    "v_mfma_f32_16x16x32_bf16 v[104:107], a[176:179], a[128:131], v[104:107]\n"
    "buffer_load_dwordx4 v17, s[44:47], s74 offen offset:1024 lds\n"
    "v_mfma_f32_16x16x32_bf16 v[132:135], a[180:183], a[128:131], v[132:135]\n"
    "buffer_load_dwordx4 v18, s[44:47], s74 offen offset:2048 lds\n"
    "v_mfma_f32_16x16x32_bf16 v[52:55], a[168:171], a[132:135], v[52:55]\n"
    "v_mfma_f32_16x16x32_bf16 v[80:83], a[172:175], a[132:135], v[80:83]\n"
    "buffer_load_dwordx4 v19, s[44:47], s74 offen offset:3072 lds\n"
    "v_mfma_f32_16x16x32_bf16 v[108:111], a[176:179], a[132:135], v[108:111]\n"
    "v_mfma_f32_16x16x32_bf16 v[136:139], a[180:183], a[132:135], v[136:139]\n"
    "v_mfma_f32_16x16x32_bf16 v[56:59], a[168:171], a[136:139], v[56:59]\n"
    "v_mfma_f32_16x16x32_bf16 v[84:87], a[172:175], a[136:139], v[84:87]\n"
    "v_mfma_f32_16x16x32_bf16 v[112:115], a[176:179], a[136:139], v[112:115]\n"
    "v_mfma_f32_16x16x32_bf16 v[140:143], a[180:183], a[136:139], v[140:143]\n"
    "s_waitcnt lgkmcnt(0)\n"
    "s_waitcnt vmcnt(4)\n"
    "v_mfma_f32_16x16x32_bf16 v[144:147], a[184:187], a[112:115], v[144:147]\n"
    "ds_read_b64_tr_b16 a[168:169], v12 offset:0\n"
    "v_mfma_f32_16x16x32_bf16 v[172:175], a[188:191], a[112:115], v[172:175]\n"
    "v_mfma_f32_16x16x32_bf16 v[200:203], a[192:195], a[112:115], v[200:203]\n"
    "ds_read_b64_tr_b16 a[170:171], v12 offset:2048\n"
    "v_mfma_f32_16x16x32_bf16 v[228:231], a[196:199], a[112:115], v[228:231]\n"
    "ds_read_b64_tr_b16 a[172:173], v13 offset:0\n"
    "v_mfma_f32_16x16x32_bf16 v[148:151], a[184:187], a[116:119], v[148:151]\n"
    "v_mfma_f32_16x16x32_bf16 v[176:179], a[188:191], a[116:119], v[176:179]\n"
    "ds_read_b64_tr_b16 a[174:175], v13 offset:2048\n"
    "v_mfma_f32_16x16x32_bf16 v[204:207], a[192:195], a[116:119], v[204:207]\n"
    "v_mfma_f32_16x16x32_bf16 v[232:235], a[196:199], a[116:119], v[232:235]\n"
    "ds_read_b64_tr_b16 a[176:177], v14 offset:0\n"
    "v_mfma_f32_16x16x32_bf16 v[152:155], a[184:187], a[120:123], v[152:155]\n"
    "ds_read_b64_tr_b16 a[178:179], v14 offset:2048\n"
    "v_mfma_f32_16x16x32_bf16 v[180:183], a[188:191], a[120:123], v[180:183]\n"
    "v_mfma_f32_16x16x32_bf16 v[208:211], a[192:195], a[120:123], v[208:211]\n"
    "ds_read_b64_tr_b16 a[180:181], v15 offset:0\n"
    "v_mfma_f32_16x16x32_bf16 v[236:239], a[196:199], a[120:123], v[236:239]\n"
    "ds_read_b64_tr_b16 a[182:183], v15 offset:2048\n"
    "v_mfma_f32_16x16x32_bf16 v[156:159], a[184:187], a[124:127], v[156:159]\n"
    "v_mfma_f32_16x16x32_bf16 v[184:187], a[188:191], a[124:127], v[184:187]\n"
    "s_add_u32 s74, s64, 128\n"
    "v_mfma_f32_16x16x32_bf16 v[212:215], a[192:195], a[124:127], v[212:215]\n"
    "v_mfma_f32_16x16x32_bf16 v[240:243], a[196:199], a[124:127], v[240:243]\n"
    "s_add_u32 m0, s66, 8192\n"
    "v_mfma_f32_16x16x32_bf16 v[160:163], a[184:187], a[128:131], v[160:163]\n"
    "buffer_load_dwordx4 v16, s[44:47], s74 offen offset:0 lds\n"
    "v_mfma_f32_16x16x32_bf16 v[188:191], a[188:191], a[128:131], v[188:191]\n"
    "v_mfma_f32_16x16x32_bf16 v[216:219], a[192:195], a[128:131], v[216:219]\n"
    "buffer_load_dwordx4 v17, s[44:47], s74 offen offset:1024 lds\n"
    "v_mfma_f32_16x16x32_bf16 v[244:247], a[196:199], a[128:131], v[244:247]\n"
    "buffer_load_dwordx4 v18, s[44:47], s74 offen offset:2048 lds\n"
    "v_mfma_f32_16x16x32_bf16 v[164:167], a[184:187], a[132:135], v[164:167]\n"
    "v_mfma_f32_16x16x32_bf16 v[192:195], a[188:191], a[132:135], v[192:195]\n"
    "buffer_load_dwordx4 v19, s[44:47], s74 offen offset:3072 lds\n"
    "v_mfma_f32_16x16x32_bf16 v[220:223], a[192:195], a[132:135], v[220:223]\n"
    "v_mfma_f32_16x16x32_bf16 v[248:251], a[196:199], a[132:135], v[248:251]\n"
    "v_mfma_f32_16x16x32_bf16 v[168:171], a[184:187], a[136:139], v[168:171]\n"
    "v_mfma_f32_16x16x32_bf16 v[196:199], a[188:191], a[136:139], v[196:199]\n"
    "v_mfma_f32_16x16x32_bf16 v[224:227], a[192:195], a[136:139], v[224:227]\n"
    "v_mfma_f32_16x16x32_bf16 v[252:255], a[196:199], a[136:139], v[252:255]\n"
    "s_waitcnt lgkmcnt(0)\n"
    "s_waitcnt vmcnt(4)\n"
    "v_mfma_f32_16x16x32_bf16 a[0:3], a[168:171], a[112:115], a[0:3]\n"
    "v_add_u32 v20, 1024, v20\n"
    "v_mfma_f32_16x16x32_bf16 a[28:31], a[172:175], a[112:115], a[28:31]\n"
    "ds_read_b64_tr_b16 a[184:185], v8 offset:0\n"
    "v_mfma_f32_16x16x32_bf16 a[56:59], a[176:179], a[112:115], a[56:59]\n"
    "ds_read_b64_tr_b16 a[186:187], v8 offset:2048\n"
    "v_mfma_f32_16x16x32_bf16 a[84:87], a[180:183], a[112:115], a[84:87]\n"
    "ds_read_b64_tr_b16 a[188:189], v9 offset:0\n"
    "v_mfma_f32_16x16x32_bf16 a[4:7], a[168:171], a[116:119], a[4:7]\n"
    "ds_read_b64_tr_b16 a[190:191], v9 offset:2048\n"
    "v_mfma_f32_16x16x32_bf16 a[32:35], a[172:175], a[116:119], a[32:35]\n"
    "ds_read_b64_tr_b16 a[192:193], v10 offset:0\n"
    "v_mfma_f32_16x16x32_bf16 a[60:63], a[176:179], a[116:119], a[60:63]\n"
    "ds_read_b64_tr_b16 a[194:195], v10 offset:2048\n"
    "v_mfma_f32_16x16x32_bf16 a[88:91], a[180:183], a[116:119], a[88:91]\n"
    "ds_read_b64_tr_b16 a[196:197], v11 offset:0\n"
    "v_mfma_f32_16x16x32_bf16 a[8:11], a[168:171], a[120:123], a[8:11]\n"
    "ds_read_b64_tr_b16 a[198:199], v11 offset:2048\n"
    "v_mfma_f32_16x16x32_bf16 a[36:39], a[172:175], a[120:123], a[36:39]\n"
    "ds_read_b128 a[140:143], v20 offset:0\n"
    "v_mfma_f32_16x16x32_bf16 a[64:67], a[176:179], a[120:123], a[64:67]\n"
    "ds_read_b128 a[144:147], v20 offset:4096\n"
    "v_mfma_f32_16x16x32_bf16 a[92:95], a[180:183], a[120:123], a[92:95]\n"
    "ds_read_b128 a[148:151], v20 offset:8192\n"
    "v_mfma_f32_16x16x32_bf16 a[12:15], a[168:171], a[124:127], a[12:15]\n"
    "ds_read_b128 a[152:155], v20 offset:12288\n"
    "v_mfma_f32_16x16x32_bf16 a[40:43], a[172:175], a[124:127], a[40:43]\n"
    "ds_read_b128 a[156:159], v20 offset:16384\n"
    "v_mfma_f32_16x16x32_bf16 a[68:71], a[176:179], a[124:127], a[68:71]\n"
    "ds_read_b128 a[160:163], v20 offset:20480\n"
    "v_mfma_f32_16x16x32_bf16 a[96:99], a[180:183], a[124:127], a[96:99]\n"
    "ds_read_b128 a[164:167], v20 offset:24576\n"
    "v_mfma_f32_16x16x32_bf16 a[16:19], a[168:171], a[128:131], a[16:19]\n"
    "s_add_u32 s74, s64, 256\n"
    "v_mfma_f32_16x16x32_bf16 a[44:47], a[172:175], a[128:131], a[44:47]\n"
    "s_mov_b32 m0, s67\n"
    "v_mfma_f32_16x16x32_bf16 a[72:75], a[176:179], a[128:131], a[72:75]\n"
    "buffer_load_dwordx4 v16, s[44:47], s74 offen offset:0 lds\n"
    "buffer_load_dwordx4 v17, s[44:47], s74 offen offset:1024 lds\n"
    "v_mfma_f32_16x16x32_bf16 a[100:103], a[180:183], a[128:131], a[100:103]\n"
    "buffer_load_dwordx4 v18, s[44:47], s74 offen offset:2048 lds\n"
    "v_mfma_f32_16x16x32_bf16 a[20:23], a[168:171], a[132:135], a[20:23]\n"
    "buffer_load_dwordx4 v19, s[44:47], s74 offen offset:3072 lds\n"
    "v_mfma_f32_16x16x32_bf16 a[48:51], a[172:175], a[132:135], a[48:51]\n"
    "s_add_u32 s64, s64, 32768\n"
    "v_mfma_f32_16x16x32_bf16 a[76:79], a[176:179], a[132:135], a[76:79]\n"
    "v_mfma_f32_16x16x32_bf16 a[104:107], a[180:183], a[132:135], a[104:107]\n"
    "v_mfma_f32_16x16x32_bf16 a[24:27], a[168:171], a[136:139], a[24:27]\n"
    "v_mfma_f32_16x16x32_bf16 a[52:55], a[172:175], a[136:139], a[52:55]\n"
    "v_mfma_f32_16x16x32_bf16 a[80:83], a[176:179], a[136:139], a[80:83]\n"
    "v_mfma_f32_16x16x32_bf16 a[108:111], a[180:183], a[136:139], a[108:111]\n"
    "s_waitcnt lgkmcnt(0)\n"
    "s_waitcnt vmcnt(4)\n"
    "v_mfma_f32_16x16x32_bf16 v[32:35], a[184:187], a[140:143], v[32:35]\n"
    "ds_read_b64_tr_b16 a[168:169], v8 offset:8192\n"
    "v_mfma_f32_16x16x32_bf16 v[60:63], a[188:191], a[140:143], v[60:63]\n"
    "v_mfma_f32_16x16x32_bf16 v[88:91], a[192:195], a[140:143], v[88:91]\n"
    "ds_read_b64_tr_b16 a[170:171], v8 offset:10240\n"
    "v_mfma_f32_16x16x32_bf16 v[116:119], a[196:199], a[140:143], v[116:119]\n"
    "ds_read_b64_tr_b16 a[172:173], v9 offset:8192\n"
    "v_mfma_f32_16x16x32_bf16 v[36:39], a[184:187], a[144:147], v[36:39]\n"
    "v_mfma_f32_16x16x32_bf16 v[64:67], a[188:191], a[144:147], v[64:67]\n"
    "ds_read_b64_tr_b16 a[174:175], v9 offset:10240\n"
    "v_mfma_f32_16x16x32_bf16 v[92:95], a[192:195], a[144:147], v[92:95]\n"
    "v_mfma_f32_16x16x32_bf16 v[120:123], a[196:199], a[144:147], v[120:123]\n"
    "ds_read_b64_tr_b16 a[176:177], v10 offset:8192\n"
    "v_mfma_f32_16x16x32_bf16 v[40:43], a[184:187], a[148:151], v[40:43]\n"
    "ds_read_b64_tr_b16 a[178:179], v10 offset:10240\n"
    "v_mfma_f32_16x16x32_bf16 v[68:71], a[188:191], a[148:151], v[68:71]\n"
    "v_mfma_f32_16x16x32_bf16 v[96:99], a[192:195], a[148:151], v[96:99]\n"
    "ds_read_b64_tr_b16 a[180:181], v11 offset:8192\n"
    "v_mfma_f32_16x16x32_bf16 v[124:127], a[196:199], a[148:151], v[124:127]\n"
    "ds_read_b64_tr_b16 a[182:183], v11 offset:10240\n"
    "v_mfma_f32_16x16x32_bf16 v[44:47], a[184:187], a[152:155], v[44:47]\n"
    "v_mfma_f32_16x16x32_bf16 v[72:75], a[188:191], a[152:155], v[72:75]\n"
    "s_add_u32 s74, s64, 0\n"
    "v_mfma_f32_16x16x32_bf16 v[100:103], a[192:195], a[152:155], v[100:103]\n"
    "v_mfma_f32_16x16x32_bf16 v[128:131], a[196:199], a[152:155], v[128:131]\n"
    "s_add_u32 m0, s66, 0\n"
    "v_mfma_f32_16x16x32_bf16 v[48:51], a[184:187], a[156:159], v[48:51]\n"
    "buffer_load_dwordx4 v16, s[44:47], s74 offen offset:0 lds\n"
    "v_mfma_f32_16x16x32_bf16 v[76:79], a[188:191], a[156:159], v[76:79]\n"
    "v_mfma_f32_16x16x32_bf16 v[104:107], a[192:195], a[156:159], v[104:107]\n"
    "buffer_load_dwordx4 v17, s[44:47], s74 offen offset:1024 lds\n"
    "v_mfma_f32_16x16x32_bf16 v[132:135], a[196:199], a[156:159], v[132:135]\n"
    "buffer_load_dwordx4 v18, s[44:47], s74 offen offset:2048 lds\n"
    "v_mfma_f32_16x16x32_bf16 v[52:55], a[184:187], a[160:163], v[52:55]\n"
    "v_mfma_f32_16x16x32_bf16 v[80:83], a[188:191], a[160:163], v[80:83]\n"
    "buffer_load_dwordx4 v19, s[44:47], s74 offen offset:3072 lds\n"
    "v_mfma_f32_16x16x32_bf16 v[108:111], a[192:195], a[160:163], v[108:111]\n"
    "v_mfma_f32_16x16x32_bf16 v[136:139], a[196:199], a[160:163], v[136:139]\n"
    "v_mfma_f32_16x16x32_bf16 v[56:59], a[184:187], a[164:167], v[56:59]\n"
    "v_mfma_f32_16x16x32_bf16 v[84:87], a[188:191], a[164:167], v[84:87]\n"
    "v_mfma_f32_16x16x32_bf16 v[112:115], a[192:195], a[164:167], v[112:115]\n"
    "v_mfma_f32_16x16x32_bf16 v[140:143], a[196:199], a[164:167], v[140:143]\n"
    "s_waitcnt lgkmcnt(0)\n"
    "s_waitcnt vmcnt(4)\n"
    "v_mfma_f32_16x16x32_bf16 v[144:147], a[168:171], a[140:143], v[144:147]\n"
    "ds_read_b64_tr_b16 a[184:185], v12 offset:0\n"
    "v_mfma_f32_16x16x32_bf16 v[172:175], a[172:175], a[140:143], v[172:175]\n"
    "v_mfma_f32_16x16x32_bf16 v[200:203], a[176:179], a[140:143], v[200:203]\n"
    "ds_read_b64_tr_b16 a[186:187], v12 offset:2048\n"
    "v_mfma_f32_16x16x32_bf16 v[228:231], a[180:183], a[140:143], v[228:231]\n"
    "ds_read_b64_tr_b16 a[188:189], v13 offset:0\n"
    "v_mfma_f32_16x16x32_bf16 v[148:151], a[168:171], a[144:147], v[148:151]\n"
    "v_mfma_f32_16x16x32_bf16 v[176:179], a[172:175], a[144:147], v[176:179]\n"
    "ds_read_b64_tr_b16 a[190:191], v13 offset:2048\n"
    "v_mfma_f32_16x16x32_bf16 v[204:207], a[176:179], a[144:147], v[204:207]\n"
    "v_mfma_f32_16x16x32_bf16 v[232:235], a[180:183], a[144:147], v[232:235]\n"
    "ds_read_b64_tr_b16 a[192:193], v14 offset:0\n"
    "v_mfma_f32_16x16x32_bf16 v[152:155], a[168:171], a[148:151], v[152:155]\n"
    "ds_read_b64_tr_b16 a[194:195], v14 offset:2048\n"
    "v_mfma_f32_16x16x32_bf16 v[180:183], a[172:175], a[148:151], v[180:183]\n"
    "v_mfma_f32_16x16x32_bf16 v[208:211], a[176:179], a[148:151], v[208:211]\n"
    "ds_read_b64_tr_b16 a[196:197], v15 offset:0\n"
    "v_mfma_f32_16x16x32_bf16 v[236:239], a[180:183], a[148:151], v[236:239]\n"
    "ds_read_b64_tr_b16 a[198:199], v15 offset:2048\n"
    "v_mfma_f32_16x16x32_bf16 v[156:159], a[168:171], a[152:155], v[156:159]\n"
    "v_mfma_f32_16x16x32_bf16 v[184:187], a[172:175], a[152:155], v[184:187]\n"
    "s_add_u32 s74, s64, 128\n"
    "v_mfma_f32_16x16x32_bf16 v[212:215], a[176:179], a[152:155], v[212:215]\n"
    "v_mfma_f32_16x16x32_bf16 v[240:243], a[180:183], a[152:155], v[240:243]\n"
    "s_add_u32 m0, s66, 8192\n"
    "v_mfma_f32_16x16x32_bf16 v[160:163], a[168:171], a[156:159], v[160:163]\n"
    "buffer_load_dwordx4 v16, s[44:47], s74 offen offset:0 lds\n"
    "v_mfma_f32_16x16x32_bf16 v[188:191], a[172:175], a[156:159], v[188:191]\n"
    "v_mfma_f32_16x16x32_bf16 v[216:219], a[176:179], a[156:159], v[216:219]\n"
    "buffer_load_dwordx4 v17, s[44:47], s74 offen offset:1024 lds\n"
    "v_mfma_f32_16x16x32_bf16 v[244:247], a[180:183], a[156:159], v[244:247]\n"
    "buffer_load_dwordx4 v18, s[44:47], s74 offen offset:2048 lds\n"
    "v_mfma_f32_16x16x32_bf16 v[164:167], a[168:171], a[160:163], v[164:167]\n"
    "v_mfma_f32_16x16x32_bf16 v[192:195], a[172:175], a[160:163], v[192:195]\n"
    "buffer_load_dwordx4 v19, s[44:47], s74 offen offset:3072 lds\n"
    "v_mfma_f32_16x16x32_bf16 v[220:223], a[176:179], a[160:163], v[220:223]\n"
    "v_mfma_f32_16x16x32_bf16 v[248:251], a[180:183], a[160:163], v[248:251]\n"
    "v_mfma_f32_16x16x32_bf16 v[168:171], a[168:171], a[164:167], v[168:171]\n"
    "v_mfma_f32_16x16x32_bf16 v[196:199], a[172:175], a[164:167], v[196:199]\n"
    "v_mfma_f32_16x16x32_bf16 v[224:227], a[176:179], a[164:167], v[224:227]\n"
    "v_mfma_f32_16x16x32_bf16 v[252:255], a[180:183], a[164:167], v[252:255]\n"
    "s_waitcnt lgkmcnt(0)\n"
    "s_waitcnt vmcnt(4)\n"
    "v_mfma_f32_16x16x32_bf16 a[0:3], a[184:187], a[140:143], a[0:3]\n"
    "s_cmp_eq_u32 s70, 1\n"
    "s_cselect_b32 s78, s79, 1024\n"
    "v_mfma_f32_16x16x32_bf16 a[28:31], a[188:191], a[140:143], a[28:31]\n"
    "v_mfma_f32_16x16x32_bf16 a[56:59], a[192:195], a[140:143], a[56:59]\n"
    "v_add_u32 v20, s78, v20\n"
    "v_mfma_f32_16x16x32_bf16 a[84:87], a[196:199], a[140:143], a[84:87]\n"
    "ds_read_b64_tr_b16 a[168:169], v8 offset:0\n"
    "v_mfma_f32_16x16x32_bf16 a[4:7], a[184:187], a[144:147], a[4:7]\n"
    "ds_read_b64_tr_b16 a[170:171], v8 offset:2048\n"
    "ds_read_b64_tr_b16 a[172:173], v9 offset:0\n"
    "v_mfma_f32_16x16x32_bf16 a[32:35], a[188:191], a[144:147], a[32:35]\n"
    "ds_read_b64_tr_b16 a[174:175], v9 offset:2048\n"
    "v_mfma_f32_16x16x32_bf16 a[60:63], a[192:195], a[144:147], a[60:63]\n"
    "ds_read_b64_tr_b16 a[176:177], v10 offset:0\n"
    "v_mfma_f32_16x16x32_bf16 a[88:91], a[196:199], a[144:147], a[88:91]\n"
    "ds_read_b64_tr_b16 a[178:179], v10 offset:2048\n"
    "v_mfma_f32_16x16x32_bf16 a[8:11], a[184:187], a[148:151], a[8:11]\n"
    "ds_read_b64_tr_b16 a[180:181], v11 offset:0\n"
    "v_mfma_f32_16x16x32_bf16 a[36:39], a[188:191], a[148:151], a[36:39]\n"
    "ds_read_b64_tr_b16 a[182:183], v11 offset:2048\n"
    "v_mfma_f32_16x16x32_bf16 a[64:67], a[192:195], a[148:151], a[64:67]\n"
    "ds_read_b128 a[112:115], v20 offset:0\n"
    "v_mfma_f32_16x16x32_bf16 a[92:95], a[196:199], a[148:151], a[92:95]\n"
    "ds_read_b128 a[116:119], v20 offset:4096\n"
    "v_mfma_f32_16x16x32_bf16 a[12:15], a[184:187], a[152:155], a[12:15]\n"
    "ds_read_b128 a[120:123], v20 offset:8192\n"
    "ds_read_b128 a[124:127], v20 offset:12288\n"
    "v_mfma_f32_16x16x32_bf16 a[40:43], a[188:191], a[152:155], a[40:43]\n"
    "ds_read_b128 a[128:131], v20 offset:16384\n"
    "v_mfma_f32_16x16x32_bf16 a[68:71], a[192:195], a[152:155], a[68:71]\n"
    "ds_read_b128 a[132:135], v20 offset:20480\n"
    "v_mfma_f32_16x16x32_bf16 a[96:99], a[196:199], a[152:155], a[96:99]\n"
    "ds_read_b128 a[136:139], v20 offset:24576\n"
    "v_mfma_f32_16x16x32_bf16 a[16:19], a[184:187], a[156:159], a[16:19]\n"
    "s_add_u32 s74, s64, 256\n"
    "v_mfma_f32_16x16x32_bf16 a[44:47], a[188:191], a[156:159], a[44:47]\n"
    "s_mov_b32 m0, s67\n"
    "v_mfma_f32_16x16x32_bf16 a[72:75], a[192:195], a[156:159], a[72:75]\n"
    "buffer_load_dwordx4 v16, s[44:47], s74 offen offset:0 lds\n"
    "v_mfma_f32_16x16x32_bf16 a[100:103], a[196:199], a[156:159], a[100:103]\n"
    "buffer_load_dwordx4 v17, s[44:47], s74 offen offset:1024 lds\n"
    "v_mfma_f32_16x16x32_bf16 a[20:23], a[184:187], a[160:163], a[20:23]\n"
    "buffer_load_dwordx4 v18, s[44:47], s74 offen offset:2048 lds\n"
    "v_mfma_f32_16x16x32_bf16 a[48:51], a[188:191], a[160:163], a[48:51]\n"
    "buffer_load_dwordx4 v19, s[44:47], s74 offen offset:3072 lds\n"
    "s_add_u32 s64, s64, 32768\n"
    "v_mfma_f32_16x16x32_bf16 a[76:79], a[192:195], a[160:163], a[76:79]\n"
    "v_mfma_f32_16x16x32_bf16 a[104:107], a[196:199], a[160:163], a[104:107]\n"
    "v_mfma_f32_16x16x32_bf16 a[24:27], a[184:187], a[164:167], a[24:27]\n"
    "v_mfma_f32_16x16x32_bf16 a[52:55], a[188:191], a[164:167], a[52:55]\n"
    "v_mfma_f32_16x16x32_bf16 a[80:83], a[192:195], a[164:167], a[80:83]\n"
    "v_mfma_f32_16x16x32_bf16 a[108:111], a[196:199], a[164:167], a[108:111]\n"
    "s_waitcnt lgkmcnt(0)\n"
    "s_add_u32 s70, s70, 1\n"
    "s_cmp_lt_u32 s70, 3\n"
    "s_cbranch_scc1 .LX_loop_%=\n"
    "s_cmp_eq_u32 s73, 1\n"
    "s_cbranch_scc1 .LX_tail2_%=\n"
    "s_waitcnt vmcnt(4)\n"
    "v_mfma_f32_16x16x32_bf16 v[32:35], a[168:171], a[112:115], v[32:35]\n"
    "ds_read_b64_tr_b16 a[184:185], v8 offset:8192\n"
    "v_mfma_f32_16x16x32_bf16 v[60:63], a[172:175], a[112:115], v[60:63]\n"
    "v_mfma_f32_16x16x32_bf16 v[88:91], a[176:179], a[112:115], v[88:91]\n"
    "ds_read_b64_tr_b16 a[186:187], v8 offset:10240\n"
    "v_mfma_f32_16x16x32_bf16 v[116:119], a[180:183], a[112:115], v[116:119]\n"
    "ds_read_b64_tr_b16 a[188:189], v9 offset:8192\n"
    "v_mfma_f32_16x16x32_bf16 v[36:39], a[168:171], a[116:119], v[36:39]\n"
    "v_mfma_f32_16x16x32_bf16 v[64:67], a[172:175], a[116:119], v[64:67]\n"
    "ds_read_b64_tr_b16 a[190:191], v9 offset:10240\n"
    "v_mfma_f32_16x16x32_bf16 v[92:95], a[176:179], a[116:119], v[92:95]\n"
    "v_mfma_f32_16x16x32_bf16 v[120:123], a[180:183], a[116:119], v[120:123]\n"
    "ds_read_b64_tr_b16 a[192:193], v10 offset:8192\n"
    "v_mfma_f32_16x16x32_bf16 v[40:43], a[168:171], a[120:123], v[40:43]\n"
    "ds_read_b64_tr_b16 a[194:195], v10 offset:10240\n"
    "v_mfma_f32_16x16x32_bf16 v[68:71], a[172:175], a[120:123], v[68:71]\n"
    "v_mfma_f32_16x16x32_bf16 v[96:99], a[176:179], a[120:123], v[96:99]\n"
    "ds_read_b64_tr_b16 a[196:197], v11 offset:8192\n"
    "v_mfma_f32_16x16x32_bf16 v[124:127], a[180:183], a[120:123], v[124:127]\n"
    "ds_read_b64_tr_b16 a[198:199], v11 offset:10240\n"
    "v_mfma_f32_16x16x32_bf16 v[44:47], a[168:171], a[124:127], v[44:47]\n"
    "v_mfma_f32_16x16x32_bf16 v[72:75], a[172:175], a[124:127], v[72:75]\n"
    "s_add_u32 s74, s64, 0\n"
    "v_mfma_f32_16x16x32_bf16 v[100:103], a[176:179], a[124:127], v[100:103]\n"
    "v_mfma_f32_16x16x32_bf16 v[128:131], a[180:183], a[124:127], v[128:131]\n"
    "s_add_u32 m0, s66, 0\n"
    "v_mfma_f32_16x16x32_bf16 v[48:51], a[168:171], a[128:131], v[48:51]\n"
    "buffer_load_dwordx4 v16, s[44:47], s74 offen offset:0 lds\n"
    "v_mfma_f32_16x16x32_bf16 v[76:79], a[172:175], a[128:131], v[76:79]\n"
    "v_mfma_f32_16x16x32_bf16 v[104:107], a[176:179], a[128:131], v[104:107]\n"
    "buffer_load_dwordx4 v17, s[44:47], s74 offen offset:1024 lds\n"
    "v_mfma_f32_16x16x32_bf16 v[132:135], a[180:183], a[128:131], v[132:135]\n"
    "buffer_load_dwordx4 v18, s[44:47], s74 offen offset:2048 lds\n"
    "v_mfma_f32_16x16x32_bf16 v[52:55], a[168:171], a[132:135], v[52:55]\n"
    "v_mfma_f32_16x16x32_bf16 v[80:83], a[172:175], a[132:135], v[80:83]\n"
    "buffer_load_dwordx4 v19, s[44:47], s74 offen offset:3072 lds\n"
    "v_mfma_f32_16x16x32_bf16 v[108:111], a[176:179], a[132:135], v[108:111]\n"
    "v_mfma_f32_16x16x32_bf16 v[136:139], a[180:183], a[132:135], v[136:139]\n"
    "v_mfma_f32_16x16x32_bf16 v[56:59], a[168:171], a[136:139], v[56:59]\n"
    "v_mfma_f32_16x16x32_bf16 v[84:87], a[172:175], a[136:139], v[84:87]\n"
    "v_mfma_f32_16x16x32_bf16 v[112:115], a[176:179], a[136:139], v[112:115]\n"
    "v_mfma_f32_16x16x32_bf16 v[140:143], a[180:183], a[136:139], v[140:143]\n"
    "s_waitcnt lgkmcnt(0)\n"
    "s_waitcnt vmcnt(4)\n"
    "v_mfma_f32_16x16x32_bf16 v[144:147], a[184:187], a[112:115], v[144:147]\n"
    "ds_read_b64_tr_b16 a[168:169], v12 offset:0\n"
    "v_mfma_f32_16x16x32_bf16 v[172:175], a[188:191], a[112:115], v[172:175]\n"
    "v_mfma_f32_16x16x32_bf16 v[200:203], a[192:195], a[112:115], v[200:203]\n"
    "ds_read_b64_tr_b16 a[170:171], v12 offset:2048\n"
    "v_mfma_f32_16x16x32_bf16 v[228:231], a[196:199], a[112:115], v[228:231]\n"
    "ds_read_b64_tr_b16 a[172:173], v13 offset:0\n"
    "v_mfma_f32_16x16x32_bf16 v[148:151], a[184:187], a[116:119], v[148:151]\n"
    "v_mfma_f32_16x16x32_bf16 v[176:179], a[188:191], a[116:119], v[176:179]\n"
    "ds_read_b64_tr_b16 a[174:175], v13 offset:2048\n"
    "v_mfma_f32_16x16x32_bf16 v[204:207], a[192:195], a[116:119], v[204:207]\n"
    "v_mfma_f32_16x16x32_bf16 v[232:235], a[196:199], a[116:119], v[232:235]\n"
    "ds_read_b64_tr_b16 a[176:177], v14 offset:0\n"
    "v_mfma_f32_16x16x32_bf16 v[152:155], a[184:187], a[120:123], v[152:155]\n"
    "ds_read_b64_tr_b16 a[178:179], v14 offset:2048\n"
    "v_mfma_f32_16x16x32_bf16 v[180:183], a[188:191], a[120:123], v[180:183]\n"
    "v_mfma_f32_16x16x32_bf16 v[208:211], a[192:195], a[120:123], v[208:211]\n"
    "ds_read_b64_tr_b16 a[180:181], v15 offset:0\n"
    "v_mfma_f32_16x16x32_bf16 v[236:239], a[196:199], a[120:123], v[236:239]\n"
    "ds_read_b64_tr_b16 a[182:183], v15 offset:2048\n"
    "v_mfma_f32_16x16x32_bf16 v[156:159], a[184:187], a[124:127], v[156:159]\n"
    "v_mfma_f32_16x16x32_bf16 v[184:187], a[188:191], a[124:127], v[184:187]\n"
    "s_add_u32 s74, s64, 128\n"
    "v_mfma_f32_16x16x32_bf16 v[212:215], a[192:195], a[124:127], v[212:215]\n"
    "v_mfma_f32_16x16x32_bf16 v[240:243], a[196:199], a[124:127], v[240:243]\n"
    "s_add_u32 m0, s66, 8192\n"
    "v_mfma_f32_16x16x32_bf16 v[160:163], a[184:187], a[128:131], v[160:163]\n"
    "buffer_load_dwordx4 v16, s[44:47], s74 offen offset:0 lds\n"
    "v_mfma_f32_16x16x32_bf16 v[188:191], a[188:191], a[128:131], v[188:191]\n"
    "v_mfma_f32_16x16x32_bf16 v[216:219], a[192:195], a[128:131], v[216:219]\n"
    "buffer_load_dwordx4 v17, s[44:47], s74 offen offset:1024 lds\n"
    "v_mfma_f32_16x16x32_bf16 v[244:247], a[196:199], a[128:131], v[244:247]\n"
    "buffer_load_dwordx4 v18, s[44:47], s74 offen offset:2048 lds\n"
    "v_mfma_f32_16x16x32_bf16 v[164:167], a[184:187], a[132:135], v[164:167]\n"
    "v_mfma_f32_16x16x32_bf16 v[192:195], a[188:191], a[132:135], v[192:195]\n"
    "buffer_load_dwordx4 v19, s[44:47], s74 offen offset:3072 lds\n"
    "v_mfma_f32_16x16x32_bf16 v[220:223], a[192:195], a[132:135], v[220:223]\n"
    "v_mfma_f32_16x16x32_bf16 v[248:251], a[196:199], a[132:135], v[248:251]\n"
    "v_mfma_f32_16x16x32_bf16 v[168:171], a[184:187], a[136:139], v[168:171]\n"
    "v_mfma_f32_16x16x32_bf16 v[196:199], a[188:191], a[136:139], v[196:199]\n"
    "v_mfma_f32_16x16x32_bf16 v[224:227], a[192:195], a[136:139], v[224:227]\n"
    "v_mfma_f32_16x16x32_bf16 v[252:255], a[196:199], a[136:139], v[252:255]\n"
    "s_waitcnt lgkmcnt(0)\n"
    "s_waitcnt vmcnt(4)\n"
    "v_mfma_f32_16x16x32_bf16 a[0:3], a[168:171], a[112:115], a[0:3]\n"
    "v_add_u32 v20, 1024, v20\n"
    "v_mfma_f32_16x16x32_bf16 a[28:31], a[172:175], a[112:115], a[28:31]\n"
    "ds_read_b64_tr_b16 a[184:185], v8 offset:0\n"
    "v_mfma_f32_16x16x32_bf16 a[56:59], a[176:179], a[112:115], a[56:59]\n"
    "ds_read_b64_tr_b16 a[186:187], v8 offset:2048\n"
    "v_mfma_f32_16x16x32_bf16 a[84:87], a[180:183], a[112:115], a[84:87]\n"
    "ds_read_b64_tr_b16 a[188:189], v9 offset:0\n"
    "v_mfma_f32_16x16x32_bf16 a[4:7], a[168:171], a[116:119], a[4:7]\n"
    "ds_read_b64_tr_b16 a[190:191], v9 offset:2048\n"
    "v_mfma_f32_16x16x32_bf16 a[32:35], a[172:175], a[116:119], a[32:35]\n"
    "ds_read_b64_tr_b16 a[192:193], v10 offset:0\n"
    "v_mfma_f32_16x16x32_bf16 a[60:63], a[176:179], a[116:119], a[60:63]\n"
    "ds_read_b64_tr_b16 a[194:195], v10 offset:2048\n"
    "v_mfma_f32_16x16x32_bf16 a[88:91], a[180:183], a[116:119], a[88:91]\n"
    "ds_read_b64_tr_b16 a[196:197], v11 offset:0\n"
    "v_mfma_f32_16x16x32_bf16 a[8:11], a[168:171], a[120:123], a[8:11]\n"
    "ds_read_b64_tr_b16 a[198:199], v11 offset:2048\n"
    "v_mfma_f32_16x16x32_bf16 a[36:39], a[172:175], a[120:123], a[36:39]\n"
    "ds_read_b128 a[140:143], v20 offset:0\n"
    "v_mfma_f32_16x16x32_bf16 a[64:67], a[176:179], a[120:123], a[64:67]\n"
    "ds_read_b128 a[144:147], v20 offset:4096\n"
    "v_mfma_f32_16x16x32_bf16 a[92:95], a[180:183], a[120:123], a[92:95]\n"
    "ds_read_b128 a[148:151], v20 offset:8192\n"
    "v_mfma_f32_16x16x32_bf16 a[12:15], a[168:171], a[124:127], a[12:15]\n"
    "ds_read_b128 a[152:155], v20 offset:12288\n"
    "v_mfma_f32_16x16x32_bf16 a[40:43], a[172:175], a[124:127], a[40:43]\n"
    "ds_read_b128 a[156:159], v20 offset:16384\n"
    "v_mfma_f32_16x16x32_bf16 a[68:71], a[176:179], a[124:127], a[68:71]\n"
    "ds_read_b128 a[160:163], v20 offset:20480\n"
    "v_mfma_f32_16x16x32_bf16 a[96:99], a[180:183], a[124:127], a[96:99]\n"
    "ds_read_b128 a[164:167], v20 offset:24576\n"
    "v_mfma_f32_16x16x32_bf16 a[16:19], a[168:171], a[128:131], a[16:19]\n"
    "s_add_u32 s74, s64, 256\n"
    "v_mfma_f32_16x16x32_bf16 a[44:47], a[172:175], a[128:131], a[44:47]\n"
    "s_mov_b32 m0, s67\n"
    "v_mfma_f32_16x16x32_bf16 a[72:75], a[176:179], a[128:131], a[72:75]\n"
    "buffer_load_dwordx4 v16, s[44:47], s74 offen offset:0 lds\n"
    "buffer_load_dwordx4 v17, s[44:47], s74 offen offset:1024 lds\n"
    "v_mfma_f32_16x16x32_bf16 a[100:103], a[180:183], a[128:131], a[100:103]\n"
    "buffer_load_dwordx4 v18, s[44:47], s74 offen offset:2048 lds\n"
    "v_mfma_f32_16x16x32_bf16 a[20:23], a[168:171], a[132:135], a[20:23]\n"
    "buffer_load_dwordx4 v19, s[44:47], s74 offen offset:3072 lds\n"
    "v_mfma_f32_16x16x32_bf16 a[48:51], a[172:175], a[132:135], a[48:51]\n"
    "s_add_u32 s64, s64, 32768\n"
    "v_mfma_f32_16x16x32_bf16 a[76:79], a[176:179], a[132:135], a[76:79]\n"
    "v_mfma_f32_16x16x32_bf16 a[104:107], a[180:183], a[132:135], a[104:107]\n"
    "v_mfma_f32_16x16x32_bf16 a[24:27], a[168:171], a[136:139], a[24:27]\n"
    "v_mfma_f32_16x16x32_bf16 a[52:55], a[172:175], a[136:139], a[52:55]\n"
    "v_mfma_f32_16x16x32_bf16 a[80:83], a[176:179], a[136:139], a[80:83]\n"
    "v_mfma_f32_16x16x32_bf16 a[108:111], a[180:183], a[136:139], a[108:111]\n"
    "s_waitcnt lgkmcnt(0)\n"
    "s_waitcnt vmcnt(4)\n"
    "v_mfma_f32_16x16x32_bf16 v[32:35], a[184:187], a[140:143], v[32:35]\n"
    "ds_read_b64_tr_b16 a[168:169], v8 offset:8192\n"
    "v_mfma_f32_16x16x32_bf16 v[60:63], a[188:191], a[140:143], v[60:63]\n"
    "v_mfma_f32_16x16x32_bf16 v[88:91], a[192:195], a[140:143], v[88:91]\n"
    "ds_read_b64_tr_b16 a[170:171], v8 offset:10240\n"
    "v_mfma_f32_16x16x32_bf16 v[116:119], a[196:199], a[140:143], v[116:119]\n"
    "ds_read_b64_tr_b16 a[172:173], v9 offset:8192\n"
    "v_mfma_f32_16x16x32_bf16 v[36:39], a[184:187], a[144:147], v[36:39]\n"
    "v_mfma_f32_16x16x32_bf16 v[64:67], a[188:191], a[144:147], v[64:67]\n"
    "ds_read_b64_tr_b16 a[174:175], v9 offset:10240\n"
    "v_mfma_f32_16x16x32_bf16 v[92:95], a[192:195], a[144:147], v[92:95]\n"
    "v_mfma_f32_16x16x32_bf16 v[120:123], a[196:199], a[144:147], v[120:123]\n"
    "ds_read_b64_tr_b16 a[176:177], v10 offset:8192\n"
    "v_mfma_f32_16x16x32_bf16 v[40:43], a[184:187], a[148:151], v[40:43]\n"
    "ds_read_b64_tr_b16 a[178:179], v10 offset:10240\n"
    "v_mfma_f32_16x16x32_bf16 v[68:71], a[188:191], a[148:151], v[68:71]\n"
    "v_mfma_f32_16x16x32_bf16 v[96:99], a[192:195], a[148:151], v[96:99]\n"
    "ds_read_b64_tr_b16 a[180:181], v11 offset:8192\n"
    "v_mfma_f32_16x16x32_bf16 v[124:127], a[196:199], a[148:151], v[124:127]\n"
    "ds_read_b64_tr_b16 a[182:183], v11 offset:10240\n"
    "v_mfma_f32_16x16x32_bf16 v[44:47], a[184:187], a[152:155], v[44:47]\n"
    "v_mfma_f32_16x16x32_bf16 v[72:75], a[188:191], a[152:155], v[72:75]\n"
    "s_add_u32 s74, s64, 0\n"
    "v_mfma_f32_16x16x32_bf16 v[100:103], a[192:195], a[152:155], v[100:103]\n"
    "v_mfma_f32_16x16x32_bf16 v[128:131], a[196:199], a[152:155], v[128:131]\n"
    "s_add_u32 m0, s66, 0\n"
    "v_mfma_f32_16x16x32_bf16 v[48:51], a[184:187], a[156:159], v[48:51]\n"
    "buffer_load_dwordx4 v16, s[44:47], s74 offen offset:0 lds\n"
    "v_mfma_f32_16x16x32_bf16 v[76:79], a[188:191], a[156:159], v[76:79]\n"
    "v_mfma_f32_16x16x32_bf16 v[104:107], a[192:195], a[156:159], v[104:107]\n"
    "buffer_load_dwordx4 v17, s[44:47], s74 offen offset:1024 lds\n"
    "v_mfma_f32_16x16x32_bf16 v[132:135], a[196:199], a[156:159], v[132:135]\n"
    "buffer_load_dwordx4 v18, s[44:47], s74 offen offset:2048 lds\n"
    "v_mfma_f32_16x16x32_bf16 v[52:55], a[184:187], a[160:163], v[52:55]\n"
    "v_mfma_f32_16x16x32_bf16 v[80:83], a[188:191], a[160:163], v[80:83]\n"
    "buffer_load_dwordx4 v19, s[44:47], s74 offen offset:3072 lds\n"
    "v_mfma_f32_16x16x32_bf16 v[108:111], a[192:195], a[160:163], v[108:111]\n"
    "v_mfma_f32_16x16x32_bf16 v[136:139], a[196:199], a[160:163], v[136:139]\n"
    "v_mfma_f32_16x16x32_bf16 v[56:59], a[184:187], a[164:167], v[56:59]\n"
    "v_mfma_f32_16x16x32_bf16 v[84:87], a[188:191], a[164:167], v[84:87]\n"
    "v_mfma_f32_16x16x32_bf16 v[112:115], a[192:195], a[164:167], v[112:115]\n"
    "v_mfma_f32_16x16x32_bf16 v[140:143], a[196:199], a[164:167], v[140:143]\n"
    "s_waitcnt lgkmcnt(0)\n"
    "s_waitcnt vmcnt(4)\n"
    "v_mfma_f32_16x16x32_bf16 v[144:147], a[168:171], a[140:143], v[144:147]\n"
    "ds_read_b64_tr_b16 a[184:185], v12 offset:0\n"
    "v_mfma_f32_16x16x32_bf16 v[172:175], a[172:175], a[140:143], v[172:175]\n"
    "v_mfma_f32_16x16x32_bf16 v[200:203], a[176:179], a[140:143], v[200:203]\n"
    "ds_read_b64_tr_b16 a[186:187], v12 offset:2048\n"
    "v_mfma_f32_16x16x32_bf16 v[228:231], a[180:183], a[140:143], v[228:231]\n"
    "ds_read_b64_tr_b16 a[188:189], v13 offset:0\n"
    "v_mfma_f32_16x16x32_bf16 v[148:151], a[168:171], a[144:147], v[148:151]\n"
    "v_mfma_f32_16x16x32_bf16 v[176:179], a[172:175], a[144:147], v[176:179]\n"
    "ds_read_b64_tr_b16 a[190:191], v13 offset:2048\n"
    "v_mfma_f32_16x16x32_bf16 v[204:207], a[176:179], a[144:147], v[204:207]\n"
    "v_mfma_f32_16x16x32_bf16 v[232:235], a[180:183], a[144:147], v[232:235]\n"
    "ds_read_b64_tr_b16 a[192:193], v14 offset:0\n"
    "v_mfma_f32_16x16x32_bf16 v[152:155], a[168:171], a[148:151], v[152:155]\n"
    "ds_read_b64_tr_b16 a[194:195], v14 offset:2048\n"
    "v_mfma_f32_16x16x32_bf16 v[180:183], a[172:175], a[148:151], v[180:183]\n"
    "v_mfma_f32_16x16x32_bf16 v[208:211], a[176:179], a[148:151], v[208:211]\n"
    "ds_read_b64_tr_b16 a[196:197], v15 offset:0\n"
    "v_mfma_f32_16x16x32_bf16 v[236:239], a[180:183], a[148:151], v[236:239]\n"
    "ds_read_b64_tr_b16 a[198:199], v15 offset:2048\n"
    "v_mfma_f32_16x16x32_bf16 v[156:159], a[168:171], a[152:155], v[156:159]\n"
    "v_mfma_f32_16x16x32_bf16 v[184:187], a[172:175], a[152:155], v[184:187]\n"
    "s_add_u32 s74, s64, 128\n"
    "v_mfma_f32_16x16x32_bf16 v[212:215], a[176:179], a[152:155], v[212:215]\n"
    "v_mfma_f32_16x16x32_bf16 v[240:243], a[180:183], a[152:155], v[240:243]\n"
    "s_add_u32 m0, s66, 8192\n"
    "v_mfma_f32_16x16x32_bf16 v[160:163], a[168:171], a[156:159], v[160:163]\n"
    "buffer_load_dwordx4 v16, s[44:47], s74 offen offset:0 lds\n"
    "v_mfma_f32_16x16x32_bf16 v[188:191], a[172:175], a[156:159], v[188:191]\n"
    "v_mfma_f32_16x16x32_bf16 v[216:219], a[176:179], a[156:159], v[216:219]\n"
    "buffer_load_dwordx4 v17, s[44:47], s74 offen offset:1024 lds\n"
    "v_mfma_f32_16x16x32_bf16 v[244:247], a[180:183], a[156:159], v[244:247]\n"
    "buffer_load_dwordx4 v18, s[44:47], s74 offen offset:2048 lds\n"
    "v_mfma_f32_16x16x32_bf16 v[164:167], a[168:171], a[160:163], v[164:167]\n"
    "v_mfma_f32_16x16x32_bf16 v[192:195], a[172:175], a[160:163], v[192:195]\n"
    "buffer_load_dwordx4 v19, s[44:47], s74 offen offset:3072 lds\n"
    "v_mfma_f32_16x16x32_bf16 v[220:223], a[176:179], a[160:163], v[220:223]\n"
    "v_mfma_f32_16x16x32_bf16 v[248:251], a[180:183], a[160:163], v[248:251]\n"
    "v_mfma_f32_16x16x32_bf16 v[168:171], a[168:171], a[164:167], v[168:171]\n"
    "v_mfma_f32_16x16x32_bf16 v[196:199], a[172:175], a[164:167], v[196:199]\n"
    "v_mfma_f32_16x16x32_bf16 v[224:227], a[176:179], a[164:167], v[224:227]\n"
    "v_mfma_f32_16x16x32_bf16 v[252:255], a[180:183], a[164:167], v[252:255]\n"
    "s_waitcnt lgkmcnt(0)\n"
    "s_waitcnt vmcnt(4)\n"
    "v_mfma_f32_16x16x32_bf16 a[0:3], a[184:187], a[140:143], a[0:3]\n"
    "ds_read_b64_tr_b16 a[168:169], v8 offset:0\n"
    "v_mfma_f32_16x16x32_bf16 a[28:31], a[188:191], a[140:143], a[28:31]\n"
    "v_mfma_f32_16x16x32_bf16 a[56:59], a[192:195], a[140:143], a[56:59]\n"
    "ds_read_b64_tr_b16 a[170:171], v8 offset:2048\n"
    "v_mfma_f32_16x16x32_bf16 a[84:87], a[196:199], a[140:143], a[84:87]\n"
    "ds_read_b64_tr_b16 a[172:173], v9 offset:0\n"
    "v_mfma_f32_16x16x32_bf16 a[4:7], a[184:187], a[144:147], a[4:7]\n"
    "v_mfma_f32_16x16x32_bf16 a[32:35], a[188:191], a[144:147], a[32:35]\n"
    "ds_read_b64_tr_b16 a[174:175], v9 offset:2048\n"
    "v_mfma_f32_16x16x32_bf16 a[60:63], a[192:195], a[144:147], a[60:63]\n"
    "ds_read_b64_tr_b16 a[176:177], v10 offset:0\n"
    "v_mfma_f32_16x16x32_bf16 a[88:91], a[196:199], a[144:147], a[88:91]\n"
    "v_mfma_f32_16x16x32_bf16 a[8:11], a[184:187], a[148:151], a[8:11]\n"
    "ds_read_b64_tr_b16 a[178:179], v10 offset:2048\n"
    "v_mfma_f32_16x16x32_bf16 a[36:39], a[188:191], a[148:151], a[36:39]\n"
    "ds_read_b64_tr_b16 a[180:181], v11 offset:0\n"
    "v_mfma_f32_16x16x32_bf16 a[64:67], a[192:195], a[148:151], a[64:67]\n"
    "v_mfma_f32_16x16x32_bf16 a[92:95], a[196:199], a[148:151], a[92:95]\n"
    "ds_read_b64_tr_b16 a[182:183], v11 offset:2048\n"
    "v_mfma_f32_16x16x32_bf16 a[12:15], a[184:187], a[152:155], a[12:15]\n"
    "s_add_u32 s74, s64, 256\n"
    "v_mfma_f32_16x16x32_bf16 a[40:43], a[188:191], a[152:155], a[40:43]\n"
    "v_mfma_f32_16x16x32_bf16 a[68:71], a[192:195], a[152:155], a[68:71]\n"
    "s_mov_b32 m0, s67\n"
    "v_mfma_f32_16x16x32_bf16 a[96:99], a[196:199], a[152:155], a[96:99]\n"
    "buffer_load_dwordx4 v16, s[44:47], s74 offen offset:0 lds\n"
    "v_mfma_f32_16x16x32_bf16 a[16:19], a[184:187], a[156:159], a[16:19]\n"
    "v_mfma_f32_16x16x32_bf16 a[44:47], a[188:191], a[156:159], a[44:47]\n"
    "buffer_load_dwordx4 v17, s[44:47], s74 offen offset:1024 lds\n"
    "v_mfma_f32_16x16x32_bf16 a[72:75], a[192:195], a[156:159], a[72:75]\n"
    "buffer_load_dwordx4 v18, s[44:47], s74 offen offset:2048 lds\n"
    "v_mfma_f32_16x16x32_bf16 a[100:103], a[196:199], a[156:159], a[100:103]\n"
    "v_mfma_f32_16x16x32_bf16 a[20:23], a[184:187], a[160:163], a[20:23]\n"
    "buffer_load_dwordx4 v19, s[44:47], s74 offen offset:3072 lds\n"
    "v_mfma_f32_16x16x32_bf16 a[48:51], a[188:191], a[160:163], a[48:51]\n"
    "s_add_u32 s64, s64, 32768\n"
    "v_mfma_f32_16x16x32_bf16 a[76:79], a[192:195], a[160:163], a[76:79]\n"
    "v_mfma_f32_16x16x32_bf16 a[104:107], a[196:199], a[160:163], a[104:107]\n"
    "v_mfma_f32_16x16x32_bf16 a[24:27], a[184:187], a[164:167], a[24:27]\n"
    "v_mfma_f32_16x16x32_bf16 a[52:55], a[188:191], a[164:167], a[52:55]\n"
    "v_mfma_f32_16x16x32_bf16 a[80:83], a[192:195], a[164:167], a[80:83]\n"
    "v_mfma_f32_16x16x32_bf16 a[108:111], a[196:199], a[164:167], a[108:111]\n"
    "s_waitcnt lgkmcnt(0)\n"
    "v_add_u32 v20, s79, v20\n"
    "s_mov_b32 s73, 1\n"
    "s_branch .LX_trip_%=\n"
    ".LX_tail2_%=:\n"
    "s_waitcnt vmcnt(4)\n"
    "v_mfma_f32_16x16x32_bf16 v[32:35], a[168:171], a[112:115], v[32:35]\n"
    "ds_read_b64_tr_b16 a[184:185], v8 offset:8192\n"
    "v_mfma_f32_16x16x32_bf16 v[60:63], a[172:175], a[112:115], v[60:63]\n"
    "v_mfma_f32_16x16x32_bf16 v[88:91], a[176:179], a[112:115], v[88:91]\n"
    "ds_read_b64_tr_b16 a[186:187], v8 offset:10240\n"
    "v_mfma_f32_16x16x32_bf16 v[116:119], a[180:183], a[112:115], v[116:119]\n"
    "ds_read_b64_tr_b16 a[188:189], v9 offset:8192\n"
    "v_mfma_f32_16x16x32_bf16 v[36:39], a[168:171], a[116:119], v[36:39]\n"
    "v_mfma_f32_16x16x32_bf16 v[64:67], a[172:175], a[116:119], v[64:67]\n"
    "ds_read_b64_tr_b16 a[190:191], v9 offset:10240\n"
    "v_mfma_f32_16x16x32_bf16 v[92:95], a[176:179], a[116:119], v[92:95]\n"
    "v_mfma_f32_16x16x32_bf16 v[120:123], a[180:183], a[116:119], v[120:123]\n"
    "ds_read_b64_tr_b16 a[192:193], v10 offset:8192\n"
    "v_mfma_f32_16x16x32_bf16 v[40:43], a[168:171], a[120:123], v[40:43]\n"
    "ds_read_b64_tr_b16 a[194:195], v10 offset:10240\n"
    "v_mfma_f32_16x16x32_bf16 v[68:71], a[172:175], a[120:123], v[68:71]\n"
    "v_mfma_f32_16x16x32_bf16 v[96:99], a[176:179], a[120:123], v[96:99]\n"
    "ds_read_b64_tr_b16 a[196:197], v11 offset:8192\n"
    "v_mfma_f32_16x16x32_bf16 v[124:127], a[180:183], a[120:123], v[124:127]\n"
    "ds_read_b64_tr_b16 a[198:199], v11 offset:10240\n"
    "v_mfma_f32_16x16x32_bf16 v[44:47], a[168:171], a[124:127], v[44:47]\n"
    "v_mfma_f32_16x16x32_bf16 v[72:75], a[172:175], a[124:127], v[72:75]\n"
    "s_add_u32 s74, s64, 0\n"
    "v_mfma_f32_16x16x32_bf16 v[100:103], a[176:179], a[124:127], v[100:103]\n"
    "v_mfma_f32_16x16x32_bf16 v[128:131], a[180:183], a[124:127], v[128:131]\n"
    "s_add_u32 m0, s66, 0\n"
    "v_mfma_f32_16x16x32_bf16 v[48:51], a[168:171], a[128:131], v[48:51]\n"
    "buffer_load_dwordx4 v16, s[44:47], s74 offen offset:0 lds\n"
    "v_mfma_f32_16x16x32_bf16 v[76:79], a[172:175], a[128:131], v[76:79]\n"
    "v_mfma_f32_16x16x32_bf16 v[104:107], a[176:179], a[128:131], v[104:107]\n"
    "buffer_load_dwordx4 v17, s[44:47], s74 offen offset:1024 lds\n"
    "v_mfma_f32_16x16x32_bf16 v[132:135], a[180:183], a[128:131], v[132:135]\n"
    "buffer_load_dwordx4 v18, s[44:47], s74 offen offset:2048 lds\n"
    "v_mfma_f32_16x16x32_bf16 v[52:55], a[168:171], a[132:135], v[52:55]\n"
    "v_mfma_f32_16x16x32_bf16 v[80:83], a[172:175], a[132:135], v[80:83]\n"
    "buffer_load_dwordx4 v19, s[44:47], s74 offen offset:3072 lds\n"
    "v_mfma_f32_16x16x32_bf16 v[108:111], a[176:179], a[132:135], v[108:111]\n"
    "v_mfma_f32_16x16x32_bf16 v[136:139], a[180:183], a[132:135], v[136:139]\n"
    "v_mfma_f32_16x16x32_bf16 v[56:59], a[168:171], a[136:139], v[56:59]\n"
    "v_mfma_f32_16x16x32_bf16 v[84:87], a[172:175], a[136:139], v[84:87]\n"
    "v_mfma_f32_16x16x32_bf16 v[112:115], a[176:179], a[136:139], v[112:115]\n"
    "v_mfma_f32_16x16x32_bf16 v[140:143], a[180:183], a[136:139], v[140:143]\n"
    "s_waitcnt lgkmcnt(0)\n"
    "s_waitcnt vmcnt(4)\n"
    "v_mfma_f32_16x16x32_bf16 v[144:147], a[184:187], a[112:115], v[144:147]\n"
    "ds_read_b64_tr_b16 a[168:169], v12 offset:0\n"
    "v_mfma_f32_16x16x32_bf16 v[172:175], a[188:191], a[112:115], v[172:175]\n"
    "v_mfma_f32_16x16x32_bf16 v[200:203], a[192:195], a[112:115], v[200:203]\n"
    "ds_read_b64_tr_b16 a[170:171], v12 offset:2048\n"
    "v_mfma_f32_16x16x32_bf16 v[228:231], a[196:199], a[112:115], v[228:231]\n"
    "ds_read_b64_tr_b16 a[172:173], v13 offset:0\n"
    "v_mfma_f32_16x16x32_bf16 v[148:151], a[184:187], a[116:119], v[148:151]\n"
    "v_mfma_f32_16x16x32_bf16 v[176:179], a[188:191], a[116:119], v[176:179]\n"
    "ds_read_b64_tr_b16 a[174:175], v13 offset:2048\n"
    "v_mfma_f32_16x16x32_bf16 v[204:207], a[192:195], a[116:119], v[204:207]\n"
    "v_mfma_f32_16x16x32_bf16 v[232:235], a[196:199], a[116:119], v[232:235]\n"
    "ds_read_b64_tr_b16 a[176:177], v14 offset:0\n"
    "v_mfma_f32_16x16x32_bf16 v[152:155], a[184:187], a[120:123], v[152:155]\n"
    "ds_read_b64_tr_b16 a[178:179], v14 offset:2048\n"
    "v_mfma_f32_16x16x32_bf16 v[180:183], a[188:191], a[120:123], v[180:183]\n"
    "v_mfma_f32_16x16x32_bf16 v[208:211], a[192:195], a[120:123], v[208:211]\n"
    "ds_read_b64_tr_b16 a[180:181], v15 offset:0\n"
    "v_mfma_f32_16x16x32_bf16 v[236:239], a[196:199], a[120:123], v[236:239]\n"
    "ds_read_b64_tr_b16 a[182:183], v15 offset:2048\n"
    "v_mfma_f32_16x16x32_bf16 v[156:159], a[184:187], a[124:127], v[156:159]\n"
    "v_mfma_f32_16x16x32_bf16 v[184:187], a[188:191], a[124:127], v[184:187]\n"
    "s_add_u32 s74, s64, 128\n"
    "v_mfma_f32_16x16x32_bf16 v[212:215], a[192:195], a[124:127], v[212:215]\n"
    "v_mfma_f32_16x16x32_bf16 v[240:243], a[196:199], a[124:127], v[240:243]\n"
    "s_add_u32 m0, s66, 8192\n"
    "v_mfma_f32_16x16x32_bf16 v[160:163], a[184:187], a[128:131], v[160:163]\n"
    "buffer_load_dwordx4 v16, s[44:47], s74 offen offset:0 lds\n"
    "v_mfma_f32_16x16x32_bf16 v[188:191], a[188:191], a[128:131], v[188:191]\n"
    "v_mfma_f32_16x16x32_bf16 v[216:219], a[192:195], a[128:131], v[216:219]\n"
    "buffer_load_dwordx4 v17, s[44:47], s74 offen offset:1024 lds\n"
    "v_mfma_f32_16x16x32_bf16 v[244:247], a[196:199], a[128:131], v[244:247]\n"
    "buffer_load_dwordx4 v18, s[44:47], s74 offen offset:2048 lds\n"
    "v_mfma_f32_16x16x32_bf16 v[164:167], a[184:187], a[132:135], v[164:167]\n"
    "v_mfma_f32_16x16x32_bf16 v[192:195], a[188:191], a[132:135], v[192:195]\n"
    "buffer_load_dwordx4 v19, s[44:47], s74 offen offset:3072 lds\n"
    "v_mfma_f32_16x16x32_bf16 v[220:223], a[192:195], a[132:135], v[220:223]\n"
    "v_mfma_f32_16x16x32_bf16 v[248:251], a[196:199], a[132:135], v[248:251]\n"
    "v_mfma_f32_16x16x32_bf16 v[168:171], a[184:187], a[136:139], v[168:171]\n"
    "v_mfma_f32_16x16x32_bf16 v[196:199], a[188:191], a[136:139], v[196:199]\n"
    "v_mfma_f32_16x16x32_bf16 v[224:227], a[192:195], a[136:139], v[224:227]\n"
    "v_mfma_f32_16x16x32_bf16 v[252:255], a[196:199], a[136:139], v[252:255]\n"
    "s_waitcnt lgkmcnt(0)\n"
    "s_waitcnt vmcnt(4)\n"
    "v_mfma_f32_16x16x32_bf16 a[0:3], a[168:171], a[112:115], a[0:3]\n"
    "v_add_u32 v20, 1024, v20\n"
    "v_mfma_f32_16x16x32_bf16 a[28:31], a[172:175], a[112:115], a[28:31]\n"
    "ds_read_b64_tr_b16 a[184:185], v8 offset:0\n"
    "v_mfma_f32_16x16x32_bf16 a[56:59], a[176:179], a[112:115], a[56:59]\n"
    "ds_read_b64_tr_b16 a[186:187], v8 offset:2048\n"
    "v_mfma_f32_16x16x32_bf16 a[84:87], a[180:183], a[112:115], a[84:87]\n"
    "ds_read_b64_tr_b16 a[188:189], v9 offset:0\n"
    "v_mfma_f32_16x16x32_bf16 a[4:7], a[168:171], a[116:119], a[4:7]\n"
    "ds_read_b64_tr_b16 a[190:191], v9 offset:2048\n"
    "v_mfma_f32_16x16x32_bf16 a[32:35], a[172:175], a[116:119], a[32:35]\n"
    "ds_read_b64_tr_b16 a[192:193], v10 offset:0\n"
    "v_mfma_f32_16x16x32_bf16 a[60:63], a[176:179], a[116:119], a[60:63]\n"
    "ds_read_b64_tr_b16 a[194:195], v10 offset:2048\n"
    "v_mfma_f32_16x16x32_bf16 a[88:91], a[180:183], a[116:119], a[88:91]\n"
    "ds_read_b64_tr_b16 a[196:197], v11 offset:0\n"
    "v_mfma_f32_16x16x32_bf16 a[8:11], a[168:171], a[120:123], a[8:11]\n"
    "ds_read_b64_tr_b16 a[198:199], v11 offset:2048\n"
    "v_mfma_f32_16x16x32_bf16 a[36:39], a[172:175], a[120:123], a[36:39]\n"
    "ds_read_b128 a[140:143], v20 offset:0\n"
    "v_mfma_f32_16x16x32_bf16 a[64:67], a[176:179], a[120:123], a[64:67]\n"
    "ds_read_b128 a[144:147], v20 offset:4096\n"
    "v_mfma_f32_16x16x32_bf16 a[92:95], a[180:183], a[120:123], a[92:95]\n"
    "ds_read_b128 a[148:151], v20 offset:8192\n"
    "v_mfma_f32_16x16x32_bf16 a[12:15], a[168:171], a[124:127], a[12:15]\n"
    "ds_read_b128 a[152:155], v20 offset:12288\n"
    "v_mfma_f32_16x16x32_bf16 a[40:43], a[172:175], a[124:127], a[40:43]\n"
    "ds_read_b128 a[156:159], v20 offset:16384\n"
    "v_mfma_f32_16x16x32_bf16 a[68:71], a[176:179], a[124:127], a[68:71]\n"
    "ds_read_b128 a[160:163], v20 offset:20480\n"
    "v_mfma_f32_16x16x32_bf16 a[96:99], a[180:183], a[124:127], a[96:99]\n"
    "ds_read_b128 a[164:167], v20 offset:24576\n"
    "v_mfma_f32_16x16x32_bf16 a[16:19], a[168:171], a[128:131], a[16:19]\n"
    "s_add_u32 s74, s64, 256\n"
    "v_mfma_f32_16x16x32_bf16 a[44:47], a[172:175], a[128:131], a[44:47]\n"
    "s_mov_b32 m0, s67\n"
    "v_mfma_f32_16x16x32_bf16 a[72:75], a[176:179], a[128:131], a[72:75]\n"
    "buffer_load_dwordx4 v16, s[44:47], s74 offen offset:0 lds\n"
    "buffer_load_dwordx4 v17, s[44:47], s74 offen offset:1024 lds\n"
    "v_mfma_f32_16x16x32_bf16 a[100:103], a[180:183], a[128:131], a[100:103]\n"
    "buffer_load_dwordx4 v18, s[44:47], s74 offen offset:2048 lds\n"
    "v_mfma_f32_16x16x32_bf16 a[20:23], a[168:171], a[132:135], a[20:23]\n"
    "buffer_load_dwordx4 v19, s[44:47], s74 offen offset:3072 lds\n"
    "v_mfma_f32_16x16x32_bf16 a[48:51], a[172:175], a[132:135], a[48:51]\n"
    "s_add_u32 s64, s64, 32768\n"
    "v_mfma_f32_16x16x32_bf16 a[76:79], a[176:179], a[132:135], a[76:79]\n"
    "v_mfma_f32_16x16x32_bf16 a[104:107], a[180:183], a[132:135], a[104:107]\n"
    "v_mfma_f32_16x16x32_bf16 a[24:27], a[168:171], a[136:139], a[24:27]\n"
    "v_mfma_f32_16x16x32_bf16 a[52:55], a[172:175], a[136:139], a[52:55]\n"
    "v_mfma_f32_16x16x32_bf16 a[80:83], a[176:179], a[136:139], a[80:83]\n"
    "v_mfma_f32_16x16x32_bf16 a[108:111], a[180:183], a[136:139], a[108:111]\n"
    "s_waitcnt lgkmcnt(0)\n"
    "s_waitcnt vmcnt(4)\n"
    "v_mfma_f32_16x16x32_bf16 v[32:35], a[184:187], a[140:143], v[32:35]\n"
    "v_mfma_f32_16x16x32_bf16 v[60:63], a[188:191], a[140:143], v[60:63]\n"
    "ds_read_b64_tr_b16 a[168:169], v8 offset:8192\n"
    "v_mfma_f32_16x16x32_bf16 v[88:91], a[192:195], a[140:143], v[88:91]\n"
    "v_mfma_f32_16x16x32_bf16 v[116:119], a[196:199], a[140:143], v[116:119]\n"
    "v_mfma_f32_16x16x32_bf16 v[36:39], a[184:187], a[144:147], v[36:39]\n"
    "ds_read_b64_tr_b16 a[170:171], v8 offset:10240\n"
    "v_mfma_f32_16x16x32_bf16 v[64:67], a[188:191], a[144:147], v[64:67]\n"
    "v_mfma_f32_16x16x32_bf16 v[92:95], a[192:195], a[144:147], v[92:95]\n"
    "ds_read_b64_tr_b16 a[172:173], v9 offset:8192\n"
    "v_mfma_f32_16x16x32_bf16 v[120:123], a[196:199], a[144:147], v[120:123]\n"
    "v_mfma_f32_16x16x32_bf16 v[40:43], a[184:187], a[148:151], v[40:43]\n"
    "v_mfma_f32_16x16x32_bf16 v[68:71], a[188:191], a[148:151], v[68:71]\n"
    "ds_read_b64_tr_b16 a[174:175], v9 offset:10240\n"
    "v_mfma_f32_16x16x32_bf16 v[96:99], a[192:195], a[148:151], v[96:99]\n"
    "v_mfma_f32_16x16x32_bf16 v[124:127], a[196:199], a[148:151], v[124:127]\n"
    "v_mfma_f32_16x16x32_bf16 v[44:47], a[184:187], a[152:155], v[44:47]\n"
    "ds_read_b64_tr_b16 a[176:177], v10 offset:8192\n"
    "v_mfma_f32_16x16x32_bf16 v[72:75], a[188:191], a[152:155], v[72:75]\n"
    "v_mfma_f32_16x16x32_bf16 v[100:103], a[192:195], a[152:155], v[100:103]\n"
    "v_mfma_f32_16x16x32_bf16 v[128:131], a[196:199], a[152:155], v[128:131]\n"
    "ds_read_b64_tr_b16 a[178:179], v10 offset:10240\n"
    "v_mfma_f32_16x16x32_bf16 v[48:51], a[184:187], a[156:159], v[48:51]\n"
    "v_mfma_f32_16x16x32_bf16 v[76:79], a[188:191], a[156:159], v[76:79]\n"
    "v_mfma_f32_16x16x32_bf16 v[104:107], a[192:195], a[156:159], v[104:107]\n"
    "ds_read_b64_tr_b16 a[180:181], v11 offset:8192\n"
    "v_mfma_f32_16x16x32_bf16 v[132:135], a[196:199], a[156:159], v[132:135]\n"
    "v_mfma_f32_16x16x32_bf16 v[52:55], a[184:187], a[160:163], v[52:55]\n"
    "ds_read_b64_tr_b16 a[182:183], v11 offset:10240\n"
    "v_mfma_f32_16x16x32_bf16 v[80:83], a[188:191], a[160:163], v[80:83]\n"
    "v_mfma_f32_16x16x32_bf16 v[108:111], a[192:195], a[160:163], v[108:111]\n"
    "v_mfma_f32_16x16x32_bf16 v[136:139], a[196:199], a[160:163], v[136:139]\n"
    "v_mfma_f32_16x16x32_bf16 v[56:59], a[184:187], a[164:167], v[56:59]\n"
    "v_mfma_f32_16x16x32_bf16 v[84:87], a[188:191], a[164:167], v[84:87]\n"
    "v_mfma_f32_16x16x32_bf16 v[112:115], a[192:195], a[164:167], v[112:115]\n"
    "v_mfma_f32_16x16x32_bf16 v[140:143], a[196:199], a[164:167], v[140:143]\n"
    "s_waitcnt lgkmcnt(0)\n"
    "s_waitcnt vmcnt(0)\n"
    "v_mfma_f32_16x16x32_bf16 v[144:147], a[168:171], a[140:143], v[144:147]\n"
    "ds_read_b64_tr_b16 a[184:185], v12 offset:0\n"
    "ds_read_b64_tr_b16 a[186:187], v12 offset:2048\n"
    "ds_read_b64_tr_b16 a[188:189], v13 offset:0\n"
    "v_mfma_f32_16x16x32_bf16 v[172:175], a[172:175], a[140:143], v[172:175]\n"
    "ds_read_b64_tr_b16 a[190:191], v13 offset:2048\n"
    "ds_read_b64_tr_b16 a[192:193], v14 offset:0\n"
    "ds_read_b64_tr_b16 a[194:195], v14 offset:2048\n"
    "ds_read_b64_tr_b16 a[196:197], v15 offset:0\n"
    "v_mfma_f32_16x16x32_bf16 v[200:203], a[176:179], a[140:143], v[200:203]\n"
    "ds_read_b64_tr_b16 a[198:199], v15 offset:2048\n"
    "v_cvt_pk_bf16_f32 v32, v32, v33\n"
    "v_cvt_pk_bf16_f32 v33, v34, v35\n"
    "v_mfma_f32_16x16x32_bf16 v[228:231], a[180:183], a[140:143], v[228:231]\n"
    "v_cvt_pk_bf16_f32 v34, v60, v61\n"
    "v_cvt_pk_bf16_f32 v35, v62, v63\n"
    "s_add_u32 s77, s69, 0\n"
    "v_mfma_f32_16x16x32_bf16 v[148:151], a[168:171], a[144:147], v[148:151]\n"
    "buffer_store_dwordx4 v[32:35], v24, s[40:43], s77 offen sc1\n"
    "v_cvt_pk_bf16_f32 v36, v36, v37\n"
    "v_cvt_pk_bf16_f32 v37, v38, v39\n"
    "v_mfma_f32_16x16x32_bf16 v[176:179], a[172:175], a[144:147], v[176:179]\n"
    "v_cvt_pk_bf16_f32 v38, v64, v65\n"
    "v_cvt_pk_bf16_f32 v39, v66, v67\n"
    "s_add_u32 s77, s69, 1024\n"
    "buffer_store_dwordx4 v[36:39], v24, s[40:43], s77 offen sc1\n"
    "v_mfma_f32_16x16x32_bf16 v[204:207], a[176:179], a[144:147], v[204:207]\n"
    "v_cvt_pk_bf16_f32 v40, v40, v41\n"
    "v_cvt_pk_bf16_f32 v41, v42, v43\n"
    "v_cvt_pk_bf16_f32 v42, v68, v69\n"
    "v_mfma_f32_16x16x32_bf16 v[232:235], a[180:183], a[144:147], v[232:235]\n"
    "v_cvt_pk_bf16_f32 v43, v70, v71\n"
    "s_add_u32 s77, s69, 2048\n"
    "buffer_store_dwordx4 v[40:43], v24, s[40:43], s77 offen sc1\n"
    "v_mfma_f32_16x16x32_bf16 v[152:155], a[168:171], a[148:151], v[152:155]\n"
    "v_cvt_pk_bf16_f32 v44, v44, v45\n"
    "v_cvt_pk_bf16_f32 v45, v46, v47\n"
    "v_cvt_pk_bf16_f32 v46, v72, v73\n"
    "v_cvt_pk_bf16_f32 v47, v74, v75\n"
    "v_mfma_f32_16x16x32_bf16 v[180:183], a[172:175], a[148:151], v[180:183]\n"
    "s_add_u32 s77, s69, 3072\n"
    "buffer_store_dwordx4 v[44:47], v24, s[40:43], s77 offen sc1\n"
    "v_cvt_pk_bf16_f32 v48, v48, v49\n"
    "v_mfma_f32_16x16x32_bf16 v[208:211], a[176:179], a[148:151], v[208:211]\n"
    "v_cvt_pk_bf16_f32 v49, v50, v51\n"
    "v_cvt_pk_bf16_f32 v50, v76, v77\n"
    "v_cvt_pk_bf16_f32 v51, v78, v79\n"
    "v_mfma_f32_16x16x32_bf16 v[236:239], a[180:183], a[148:151], v[236:239]\n"
    "s_add_u32 s77, s69, 4096\n"
    "buffer_store_dwordx4 v[48:51], v24, s[40:43], s77 offen sc1\n"
    "v_cvt_pk_bf16_f32 v52, v52, v53\n"
    "v_mfma_f32_16x16x32_bf16 v[156:159], a[168:171], a[152:155], v[156:159]\n"
    "v_cvt_pk_bf16_f32 v53, v54, v55\n"
    "v_cvt_pk_bf16_f32 v54, v80, v81\n"
    "v_cvt_pk_bf16_f32 v55, v82, v83\n"
    "s_add_u32 s77, s69, 5120\n"
    "v_mfma_f32_16x16x32_bf16 v[184:187], a[172:175], a[152:155], v[184:187]\n"
    "buffer_store_dwordx4 v[52:55], v24, s[40:43], s77 offen sc1\n"
    "v_cvt_pk_bf16_f32 v56, v56, v57\n"
    "v_cvt_pk_bf16_f32 v57, v58, v59\n"
    "v_mfma_f32_16x16x32_bf16 v[212:215], a[176:179], a[152:155], v[212:215]\n"
    "v_cvt_pk_bf16_f32 v58, v84, v85\n"
    "v_cvt_pk_bf16_f32 v59, v86, v87\n"
    "s_add_u32 s77, s69, 6144\n"
    "v_mfma_f32_16x16x32_bf16 v[240:243], a[180:183], a[152:155], v[240:243]\n"
    "buffer_store_dwordx4 v[56:59], v24, s[40:43], s77 offen sc1\n"
    "v_cvt_pk_bf16_f32 v88, v88, v89\n"
    "v_cvt_pk_bf16_f32 v89, v90, v91\n"
    "v_cvt_pk_bf16_f32 v90, v116, v117\n"
    "v_mfma_f32_16x16x32_bf16 v[160:163], a[168:171], a[156:159], v[160:163]\n"
    "v_cvt_pk_bf16_f32 v91, v118, v119\n"
    "s_add_u32 s77, s69, 7168\n"
    "buffer_store_dwordx4 v[88:91], v24, s[40:43], s77 offen sc1\n"
    "v_mfma_f32_16x16x32_bf16 v[188:191], a[172:175], a[156:159], v[188:191]\n"
    "v_cvt_pk_bf16_f32 v92, v92, v93\n"
    "v_cvt_pk_bf16_f32 v93, v94, v95\n"
    "v_cvt_pk_bf16_f32 v94, v120, v121\n"
    "v_mfma_f32_16x16x32_bf16 v[216:219], a[176:179], a[156:159], v[216:219]\n"
    "v_cvt_pk_bf16_f32 v95, v122, v123\n"
    "s_add_u32 s77, s69, 8192\n"
    "buffer_store_dwordx4 v[92:95], v24, s[40:43], s77 offen sc1\n"
    "v_mfma_f32_16x16x32_bf16 v[244:247], a[180:183], a[156:159], v[244:247]\n"
    "v_cvt_pk_bf16_f32 v96, v96, v97\n"
    "v_cvt_pk_bf16_f32 v97, v98, v99\n"
    "v_cvt_pk_bf16_f32 v98, v124, v125\n"
    "v_cvt_pk_bf16_f32 v99, v126, v127\n"
    "v_mfma_f32_16x16x32_bf16 v[164:167], a[168:171], a[160:163], v[164:167]\n"
    "s_add_u32 s77, s69, 9216\n"
    "buffer_store_dwordx4 v[96:99], v24, s[40:43], s77 offen sc1\n"
    "v_cvt_pk_bf16_f32 v100, v100, v101\n"
    "v_mfma_f32_16x16x32_bf16 v[192:195], a[172:175], a[160:163], v[192:195]\n"
    "v_cvt_pk_bf16_f32 v101, v102, v103\n"
    "v_cvt_pk_bf16_f32 v102, v128, v129\n"
    "v_cvt_pk_bf16_f32 v103, v130, v131\n"
    "v_mfma_f32_16x16x32_bf16 v[220:223], a[176:179], a[160:163], v[220:223]\n"
    "s_add_u32 s77, s69, 10240\n"
    "buffer_store_dwordx4 v[100:103], v24, s[40:43], s77 offen sc1\n"
    "v_cvt_pk_bf16_f32 v104, v104, v105\n"
    "v_cvt_pk_bf16_f32 v105, v106, v107\n"
    "v_mfma_f32_16x16x32_bf16 v[248:251], a[180:183], a[160:163], v[248:251]\n"
    "v_cvt_pk_bf16_f32 v106, v132, v133\n"
    "v_cvt_pk_bf16_f32 v107, v134, v135\n"
    "s_add_u32 s77, s69, 11264\n"
    "v_mfma_f32_16x16x32_bf16 v[168:171], a[168:171], a[164:167], v[168:171]\n"
    "buffer_store_dwordx4 v[104:107], v24, s[40:43], s77 offen sc1\n"
    "v_cvt_pk_bf16_f32 v108, v108, v109\n"
    "v_cvt_pk_bf16_f32 v109, v110, v111\n"
    "v_mfma_f32_16x16x32_bf16 v[196:199], a[172:175], a[164:167], v[196:199]\n"
    "v_cvt_pk_bf16_f32 v110, v136, v137\n"
    "v_cvt_pk_bf16_f32 v111, v138, v139\n"
    "s_add_u32 s77, s69, 12288\n"
    "v_mfma_f32_16x16x32_bf16 v[224:227], a[176:179], a[164:167], v[224:227]\n"
    "buffer_store_dwordx4 v[108:111], v24, s[40:43], s77 offen sc1\n"
    "v_cvt_pk_bf16_f32 v112, v112, v113\n"
    "v_cvt_pk_bf16_f32 v113, v114, v115\n"
    "v_cvt_pk_bf16_f32 v114, v140, v141\n"
    "v_mfma_f32_16x16x32_bf16 v[252:255], a[180:183], a[164:167], v[252:255]\n"
    "v_cvt_pk_bf16_f32 v115, v142, v143\n"
    "s_add_u32 s77, s69, 13312\n"
    "buffer_store_dwordx4 v[112:115], v24, s[40:43], s77 offen sc1\n"
    "s_waitcnt lgkmcnt(0)\n"
    "v_mfma_f32_16x16x32_bf16 a[0:3], a[184:187], a[140:143], a[0:3]\n"
    "v_cvt_pk_bf16_f32 v144, v144, v145\n"
    "v_cvt_pk_bf16_f32 v145, v146, v147\n"
    "v_cvt_pk_bf16_f32 v146, v172, v173\n"
    "v_mfma_f32_16x16x32_bf16 a[28:31], a[188:191], a[140:143], a[28:31]\n"
    "v_cvt_pk_bf16_f32 v147, v174, v175\n"
    "s_add_u32 s77, s69, 14336\n"
    "buffer_store_dwordx4 v[144:147], v24, s[40:43], s77 offen sc1\n"
    "v_mfma_f32_16x16x32_bf16 a[56:59], a[192:195], a[140:143], a[56:59]\n"
    "v_cvt_pk_bf16_f32 v148, v148, v149\n"
    "v_cvt_pk_bf16_f32 v149, v150, v151\n"
    "v_cvt_pk_bf16_f32 v150, v176, v177\n"
    "v_mfma_f32_16x16x32_bf16 a[84:87], a[196:199], a[140:143], a[84:87]\n"
    "v_cvt_pk_bf16_f32 v151, v178, v179\n"
    "s_add_u32 s77, s69, 15360\n"
    "buffer_store_dwordx4 v[148:151], v24, s[40:43], s77 offen sc1\n"
    "v_mfma_f32_16x16x32_bf16 a[4:7], a[184:187], a[144:147], a[4:7]\n"
    "v_cvt_pk_bf16_f32 v152, v152, v153\n"
    "v_cvt_pk_bf16_f32 v153, v154, v155\n"
    "v_cvt_pk_bf16_f32 v154, v180, v181\n"
    "v_mfma_f32_16x16x32_bf16 a[32:35], a[188:191], a[144:147], a[32:35]\n"
    "v_cvt_pk_bf16_f32 v155, v182, v183\n"
    "s_add_u32 s77, s69, 16384\n"
    "buffer_store_dwordx4 v[152:155], v24, s[40:43], s77 offen sc1\n"
    "v_mfma_f32_16x16x32_bf16 a[60:63], a[192:195], a[144:147], a[60:63]\n"
    "v_cvt_pk_bf16_f32 v156, v156, v157\n"
    "v_cvt_pk_bf16_f32 v157, v158, v159\n"
    "v_cvt_pk_bf16_f32 v158, v184, v185\n"
    "v_mfma_f32_16x16x32_bf16 a[88:91], a[196:199], a[144:147], a[88:91]\n"
    "v_cvt_pk_bf16_f32 v159, v186, v187\n"
    "s_add_u32 s77, s69, 17408\n"
    "buffer_store_dwordx4 v[156:159], v24, s[40:43], s77 offen sc1\n"
    "v_mfma_f32_16x16x32_bf16 a[8:11], a[184:187], a[148:151], a[8:11]\n"
    "v_cvt_pk_bf16_f32 v160, v160, v161\n"
    "v_cvt_pk_bf16_f32 v161, v162, v163\n"
    "v_cvt_pk_bf16_f32 v162, v188, v189\n"
    "v_mfma_f32_16x16x32_bf16 a[36:39], a[188:191], a[148:151], a[36:39]\n"
    "v_cvt_pk_bf16_f32 v163, v190, v191\n"
    "s_add_u32 s77, s69, 18432\n"
    "buffer_store_dwordx4 v[160:163], v24, s[40:43], s77 offen sc1\n"
    "v_mfma_f32_16x16x32_bf16 a[64:67], a[192:195], a[148:151], a[64:67]\n"
    "v_cvt_pk_bf16_f32 v164, v164, v165\n"
    "v_cvt_pk_bf16_f32 v165, v166, v167\n"
    "v_cvt_pk_bf16_f32 v166, v192, v193\n"
    "v_mfma_f32_16x16x32_bf16 a[92:95], a[196:199], a[148:151], a[92:95]\n"
    "v_cvt_pk_bf16_f32 v167, v194, v195\n"
    "s_add_u32 s77, s69, 19456\n"
    "buffer_store_dwordx4 v[164:167], v24, s[40:43], s77 offen sc1\n"
    "v_mfma_f32_16x16x32_bf16 a[12:15], a[184:187], a[152:155], a[12:15]\n"
    "v_cvt_pk_bf16_f32 v168, v168, v169\n"
    "v_cvt_pk_bf16_f32 v169, v170, v171\n"
    "v_cvt_pk_bf16_f32 v170, v196, v197\n"
    "v_mfma_f32_16x16x32_bf16 a[40:43], a[188:191], a[152:155], a[40:43]\n"
    "v_cvt_pk_bf16_f32 v171, v198, v199\n"
    "s_add_u32 s77, s69, 20480\n"
    "buffer_store_dwordx4 v[168:171], v24, s[40:43], s77 offen sc1\n"
    "v_mfma_f32_16x16x32_bf16 a[68:71], a[192:195], a[152:155], a[68:71]\n"
    "v_cvt_pk_bf16_f32 v200, v200, v201\n"
    "v_cvt_pk_bf16_f32 v201, v202, v203\n"
    "v_cvt_pk_bf16_f32 v202, v228, v229\n"
    "v_mfma_f32_16x16x32_bf16 a[96:99], a[196:199], a[152:155], a[96:99]\n"
    "v_cvt_pk_bf16_f32 v203, v230, v231\n"
    "s_add_u32 s77, s69, 21504\n"
    "buffer_store_dwordx4 v[200:203], v24, s[40:43], s77 offen sc1\n"
    "v_mfma_f32_16x16x32_bf16 a[16:19], a[184:187], a[156:159], a[16:19]\n"
    "v_cvt_pk_bf16_f32 v204, v204, v205\n"
    "v_cvt_pk_bf16_f32 v205, v206, v207\n"
    "v_cvt_pk_bf16_f32 v206, v232, v233\n"
    "v_mfma_f32_16x16x32_bf16 a[44:47], a[188:191], a[156:159], a[44:47]\n"
    "v_cvt_pk_bf16_f32 v207, v234, v235\n"
    "s_add_u32 s77, s69, 22528\n"
    "buffer_store_dwordx4 v[204:207], v24, s[40:43], s77 offen sc1\n"
    "v_mfma_f32_16x16x32_bf16 a[72:75], a[192:195], a[156:159], a[72:75]\n"
    "v_cvt_pk_bf16_f32 v208, v208, v209\n"
    "v_cvt_pk_bf16_f32 v209, v210, v211\n"
    "v_cvt_pk_bf16_f32 v210, v236, v237\n"
    "v_mfma_f32_16x16x32_bf16 a[100:103], a[196:199], a[156:159], a[100:103]\n"
    "v_cvt_pk_bf16_f32 v211, v238, v239\n"
    "s_add_u32 s77, s69, 23552\n"
    "buffer_store_dwordx4 v[208:211], v24, s[40:43], s77 offen sc1\n"
    "v_mfma_f32_16x16x32_bf16 a[20:23], a[184:187], a[160:163], a[20:23]\n"
    "v_cvt_pk_bf16_f32 v212, v212, v213\n"
    "v_cvt_pk_bf16_f32 v213, v214, v215\n"
    "v_cvt_pk_bf16_f32 v214, v240, v241\n"
    "v_mfma_f32_16x16x32_bf16 a[48:51], a[188:191], a[160:163], a[48:51]\n"
    "v_cvt_pk_bf16_f32 v215, v242, v243\n"
    "s_add_u32 s77, s69, 24576\n"
    "buffer_store_dwordx4 v[212:215], v24, s[40:43], s77 offen sc1\n"
    "v_mfma_f32_16x16x32_bf16 a[76:79], a[192:195], a[160:163], a[76:79]\n"
    "v_cvt_pk_bf16_f32 v216, v216, v217\n"
    "v_cvt_pk_bf16_f32 v217, v218, v219\n"
    "v_cvt_pk_bf16_f32 v218, v244, v245\n"
    "v_mfma_f32_16x16x32_bf16 a[104:107], a[196:199], a[160:163], a[104:107]\n"
    "v_cvt_pk_bf16_f32 v219, v246, v247\n"
    "s_add_u32 s77, s69, 25600\n"
    "buffer_store_dwordx4 v[216:219], v24, s[40:43], s77 offen sc1\n"
    "v_mfma_f32_16x16x32_bf16 a[24:27], a[184:187], a[164:167], a[24:27]\n"
    "v_cvt_pk_bf16_f32 v220, v220, v221\n"
    "v_cvt_pk_bf16_f32 v221, v222, v223\n"
    "v_cvt_pk_bf16_f32 v222, v248, v249\n"
    "v_mfma_f32_16x16x32_bf16 a[52:55], a[188:191], a[164:167], a[52:55]\n"
    "v_cvt_pk_bf16_f32 v223, v250, v251\n"
    "s_add_u32 s77, s69, 26624\n"
    "buffer_store_dwordx4 v[220:223], v24, s[40:43], s77 offen sc1\n"
    "v_mfma_f32_16x16x32_bf16 a[80:83], a[192:195], a[164:167], a[80:83]\n"
    "v_cvt_pk_bf16_f32 v224, v224, v225\n"
    "v_cvt_pk_bf16_f32 v225, v226, v227\n"
    "v_cvt_pk_bf16_f32 v226, v252, v253\n"
    "v_mfma_f32_16x16x32_bf16 a[108:111], a[196:199], a[164:167], a[108:111]\n"
    "v_cvt_pk_bf16_f32 v227, v254, v255\n"
    "s_add_u32 s77, s69, 27648\n"
    "buffer_store_dwordx4 v[224:227], v24, s[40:43], s77 offen sc1\n"
    "s_waitcnt lgkmcnt(0)\n"
    "s_nop 7\n"
    "v_accvgpr_read_b32 v32, a0\n"
    "v_accvgpr_read_b32 v33, a1\n"
    "v_accvgpr_read_b32 v34, a2\n"
    "v_accvgpr_read_b32 v35, a3\n"
    "v_accvgpr_read_b32 v36, a28\n"
    "v_accvgpr_read_b32 v37, a29\n"
    "v_accvgpr_read_b32 v38, a30\n"
    "v_accvgpr_read_b32 v39, a31\n"
    "v_cvt_pk_bf16_f32 v32, v32, v33\n"
    "v_cvt_pk_bf16_f32 v33, v34, v35\n"
    "v_cvt_pk_bf16_f32 v34, v36, v37\n"
    "v_cvt_pk_bf16_f32 v35, v38, v39\n"
    "s_add_u32 s77, s69, 28672\n"
    "buffer_store_dwordx4 v[32:35], v24, s[40:43], s77 offen sc1\n"
    "v_accvgpr_read_b32 v40, a4\n"
    "v_accvgpr_read_b32 v41, a5\n"
    "v_accvgpr_read_b32 v42, a6\n"
    "v_accvgpr_read_b32 v43, a7\n"
    "v_accvgpr_read_b32 v44, a32\n"
    "v_accvgpr_read_b32 v45, a33\n"
    "v_accvgpr_read_b32 v46, a34\n"
    "v_accvgpr_read_b32 v47, a35\n"
    "v_cvt_pk_bf16_f32 v40, v40, v41\n"
    "v_cvt_pk_bf16_f32 v41, v42, v43\n"
    "v_cvt_pk_bf16_f32 v42, v44, v45\n"
    "v_cvt_pk_bf16_f32 v43, v46, v47\n"
    "s_add_u32 s77, s69, 29696\n"
    "buffer_store_dwordx4 v[40:43], v24, s[40:43], s77 offen sc1\n"
    "v_accvgpr_read_b32 v48, a8\n"
    "v_accvgpr_read_b32 v49, a9\n"
    "v_accvgpr_read_b32 v50, a10\n"
    "v_accvgpr_read_b32 v51, a11\n"
    "v_accvgpr_read_b32 v52, a36\n"
    "v_accvgpr_read_b32 v53, a37\n"
    "v_accvgpr_read_b32 v54, a38\n"
    "v_accvgpr_read_b32 v55, a39\n"
    "v_cvt_pk_bf16_f32 v48, v48, v49\n"
    "v_cvt_pk_bf16_f32 v49, v50, v51\n"
    "v_cvt_pk_bf16_f32 v50, v52, v53\n"
    "v_cvt_pk_bf16_f32 v51, v54, v55\n"
    "s_add_u32 s77, s69, 30720\n"
    "buffer_store_dwordx4 v[48:51], v24, s[40:43], s77 offen sc1\n"
    "v_accvgpr_read_b32 v56, a12\n"
    "v_accvgpr_read_b32 v57, a13\n"
    "v_accvgpr_read_b32 v58, a14\n"
    "v_accvgpr_read_b32 v59, a15\n"
    "v_accvgpr_read_b32 v60, a40\n"
    "v_accvgpr_read_b32 v61, a41\n"
    "v_accvgpr_read_b32 v62, a42\n"
    "v_accvgpr_read_b32 v63, a43\n"
    "v_cvt_pk_bf16_f32 v56, v56, v57\n"
    "v_cvt_pk_bf16_f32 v57, v58, v59\n"
    "v_cvt_pk_bf16_f32 v58, v60, v61\n"
    "v_cvt_pk_bf16_f32 v59, v62, v63\n"
    "s_add_u32 s77, s69, 31744\n"
    "buffer_store_dwordx4 v[56:59], v24, s[40:43], s77 offen sc1\n"
    "v_accvgpr_read_b32 v32, a16\n"
    "v_accvgpr_read_b32 v33, a17\n"
    "v_accvgpr_read_b32 v34, a18\n"
    "v_accvgpr_read_b32 v35, a19\n"
    "v_accvgpr_read_b32 v36, a44\n"
    "v_accvgpr_read_b32 v37, a45\n"
    "v_accvgpr_read_b32 v38, a46\n"
    "v_accvgpr_read_b32 v39, a47\n"
    "v_cvt_pk_bf16_f32 v32, v32, v33\n"
    "v_cvt_pk_bf16_f32 v33, v34, v35\n"
    "v_cvt_pk_bf16_f32 v34, v36, v37\n"
    "v_cvt_pk_bf16_f32 v35, v38, v39\n"
    "s_add_u32 s77, s69, 32768\n"
    "buffer_store_dwordx4 v[32:35], v24, s[40:43], s77 offen sc1\n"
    "v_accvgpr_read_b32 v40, a20\n"
    "v_accvgpr_read_b32 v41, a21\n"
    "v_accvgpr_read_b32 v42, a22\n"
    "v_accvgpr_read_b32 v43, a23\n"
    "v_accvgpr_read_b32 v44, a48\n"
    "v_accvgpr_read_b32 v45, a49\n"
    "v_accvgpr_read_b32 v46, a50\n"
    "v_accvgpr_read_b32 v47, a51\n"
    "v_cvt_pk_bf16_f32 v40, v40, v41\n"
    "v_cvt_pk_bf16_f32 v41, v42, v43\n"
    "v_cvt_pk_bf16_f32 v42, v44, v45\n"
    "v_cvt_pk_bf16_f32 v43, v46, v47\n"
    "s_add_u32 s77, s69, 33792\n"
    "buffer_store_dwordx4 v[40:43], v24, s[40:43], s77 offen sc1\n"
    "v_accvgpr_read_b32 v48, a24\n"
    "v_accvgpr_read_b32 v49, a25\n"
    "v_accvgpr_read_b32 v50, a26\n"
    "v_accvgpr_read_b32 v51, a27\n"
    "v_accvgpr_read_b32 v52, a52\n"
    "v_accvgpr_read_b32 v53, a53\n"
    "v_accvgpr_read_b32 v54, a54\n"
    "v_accvgpr_read_b32 v55, a55\n"
    "v_cvt_pk_bf16_f32 v48, v48, v49\n"
    "v_cvt_pk_bf16_f32 v49, v50, v51\n"
    "v_cvt_pk_bf16_f32 v50, v52, v53\n"
    "v_cvt_pk_bf16_f32 v51, v54, v55\n"
    "s_add_u32 s77, s69, 34816\n"
    "buffer_store_dwordx4 v[48:51], v24, s[40:43], s77 offen sc1\n"
    "v_accvgpr_read_b32 v56, a56\n"
    "v_accvgpr_read_b32 v57, a57\n"
    "v_accvgpr_read_b32 v58, a58\n"
    "v_accvgpr_read_b32 v59, a59\n"
    "v_accvgpr_read_b32 v60, a84\n"
    "v_accvgpr_read_b32 v61, a85\n"
    "v_accvgpr_read_b32 v62, a86\n"
    "v_accvgpr_read_b32 v63, a87\n"
    "v_cvt_pk_bf16_f32 v56, v56, v57\n"
    "v_cvt_pk_bf16_f32 v57, v58, v59\n"
    "v_cvt_pk_bf16_f32 v58, v60, v61\n"
    "v_cvt_pk_bf16_f32 v59, v62, v63\n"
    "s_add_u32 s77, s69, 35840\n"
    "buffer_store_dwordx4 v[56:59], v24, s[40:43], s77 offen sc1\n"
    "v_accvgpr_read_b32 v32, a60\n"
    "v_accvgpr_read_b32 v33, a61\n"
    "v_accvgpr_read_b32 v34, a62\n"
    "v_accvgpr_read_b32 v35, a63\n"
    "v_accvgpr_read_b32 v36, a88\n"
    "v_accvgpr_read_b32 v37, a89\n"
    "v_accvgpr_read_b32 v38, a90\n"
    "v_accvgpr_read_b32 v39, a91\n"
    "v_cvt_pk_bf16_f32 v32, v32, v33\n"
    "v_cvt_pk_bf16_f32 v33, v34, v35\n"
    "v_cvt_pk_bf16_f32 v34, v36, v37\n"
    "v_cvt_pk_bf16_f32 v35, v38, v39\n"
    "s_add_u32 s77, s69, 36864\n"
    "buffer_store_dwordx4 v[32:35], v24, s[40:43], s77 offen sc1\n"
    "v_accvgpr_read_b32 v40, a64\n"
    "v_accvgpr_read_b32 v41, a65\n"
    "v_accvgpr_read_b32 v42, a66\n"
    "v_accvgpr_read_b32 v43, a67\n"
    "v_accvgpr_read_b32 v44, a92\n"
    "v_accvgpr_read_b32 v45, a93\n"
    "v_accvgpr_read_b32 v46, a94\n"
    "v_accvgpr_read_b32 v47, a95\n"
    "v_cvt_pk_bf16_f32 v40, v40, v41\n"
    "v_cvt_pk_bf16_f32 v41, v42, v43\n"
    "v_cvt_pk_bf16_f32 v42, v44, v45\n"
    "v_cvt_pk_bf16_f32 v43, v46, v47\n"
    "s_add_u32 s77, s69, 37888\n"
    "buffer_store_dwordx4 v[40:43], v24, s[40:43], s77 offen sc1\n"
    "v_accvgpr_read_b32 v48, a68\n"
    "v_accvgpr_read_b32 v49, a69\n"
    "v_accvgpr_read_b32 v50, a70\n"
    "v_accvgpr_read_b32 v51, a71\n"
    "v_accvgpr_read_b32 v52, a96\n"
    "v_accvgpr_read_b32 v53, a97\n"
    "v_accvgpr_read_b32 v54, a98\n"
    "v_accvgpr_read_b32 v55, a99\n"
    "v_cvt_pk_bf16_f32 v48, v48, v49\n"
    "v_cvt_pk_bf16_f32 v49, v50, v51\n"
    "v_cvt_pk_bf16_f32 v50, v52, v53\n"
    "v_cvt_pk_bf16_f32 v51, v54, v55\n"
    "s_add_u32 s77, s69, 38912\n"
    "buffer_store_dwordx4 v[48:51], v24, s[40:43], s77 offen sc1\n"
    "v_accvgpr_read_b32 v56, a72\n"
    "v_accvgpr_read_b32 v57, a73\n"
    "v_accvgpr_read_b32 v58, a74\n"
    "v_accvgpr_read_b32 v59, a75\n"
    "v_accvgpr_read_b32 v60, a100\n"
    "v_accvgpr_read_b32 v61, a101\n"
    "v_accvgpr_read_b32 v62, a102\n"
    "v_accvgpr_read_b32 v63, a103\n"
    "v_cvt_pk_bf16_f32 v56, v56, v57\n"
    "v_cvt_pk_bf16_f32 v57, v58, v59\n"
    "v_cvt_pk_bf16_f32 v58, v60, v61\n"
    "v_cvt_pk_bf16_f32 v59, v62, v63\n"
    "s_add_u32 s77, s69, 39936\n"
    "buffer_store_dwordx4 v[56:59], v24, s[40:43], s77 offen sc1\n"
    "v_accvgpr_read_b32 v32, a76\n"
    "v_accvgpr_read_b32 v33, a77\n"
    "v_accvgpr_read_b32 v34, a78\n"
    "v_accvgpr_read_b32 v35, a79\n"
    "v_accvgpr_read_b32 v36, a104\n"
    "v_accvgpr_read_b32 v37, a105\n"
    "v_accvgpr_read_b32 v38, a106\n"
    "v_accvgpr_read_b32 v39, a107\n"
    "v_cvt_pk_bf16_f32 v32, v32, v33\n"
    "v_cvt_pk_bf16_f32 v33, v34, v35\n"
    "v_cvt_pk_bf16_f32 v34, v36, v37\n"
    "v_cvt_pk_bf16_f32 v35, v38, v39\n"
    "s_add_u32 s77, s69, 40960\n"
    "buffer_store_dwordx4 v[32:35], v24, s[40:43], s77 offen sc1\n"
    "v_accvgpr_read_b32 v40, a80\n"
    "v_accvgpr_read_b32 v41, a81\n"
    "v_accvgpr_read_b32 v42, a82\n"
    "v_accvgpr_read_b32 v43, a83\n"
    "v_accvgpr_read_b32 v44, a108\n"
    "v_accvgpr_read_b32 v45, a109\n"
    "v_accvgpr_read_b32 v46, a110\n"
    "v_accvgpr_read_b32 v47, a111\n"
    "v_cvt_pk_bf16_f32 v40, v40, v41\n"
    "v_cvt_pk_bf16_f32 v41, v42, v43\n"
    "v_cvt_pk_bf16_f32 v42, v44, v45\n"
    "v_cvt_pk_bf16_f32 v43, v46, v47\n"
    "s_add_u32 s77, s69, 41984\n"
    "buffer_store_dwordx4 v[40:43], v24, s[40:43], s77 offen sc1\n"
    "s_branch .LX_end_%=\n"
    ".LX_late_%=:\n"
    "s_waitcnt vmcnt(0) lgkmcnt(0)\n"
    "s_barrier\n"
    "s_add_u32 s74, s64, 0\n"
    "s_add_u32 m0, s66, 0\n"
    "s_nop 0\n"
    "buffer_load_dwordx4 v16, s[44:47], s74 offen offset:0 lds\n"
    "buffer_load_dwordx4 v17, s[44:47], s74 offen offset:1024 lds\n"
    "buffer_load_dwordx4 v18, s[44:47], s74 offen offset:2048 lds\n"
    "buffer_load_dwordx4 v19, s[44:47], s74 offen offset:3072 lds\n"
    "s_add_u32 s64, s64, 32768\n"
    "s_add_u32 s74, s64, 0\n"
    "s_add_u32 m0, s66, 8192\n"
    "s_nop 0\n"
    "buffer_load_dwordx4 v16, s[44:47], s74 offen offset:0 lds\n"
    "buffer_load_dwordx4 v17, s[44:47], s74 offen offset:1024 lds\n"
    "buffer_load_dwordx4 v18, s[44:47], s74 offen offset:2048 lds\n"
    "buffer_load_dwordx4 v19, s[44:47], s74 offen offset:3072 lds\n"
    "s_add_u32 s64, s64, 32768\n"
    "s_add_u32 s74, s64, 0\n"
    "s_mov_b32 m0, s67\n"
    "s_nop 0\n"
    "buffer_load_dwordx4 v16, s[44:47], s74 offen offset:0 lds\n"
    "buffer_load_dwordx4 v17, s[44:47], s74 offen offset:1024 lds\n"
    "buffer_load_dwordx4 v18, s[44:47], s74 offen offset:2048 lds\n"
    "buffer_load_dwordx4 v19, s[44:47], s74 offen offset:3072 lds\n"
    "s_add_u32 s64, s64, 32768\n"
    "s_waitcnt vmcnt(8)\n"
    "ds_read_b64_tr_b16 a[168:169], v8 offset:0\n"
    "ds_read_b64_tr_b16 a[170:171], v8 offset:2048\n"
    "ds_read_b64_tr_b16 a[172:173], v9 offset:0\n"
    "ds_read_b64_tr_b16 a[174:175], v9 offset:2048\n"
    "ds_read_b64_tr_b16 a[176:177], v10 offset:0\n"
    "ds_read_b64_tr_b16 a[178:179], v10 offset:2048\n"
    "ds_read_b64_tr_b16 a[180:181], v11 offset:0\n"
    "ds_read_b64_tr_b16 a[182:183], v11 offset:2048\n"
    "ds_read_b128 a[112:115], v20 offset:0\n"
    "ds_read_b128 a[116:119], v20 offset:4096\n"
    "ds_read_b128 a[120:123], v20 offset:8192\n"
    "ds_read_b128 a[124:127], v20 offset:12288\n"
    "ds_read_b128 a[128:131], v20 offset:16384\n"
    "ds_read_b128 a[132:135], v20 offset:20480\n"
    "ds_read_b128 a[136:139], v20 offset:24576\n"
    "s_waitcnt lgkmcnt(0)\n"
    "s_waitcnt vmcnt(4)\n"
    "v_mfma_f32_16x16x32_bf16 v[32:35], a[168:171], a[112:115], 0\n"
    "ds_read_b64_tr_b16 a[184:185], v8 offset:8192\n"
    "v_mfma_f32_16x16x32_bf16 v[60:63], a[172:175], a[112:115], 0\n"
    "ds_read_b64_tr_b16 a[186:187], v8 offset:10240\n"
    "v_mfma_f32_16x16x32_bf16 v[88:91], a[176:179], a[112:115], 0\n"
    "ds_read_b64_tr_b16 a[188:189], v9 offset:8192\n"
    "v_mfma_f32_16x16x32_bf16 v[116:119], a[180:183], a[112:115], 0\n"
    "ds_read_b64_tr_b16 a[190:191], v9 offset:10240\n"
    "v_mfma_f32_16x16x32_bf16 v[36:39], a[168:171], a[116:119], 0\n"
    "ds_read_b64_tr_b16 a[192:193], v10 offset:8192\n"
    "v_mfma_f32_16x16x32_bf16 v[64:67], a[172:175], a[116:119], 0\n"
    "ds_read_b64_tr_b16 a[194:195], v10 offset:10240\n"
    "v_mfma_f32_16x16x32_bf16 v[92:95], a[176:179], a[116:119], 0\n"
    "ds_read_b64_tr_b16 a[196:197], v11 offset:8192\n"
    "v_mfma_f32_16x16x32_bf16 v[120:123], a[180:183], a[116:119], 0\n"
    "ds_read_b64_tr_b16 a[198:199], v11 offset:10240\n"
    "v_mfma_f32_16x16x32_bf16 v[40:43], a[168:171], a[120:123], 0\n"
    "ds_read_b128 a[140:143], v20 offset:1024\n"
    "v_mfma_f32_16x16x32_bf16 v[68:71], a[172:175], a[120:123], 0\n"
    "ds_read_b128 a[144:147], v20 offset:5120\n"
    "v_mfma_f32_16x16x32_bf16 v[96:99], a[176:179], a[120:123], 0\n"
    "ds_read_b128 a[148:151], v20 offset:9216\n"
    "v_mfma_f32_16x16x32_bf16 v[124:127], a[180:183], a[120:123], 0\n"
    "ds_read_b128 a[152:155], v20 offset:13312\n"
    "v_mfma_f32_16x16x32_bf16 v[44:47], a[168:171], a[124:127], 0\n"
    "ds_read_b128 a[156:159], v20 offset:17408\n"
    "v_mfma_f32_16x16x32_bf16 v[72:75], a[172:175], a[124:127], 0\n"
    "ds_read_b128 a[160:163], v20 offset:21504\n"
    "v_mfma_f32_16x16x32_bf16 v[100:103], a[176:179], a[124:127], 0\n"
    "ds_read_b128 a[164:167], v20 offset:25600\n"
    "v_mfma_f32_16x16x32_bf16 v[128:131], a[180:183], a[124:127], 0\n"
    "s_add_u32 s74, s64, 0\n"
    "v_mfma_f32_16x16x32_bf16 v[48:51], a[168:171], a[128:131], 0\n"
    "s_add_u32 m0, s66, 0\n"
    "v_mfma_f32_16x16x32_bf16 v[76:79], a[172:175], a[128:131], 0\n"
    "buffer_load_dwordx4 v16, s[44:47], s74 offen offset:0 lds\n"
    "v_mfma_f32_16x16x32_bf16 v[104:107], a[176:179], a[128:131], 0\n"
    "buffer_load_dwordx4 v17, s[44:47], s74 offen offset:1024 lds\n"
    "v_mfma_f32_16x16x32_bf16 v[132:135], a[180:183], a[128:131], 0\n"
    "buffer_load_dwordx4 v18, s[44:47], s74 offen offset:2048 lds\n"
    "v_mfma_f32_16x16x32_bf16 v[52:55], a[168:171], a[132:135], 0\n"
    "buffer_load_dwordx4 v19, s[44:47], s74 offen offset:3072 lds\n"
    "v_mfma_f32_16x16x32_bf16 v[80:83], a[172:175], a[132:135], 0\n"
    "s_add_u32 s64, s64, 32768\n"
    "v_mfma_f32_16x16x32_bf16 v[108:111], a[176:179], a[132:135], 0\n"
    "v_mfma_f32_16x16x32_bf16 v[136:139], a[180:183], a[132:135], 0\n"
    "v_mfma_f32_16x16x32_bf16 v[56:59], a[168:171], a[136:139], 0\n"
    "v_mfma_f32_16x16x32_bf16 v[84:87], a[172:175], a[136:139], 0\n"
    "v_mfma_f32_16x16x32_bf16 v[112:115], a[176:179], a[136:139], 0\n"
    "v_mfma_f32_16x16x32_bf16 v[140:143], a[180:183], a[136:139], 0\n"
    "s_waitcnt lgkmcnt(0)\n"
    "s_waitcnt vmcnt(4)\n"
    "v_mfma_f32_16x16x32_bf16 v[32:35], a[184:187], a[140:143], v[32:35]\n"
    "ds_read_b64_tr_b16 a[168:169], v12 offset:0\n"
    "v_mfma_f32_16x16x32_bf16 v[60:63], a[188:191], a[140:143], v[60:63]\n"
    "ds_read_b64_tr_b16 a[170:171], v12 offset:2048\n"
    "v_mfma_f32_16x16x32_bf16 v[88:91], a[192:195], a[140:143], v[88:91]\n"
    "ds_read_b64_tr_b16 a[172:173], v13 offset:0\n"
    "v_mfma_f32_16x16x32_bf16 v[116:119], a[196:199], a[140:143], v[116:119]\n"
    "ds_read_b64_tr_b16 a[174:175], v13 offset:2048\n"
    "v_mfma_f32_16x16x32_bf16 v[36:39], a[184:187], a[144:147], v[36:39]\n"
    "ds_read_b64_tr_b16 a[176:177], v14 offset:0\n"
    "v_mfma_f32_16x16x32_bf16 v[64:67], a[188:191], a[144:147], v[64:67]\n"
    "ds_read_b64_tr_b16 a[178:179], v14 offset:2048\n"
    "v_mfma_f32_16x16x32_bf16 v[92:95], a[192:195], a[144:147], v[92:95]\n"
    "ds_read_b64_tr_b16 a[180:181], v15 offset:0\n"
    "v_mfma_f32_16x16x32_bf16 v[120:123], a[196:199], a[144:147], v[120:123]\n"
    "ds_read_b64_tr_b16 a[182:183], v15 offset:2048\n"
    "v_mfma_f32_16x16x32_bf16 v[40:43], a[184:187], a[148:151], v[40:43]\n"
    "ds_read_b128 a[112:115], v20 offset:2048\n"
    "v_mfma_f32_16x16x32_bf16 v[68:71], a[188:191], a[148:151], v[68:71]\n"
    "ds_read_b128 a[116:119], v20 offset:6144\n"
    "v_mfma_f32_16x16x32_bf16 v[96:99], a[192:195], a[148:151], v[96:99]\n"
    "ds_read_b128 a[120:123], v20 offset:10240\n"
    "v_mfma_f32_16x16x32_bf16 v[124:127], a[196:199], a[148:151], v[124:127]\n"
    "ds_read_b128 a[124:127], v20 offset:14336\n"
    "v_mfma_f32_16x16x32_bf16 v[44:47], a[184:187], a[152:155], v[44:47]\n"
    "ds_read_b128 a[128:131], v20 offset:18432\n"
    "v_mfma_f32_16x16x32_bf16 v[72:75], a[188:191], a[152:155], v[72:75]\n"
    "ds_read_b128 a[132:135], v20 offset:22528\n"
    "v_mfma_f32_16x16x32_bf16 v[100:103], a[192:195], a[152:155], v[100:103]\n"
    "ds_read_b128 a[136:139], v20 offset:26624\n"
    "v_mfma_f32_16x16x32_bf16 v[128:131], a[196:199], a[152:155], v[128:131]\n"
    "s_add_u32 s74, s64, 0\n"
    "v_mfma_f32_16x16x32_bf16 v[48:51], a[184:187], a[156:159], v[48:51]\n"
    "s_add_u32 m0, s66, 8192\n"
    "v_mfma_f32_16x16x32_bf16 v[76:79], a[188:191], a[156:159], v[76:79]\n"
    "buffer_load_dwordx4 v16, s[44:47], s74 offen offset:0 lds\n"
    "v_mfma_f32_16x16x32_bf16 v[104:107], a[192:195], a[156:159], v[104:107]\n"
    "buffer_load_dwordx4 v17, s[44:47], s74 offen offset:1024 lds\n"
    "v_mfma_f32_16x16x32_bf16 v[132:135], a[196:199], a[156:159], v[132:135]\n"
    "buffer_load_dwordx4 v18, s[44:47], s74 offen offset:2048 lds\n"
    "v_mfma_f32_16x16x32_bf16 v[52:55], a[184:187], a[160:163], v[52:55]\n"
    "buffer_load_dwordx4 v19, s[44:47], s74 offen offset:3072 lds\n"
    "v_mfma_f32_16x16x32_bf16 v[80:83], a[188:191], a[160:163], v[80:83]\n"
    "s_add_u32 s64, s64, 32768\n"
    "v_mfma_f32_16x16x32_bf16 v[108:111], a[192:195], a[160:163], v[108:111]\n"
    "v_mfma_f32_16x16x32_bf16 v[136:139], a[196:199], a[160:163], v[136:139]\n"
    "v_mfma_f32_16x16x32_bf16 v[56:59], a[184:187], a[164:167], v[56:59]\n"
    "v_mfma_f32_16x16x32_bf16 v[84:87], a[188:191], a[164:167], v[84:87]\n"
    "v_mfma_f32_16x16x32_bf16 v[112:115], a[192:195], a[164:167], v[112:115]\n"
    "v_mfma_f32_16x16x32_bf16 v[140:143], a[196:199], a[164:167], v[140:143]\n"
    "s_waitcnt lgkmcnt(0)\n"
    "s_waitcnt vmcnt(4)\n"
    "v_mfma_f32_16x16x32_bf16 v[32:35], a[168:171], a[112:115], v[32:35]\n"
    "ds_read_b64_tr_b16 a[184:185], v8 offset:0\n"
    "v_mfma_f32_16x16x32_bf16 v[60:63], a[172:175], a[112:115], v[60:63]\n"
    "ds_read_b64_tr_b16 a[186:187], v8 offset:2048\n"
    "v_mfma_f32_16x16x32_bf16 v[88:91], a[176:179], a[112:115], v[88:91]\n"
    "ds_read_b64_tr_b16 a[188:189], v9 offset:0\n"
    "v_mfma_f32_16x16x32_bf16 v[116:119], a[180:183], a[112:115], v[116:119]\n"
    "ds_read_b64_tr_b16 a[190:191], v9 offset:2048\n"
    "v_mfma_f32_16x16x32_bf16 v[36:39], a[168:171], a[116:119], v[36:39]\n"
    "ds_read_b64_tr_b16 a[192:193], v10 offset:0\n"
    "v_mfma_f32_16x16x32_bf16 v[64:67], a[172:175], a[116:119], v[64:67]\n"
    "ds_read_b64_tr_b16 a[194:195], v10 offset:2048\n"
    "v_mfma_f32_16x16x32_bf16 v[92:95], a[176:179], a[116:119], v[92:95]\n"
    "ds_read_b64_tr_b16 a[196:197], v11 offset:0\n"
    "v_mfma_f32_16x16x32_bf16 v[120:123], a[180:183], a[116:119], v[120:123]\n"
    "ds_read_b64_tr_b16 a[198:199], v11 offset:2048\n"
    "v_mfma_f32_16x16x32_bf16 v[40:43], a[168:171], a[120:123], v[40:43]\n"
    "ds_read_b128 a[140:143], v20 offset:3072\n"
    "v_mfma_f32_16x16x32_bf16 v[68:71], a[172:175], a[120:123], v[68:71]\n"
    "ds_read_b128 a[144:147], v20 offset:7168\n"
    "v_mfma_f32_16x16x32_bf16 v[96:99], a[176:179], a[120:123], v[96:99]\n"
    "ds_read_b128 a[148:151], v20 offset:11264\n"
    "v_mfma_f32_16x16x32_bf16 v[124:127], a[180:183], a[120:123], v[124:127]\n"
    "ds_read_b128 a[152:155], v20 offset:15360\n"
    "v_mfma_f32_16x16x32_bf16 v[44:47], a[168:171], a[124:127], v[44:47]\n"
    "ds_read_b128 a[156:159], v20 offset:19456\n"
    "v_mfma_f32_16x16x32_bf16 v[72:75], a[172:175], a[124:127], v[72:75]\n"
    "ds_read_b128 a[160:163], v20 offset:23552\n"
    "v_mfma_f32_16x16x32_bf16 v[100:103], a[176:179], a[124:127], v[100:103]\n"
    "ds_read_b128 a[164:167], v20 offset:27648\n"
    "v_mfma_f32_16x16x32_bf16 v[128:131], a[180:183], a[124:127], v[128:131]\n"
    "s_add_u32 s74, s64, 0\n"
    "v_mfma_f32_16x16x32_bf16 v[48:51], a[168:171], a[128:131], v[48:51]\n"
    "s_mov_b32 m0, s67\n"
    "v_mfma_f32_16x16x32_bf16 v[76:79], a[172:175], a[128:131], v[76:79]\n"
    "buffer_load_dwordx4 v16, s[44:47], s74 offen offset:0 lds\n"
    "v_mfma_f32_16x16x32_bf16 v[104:107], a[176:179], a[128:131], v[104:107]\n"
    "buffer_load_dwordx4 v17, s[44:47], s74 offen offset:1024 lds\n"
    "v_mfma_f32_16x16x32_bf16 v[132:135], a[180:183], a[128:131], v[132:135]\n"
    "buffer_load_dwordx4 v18, s[44:47], s74 offen offset:2048 lds\n"
    "v_mfma_f32_16x16x32_bf16 v[52:55], a[168:171], a[132:135], v[52:55]\n"
    "buffer_load_dwordx4 v19, s[44:47], s74 offen offset:3072 lds\n"
    "v_mfma_f32_16x16x32_bf16 v[80:83], a[172:175], a[132:135], v[80:83]\n"
    "s_add_u32 s64, s64, 32768\n"
    "v_mfma_f32_16x16x32_bf16 v[108:111], a[176:179], a[132:135], v[108:111]\n"
    "v_mfma_f32_16x16x32_bf16 v[136:139], a[180:183], a[132:135], v[136:139]\n"
    "v_mfma_f32_16x16x32_bf16 v[56:59], a[168:171], a[136:139], v[56:59]\n"
    "v_mfma_f32_16x16x32_bf16 v[84:87], a[172:175], a[136:139], v[84:87]\n"
    "v_mfma_f32_16x16x32_bf16 v[112:115], a[176:179], a[136:139], v[112:115]\n"
    "v_mfma_f32_16x16x32_bf16 v[140:143], a[180:183], a[136:139], v[140:143]\n"
    "s_waitcnt lgkmcnt(0)\n"
    "s_waitcnt vmcnt(4)\n"
    "v_mfma_f32_16x16x32_bf16 v[32:35], a[184:187], a[140:143], v[32:35]\n"
    "v_add_u32 v20, 32768, v20\n"
    "v_mfma_f32_16x16x32_bf16 v[60:63], a[188:191], a[140:143], v[60:63]\n"
    "ds_read_b64_tr_b16 a[168:169], v8 offset:8192\n"
    "v_mfma_f32_16x16x32_bf16 v[88:91], a[192:195], a[140:143], v[88:91]\n"
    "ds_read_b64_tr_b16 a[170:171], v8 offset:10240\n"
    "v_mfma_f32_16x16x32_bf16 v[116:119], a[196:199], a[140:143], v[116:119]\n"
    "ds_read_b64_tr_b16 a[172:173], v9 offset:8192\n"
    "v_mfma_f32_16x16x32_bf16 v[36:39], a[184:187], a[144:147], v[36:39]\n"
    "ds_read_b64_tr_b16 a[174:175], v9 offset:10240\n"
    "v_mfma_f32_16x16x32_bf16 v[64:67], a[188:191], a[144:147], v[64:67]\n"
    "ds_read_b64_tr_b16 a[176:177], v10 offset:8192\n"
    "v_mfma_f32_16x16x32_bf16 v[92:95], a[192:195], a[144:147], v[92:95]\n"
    "ds_read_b64_tr_b16 a[178:179], v10 offset:10240\n"
    "v_mfma_f32_16x16x32_bf16 v[120:123], a[196:199], a[144:147], v[120:123]\n"
    "ds_read_b64_tr_b16 a[180:181], v11 offset:8192\n"
    "v_mfma_f32_16x16x32_bf16 v[40:43], a[184:187], a[148:151], v[40:43]\n"
    "ds_read_b64_tr_b16 a[182:183], v11 offset:10240\n"
    "v_mfma_f32_16x16x32_bf16 v[68:71], a[188:191], a[148:151], v[68:71]\n"
    "ds_read_b128 a[112:115], v20 offset:0\n"
    "v_mfma_f32_16x16x32_bf16 v[96:99], a[192:195], a[148:151], v[96:99]\n"
    "ds_read_b128 a[116:119], v20 offset:4096\n"
    "v_mfma_f32_16x16x32_bf16 v[124:127], a[196:199], a[148:151], v[124:127]\n"
    "ds_read_b128 a[120:123], v20 offset:8192\n"
    "v_mfma_f32_16x16x32_bf16 v[44:47], a[184:187], a[152:155], v[44:47]\n"
    "ds_read_b128 a[124:127], v20 offset:12288\n"
    "v_mfma_f32_16x16x32_bf16 v[72:75], a[188:191], a[152:155], v[72:75]\n"
    "ds_read_b128 a[128:131], v20 offset:16384\n"
    "v_mfma_f32_16x16x32_bf16 v[100:103], a[192:195], a[152:155], v[100:103]\n"
    "ds_read_b128 a[132:135], v20 offset:20480\n"
    "v_mfma_f32_16x16x32_bf16 v[128:131], a[196:199], a[152:155], v[128:131]\n"
    "ds_read_b128 a[136:139], v20 offset:24576\n"
    "v_mfma_f32_16x16x32_bf16 v[48:51], a[184:187], a[156:159], v[48:51]\n"
    "s_add_u32 s74, s64, 0\n"
    "v_mfma_f32_16x16x32_bf16 v[76:79], a[188:191], a[156:159], v[76:79]\n"
    "s_add_u32 m0, s66, 0\n"
    "v_mfma_f32_16x16x32_bf16 v[104:107], a[192:195], a[156:159], v[104:107]\n"
    "buffer_load_dwordx4 v16, s[44:47], s74 offen offset:0 lds\n"
    "buffer_load_dwordx4 v17, s[44:47], s74 offen offset:1024 lds\n"
    "v_mfma_f32_16x16x32_bf16 v[132:135], a[196:199], a[156:159], v[132:135]\n"
    "buffer_load_dwordx4 v18, s[44:47], s74 offen offset:2048 lds\n"
    "v_mfma_f32_16x16x32_bf16 v[52:55], a[184:187], a[160:163], v[52:55]\n"
    "buffer_load_dwordx4 v19, s[44:47], s74 offen offset:3072 lds\n"
    "v_mfma_f32_16x16x32_bf16 v[80:83], a[188:191], a[160:163], v[80:83]\n"
    "s_add_u32 s64, s64, 32768\n"
    "v_mfma_f32_16x16x32_bf16 v[108:111], a[192:195], a[160:163], v[108:111]\n"
    "v_mfma_f32_16x16x32_bf16 v[136:139], a[196:199], a[160:163], v[136:139]\n"
    "v_mfma_f32_16x16x32_bf16 v[56:59], a[184:187], a[164:167], v[56:59]\n"
    "v_mfma_f32_16x16x32_bf16 v[84:87], a[188:191], a[164:167], v[84:87]\n"
    "v_mfma_f32_16x16x32_bf16 v[112:115], a[192:195], a[164:167], v[112:115]\n"
    "v_mfma_f32_16x16x32_bf16 v[140:143], a[196:199], a[164:167], v[140:143]\n"
    "s_waitcnt lgkmcnt(0)\n"
    "s_waitcnt vmcnt(4)\n"
    "v_mfma_f32_16x16x32_bf16 v[32:35], a[168:171], a[112:115], v[32:35]\n"
    "ds_read_b64_tr_b16 a[184:185], v12 offset:0\n"
    "v_mfma_f32_16x16x32_bf16 v[60:63], a[172:175], a[112:115], v[60:63]\n"
    "ds_read_b64_tr_b16 a[186:187], v12 offset:2048\n"
    "v_mfma_f32_16x16x32_bf16 v[88:91], a[176:179], a[112:115], v[88:91]\n"
    "ds_read_b64_tr_b16 a[188:189], v13 offset:0\n"
    "v_mfma_f32_16x16x32_bf16 v[116:119], a[180:183], a[112:115], v[116:119]\n"
    "ds_read_b64_tr_b16 a[190:191], v13 offset:2048\n"
    "v_mfma_f32_16x16x32_bf16 v[36:39], a[168:171], a[116:119], v[36:39]\n"
    "ds_read_b64_tr_b16 a[192:193], v14 offset:0\n"
    "v_mfma_f32_16x16x32_bf16 v[64:67], a[172:175], a[116:119], v[64:67]\n"
    "ds_read_b64_tr_b16 a[194:195], v14 offset:2048\n"
    "v_mfma_f32_16x16x32_bf16 v[92:95], a[176:179], a[116:119], v[92:95]\n"
    "ds_read_b64_tr_b16 a[196:197], v15 offset:0\n"
    "v_mfma_f32_16x16x32_bf16 v[120:123], a[180:183], a[116:119], v[120:123]\n"
    "ds_read_b64_tr_b16 a[198:199], v15 offset:2048\n"
    "v_mfma_f32_16x16x32_bf16 v[40:43], a[168:171], a[120:123], v[40:43]\n"
    "ds_read_b128 a[140:143], v20 offset:1024\n"
    "v_mfma_f32_16x16x32_bf16 v[68:71], a[172:175], a[120:123], v[68:71]\n"
    "ds_read_b128 a[144:147], v20 offset:5120\n"
    "v_mfma_f32_16x16x32_bf16 v[96:99], a[176:179], a[120:123], v[96:99]\n"
    "ds_read_b128 a[148:151], v20 offset:9216\n"
    "v_mfma_f32_16x16x32_bf16 v[124:127], a[180:183], a[120:123], v[124:127]\n"
    "ds_read_b128 a[152:155], v20 offset:13312\n"
    "v_mfma_f32_16x16x32_bf16 v[44:47], a[168:171], a[124:127], v[44:47]\n"
    "ds_read_b128 a[156:159], v20 offset:17408\n"
    "v_mfma_f32_16x16x32_bf16 v[72:75], a[172:175], a[124:127], v[72:75]\n"
    "ds_read_b128 a[160:163], v20 offset:21504\n"
    "v_mfma_f32_16x16x32_bf16 v[100:103], a[176:179], a[124:127], v[100:103]\n"
    "ds_read_b128 a[164:167], v20 offset:25600\n"
    "v_mfma_f32_16x16x32_bf16 v[128:131], a[180:183], a[124:127], v[128:131]\n"
    "s_add_u32 s74, s64, 0\n"
    "v_mfma_f32_16x16x32_bf16 v[48:51], a[168:171], a[128:131], v[48:51]\n"
    "s_add_u32 m0, s66, 8192\n"
    "v_mfma_f32_16x16x32_bf16 v[76:79], a[172:175], a[128:131], v[76:79]\n"
    "buffer_load_dwordx4 v16, s[44:47], s74 offen offset:0 lds\n"
    "v_mfma_f32_16x16x32_bf16 v[104:107], a[176:179], a[128:131], v[104:107]\n"
    "buffer_load_dwordx4 v17, s[44:47], s74 offen offset:1024 lds\n"
    "v_mfma_f32_16x16x32_bf16 v[132:135], a[180:183], a[128:131], v[132:135]\n"
    "buffer_load_dwordx4 v18, s[44:47], s74 offen offset:2048 lds\n"
    "v_mfma_f32_16x16x32_bf16 v[52:55], a[168:171], a[132:135], v[52:55]\n"
    "buffer_load_dwordx4 v19, s[44:47], s74 offen offset:3072 lds\n"
    "v_mfma_f32_16x16x32_bf16 v[80:83], a[172:175], a[132:135], v[80:83]\n"
    "s_add_u32 s64, s64, 32768\n"
    "v_mfma_f32_16x16x32_bf16 v[108:111], a[176:179], a[132:135], v[108:111]\n"
    "v_mfma_f32_16x16x32_bf16 v[136:139], a[180:183], a[132:135], v[136:139]\n"
    "v_mfma_f32_16x16x32_bf16 v[56:59], a[168:171], a[136:139], v[56:59]\n"
    "v_mfma_f32_16x16x32_bf16 v[84:87], a[172:175], a[136:139], v[84:87]\n"
    "v_mfma_f32_16x16x32_bf16 v[112:115], a[176:179], a[136:139], v[112:115]\n"
    "v_mfma_f32_16x16x32_bf16 v[140:143], a[180:183], a[136:139], v[140:143]\n"
    "s_waitcnt lgkmcnt(0)\n"
    "s_waitcnt vmcnt(4)\n"
    "v_mfma_f32_16x16x32_bf16 v[32:35], a[184:187], a[140:143], v[32:35]\n"
    "ds_read_b64_tr_b16 a[168:169], v8 offset:0\n"
    "v_mfma_f32_16x16x32_bf16 v[60:63], a[188:191], a[140:143], v[60:63]\n"
    "ds_read_b64_tr_b16 a[170:171], v8 offset:2048\n"
    "v_mfma_f32_16x16x32_bf16 v[88:91], a[192:195], a[140:143], v[88:91]\n"
    "ds_read_b64_tr_b16 a[172:173], v9 offset:0\n"
    "v_mfma_f32_16x16x32_bf16 v[116:119], a[196:199], a[140:143], v[116:119]\n"
    "ds_read_b64_tr_b16 a[174:175], v9 offset:2048\n"
    "v_mfma_f32_16x16x32_bf16 v[36:39], a[184:187], a[144:147], v[36:39]\n"
    "ds_read_b64_tr_b16 a[176:177], v10 offset:0\n"
    "v_mfma_f32_16x16x32_bf16 v[64:67], a[188:191], a[144:147], v[64:67]\n"
    "ds_read_b64_tr_b16 a[178:179], v10 offset:2048\n"
    "v_mfma_f32_16x16x32_bf16 v[92:95], a[192:195], a[144:147], v[92:95]\n"
    "ds_read_b64_tr_b16 a[180:181], v11 offset:0\n"
    "v_mfma_f32_16x16x32_bf16 v[120:123], a[196:199], a[144:147], v[120:123]\n"
    "ds_read_b64_tr_b16 a[182:183], v11 offset:2048\n"
    "v_mfma_f32_16x16x32_bf16 v[40:43], a[184:187], a[148:151], v[40:43]\n"
    "ds_read_b128 a[112:115], v20 offset:2048\n"
    "v_mfma_f32_16x16x32_bf16 v[68:71], a[188:191], a[148:151], v[68:71]\n"
    "ds_read_b128 a[116:119], v20 offset:6144\n"
    "v_mfma_f32_16x16x32_bf16 v[96:99], a[192:195], a[148:151], v[96:99]\n"
    "ds_read_b128 a[120:123], v20 offset:10240\n"
    "v_mfma_f32_16x16x32_bf16 v[124:127], a[196:199], a[148:151], v[124:127]\n"
    "ds_read_b128 a[124:127], v20 offset:14336\n"
    "v_mfma_f32_16x16x32_bf16 v[44:47], a[184:187], a[152:155], v[44:47]\n"
    "ds_read_b128 a[128:131], v20 offset:18432\n"
    "v_mfma_f32_16x16x32_bf16 v[72:75], a[188:191], a[152:155], v[72:75]\n"
    "ds_read_b128 a[132:135], v20 offset:22528\n"
    "v_mfma_f32_16x16x32_bf16 v[100:103], a[192:195], a[152:155], v[100:103]\n"
    "ds_read_b128 a[136:139], v20 offset:26624\n"
    "v_mfma_f32_16x16x32_bf16 v[128:131], a[196:199], a[152:155], v[128:131]\n"
    "s_add_u32 s74, s64, 0\n"
    "v_mfma_f32_16x16x32_bf16 v[48:51], a[184:187], a[156:159], v[48:51]\n"
    "s_mov_b32 m0, s67\n"
    "v_mfma_f32_16x16x32_bf16 v[76:79], a[188:191], a[156:159], v[76:79]\n"
    "buffer_load_dwordx4 v16, s[44:47], s74 offen offset:0 lds\n"
    "v_mfma_f32_16x16x32_bf16 v[104:107], a[192:195], a[156:159], v[104:107]\n"
    "buffer_load_dwordx4 v17, s[44:47], s74 offen offset:1024 lds\n"
    "v_mfma_f32_16x16x32_bf16 v[132:135], a[196:199], a[156:159], v[132:135]\n"
    "buffer_load_dwordx4 v18, s[44:47], s74 offen offset:2048 lds\n"
    "v_mfma_f32_16x16x32_bf16 v[52:55], a[184:187], a[160:163], v[52:55]\n"
    "buffer_load_dwordx4 v19, s[44:47], s74 offen offset:3072 lds\n"
    "v_mfma_f32_16x16x32_bf16 v[80:83], a[188:191], a[160:163], v[80:83]\n"
    "s_add_u32 s64, s64, 32768\n"
    "v_mfma_f32_16x16x32_bf16 v[108:111], a[192:195], a[160:163], v[108:111]\n"
    "v_mfma_f32_16x16x32_bf16 v[136:139], a[196:199], a[160:163], v[136:139]\n"
    "v_mfma_f32_16x16x32_bf16 v[56:59], a[184:187], a[164:167], v[56:59]\n"
    "v_mfma_f32_16x16x32_bf16 v[84:87], a[188:191], a[164:167], v[84:87]\n"
    "v_mfma_f32_16x16x32_bf16 v[112:115], a[192:195], a[164:167], v[112:115]\n"
    "v_mfma_f32_16x16x32_bf16 v[140:143], a[196:199], a[164:167], v[140:143]\n"
    "s_waitcnt lgkmcnt(0)\n"
    "s_waitcnt vmcnt(4)\n"
    "v_mfma_f32_16x16x32_bf16 v[32:35], a[168:171], a[112:115], v[32:35]\n"
    "ds_read_b64_tr_b16 a[184:185], v8 offset:8192\n"
    "v_mfma_f32_16x16x32_bf16 v[60:63], a[172:175], a[112:115], v[60:63]\n"
    "ds_read_b64_tr_b16 a[186:187], v8 offset:10240\n"
    "v_mfma_f32_16x16x32_bf16 v[88:91], a[176:179], a[112:115], v[88:91]\n"
    "ds_read_b64_tr_b16 a[188:189], v9 offset:8192\n"
    "v_mfma_f32_16x16x32_bf16 v[116:119], a[180:183], a[112:115], v[116:119]\n"
    "ds_read_b64_tr_b16 a[190:191], v9 offset:10240\n"
    "v_mfma_f32_16x16x32_bf16 v[36:39], a[168:171], a[116:119], v[36:39]\n"
    "ds_read_b64_tr_b16 a[192:193], v10 offset:8192\n"
    "v_mfma_f32_16x16x32_bf16 v[64:67], a[172:175], a[116:119], v[64:67]\n"
    "ds_read_b64_tr_b16 a[194:195], v10 offset:10240\n"
    "v_mfma_f32_16x16x32_bf16 v[92:95], a[176:179], a[116:119], v[92:95]\n"
    "ds_read_b64_tr_b16 a[196:197], v11 offset:8192\n"
    "v_mfma_f32_16x16x32_bf16 v[120:123], a[180:183], a[116:119], v[120:123]\n"
    "ds_read_b64_tr_b16 a[198:199], v11 offset:10240\n"
    "v_mfma_f32_16x16x32_bf16 v[40:43], a[168:171], a[120:123], v[40:43]\n"
    "ds_read_b128 a[140:143], v20 offset:3072\n"
    "v_mfma_f32_16x16x32_bf16 v[68:71], a[172:175], a[120:123], v[68:71]\n"
    "ds_read_b128 a[144:147], v20 offset:7168\n"
    "v_mfma_f32_16x16x32_bf16 v[96:99], a[176:179], a[120:123], v[96:99]\n"
    "ds_read_b128 a[148:151], v20 offset:11264\n"
    "v_mfma_f32_16x16x32_bf16 v[124:127], a[180:183], a[120:123], v[124:127]\n"
    "ds_read_b128 a[152:155], v20 offset:15360\n"
    "v_mfma_f32_16x16x32_bf16 v[44:47], a[168:171], a[124:127], v[44:47]\n"
    "ds_read_b128 a[156:159], v20 offset:19456\n"
    "v_mfma_f32_16x16x32_bf16 v[72:75], a[172:175], a[124:127], v[72:75]\n"
    "ds_read_b128 a[160:163], v20 offset:23552\n"
    "v_mfma_f32_16x16x32_bf16 v[100:103], a[176:179], a[124:127], v[100:103]\n"
    "ds_read_b128 a[164:167], v20 offset:27648\n"
    "v_mfma_f32_16x16x32_bf16 v[128:131], a[180:183], a[124:127], v[128:131]\n"
    "s_add_u32 s74, s64, 0\n"
    "v_mfma_f32_16x16x32_bf16 v[48:51], a[168:171], a[128:131], v[48:51]\n"
    "s_add_u32 m0, s66, 0\n"
    "v_mfma_f32_16x16x32_bf16 v[76:79], a[172:175], a[128:131], v[76:79]\n"
    "buffer_load_dwordx4 v16, s[44:47], s74 offen offset:0 lds\n"
    "v_mfma_f32_16x16x32_bf16 v[104:107], a[176:179], a[128:131], v[104:107]\n"
    "buffer_load_dwordx4 v17, s[44:47], s74 offen offset:1024 lds\n"
    "v_mfma_f32_16x16x32_bf16 v[132:135], a[180:183], a[128:131], v[132:135]\n"
    "buffer_load_dwordx4 v18, s[44:47], s74 offen offset:2048 lds\n"
    "v_mfma_f32_16x16x32_bf16 v[52:55], a[168:171], a[132:135], v[52:55]\n"
    "buffer_load_dwordx4 v19, s[44:47], s74 offen offset:3072 lds\n"
    "v_mfma_f32_16x16x32_bf16 v[80:83], a[172:175], a[132:135], v[80:83]\n"
    "s_add_u32 s64, s64, 32768\n"
    "v_mfma_f32_16x16x32_bf16 v[108:111], a[176:179], a[132:135], v[108:111]\n"
    "v_mfma_f32_16x16x32_bf16 v[136:139], a[180:183], a[132:135], v[136:139]\n"
    "v_mfma_f32_16x16x32_bf16 v[56:59], a[168:171], a[136:139], v[56:59]\n"
    "v_mfma_f32_16x16x32_bf16 v[84:87], a[172:175], a[136:139], v[84:87]\n"
    "v_mfma_f32_16x16x32_bf16 v[112:115], a[176:179], a[136:139], v[112:115]\n"
    "v_mfma_f32_16x16x32_bf16 v[140:143], a[180:183], a[136:139], v[140:143]\n"
    "s_waitcnt lgkmcnt(0)\n"
    "s_waitcnt vmcnt(4)\n"
    "v_mfma_f32_16x16x32_bf16 v[32:35], a[184:187], a[140:143], v[32:35]\n"
    "v_add_u32 v20, 32768, v20\n"
    "v_mfma_f32_16x16x32_bf16 v[60:63], a[188:191], a[140:143], v[60:63]\n"
    "ds_read_b64_tr_b16 a[168:169], v12 offset:0\n"
    "v_mfma_f32_16x16x32_bf16 v[88:91], a[192:195], a[140:143], v[88:91]\n"
    "ds_read_b64_tr_b16 a[170:171], v12 offset:2048\n"
    "v_mfma_f32_16x16x32_bf16 v[116:119], a[196:199], a[140:143], v[116:119]\n"
    "ds_read_b64_tr_b16 a[172:173], v13 offset:0\n"
    "v_mfma_f32_16x16x32_bf16 v[36:39], a[184:187], a[144:147], v[36:39]\n"
    "ds_read_b64_tr_b16 a[174:175], v13 offset:2048\n"
    "v_mfma_f32_16x16x32_bf16 v[64:67], a[188:191], a[144:147], v[64:67]\n"
    "ds_read_b64_tr_b16 a[176:177], v14 offset:0\n"
    "v_mfma_f32_16x16x32_bf16 v[92:95], a[192:195], a[144:147], v[92:95]\n"
    "ds_read_b64_tr_b16 a[178:179], v14 offset:2048\n"
    "v_mfma_f32_16x16x32_bf16 v[120:123], a[196:199], a[144:147], v[120:123]\n"
    "ds_read_b64_tr_b16 a[180:181], v15 offset:0\n"
    "v_mfma_f32_16x16x32_bf16 v[40:43], a[184:187], a[148:151], v[40:43]\n"
    "ds_read_b64_tr_b16 a[182:183], v15 offset:2048\n"
    "v_mfma_f32_16x16x32_bf16 v[68:71], a[188:191], a[148:151], v[68:71]\n"
    "ds_read_b128 a[112:115], v20 offset:0\n"
    "v_mfma_f32_16x16x32_bf16 v[96:99], a[192:195], a[148:151], v[96:99]\n"
    "ds_read_b128 a[116:119], v20 offset:4096\n"
    "v_mfma_f32_16x16x32_bf16 v[124:127], a[196:199], a[148:151], v[124:127]\n"
    "ds_read_b128 a[120:123], v20 offset:8192\n"
    "v_mfma_f32_16x16x32_bf16 v[44:47], a[184:187], a[152:155], v[44:47]\n"
    "ds_read_b128 a[124:127], v20 offset:12288\n"
    "v_mfma_f32_16x16x32_bf16 v[72:75], a[188:191], a[152:155], v[72:75]\n"
    "ds_read_b128 a[128:131], v20 offset:16384\n"
    "v_mfma_f32_16x16x32_bf16 v[100:103], a[192:195], a[152:155], v[100:103]\n"
    "ds_read_b128 a[132:135], v20 offset:20480\n"
    "v_mfma_f32_16x16x32_bf16 v[128:131], a[196:199], a[152:155], v[128:131]\n"
    "ds_read_b128 a[136:139], v20 offset:24576\n"
    "v_mfma_f32_16x16x32_bf16 v[48:51], a[184:187], a[156:159], v[48:51]\n"
    "s_add_u32 s74, s64, 0\n"
    "v_mfma_f32_16x16x32_bf16 v[76:79], a[188:191], a[156:159], v[76:79]\n"
    "s_add_u32 m0, s66, 8192\n"
    "v_mfma_f32_16x16x32_bf16 v[104:107], a[192:195], a[156:159], v[104:107]\n"
    "buffer_load_dwordx4 v16, s[44:47], s74 offen offset:0 lds\n"
    "buffer_load_dwordx4 v17, s[44:47], s74 offen offset:1024 lds\n"
    "v_mfma_f32_16x16x32_bf16 v[132:135], a[196:199], a[156:159], v[132:135]\n"
    "buffer_load_dwordx4 v18, s[44:47], s74 offen offset:2048 lds\n"
    "v_mfma_f32_16x16x32_bf16 v[52:55], a[184:187], a[160:163], v[52:55]\n"
    "buffer_load_dwordx4 v19, s[44:47], s74 offen offset:3072 lds\n"
    "v_mfma_f32_16x16x32_bf16 v[80:83], a[188:191], a[160:163], v[80:83]\n"
    "s_add_u32 s64, s64, 32768\n"
    "v_mfma_f32_16x16x32_bf16 v[108:111], a[192:195], a[160:163], v[108:111]\n"
    "v_mfma_f32_16x16x32_bf16 v[136:139], a[196:199], a[160:163], v[136:139]\n"
    "v_mfma_f32_16x16x32_bf16 v[56:59], a[184:187], a[164:167], v[56:59]\n"
    "v_mfma_f32_16x16x32_bf16 v[84:87], a[188:191], a[164:167], v[84:87]\n"
    "v_mfma_f32_16x16x32_bf16 v[112:115], a[192:195], a[164:167], v[112:115]\n"
    "v_mfma_f32_16x16x32_bf16 v[140:143], a[196:199], a[164:167], v[140:143]\n"
    "s_waitcnt lgkmcnt(0)\n"
    "s_waitcnt vmcnt(4)\n"
    "v_mfma_f32_16x16x32_bf16 v[32:35], a[168:171], a[112:115], v[32:35]\n"
    "ds_read_b64_tr_b16 a[184:185], v8 offset:0\n"
    "v_mfma_f32_16x16x32_bf16 v[60:63], a[172:175], a[112:115], v[60:63]\n"
    "ds_read_b64_tr_b16 a[186:187], v8 offset:2048\n"
    "v_mfma_f32_16x16x32_bf16 v[88:91], a[176:179], a[112:115], v[88:91]\n"
    "ds_read_b64_tr_b16 a[188:189], v9 offset:0\n"
    "v_mfma_f32_16x16x32_bf16 v[116:119], a[180:183], a[112:115], v[116:119]\n"
    "ds_read_b64_tr_b16 a[190:191], v9 offset:2048\n"
    "v_mfma_f32_16x16x32_bf16 v[36:39], a[168:171], a[116:119], v[36:39]\n"
    "ds_read_b64_tr_b16 a[192:193], v10 offset:0\n"
    "v_mfma_f32_16x16x32_bf16 v[64:67], a[172:175], a[116:119], v[64:67]\n"
    "ds_read_b64_tr_b16 a[194:195], v10 offset:2048\n"
    "v_mfma_f32_16x16x32_bf16 v[92:95], a[176:179], a[116:119], v[92:95]\n"
    "ds_read_b64_tr_b16 a[196:197], v11 offset:0\n"
    "v_mfma_f32_16x16x32_bf16 v[120:123], a[180:183], a[116:119], v[120:123]\n"
    "ds_read_b64_tr_b16 a[198:199], v11 offset:2048\n"
    "v_mfma_f32_16x16x32_bf16 v[40:43], a[168:171], a[120:123], v[40:43]\n"
    "ds_read_b128 a[140:143], v20 offset:1024\n"
    "v_mfma_f32_16x16x32_bf16 v[68:71], a[172:175], a[120:123], v[68:71]\n"
    "ds_read_b128 a[144:147], v20 offset:5120\n"
    "v_mfma_f32_16x16x32_bf16 v[96:99], a[176:179], a[120:123], v[96:99]\n"
    "ds_read_b128 a[148:151], v20 offset:9216\n"
    "v_mfma_f32_16x16x32_bf16 v[124:127], a[180:183], a[120:123], v[124:127]\n"
    "ds_read_b128 a[152:155], v20 offset:13312\n"
    "v_mfma_f32_16x16x32_bf16 v[44:47], a[168:171], a[124:127], v[44:47]\n"
    "ds_read_b128 a[156:159], v20 offset:17408\n"
    "v_mfma_f32_16x16x32_bf16 v[72:75], a[172:175], a[124:127], v[72:75]\n"
    "ds_read_b128 a[160:163], v20 offset:21504\n"
    "v_mfma_f32_16x16x32_bf16 v[100:103], a[176:179], a[124:127], v[100:103]\n"
    "ds_read_b128 a[164:167], v20 offset:25600\n"
    "v_mfma_f32_16x16x32_bf16 v[128:131], a[180:183], a[124:127], v[128:131]\n"
    "s_add_u32 s74, s64, 0\n"
    "v_mfma_f32_16x16x32_bf16 v[48:51], a[168:171], a[128:131], v[48:51]\n"
    "s_mov_b32 m0, s67\n"
    "v_mfma_f32_16x16x32_bf16 v[76:79], a[172:175], a[128:131], v[76:79]\n"
    "buffer_load_dwordx4 v16, s[44:47], s74 offen offset:0 lds\n"
    "v_mfma_f32_16x16x32_bf16 v[104:107], a[176:179], a[128:131], v[104:107]\n"
    "buffer_load_dwordx4 v17, s[44:47], s74 offen offset:1024 lds\n"
    "v_mfma_f32_16x16x32_bf16 v[132:135], a[180:183], a[128:131], v[132:135]\n"
    "buffer_load_dwordx4 v18, s[44:47], s74 offen offset:2048 lds\n"
    "v_mfma_f32_16x16x32_bf16 v[52:55], a[168:171], a[132:135], v[52:55]\n"
    "buffer_load_dwordx4 v19, s[44:47], s74 offen offset:3072 lds\n"
    "v_mfma_f32_16x16x32_bf16 v[80:83], a[172:175], a[132:135], v[80:83]\n"
    "s_add_u32 s64, s64, 32768\n"
    "v_mfma_f32_16x16x32_bf16 v[108:111], a[176:179], a[132:135], v[108:111]\n"
    "v_mfma_f32_16x16x32_bf16 v[136:139], a[180:183], a[132:135], v[136:139]\n"
    "v_mfma_f32_16x16x32_bf16 v[56:59], a[168:171], a[136:139], v[56:59]\n"
    "v_mfma_f32_16x16x32_bf16 v[84:87], a[172:175], a[136:139], v[84:87]\n"
    "v_mfma_f32_16x16x32_bf16 v[112:115], a[176:179], a[136:139], v[112:115]\n"
    "v_mfma_f32_16x16x32_bf16 v[140:143], a[180:183], a[136:139], v[140:143]\n"
    "s_waitcnt lgkmcnt(0)\n"
    "s_waitcnt vmcnt(4)\n"
    "v_mfma_f32_16x16x32_bf16 v[32:35], a[184:187], a[140:143], v[32:35]\n"
    "ds_read_b64_tr_b16 a[168:169], v8 offset:8192\n"
    "v_mfma_f32_16x16x32_bf16 v[60:63], a[188:191], a[140:143], v[60:63]\n"
    "ds_read_b64_tr_b16 a[170:171], v8 offset:10240\n"
    "v_mfma_f32_16x16x32_bf16 v[88:91], a[192:195], a[140:143], v[88:91]\n"
    "ds_read_b64_tr_b16 a[172:173], v9 offset:8192\n"
    "v_mfma_f32_16x16x32_bf16 v[116:119], a[196:199], a[140:143], v[116:119]\n"
    "ds_read_b64_tr_b16 a[174:175], v9 offset:10240\n"
    "v_mfma_f32_16x16x32_bf16 v[36:39], a[184:187], a[144:147], v[36:39]\n"
    "ds_read_b64_tr_b16 a[176:177], v10 offset:8192\n"
    "v_mfma_f32_16x16x32_bf16 v[64:67], a[188:191], a[144:147], v[64:67]\n"
    "ds_read_b64_tr_b16 a[178:179], v10 offset:10240\n"
    "v_mfma_f32_16x16x32_bf16 v[92:95], a[192:195], a[144:147], v[92:95]\n"
    "ds_read_b64_tr_b16 a[180:181], v11 offset:8192\n"
    "v_mfma_f32_16x16x32_bf16 v[120:123], a[196:199], a[144:147], v[120:123]\n"
    "ds_read_b64_tr_b16 a[182:183], v11 offset:10240\n"
    "v_mfma_f32_16x16x32_bf16 v[40:43], a[184:187], a[148:151], v[40:43]\n"
    "ds_read_b128 a[112:115], v20 offset:2048\n"
    "v_mfma_f32_16x16x32_bf16 v[68:71], a[188:191], a[148:151], v[68:71]\n"
    "ds_read_b128 a[116:119], v20 offset:6144\n"
    "v_mfma_f32_16x16x32_bf16 v[96:99], a[192:195], a[148:151], v[96:99]\n"
    "ds_read_b128 a[120:123], v20 offset:10240\n"
    "v_mfma_f32_16x16x32_bf16 v[124:127], a[196:199], a[148:151], v[124:127]\n"
    "ds_read_b128 a[124:127], v20 offset:14336\n"
    "v_mfma_f32_16x16x32_bf16 v[44:47], a[184:187], a[152:155], v[44:47]\n"
    "ds_read_b128 a[128:131], v20 offset:18432\n"
    "v_mfma_f32_16x16x32_bf16 v[72:75], a[188:191], a[152:155], v[72:75]\n"
    "ds_read_b128 a[132:135], v20 offset:22528\n"
    "v_mfma_f32_16x16x32_bf16 v[100:103], a[192:195], a[152:155], v[100:103]\n"
    "ds_read_b128 a[136:139], v20 offset:26624\n"
    "v_mfma_f32_16x16x32_bf16 v[128:131], a[196:199], a[152:155], v[128:131]\n"
    "s_add_u32 s74, s64, 0\n"
    "v_mfma_f32_16x16x32_bf16 v[48:51], a[184:187], a[156:159], v[48:51]\n"
    "s_add_u32 m0, s66, 0\n"
    "v_mfma_f32_16x16x32_bf16 v[76:79], a[188:191], a[156:159], v[76:79]\n"
    "buffer_load_dwordx4 v16, s[44:47], s74 offen offset:0 lds\n"
    "v_mfma_f32_16x16x32_bf16 v[104:107], a[192:195], a[156:159], v[104:107]\n"
    "buffer_load_dwordx4 v17, s[44:47], s74 offen offset:1024 lds\n"
    "v_mfma_f32_16x16x32_bf16 v[132:135], a[196:199], a[156:159], v[132:135]\n"
    "buffer_load_dwordx4 v18, s[44:47], s74 offen offset:2048 lds\n"
    "v_mfma_f32_16x16x32_bf16 v[52:55], a[184:187], a[160:163], v[52:55]\n"
    "buffer_load_dwordx4 v19, s[44:47], s74 offen offset:3072 lds\n"
    "v_mfma_f32_16x16x32_bf16 v[80:83], a[188:191], a[160:163], v[80:83]\n"
    "s_add_u32 s64, s64, 32768\n"
    "v_mfma_f32_16x16x32_bf16 v[108:111], a[192:195], a[160:163], v[108:111]\n"
    "v_mfma_f32_16x16x32_bf16 v[136:139], a[196:199], a[160:163], v[136:139]\n"
    "v_mfma_f32_16x16x32_bf16 v[56:59], a[184:187], a[164:167], v[56:59]\n"
    "v_mfma_f32_16x16x32_bf16 v[84:87], a[188:191], a[164:167], v[84:87]\n"
    "v_mfma_f32_16x16x32_bf16 v[112:115], a[192:195], a[164:167], v[112:115]\n"
    "v_mfma_f32_16x16x32_bf16 v[140:143], a[196:199], a[164:167], v[140:143]\n"
    "s_waitcnt lgkmcnt(0)\n"
    "s_waitcnt vmcnt(4)\n"
    "v_mfma_f32_16x16x32_bf16 v[32:35], a[168:171], a[112:115], v[32:35]\n"
    "ds_read_b64_tr_b16 a[184:185], v12 offset:0\n"
    "v_mfma_f32_16x16x32_bf16 v[60:63], a[172:175], a[112:115], v[60:63]\n"
    "ds_read_b64_tr_b16 a[186:187], v12 offset:2048\n"
    "v_mfma_f32_16x16x32_bf16 v[88:91], a[176:179], a[112:115], v[88:91]\n"
    "ds_read_b64_tr_b16 a[188:189], v13 offset:0\n"
    "v_mfma_f32_16x16x32_bf16 v[116:119], a[180:183], a[112:115], v[116:119]\n"
    "ds_read_b64_tr_b16 a[190:191], v13 offset:2048\n"
    "v_mfma_f32_16x16x32_bf16 v[36:39], a[168:171], a[116:119], v[36:39]\n"
    "ds_read_b64_tr_b16 a[192:193], v14 offset:0\n"
    "v_mfma_f32_16x16x32_bf16 v[64:67], a[172:175], a[116:119], v[64:67]\n"
    "ds_read_b64_tr_b16 a[194:195], v14 offset:2048\n"
    "v_mfma_f32_16x16x32_bf16 v[92:95], a[176:179], a[116:119], v[92:95]\n"
    "ds_read_b64_tr_b16 a[196:197], v15 offset:0\n"
    "v_mfma_f32_16x16x32_bf16 v[120:123], a[180:183], a[116:119], v[120:123]\n"
    "ds_read_b64_tr_b16 a[198:199], v15 offset:2048\n"
    "v_mfma_f32_16x16x32_bf16 v[40:43], a[168:171], a[120:123], v[40:43]\n"
    "ds_read_b128 a[140:143], v20 offset:3072\n"
    "v_mfma_f32_16x16x32_bf16 v[68:71], a[172:175], a[120:123], v[68:71]\n"
    "ds_read_b128 a[144:147], v20 offset:7168\n"
    "v_mfma_f32_16x16x32_bf16 v[96:99], a[176:179], a[120:123], v[96:99]\n"
    "ds_read_b128 a[148:151], v20 offset:11264\n"
    "v_mfma_f32_16x16x32_bf16 v[124:127], a[180:183], a[120:123], v[124:127]\n"
    "ds_read_b128 a[152:155], v20 offset:15360\n"
    "v_mfma_f32_16x16x32_bf16 v[44:47], a[168:171], a[124:127], v[44:47]\n"
    "ds_read_b128 a[156:159], v20 offset:19456\n"
    "v_mfma_f32_16x16x32_bf16 v[72:75], a[172:175], a[124:127], v[72:75]\n"
    "ds_read_b128 a[160:163], v20 offset:23552\n"
    "v_mfma_f32_16x16x32_bf16 v[100:103], a[176:179], a[124:127], v[100:103]\n"
    "ds_read_b128 a[164:167], v20 offset:27648\n"
    "v_mfma_f32_16x16x32_bf16 v[128:131], a[180:183], a[124:127], v[128:131]\n"
    "s_add_u32 s74, s64, 0\n"
    "v_mfma_f32_16x16x32_bf16 v[48:51], a[168:171], a[128:131], v[48:51]\n"
    "s_add_u32 m0, s66, 8192\n"
    "v_mfma_f32_16x16x32_bf16 v[76:79], a[172:175], a[128:131], v[76:79]\n"
    "buffer_load_dwordx4 v16, s[44:47], s74 offen offset:0 lds\n"
    "v_mfma_f32_16x16x32_bf16 v[104:107], a[176:179], a[128:131], v[104:107]\n"
    "buffer_load_dwordx4 v17, s[44:47], s74 offen offset:1024 lds\n"
    "v_mfma_f32_16x16x32_bf16 v[132:135], a[180:183], a[128:131], v[132:135]\n"
    "buffer_load_dwordx4 v18, s[44:47], s74 offen offset:2048 lds\n"
    "v_mfma_f32_16x16x32_bf16 v[52:55], a[168:171], a[132:135], v[52:55]\n"
    "buffer_load_dwordx4 v19, s[44:47], s74 offen offset:3072 lds\n"
    "v_mfma_f32_16x16x32_bf16 v[80:83], a[172:175], a[132:135], v[80:83]\n"
    "s_add_u32 s64, s64, 32768\n"
    "v_mfma_f32_16x16x32_bf16 v[108:111], a[176:179], a[132:135], v[108:111]\n"
    "v_mfma_f32_16x16x32_bf16 v[136:139], a[180:183], a[132:135], v[136:139]\n"
    "v_mfma_f32_16x16x32_bf16 v[56:59], a[168:171], a[136:139], v[56:59]\n"
    "v_mfma_f32_16x16x32_bf16 v[84:87], a[172:175], a[136:139], v[84:87]\n"
    "v_mfma_f32_16x16x32_bf16 v[112:115], a[176:179], a[136:139], v[112:115]\n"
    "v_mfma_f32_16x16x32_bf16 v[140:143], a[180:183], a[136:139], v[140:143]\n"
    "s_waitcnt lgkmcnt(0)\n"
    "s_waitcnt vmcnt(4)\n"
    "v_mfma_f32_16x16x32_bf16 v[32:35], a[184:187], a[140:143], v[32:35]\n"
    "v_add_u32 v20, 32768, v20\n"
    "v_mfma_f32_16x16x32_bf16 v[60:63], a[188:191], a[140:143], v[60:63]\n"
    "ds_read_b64_tr_b16 a[168:169], v8 offset:0\n"
    "v_mfma_f32_16x16x32_bf16 v[88:91], a[192:195], a[140:143], v[88:91]\n"
    "ds_read_b64_tr_b16 a[170:171], v8 offset:2048\n"
    "v_mfma_f32_16x16x32_bf16 v[116:119], a[196:199], a[140:143], v[116:119]\n"
    "ds_read_b64_tr_b16 a[172:173], v9 offset:0\n"
    "v_mfma_f32_16x16x32_bf16 v[36:39], a[184:187], a[144:147], v[36:39]\n"
    "ds_read_b64_tr_b16 a[174:175], v9 offset:2048\n"
    "v_mfma_f32_16x16x32_bf16 v[64:67], a[188:191], a[144:147], v[64:67]\n"
    "ds_read_b64_tr_b16 a[176:177], v10 offset:0\n"
    "v_mfma_f32_16x16x32_bf16 v[92:95], a[192:195], a[144:147], v[92:95]\n"
    "ds_read_b64_tr_b16 a[178:179], v10 offset:2048\n"
    "v_mfma_f32_16x16x32_bf16 v[120:123], a[196:199], a[144:147], v[120:123]\n"
    "ds_read_b64_tr_b16 a[180:181], v11 offset:0\n"
    "v_mfma_f32_16x16x32_bf16 v[40:43], a[184:187], a[148:151], v[40:43]\n"
    "ds_read_b64_tr_b16 a[182:183], v11 offset:2048\n"
    "v_mfma_f32_16x16x32_bf16 v[68:71], a[188:191], a[148:151], v[68:71]\n"
    "ds_read_b128 a[112:115], v20 offset:0\n"
    "v_mfma_f32_16x16x32_bf16 v[96:99], a[192:195], a[148:151], v[96:99]\n"
    "ds_read_b128 a[116:119], v20 offset:4096\n"
    "v_mfma_f32_16x16x32_bf16 v[124:127], a[196:199], a[148:151], v[124:127]\n"
    "ds_read_b128 a[120:123], v20 offset:8192\n"
    "v_mfma_f32_16x16x32_bf16 v[44:47], a[184:187], a[152:155], v[44:47]\n"
    "ds_read_b128 a[124:127], v20 offset:12288\n"
    "v_mfma_f32_16x16x32_bf16 v[72:75], a[188:191], a[152:155], v[72:75]\n"
    "ds_read_b128 a[128:131], v20 offset:16384\n"
    "v_mfma_f32_16x16x32_bf16 v[100:103], a[192:195], a[152:155], v[100:103]\n"
    "ds_read_b128 a[132:135], v20 offset:20480\n"
    "v_mfma_f32_16x16x32_bf16 v[128:131], a[196:199], a[152:155], v[128:131]\n"
    "ds_read_b128 a[136:139], v20 offset:24576\n"
    "v_mfma_f32_16x16x32_bf16 v[48:51], a[184:187], a[156:159], v[48:51]\n"
    "s_add_u32 s74, s64, 0\n"
    "v_mfma_f32_16x16x32_bf16 v[76:79], a[188:191], a[156:159], v[76:79]\n"
    "s_mov_b32 m0, s67\n"
    "v_mfma_f32_16x16x32_bf16 v[104:107], a[192:195], a[156:159], v[104:107]\n"
    "buffer_load_dwordx4 v16, s[44:47], s74 offen offset:0 lds\n"
    "buffer_load_dwordx4 v17, s[44:47], s74 offen offset:1024 lds\n"
    "v_mfma_f32_16x16x32_bf16 v[132:135], a[196:199], a[156:159], v[132:135]\n"
    "buffer_load_dwordx4 v18, s[44:47], s74 offen offset:2048 lds\n"
    "v_mfma_f32_16x16x32_bf16 v[52:55], a[184:187], a[160:163], v[52:55]\n"
    "buffer_load_dwordx4 v19, s[44:47], s74 offen offset:3072 lds\n"
    "v_mfma_f32_16x16x32_bf16 v[80:83], a[188:191], a[160:163], v[80:83]\n"
    "s_add_u32 s64, s64, 32768\n"
    "v_mfma_f32_16x16x32_bf16 v[108:111], a[192:195], a[160:163], v[108:111]\n"
    "v_mfma_f32_16x16x32_bf16 v[136:139], a[196:199], a[160:163], v[136:139]\n"
    "v_mfma_f32_16x16x32_bf16 v[56:59], a[184:187], a[164:167], v[56:59]\n"
    "v_mfma_f32_16x16x32_bf16 v[84:87], a[188:191], a[164:167], v[84:87]\n"
    "v_mfma_f32_16x16x32_bf16 v[112:115], a[192:195], a[164:167], v[112:115]\n"
    "v_mfma_f32_16x16x32_bf16 v[140:143], a[196:199], a[164:167], v[140:143]\n"
    "s_waitcnt lgkmcnt(0)\n"
    "s_waitcnt vmcnt(4)\n"
    "v_mfma_f32_16x16x32_bf16 v[32:35], a[168:171], a[112:115], v[32:35]\n"
    "ds_read_b64_tr_b16 a[184:185], v8 offset:8192\n"
    "v_mfma_f32_16x16x32_bf16 v[60:63], a[172:175], a[112:115], v[60:63]\n"
    "ds_read_b64_tr_b16 a[186:187], v8 offset:10240\n"
    "v_mfma_f32_16x16x32_bf16 v[88:91], a[176:179], a[112:115], v[88:91]\n"
    "ds_read_b64_tr_b16 a[188:189], v9 offset:8192\n"
    "v_mfma_f32_16x16x32_bf16 v[116:119], a[180:183], a[112:115], v[116:119]\n"
    "ds_read_b64_tr_b16 a[190:191], v9 offset:10240\n"
    "v_mfma_f32_16x16x32_bf16 v[36:39], a[168:171], a[116:119], v[36:39]\n"
    "ds_read_b64_tr_b16 a[192:193], v10 offset:8192\n"
    "v_mfma_f32_16x16x32_bf16 v[64:67], a[172:175], a[116:119], v[64:67]\n"
    "ds_read_b64_tr_b16 a[194:195], v10 offset:10240\n"
    "v_mfma_f32_16x16x32_bf16 v[92:95], a[176:179], a[116:119], v[92:95]\n"
    "ds_read_b64_tr_b16 a[196:197], v11 offset:8192\n"
    "v_mfma_f32_16x16x32_bf16 v[120:123], a[180:183], a[116:119], v[120:123]\n"
    "ds_read_b64_tr_b16 a[198:199], v11 offset:10240\n"
    "v_mfma_f32_16x16x32_bf16 v[40:43], a[168:171], a[120:123], v[40:43]\n"
    "ds_read_b128 a[140:143], v20 offset:1024\n"
    "v_mfma_f32_16x16x32_bf16 v[68:71], a[172:175], a[120:123], v[68:71]\n"
    "ds_read_b128 a[144:147], v20 offset:5120\n"
    "v_mfma_f32_16x16x32_bf16 v[96:99], a[176:179], a[120:123], v[96:99]\n"
    "ds_read_b128 a[148:151], v20 offset:9216\n"
    "v_mfma_f32_16x16x32_bf16 v[124:127], a[180:183], a[120:123], v[124:127]\n"
    "ds_read_b128 a[152:155], v20 offset:13312\n"
    "v_mfma_f32_16x16x32_bf16 v[44:47], a[168:171], a[124:127], v[44:47]\n"
    "ds_read_b128 a[156:159], v20 offset:17408\n"
    "v_mfma_f32_16x16x32_bf16 v[72:75], a[172:175], a[124:127], v[72:75]\n"
    "ds_read_b128 a[160:163], v20 offset:21504\n"
    "v_mfma_f32_16x16x32_bf16 v[100:103], a[176:179], a[124:127], v[100:103]\n"
    "ds_read_b128 a[164:167], v20 offset:25600\n"
    "v_mfma_f32_16x16x32_bf16 v[128:131], a[180:183], a[124:127], v[128:131]\n"
    "s_add_u32 s74, s64, 0\n"
    "v_mfma_f32_16x16x32_bf16 v[48:51], a[168:171], a[128:131], v[48:51]\n"
    "s_add_u32 m0, s66, 0\n"
    "v_mfma_f32_16x16x32_bf16 v[76:79], a[172:175], a[128:131], v[76:79]\n"
    "buffer_load_dwordx4 v16, s[44:47], s74 offen offset:0 lds\n"
    "v_mfma_f32_16x16x32_bf16 v[104:107], a[176:179], a[128:131], v[104:107]\n"
    "buffer_load_dwordx4 v17, s[44:47], s74 offen offset:1024 lds\n"
    "v_mfma_f32_16x16x32_bf16 v[132:135], a[180:183], a[128:131], v[132:135]\n"
    "buffer_load_dwordx4 v18, s[44:47], s74 offen offset:2048 lds\n"
    "v_mfma_f32_16x16x32_bf16 v[52:55], a[168:171], a[132:135], v[52:55]\n"
    "buffer_load_dwordx4 v19, s[44:47], s74 offen offset:3072 lds\n"
    "v_mfma_f32_16x16x32_bf16 v[80:83], a[172:175], a[132:135], v[80:83]\n"
    "s_add_u32 s64, s64, 32768\n"
    "v_mfma_f32_16x16x32_bf16 v[108:111], a[176:179], a[132:135], v[108:111]\n"
    "v_mfma_f32_16x16x32_bf16 v[136:139], a[180:183], a[132:135], v[136:139]\n"
    "v_mfma_f32_16x16x32_bf16 v[56:59], a[168:171], a[136:139], v[56:59]\n"
    "v_mfma_f32_16x16x32_bf16 v[84:87], a[172:175], a[136:139], v[84:87]\n"
    "v_mfma_f32_16x16x32_bf16 v[112:115], a[176:179], a[136:139], v[112:115]\n"
    "v_mfma_f32_16x16x32_bf16 v[140:143], a[180:183], a[136:139], v[140:143]\n"
    "s_waitcnt lgkmcnt(0)\n"
    "s_waitcnt vmcnt(4)\n"
    "v_mfma_f32_16x16x32_bf16 v[32:35], a[184:187], a[140:143], v[32:35]\n"
    "ds_read_b64_tr_b16 a[168:169], v12 offset:0\n"
    "v_mfma_f32_16x16x32_bf16 v[60:63], a[188:191], a[140:143], v[60:63]\n"
    "ds_read_b64_tr_b16 a[170:171], v12 offset:2048\n"
    "v_mfma_f32_16x16x32_bf16 v[88:91], a[192:195], a[140:143], v[88:91]\n"
    "ds_read_b64_tr_b16 a[172:173], v13 offset:0\n"
    "v_mfma_f32_16x16x32_bf16 v[116:119], a[196:199], a[140:143], v[116:119]\n"
    "ds_read_b64_tr_b16 a[174:175], v13 offset:2048\n"
    "v_mfma_f32_16x16x32_bf16 v[36:39], a[184:187], a[144:147], v[36:39]\n"
    "ds_read_b64_tr_b16 a[176:177], v14 offset:0\n"
    "v_mfma_f32_16x16x32_bf16 v[64:67], a[188:191], a[144:147], v[64:67]\n"
    "ds_read_b64_tr_b16 a[178:179], v14 offset:2048\n"
    "v_mfma_f32_16x16x32_bf16 v[92:95], a[192:195], a[144:147], v[92:95]\n"
    "ds_read_b64_tr_b16 a[180:181], v15 offset:0\n"
    "v_mfma_f32_16x16x32_bf16 v[120:123], a[196:199], a[144:147], v[120:123]\n"
    "ds_read_b64_tr_b16 a[182:183], v15 offset:2048\n"
    "v_mfma_f32_16x16x32_bf16 v[40:43], a[184:187], a[148:151], v[40:43]\n"
    "ds_read_b128 a[112:115], v20 offset:2048\n"
    "v_mfma_f32_16x16x32_bf16 v[68:71], a[188:191], a[148:151], v[68:71]\n"
    "ds_read_b128 a[116:119], v20 offset:6144\n"
    "v_mfma_f32_16x16x32_bf16 v[96:99], a[192:195], a[148:151], v[96:99]\n"
    "ds_read_b128 a[120:123], v20 offset:10240\n"
    "v_mfma_f32_16x16x32_bf16 v[124:127], a[196:199], a[148:151], v[124:127]\n"
    "ds_read_b128 a[124:127], v20 offset:14336\n"
    "v_mfma_f32_16x16x32_bf16 v[44:47], a[184:187], a[152:155], v[44:47]\n"
    "ds_read_b128 a[128:131], v20 offset:18432\n"
    "v_mfma_f32_16x16x32_bf16 v[72:75], a[188:191], a[152:155], v[72:75]\n"
    "ds_read_b128 a[132:135], v20 offset:22528\n"
    "v_mfma_f32_16x16x32_bf16 v[100:103], a[192:195], a[152:155], v[100:103]\n"
    "ds_read_b128 a[136:139], v20 offset:26624\n"
    "v_mfma_f32_16x16x32_bf16 v[128:131], a[196:199], a[152:155], v[128:131]\n"
    "s_add_u32 s74, s64, 0\n"
    "v_mfma_f32_16x16x32_bf16 v[48:51], a[184:187], a[156:159], v[48:51]\n"
    "s_add_u32 m0, s66, 8192\n"
    "v_mfma_f32_16x16x32_bf16 v[76:79], a[188:191], a[156:159], v[76:79]\n"
    "buffer_load_dwordx4 v16, s[44:47], s74 offen offset:0 lds\n"
    "v_mfma_f32_16x16x32_bf16 v[104:107], a[192:195], a[156:159], v[104:107]\n"
    "buffer_load_dwordx4 v17, s[44:47], s74 offen offset:1024 lds\n"
    "v_mfma_f32_16x16x32_bf16 v[132:135], a[196:199], a[156:159], v[132:135]\n"
    "buffer_load_dwordx4 v18, s[44:47], s74 offen offset:2048 lds\n"
    "v_mfma_f32_16x16x32_bf16 v[52:55], a[184:187], a[160:163], v[52:55]\n"
    "buffer_load_dwordx4 v19, s[44:47], s74 offen offset:3072 lds\n"
    "v_mfma_f32_16x16x32_bf16 v[80:83], a[188:191], a[160:163], v[80:83]\n"
    "s_add_u32 s64, s64, 32768\n"
    "v_mfma_f32_16x16x32_bf16 v[108:111], a[192:195], a[160:163], v[108:111]\n"
    "v_mfma_f32_16x16x32_bf16 v[136:139], a[196:199], a[160:163], v[136:139]\n"
    "v_mfma_f32_16x16x32_bf16 v[56:59], a[184:187], a[164:167], v[56:59]\n"
    "v_mfma_f32_16x16x32_bf16 v[84:87], a[188:191], a[164:167], v[84:87]\n"
    "v_mfma_f32_16x16x32_bf16 v[112:115], a[192:195], a[164:167], v[112:115]\n"
    "v_mfma_f32_16x16x32_bf16 v[140:143], a[196:199], a[164:167], v[140:143]\n"
    "s_waitcnt lgkmcnt(0)\n"
    "s_waitcnt vmcnt(4)\n"
    "v_mfma_f32_16x16x32_bf16 v[32:35], a[168:171], a[112:115], v[32:35]\n"
    "ds_read_b64_tr_b16 a[184:185], v8 offset:0\n"
    "v_mfma_f32_16x16x32_bf16 v[60:63], a[172:175], a[112:115], v[60:63]\n"
    "ds_read_b64_tr_b16 a[186:187], v8 offset:2048\n"
    "v_mfma_f32_16x16x32_bf16 v[88:91], a[176:179], a[112:115], v[88:91]\n"
    "ds_read_b64_tr_b16 a[188:189], v9 offset:0\n"
    "v_mfma_f32_16x16x32_bf16 v[116:119], a[180:183], a[112:115], v[116:119]\n"
    "ds_read_b64_tr_b16 a[190:191], v9 offset:2048\n"
    "v_mfma_f32_16x16x32_bf16 v[36:39], a[168:171], a[116:119], v[36:39]\n"
    "ds_read_b64_tr_b16 a[192:193], v10 offset:0\n"
    "v_mfma_f32_16x16x32_bf16 v[64:67], a[172:175], a[116:119], v[64:67]\n"
    "ds_read_b64_tr_b16 a[194:195], v10 offset:2048\n"
    "v_mfma_f32_16x16x32_bf16 v[92:95], a[176:179], a[116:119], v[92:95]\n"
    "ds_read_b64_tr_b16 a[196:197], v11 offset:0\n"
    "v_mfma_f32_16x16x32_bf16 v[120:123], a[180:183], a[116:119], v[120:123]\n"
    "ds_read_b64_tr_b16 a[198:199], v11 offset:2048\n"
    "v_mfma_f32_16x16x32_bf16 v[40:43], a[168:171], a[120:123], v[40:43]\n"
    "ds_read_b128 a[140:143], v20 offset:3072\n"
    "v_mfma_f32_16x16x32_bf16 v[68:71], a[172:175], a[120:123], v[68:71]\n"
    "ds_read_b128 a[144:147], v20 offset:7168\n"
    "v_mfma_f32_16x16x32_bf16 v[96:99], a[176:179], a[120:123], v[96:99]\n"
    "ds_read_b128 a[148:151], v20 offset:11264\n"
    "v_mfma_f32_16x16x32_bf16 v[124:127], a[180:183], a[120:123], v[124:127]\n"
    "ds_read_b128 a[152:155], v20 offset:15360\n"
    "v_mfma_f32_16x16x32_bf16 v[44:47], a[168:171], a[124:127], v[44:47]\n"
    "ds_read_b128 a[156:159], v20 offset:19456\n"
    "v_mfma_f32_16x16x32_bf16 v[72:75], a[172:175], a[124:127], v[72:75]\n"
    "ds_read_b128 a[160:163], v20 offset:23552\n"
    "v_mfma_f32_16x16x32_bf16 v[100:103], a[176:179], a[124:127], v[100:103]\n"
    "ds_read_b128 a[164:167], v20 offset:27648\n"
    "v_mfma_f32_16x16x32_bf16 v[128:131], a[180:183], a[124:127], v[128:131]\n"
    "s_add_u32 s74, s64, 0\n"
    "v_mfma_f32_16x16x32_bf16 v[48:51], a[168:171], a[128:131], v[48:51]\n"
    "s_mov_b32 m0, s67\n"
    "v_mfma_f32_16x16x32_bf16 v[76:79], a[172:175], a[128:131], v[76:79]\n"
    "buffer_load_dwordx4 v16, s[44:47], s74 offen offset:0 lds\n"
    "v_mfma_f32_16x16x32_bf16 v[104:107], a[176:179], a[128:131], v[104:107]\n"
    "buffer_load_dwordx4 v17, s[44:47], s74 offen offset:1024 lds\n"
    "v_mfma_f32_16x16x32_bf16 v[132:135], a[180:183], a[128:131], v[132:135]\n"
    "buffer_load_dwordx4 v18, s[44:47], s74 offen offset:2048 lds\n"
    "v_mfma_f32_16x16x32_bf16 v[52:55], a[168:171], a[132:135], v[52:55]\n"
    "buffer_load_dwordx4 v19, s[44:47], s74 offen offset:3072 lds\n"
    "v_mfma_f32_16x16x32_bf16 v[80:83], a[172:175], a[132:135], v[80:83]\n"
    "s_add_u32 s64, s64, 32768\n"
    "v_mfma_f32_16x16x32_bf16 v[108:111], a[176:179], a[132:135], v[108:111]\n"
    "v_mfma_f32_16x16x32_bf16 v[136:139], a[180:183], a[132:135], v[136:139]\n"
    "v_mfma_f32_16x16x32_bf16 v[56:59], a[168:171], a[136:139], v[56:59]\n"
    "v_mfma_f32_16x16x32_bf16 v[84:87], a[172:175], a[136:139], v[84:87]\n"
    "v_mfma_f32_16x16x32_bf16 v[112:115], a[176:179], a[136:139], v[112:115]\n"
    "v_mfma_f32_16x16x32_bf16 v[140:143], a[180:183], a[136:139], v[140:143]\n"
    "s_waitcnt lgkmcnt(0)\n"
    "s_waitcnt vmcnt(4)\n"
    "v_mfma_f32_16x16x32_bf16 v[32:35], a[184:187], a[140:143], v[32:35]\n"
    "v_add_u32 v20, 32768, v20\n"
    "v_mfma_f32_16x16x32_bf16 v[60:63], a[188:191], a[140:143], v[60:63]\n"
    "ds_read_b64_tr_b16 a[168:169], v8 offset:8192\n"
    "v_mfma_f32_16x16x32_bf16 v[88:91], a[192:195], a[140:143], v[88:91]\n"
    "ds_read_b64_tr_b16 a[170:171], v8 offset:10240\n"
    "v_mfma_f32_16x16x32_bf16 v[116:119], a[196:199], a[140:143], v[116:119]\n"
    "ds_read_b64_tr_b16 a[172:173], v9 offset:8192\n"
    "v_mfma_f32_16x16x32_bf16 v[36:39], a[184:187], a[144:147], v[36:39]\n"
    "ds_read_b64_tr_b16 a[174:175], v9 offset:10240\n"
    "v_mfma_f32_16x16x32_bf16 v[64:67], a[188:191], a[144:147], v[64:67]\n"
    "ds_read_b64_tr_b16 a[176:177], v10 offset:8192\n"
    "v_mfma_f32_16x16x32_bf16 v[92:95], a[192:195], a[144:147], v[92:95]\n"
    "ds_read_b64_tr_b16 a[178:179], v10 offset:10240\n"
    "v_mfma_f32_16x16x32_bf16 v[120:123], a[196:199], a[144:147], v[120:123]\n"
    "ds_read_b64_tr_b16 a[180:181], v11 offset:8192\n"
    "v_mfma_f32_16x16x32_bf16 v[40:43], a[184:187], a[148:151], v[40:43]\n"
    "ds_read_b64_tr_b16 a[182:183], v11 offset:10240\n"
    "v_mfma_f32_16x16x32_bf16 v[68:71], a[188:191], a[148:151], v[68:71]\n"
    "ds_read_b128 a[112:115], v20 offset:0\n"
    "v_mfma_f32_16x16x32_bf16 v[96:99], a[192:195], a[148:151], v[96:99]\n"
    "ds_read_b128 a[116:119], v20 offset:4096\n"
    "v_mfma_f32_16x16x32_bf16 v[124:127], a[196:199], a[148:151], v[124:127]\n"
    "ds_read_b128 a[120:123], v20 offset:8192\n"
    "v_mfma_f32_16x16x32_bf16 v[44:47], a[184:187], a[152:155], v[44:47]\n"
    "ds_read_b128 a[124:127], v20 offset:12288\n"
    "v_mfma_f32_16x16x32_bf16 v[72:75], a[188:191], a[152:155], v[72:75]\n"
    "ds_read_b128 a[128:131], v20 offset:16384\n"
    "v_mfma_f32_16x16x32_bf16 v[100:103], a[192:195], a[152:155], v[100:103]\n"
    "ds_read_b128 a[132:135], v20 offset:20480\n"
    "v_mfma_f32_16x16x32_bf16 v[128:131], a[196:199], a[152:155], v[128:131]\n"
    "ds_read_b128 a[136:139], v20 offset:24576\n"
    "v_mfma_f32_16x16x32_bf16 v[48:51], a[184:187], a[156:159], v[48:51]\n"
    "s_add_u32 s74, s64, 0\n"
    "v_mfma_f32_16x16x32_bf16 v[76:79], a[188:191], a[156:159], v[76:79]\n"
    "s_add_u32 m0, s66, 0\n"
    "v_mfma_f32_16x16x32_bf16 v[104:107], a[192:195], a[156:159], v[104:107]\n"
    "buffer_load_dwordx4 v16, s[44:47], s74 offen offset:0 lds\n"
    "buffer_load_dwordx4 v17, s[44:47], s74 offen offset:1024 lds\n"
    "v_mfma_f32_16x16x32_bf16 v[132:135], a[196:199], a[156:159], v[132:135]\n"
    "buffer_load_dwordx4 v18, s[44:47], s74 offen offset:2048 lds\n"
    "v_mfma_f32_16x16x32_bf16 v[52:55], a[184:187], a[160:163], v[52:55]\n"
    "buffer_load_dwordx4 v19, s[44:47], s74 offen offset:3072 lds\n"
    "v_mfma_f32_16x16x32_bf16 v[80:83], a[188:191], a[160:163], v[80:83]\n"
    "s_add_u32 s64, s64, 32768\n"
    "v_mfma_f32_16x16x32_bf16 v[108:111], a[192:195], a[160:163], v[108:111]\n"
    "v_mfma_f32_16x16x32_bf16 v[136:139], a[196:199], a[160:163], v[136:139]\n"
    "v_mfma_f32_16x16x32_bf16 v[56:59], a[184:187], a[164:167], v[56:59]\n"
    "v_mfma_f32_16x16x32_bf16 v[84:87], a[188:191], a[164:167], v[84:87]\n"
    "v_mfma_f32_16x16x32_bf16 v[112:115], a[192:195], a[164:167], v[112:115]\n"
    "v_mfma_f32_16x16x32_bf16 v[140:143], a[196:199], a[164:167], v[140:143]\n"
    "s_waitcnt lgkmcnt(0)\n"
    "s_nop 7\n"
    "v_cvt_pk_bf16_f32 v32, v32, v33\n"
    "v_cvt_pk_bf16_f32 v33, v34, v35\n"
    "v_cvt_pk_bf16_f32 v34, v60, v61\n"
    "v_cvt_pk_bf16_f32 v35, v62, v63\n"
    "s_add_u32 s77, s69, 0\n"
    "buffer_store_dwordx4 v[32:35], v24, s[40:43], s77 offen sc1\n"
    "v_cvt_pk_bf16_f32 v36, v36, v37\n"
    "v_cvt_pk_bf16_f32 v37, v38, v39\n"
    "v_cvt_pk_bf16_f32 v38, v64, v65\n"
    "v_cvt_pk_bf16_f32 v39, v66, v67\n"
    "s_add_u32 s77, s69, 1024\n"
    "buffer_store_dwordx4 v[36:39], v24, s[40:43], s77 offen sc1\n"
    "v_cvt_pk_bf16_f32 v40, v40, v41\n"
    "v_cvt_pk_bf16_f32 v41, v42, v43\n"
    "v_cvt_pk_bf16_f32 v42, v68, v69\n"
    "v_cvt_pk_bf16_f32 v43, v70, v71\n"
    "s_add_u32 s77, s69, 2048\n"
    "buffer_store_dwordx4 v[40:43], v24, s[40:43], s77 offen sc1\n"
    "v_cvt_pk_bf16_f32 v44, v44, v45\n"
    "v_cvt_pk_bf16_f32 v45, v46, v47\n"
    "v_cvt_pk_bf16_f32 v46, v72, v73\n"
    "v_cvt_pk_bf16_f32 v47, v74, v75\n"
    "s_add_u32 s77, s69, 3072\n"
    "buffer_store_dwordx4 v[44:47], v24, s[40:43], s77 offen sc1\n"
    "v_cvt_pk_bf16_f32 v48, v48, v49\n"
    "v_cvt_pk_bf16_f32 v49, v50, v51\n"
    "v_cvt_pk_bf16_f32 v50, v76, v77\n"
    "v_cvt_pk_bf16_f32 v51, v78, v79\n"
    "s_add_u32 s77, s69, 4096\n"
    "buffer_store_dwordx4 v[48:51], v24, s[40:43], s77 offen sc1\n"
    "v_cvt_pk_bf16_f32 v52, v52, v53\n"
    "v_cvt_pk_bf16_f32 v53, v54, v55\n"
    "v_cvt_pk_bf16_f32 v54, v80, v81\n"
    "v_cvt_pk_bf16_f32 v55, v82, v83\n"
    "s_add_u32 s77, s69, 5120\n"
    "buffer_store_dwordx4 v[52:55], v24, s[40:43], s77 offen sc1\n"
    "v_cvt_pk_bf16_f32 v56, v56, v57\n"
    "v_cvt_pk_bf16_f32 v57, v58, v59\n"
    "v_cvt_pk_bf16_f32 v58, v84, v85\n"
    "v_cvt_pk_bf16_f32 v59, v86, v87\n"
    "s_add_u32 s77, s69, 6144\n"
    "buffer_store_dwordx4 v[56:59], v24, s[40:43], s77 offen sc1\n"
    "v_cvt_pk_bf16_f32 v88, v88, v89\n"
    "v_cvt_pk_bf16_f32 v89, v90, v91\n"
    "v_cvt_pk_bf16_f32 v90, v116, v117\n"
    "v_cvt_pk_bf16_f32 v91, v118, v119\n"
    "s_add_u32 s77, s69, 7168\n"
    "buffer_store_dwordx4 v[88:91], v24, s[40:43], s77 offen sc1\n"
    "v_cvt_pk_bf16_f32 v92, v92, v93\n"
    "v_cvt_pk_bf16_f32 v93, v94, v95\n"
    "v_cvt_pk_bf16_f32 v94, v120, v121\n"
    "v_cvt_pk_bf16_f32 v95, v122, v123\n"
    "s_add_u32 s77, s69, 8192\n"
    "buffer_store_dwordx4 v[92:95], v24, s[40:43], s77 offen sc1\n"
    "v_cvt_pk_bf16_f32 v96, v96, v97\n"
    "v_cvt_pk_bf16_f32 v97, v98, v99\n"
    "v_cvt_pk_bf16_f32 v98, v124, v125\n"
    "v_cvt_pk_bf16_f32 v99, v126, v127\n"
    "s_add_u32 s77, s69, 9216\n"
    "buffer_store_dwordx4 v[96:99], v24, s[40:43], s77 offen sc1\n"
    "v_cvt_pk_bf16_f32 v100, v100, v101\n"
    "v_cvt_pk_bf16_f32 v101, v102, v103\n"
    "v_cvt_pk_bf16_f32 v102, v128, v129\n"
    "v_cvt_pk_bf16_f32 v103, v130, v131\n"
    "s_add_u32 s77, s69, 10240\n"
    "buffer_store_dwordx4 v[100:103], v24, s[40:43], s77 offen sc1\n"
    "v_cvt_pk_bf16_f32 v104, v104, v105\n"
    "v_cvt_pk_bf16_f32 v105, v106, v107\n"
    "v_cvt_pk_bf16_f32 v106, v132, v133\n"
    "v_cvt_pk_bf16_f32 v107, v134, v135\n"
    "s_add_u32 s77, s69, 11264\n"
    "buffer_store_dwordx4 v[104:107], v24, s[40:43], s77 offen sc1\n"
    "v_cvt_pk_bf16_f32 v108, v108, v109\n"
    "v_cvt_pk_bf16_f32 v109, v110, v111\n"
    "v_cvt_pk_bf16_f32 v110, v136, v137\n"
    "v_cvt_pk_bf16_f32 v111, v138, v139\n"
    "s_add_u32 s77, s69, 12288\n"
    "buffer_store_dwordx4 v[108:111], v24, s[40:43], s77 offen sc1\n"
    "v_cvt_pk_bf16_f32 v112, v112, v113\n"
    "v_cvt_pk_bf16_f32 v113, v114, v115\n"
    "v_cvt_pk_bf16_f32 v114, v140, v141\n"
    "v_cvt_pk_bf16_f32 v115, v142, v143\n"
    "s_add_u32 s77, s69, 13312\n"
    "buffer_store_dwordx4 v[112:115], v24, s[40:43], s77 offen sc1\n"
    ".LX_end_%=:\n"
    "s_waitcnt vmcnt(0) lgkmcnt(0)\n"
    "s_waitcnt vmcnt(0)\n"
    "s_mov_b32 %[bad], s75\n"
    : [bad] "=s"(bad)
    : [wid] "s"(wid), [kp] "s"(kp), [vp] "s"(vp), [wp] "s"(wp), [xp] "s"(x), [nw] "s"(nw), [lds] "s"(ldsb), [wop] "s"(wop), [pp] "s"(P), [pto] "s"(pto), [wpa] "s"(wpa), [nwa] "s"(nwa)
    : "v6", "v7", "v8", "v9", "v10", "v11", "v12", "v13", "v14", "v15", "v16", "v17", "v18", "v19", "v20", "v21", "v22", "v23",
      "v24", "v25", "v26", "v27", "v28", "v29", "v30", "v31", "v32", "v33", "v34", "v35", "v36", "v37", "v38", "v39",
      "v40", "v41", "v42", "v43", "v44", "v45", "v46", "v47", "v48", "v49", "v50", "v51", "v52", "v53", "v54", "v55",
      "v56", "v57", "v58", "v59", "v60", "v61", "v62", "v63", "v64", "v65", "v66", "v67", "v68", "v69", "v70", "v71",
      "v72", "v73", "v74", "v75", "v76", "v77", "v78", "v79", "v80", "v81", "v82", "v83", "v84", "v85", "v86", "v87",
      "v88", "v89", "v90", "v91", "v92", "v93", "v94", "v95", "v96", "v97", "v98", "v99", "v100", "v101", "v102", "v103",
      "v104", "v105", "v106", "v107", "v108", "v109", "v110", "v111", "v112", "v113", "v114", "v115", "v116", "v117", "v118", "v119",
      "v120", "v121", "v122", "v123", "v124", "v125", "v126", "v127", "v128", "v129", "v130", "v131", "v132", "v133", "v134", "v135",
      "v136", "v137", "v138", "v139", "v140", "v141", "v142", "v143", "v144", "v145", "v146", "v147", "v148", "v149", "v150", "v151",
      "v152", "v153", "v154", "v155", "v156", "v157", "v158", "v159", "v160", "v161", "v162", "v163", "v164", "v165", "v166", "v167",
      "v168", "v169", "v170", "v171", "v172", "v173", "v174", "v175", "v176", "v177", "v178", "v179", "v180", "v181", "v182", "v183",
      "v184", "v185", "v186", "v187", "v188", "v189", "v190", "v191", "v192", "v193", "v194", "v195", "v196", "v197", "v198", "v199",
      "v200", "v201", "v202", "v203", "v204", "v205", "v206", "v207", "v208", "v209", "v210", "v211", "v212", "v213", "v214", "v215",
      "v216", "v217", "v218", "v219", "v220", "v221", "v222", "v223", "v224", "v225", "v226", "v227", "v228", "v229", "v230", "v231",
      "v232", "v233", "v234", "v235", "v236", "v237", "v238", "v239", "v240", "v241", "v242", "v243", "v244", "v245", "v246", "v247",
      "v248", "v249", "v250", "v251", "v252", "v253", "v254", "v255", "a0", "a1", "a2", "a3", "a4", "a5", "a6", "a7",
      "a8", "a9", "a10", "a11", "a12", "a13", "a14", "a15", "a16", "a17", "a18", "a19", "a20", "a21", "a22", "a23",
      "a24", "a25", "a26", "a27", "a28", "a29", "a30", "a31", "a32", "a33", "a34", "a35", "a36", "a37", "a38", "a39",
      "a40", "a41", "a42", "a43", "a44", "a45", "a46", "a47", "a48", "a49", "a50", "a51", "a52", "a53", "a54", "a55",
      "a56", "a57", "a58", "a59", "a60", "a61", "a62", "a63", "a64", "a65", "a66", "a67", "a68", "a69", "a70", "a71",
      "a72", "a73", "a74", "a75", "a76", "a77", "a78", "a79", "a80", "a81", "a82", "a83", "a84", "a85", "a86", "a87",
      "a88", "a89", "a90", "a91", "a92", "a93", "a94", "a95", "a96", "a97", "a98", "a99", "a100", "a101", "a102", "a103",
      "a104", "a105", "a106", "a107", "a108", "a109", "a110", "a111", "a112", "a113", "a114", "a115", "a116", "a117", "a118", "a119",
      "a120", "a121", "a122", "a123", "a124", "a125", "a126", "a127", "a128", "a129", "a130", "a131", "a132", "a133", "a134", "a135",
      "a136", "a137", "a138", "a139", "a140", "a141", "a142", "a143", "a144", "a145", "a146", "a147", "a148", "a149", "a150", "a151",
      "a152", "a153", "a154", "a155", "a156", "a157", "a158", "a159", "a160", "a161", "a162", "a163", "a164", "a165", "a166", "a167",
      "a168", "a169", "a170", "a171", "a172", "a173", "a174", "a175", "a176", "a177", "a178", "a179", "a180", "a181", "a182", "a183",
      "a184", "a185", "a186", "a187", "a188", "a189", "a190", "a191", "a192", "a193", "a194", "a195", "a196", "a197", "a198", "a199",
      "a200", "a201", "a202", "a203", "a204", "a205", "a206", "a207", "a208", "a209", "a210", "a211", "a212", "a213", "a214", "a215",
      "a216", "a217", "a218", "a219", "a220", "a221", "a222", "a223", "a224", "a225", "a226", "a227", "a228", "a229", "a230", "a231",
      "a232", "a233", "a234", "a235", "a236", "a237", "a238", "a239", "a240", "a241", "a242", "a243", "a244", "a245", "a246", "a247",
      "a248", "a249", "a250", "a251", "a252", "a253", "a254", "a255", "s40", "s41", "s42", "s43", "s44", "s45", "s46", "s47",
      "s48", "s49", "s50", "s51", "s52", "s53", "s54", "s55", "s56", "s57", "s58", "s59", "s60", "s61", "s62", "s63",
      "s64", "s65", "s66", "s67", "s68", "s69", "s70", "s71", "s72", "s73", "s74", "s75", "s76", "s77", "s78", "s79",
      "s80", "s81", "s82", "s83", "s84", "s85", "s86", "s87", "s88", "s89", "s90", "s91", "s92", "s93", "s94", "s95",
      "s96", "s97", "s98", "s99", "vcc", "scc", "memory");
  typedef __attribute__((address_space(3))) volatile unsigned lu32;
  lu32* flag = (lu32*)(lds + FLAG_OFF);
  __syncthreads();
  if ((threadIdx.x & 63) == 0) flag[wid] = bad;
  __syncthreads();
  if (flag[0] | flag[1] | flag[2] | flag[3]) {
    __syncthreads();
    safe_block(x, wq, K, V, wo, O, P, hg, lds);
  }
}

__global__ __launch_bounds__(256) void reduce_kernel(const u16* __restrict__ ws, u16* __restrict__ out) {
  const unsigned c = blockIdx.x * 256 + threadIdx.x;
  const unsigned lane = c & 63, t = c >> 6, j = t % 7, pm = (t / 7) & 15, grp = t / 112;
  const u32x4* p = reinterpret_cast<const u32x4*>(ws + P_OFF + (size_t)grp * 8 * P_ITEM) + (pm * 7 + j) * 64 + lane;
  u32x4 v[8];
#pragma unroll
  for (int h = 0; h < 8; ++h) v[h] = __builtin_nontemporal_load(p + h * (P_ITEM / 8));
  float a[8] = {};
#pragma unroll
  for (int h = 0; h < 8; ++h)
#pragma unroll
    for (int i = 0; i < 4; ++i) { a[2 * i] += __uint_as_float(v[h][i] << 16); a[2 * i + 1] += __uint_as_float(v[h][i] & 0xffff0000u); }
  const unsigned br = grp >> 4, b = grp & 15, g = lane >> 4, q = 16 * j + (lane & 15);
  u16* o = out + (size_t)(b * SQ + q) * (2 * DM) + br * DM + 32 * pm + 4 * g;
  u32x2 w0, w1;
  w0[0] = (unsigned)f2bf(a[0]) | ((unsigned)f2bf(a[1]) << 16); w0[1] = (unsigned)f2bf(a[2]) | ((unsigned)f2bf(a[3]) << 16);
  w1[0] = (unsigned)f2bf(a[4]) | ((unsigned)f2bf(a[5]) << 16); w1[1] = (unsigned)f2bf(a[6]) | ((unsigned)f2bf(a[7]) << 16);
  *reinterpret_cast<u32x2*>(o) = w0;
  *reinterpret_cast<u32x2*>(o + 16) = w1;
}

extern "C" void kernel_launch(void* const* d_in, const int* in_sizes, int n_in, void* d_out, int out_size, void* d_ws, size_t ws_size,
                              hipStream_t stream) {
  (void)in_sizes; (void)n_in; (void)out_size; (void)ws_size;
  u16* ws = (u16*)d_ws;
  (void)hipFuncSetAttribute((const void*)attn_kernel, hipFuncAttributeMaxDynamicSharedMemorySize, LDS1);
  attn_kernel<<<256, 256, LDS1, stream>>>((const u16*)d_in[0], (const u16*)d_in[1], (const u16*)d_in[2], (const u16*)d_in[3], (const u16*)d_in[4],
                                          (const u16*)d_in[5], (const u16*)d_in[6], (const u16*)d_in[7], (const u16*)d_in[8], (const u16*)d_in[9], ws);
  reduce_kernel<<<32 * 16 * 7 * 64 / 256, 256, 0, stream>>>(ws, (u16*)d_out);
}
```
